```python
import torch

def _softmax(x, axis=-1):
    return torch.softmax(x, dim=axis)
def _gelu(x):
    return torch.nn.functional.gelu(x, approximate="tanh")
def _silu(x):
    return torch.nn.functional.silu(x)
def _sigmoid(x):
    return torch.sigmoid(x)
def _layernorm(x, axis=-1, eps=1e-5):
    m = x.mean(dim=axis, keepdim=True)
    v = x.var(dim=axis, keepdim=True, unbiased=False)
    return (x - m) / torch.sqrt(v + eps)
def _scatter_add(tgt, idx, src, axis=0):
    return tgt.clone().index_add_(axis, idx, src)
def _segment_sum(src, seg, n):
    out = torch.zeros((n,) + src.shape[1:], dtype=src.dtype)
    return out.index_add_(0, seg, src)
def _pad(x, pw):
    flat = tuple(v for pair in reversed(pw) for v in pair)
    return torch.nn.functional.pad(x, flat)

kernel_name = 'recurrent_scan__sliding_block_cached__moe_arch_mod__prompt'

def setup_inputs():

    _w = (lambda a: (lambda t: t.to(torch.bfloat16) if t.is_floating_point() else t)(torch.as_tensor(a)))
    def _A_setup():
        B, Sq, D = 1, 2048, 640
        Hq, Hkv, Sk, Dh = 32, 32, 2048, 192
        assert Hq % Hkv == 0, (Hq, Hkv)
        return {
            "x": _w((lambda *s: torch.randn(*s, dtype=torch.bfloat16) * 0.05)(B, Sq, D)),
            "k_cache": _w((lambda *s: torch.randn(*s, dtype=torch.bfloat16) * 0.8)(B, Hkv, Sk, Dh)),
            "v_cache": _w((lambda *s: torch.randn(*s, dtype=torch.bfloat16) * 0.2)(B, Hkv, Sk, Dh)),
            "wq": _w((lambda *s: torch.randn(*s, dtype=torch.bfloat16) * 0.8)(D, Hq * Dh) * (D ** -0.5)),
            "wo": _w((lambda *s: torch.randn(*s, dtype=torch.bfloat16) * 0.2)(Hq * Dh, D) * ((Hq * Dh) ** -0.5)),
        }
    _A_in = _A_setup()

    def _B_setup():
        B, S, D, F, C = 1, 2048, 640, 1536, 2
        return {
            "x": _w((lambda *s: torch.randn(*s, dtype=torch.bfloat16) * 0.05)(B, S, D)),
            "w_r": _w((lambda *s: torch.randn(*s, dtype=torch.bfloat16) * 0.2)(D)),
            "w_gate": _w((lambda *s: torch.randn(*s, dtype=torch.bfloat16) * 0.1)(D, F)),
            "w_up": _w((lambda *s: torch.randn(*s, dtype=torch.bfloat16) * 0.1)(D, F)),
            "w_down": _w((lambda *s: torch.randn(*s, dtype=torch.bfloat16) * 0.1)(F, D)),
        }
    _B_in = _B_setup()

    return {
        "A_x": _A_in["x"],
        "A_k_cache": _A_in["k_cache"],
        "A_v_cache": _A_in["v_cache"],
        "A_wq": _A_in["wq"],
        "A_wo": _A_in["wo"],
        "B_x": _B_in["x"],
        "B_w_r": _B_in["w_r"],
        "B_w_gate": _B_in["w_gate"],
        "B_w_up": _B_in["w_up"],
        "B_w_down": _B_in["w_down"],
    }

def reference(A_x, A_k_cache, A_v_cache, A_wq, A_wo, B_x, B_w_r, B_w_gate, B_w_up, B_w_down):
    def _A_ref(x, k_cache, v_cache, wq, wo):
        B, Sq, D = x.shape
        Hq, Dh = 32, 192
        Hkv, Sk = k_cache.shape[1], k_cache.shape[2]
        W = min(4096, max(256, Sk // 4))
        rep = Hq // Hkv
        q = (lambda x, *d: x.permute(*d))((x @ wq).reshape(B, Sq, Hq, Dh), 0, 2, 1, 3)
        k = (lambda x, n, axis: torch.repeat_interleave(x, n, dim=axis))(k_cache[:, :, Sk - W:, :], rep, axis=1)
        v = (lambda x, n, axis: torch.repeat_interleave(x, n, dim=axis))(v_cache[:, :, Sk - W:, :], rep, axis=1)
        scores = torch.einsum("bhqd,bhkd->bhqk", q, k) * (Dh ** -0.5)
        p = _softmax(scores, axis=-1)
        o = torch.einsum("bhqk,bhkd->bhqd", p, v)
        return (lambda x, *d: x.permute(*d))(o, 0, 2, 1, 3).reshape(B, Sq, Hq * Dh) @ wo

    def _B_ref(x, w_r, w_gate, w_up, w_down):
        B, S, D = x.shape
        C = 2
        r = x @ w_r
        idx = torch.argsort(-r, axis=-1)[:, :C]
        idx_e = (lambda x, n, axis: torch.repeat_interleave(x, n, dim=axis))(idx.reshape(B, C, 1), D, axis=2)
        x_sel = (lambda x, i, axis: torch.take_along_dim(x, i, dim=axis))(x, idx_e, axis=1)
        g = _silu(x_sel @ w_gate)
        u = x_sel @ w_up
        y = (g * u) @ w_down
        r_sel = _sigmoid((lambda x, i, axis: torch.take_along_dim(x, i, dim=axis))(r, idx, axis=1))
        y = y * r_sel.reshape(B, C, 1)
        b_off = (torch.arange(B) * S).reshape(B, 1)
        flat_idx = (idx + b_off).reshape(B * C)
        zero = (x * 0.0).reshape(B * S, D)
        upd = _scatter_add(zero, flat_idx, y.reshape(B * C, D), axis=0)
        return x + upd.reshape(B, S, D)

    x = A_x
    xn = _layernorm(x, axis=-1)
    s = _A_ref(xn, A_k_cache, A_v_cache, A_wq, A_wo)
    g = _silu(_B_ref(xn, B_w_r, B_w_gate, B_w_up, B_w_down))
    return x + s * g
```

```cpp
#include <hip/hip_runtime.h>
#include <stdint.h>

typedef unsigned short u16;
typedef unsigned u32x2 __attribute__((ext_vector_type(2)));
typedef unsigned u32x4 __attribute__((ext_vector_type(4)));
typedef unsigned u32x8 __attribute__((ext_vector_type(8)));
typedef unsigned u32x16 __attribute__((ext_vector_type(16)));
typedef unsigned u32x32 __attribute__((ext_vector_type(32)));
typedef float f32x16 __attribute__((ext_vector_type(16)));
typedef float f32x4 __attribute__((ext_vector_type(4)));
typedef float f32x2 __attribute__((ext_vector_type(2)));
typedef __bf16 bf16x2 __attribute__((ext_vector_type(2)));
typedef __amdgpu_buffer_rsrc_t rsrc_t;
#define NTP 0
#define NT0 ""
#define NT1 ""
#define NT2 ""
#define NT3 ""
#define NTC ""
#define NTX ""
#define CPFV 1
#define CPFADDR ((bp >> 3) * 4 + w) * 128u + l * 16384u
#define BSW 0
#define TCH 0
#define PRE 0
#define CPFP 0
#define CP1 0
#define CPFN 0
#define W10 0
#define ORD10 0
#define EPZ 0
#define STCH 0
#define EPL EPZ
#define DI __device__ __forceinline__
#define XPOS 2
#define CHP 0
#define XSU(r, g) (((r) & 12) * 4 + ((g) & 2) * 4 + ((r) & 2) * 2 + ((g) & 1) * 2 + ((r) & 1))
#define OP2 1
#define FWA 1
#define STW 1
#define W1POS 1
#define X1POS 2
#define LNP 0
#define PAD(n, m, v)
#define WRL 0
#define WW0 12
#define XWAIT "s_waitcnt vmcnt(17+" STR(CPFN) ")"
#define STR_(x) #x
#define STR(x) STR_(x)
#define WWAIT "s_waitcnt vmcnt(" STR(WW0) ")"
#define G0 4u
#define RQ0 0
#define RQ1 0
#define ROTW(r) (((r) + RQ1 * ((b >> 3) & 3)) % 6u)
#define TIDX(J) (G0 == 1 ? (J) : (rb + RQ0 * (b >> 3) + (J)) & (G0 - 1))
#define FENCE() __builtin_amdgcn_sched_barrier(0)
#define NTR ""

constexpr int S_ = 2048, D_ = 640, H_ = 32, DH = 192, SK = 2048, WIN = 512, F_ = 1536;
constexpr unsigned WS_XS = 0;
constexpr unsigned WS_O = 4u << 20;
constexpr unsigned WS_WOT = 32u << 20;
constexpr unsigned WS_STAT = 44u << 20;
constexpr unsigned WS_U = WS_STAT + 0x20000;
constexpr unsigned WS_YP = 45u << 20;
constexpr unsigned WS_GP = 46u << 20;
constexpr unsigned WS_EP = 47u << 20;
constexpr unsigned WS_FLN = WS_EP + 0x10000, WS_FWO = WS_EP + 0x20000, WS_FO = WS_EP + 0x30000, WS_FM1 = WS_EP + 0x40000, WS_FM2 = WS_EP + 0x50000;
constexpr unsigned FS = 128;
constexpr unsigned WS_FX = WS_EP + 0x60000, WS_KEYS = WS_EP + 0x8000;
constexpr unsigned WS_EX = 48u << 20;
constexpr unsigned WS_SLOW = 72u << 20;
#define WSOFF 0
constexpr unsigned WS_SRD = (WSOFF ? 60u : 192u) << 20;
constexpr unsigned OOB = 0x7ffffff0u;
constexpr unsigned KSCR = 73728, XST = 98304;

DI float bf2f(u16 v) { return __uint_as_float((unsigned)v << 16); }
DI unsigned pk2(float lo, float hi) { f32x2 v = {lo, hi}; bf16x2 b = __builtin_convertvector(v, bf16x2); return __builtin_bit_cast(unsigned, b); }
DI float rbf(float x) { return __uint_as_float(pk2(x, 0.f) << 16); }
DI float lo16(unsigned u) { return __uint_as_float(u << 16); }
DI float hi16(unsigned u) { return __uint_as_float(u & 0xffff0000u); }
DI float dot2(unsigned a, unsigned b, float c) { return __builtin_amdgcn_fdot2_f32_bf16(__builtin_bit_cast(bf16x2, a), __builtin_bit_cast(bf16x2, b), c, false); }
DI float silu_f(float x) { return x / (1.f + __expf(-x)); }
DI rsrc_t mk(const void* p, unsigned n) { return __builtin_amdgcn_make_buffer_rsrc((void*)p, 0, n, 0x00020000); }
DI unsigned ld1(rsrc_t r, unsigned off) { return __builtin_amdgcn_raw_buffer_load_b32(r, off, 0, 16); }
DI u32x4 ld4(rsrc_t r, unsigned off) { return __builtin_amdgcn_raw_buffer_load_b128(r, off, 0, 16); }
DI void st1(rsrc_t r, unsigned off, unsigned v) { __builtin_amdgcn_raw_buffer_store_b32(v, r, off, 0, 16); }
DI void st4(rsrc_t r, unsigned off, u32x4 v) { __builtin_amdgcn_raw_buffer_store_b128(v, r, off, 0, 16); }
DI unsigned sc(unsigned v) { v = __builtin_amdgcn_readfirstlane(v); asm("" : "+s"(v)); return v; }
DI u32x4 s4(unsigned a, unsigned b, unsigned c, unsigned d) { u32x4 r = {sc(a), sc(b), sc(c), sc(d)}; return r; }
DI u32x4 mk4(const void* p, unsigned n) { const unsigned long long a = (unsigned long long)p; return s4((unsigned)a, (unsigned)(a >> 32) & 0xffffu, n, 0x00020000u); }
DI void drain() { asm volatile("s_waitcnt vmcnt(0)" ::: "memory"); }
DI void wgbar() { asm volatile("s_waitcnt lgkmcnt(0)\n\ts_barrier" ::: "memory"); }
DI void wait_flags(rsrc_t r, unsigned off, unsigned tag) {
  const unsigned want = off == OOB ? 0u : tag;
  for (unsigned spins = 0; spins < 0x10000u; ++spins) {
    asm volatile("" ::: "memory");
    const unsigned v = ld1(r, off);
    if (__builtin_amdgcn_ballot_w64(v != want) == 0) break;
    __builtin_amdgcn_s_sleep(1);
  }
  asm volatile("" ::: "memory");
}
template <int C> DI unsigned dppu(unsigned v) { return __builtin_amdgcn_update_dpp(0u, v, C, 0xf, 0xf, true); }
template <int C> DI float dppf(float v) { return __uint_as_float(dppu<C>(__float_as_uint(v))); }
DI float add16(float v) { const auto r = __builtin_amdgcn_permlane16_swap(__float_as_uint(v), __float_as_uint(v), false, false); return __uint_as_float(r[0]) + __uint_as_float(r[1]); }
DI float add32(float v) { const auto r = __builtin_amdgcn_permlane32_swap(__float_as_uint(v), __float_as_uint(v), false, false); return __uint_as_float(r[0]) + __uint_as_float(r[1]); }
DI float sum32(float v) { v += dppf<0x4E>(v); v += dppf<0x124>(v); v += dppf<0x128>(v); return add32(add16(v)); }
DI void top2(unsigned& a0, unsigned& a1, unsigned b0, unsigned b1) {
  const unsigned lo = min(a0, b0); a0 = max(a0, b0); a1 = max(lo, max(a1, b1));
}

constexpr float QSCALE = 0.10411759449f;

DI void attn_slow(const u16* xs, const u16* wq, const u16* kc, const u16* vc, float* scr, int h, int row) {
  __builtin_amdgcn_fence(__ATOMIC_ACQUIRE, "agent");
  float* q = scr + ((size_t)row * H_ + h) * (DH + WIN);
  float* sc = q + DH;
  for (int d = 0; d < DH; ++d) {
    float a = 0.f;
    for (int k = 0; k < D_; ++k)
      a += bf2f(xs[(size_t)(((row >> 4) * 20 + (k >> 5)) * 64 + XSU(row, (k >> 3) & 3)) * 8 + (k & 7)]) * bf2f(wq[(size_t)k * (H_ * DH) + h * DH + d]);
    q[d] = rbf(a);
  }
  const u16* kh = kc + ((size_t)h * SK + (SK - WIN)) * DH;
  const u16* vh = vc + ((size_t)h * SK + (SK - WIN)) * DH;
  float m = -INFINITY;
  for (int j = 0; j < WIN; ++j) {
    float a = 0.f;
    for (int d = 0; d < DH; ++d) a += q[d] * bf2f(kh[j * DH + d]);
    sc[j] = a; m = fmaxf(m, a);
  }
  float l = 0.f;
  for (int j = 0; j < WIN; ++j) { const float p = exp2f(sc[j] - m); l += p; sc[j] = rbf(p); }
  const float il = 1.f / l;
  for (int d = 0; d < DH; d += 2) {
    float a = 0.f, c = 0.f;
    for (int j = 0; j < WIN; ++j) { const unsigned v = *(const unsigned*)(vh + j * DH + d); a += sc[j] * lo16(v); c += sc[j] * hi16(v); }
    q[d] = a * il; q[d + 1] = c * il;
  }
}

#define CLOB_V "v64","v65","v66","v67","v68","v69","v70","v71","v72","v73","v74","v75","v76","v77","v78","v79","v80","v81","v82","v83","v84","v85","v86","v87","v88","v89","v90","v91","v92","v93","v94","v95","v96","v97","v98","v99","v100","v101","v102","v103","v104","v105","v106","v107","v108","v109","v110","v111","v112","v113","v114","v115","v116","v117","v118","v119","v120","v121","v122","v123","v124","v125","v126","v127"
#define DMA_W(J) DMA_WX(J, NT##J)
#define DMA_WX(J, NTF) asm volatile( \
    "s_add_u32 m0, %0, %5\n\ts_nop 0\n\t" \
    "buffer_load_dwordx4 %1, %4, %7 offen" NTF " lds\n\tbuffer_load_dwordx4 %2, %4, %7 offen offset:1024" NTF " lds\n\tbuffer_load_dwordx4 %3, %4, %7 offen offset:2048" NTF " lds\n\t" \
    "s_add_u32 m0, %0, %6\n\ts_add_u32 s34, %7, 98304\n\t" \
    "buffer_load_dwordx4 %1, %4, s34 offen" NTF " lds\n\tbuffer_load_dwordx4 %2, %4, s34 offen offset:1024" NTF " lds\n\tbuffer_load_dwordx4 %3, %4, s34 offen offset:2048" NTF " lds" \
    :: "s"(sM0), "v"(vWd[0]), "v"(vWd[1]), "v"(vWd[2]), "{s[44:47]}"(rwq), "i"((J) * 24576), "i"((J) * 24576 + 3072), \
       "s"(sc(TIDX(J) * 786432u)) : "s34", "scc", "memory")
#define DMA_X1(J, Q) asm volatile( \
    "s_add_u32 m0, %0, %4\n\ts_nop 0\n\t" \
    "buffer_load_dwordx4 %1, %3, %5 offen" NTR " lds\n\tbuffer_load_dwordx4 %2, %3, %5 offen offset:1024" NTR " lds" \
    :: "s"(sMx), "v"(vXr[0]), "v"(vXr[1]), "{s[56:59]}"(rxr), "i"((J) * 32768 + (Q) * 2048), "s"(sc(TIDX(J) * 128u + (xq ^ (Q)) * 20480u)) : "scc", "memory")
#define LOAD_WR() asm volatile( \
    "buffer_load_dwordx4 %0, %3, %4, 0 offen\n\tbuffer_load_dwordx4 %1, %3, %4, 0 offen offset:512\n\tbuffer_load_dwordx4 %2, %3, %4, 0 offen offset:1024" \
    : "=&v"(wv[0]), "=&v"(wv[1]), "=&v"(wv[2]) : "v"(sub * 16u), "s"(mk(wr, D_ * 2)) : "memory")
#define DMA_X(J) DMA_X1(J, 0); DMA_X1(J, 1); DMA_X1(J, 2); DMA_X1(J, 3)

#define BSCALARS \
  const unsigned kqb = (b >> 1) & 3, nq = (b >> 3) & 3, par = kqb & 1, hf = kqb >> 1, hh = (b & 1) * 4 + nq; \
  const unsigned sSrc = WS_WOT + kqb * 48 * 40960u + nq * 10240u, r0 = (b >> 5) * 256 + w * 64 + (b & 1) * 32, c0 = nq * 10 + (OP2 ? 2 * (kqb + hf) : 5 * hf); \
  const unsigned sM5 = ldsb + w * 5120u, sOst = WS_O + ((b >> 5) * 16 + w * 4) * 196608u + (kqb * 8 + hh) * 6144u;

struct Tab { unsigned v[1024][32]; };
constexpr Tab make_tab() {
  Tab t = {};
  for (unsigned b = 0; b < 256; ++b)
    for (unsigned w = 0; w < 4; ++w) {
      const unsigned ldsb = 0, h = (b & 7) * 4 + ((b >> 3) & 3), rb = b >> 5, xq = (b & 1) * 2 + ((b >> 1) & 1);
      const unsigned wi = b * 4 + w, wp = wi < 960 ? wi : 959u;
      const unsigned fc = b % 24u, kq1 = b / 24u, kq1c = kq1 < 10 ? kq1 : 9u;
      const unsigned cb = wi / 48u, fs = wi % 48u, cbc = cb < 10 ? cb : 9u;
      const unsigned t3 = wi / 40u, nb3 = wi % 40u;
      BSCALARS
      unsigned tab[2] = {0u, 0u};
      for (unsigned j = 0; j < 8; ++j) {
        const unsigned nbg = (wp % 5u) * 8u + j, n5 = nbg / 5u, nb = OP2 ? (0x3410223410ull >> (4 * (nbg % 10u))) & 15u : nbg % 5u, d = wp / 240u, jj = (d & 1) ? 4 - nb : nb;
        tab[j >> 2] |= ((n5 >> 1) * 10u + ((n5 & 1) == (d >> 1) ? 9 - jj : jj)) << (8 * (j & 3));
      }
      const unsigned e[32] = {
        (wp / 5u) * 40960u + (wp % 5u) * 256u, (h * 2048u + 1536u) * 384u + w * 6144u, (kq1c * 64u + (w & 1) * 32u) * 3072u + fc * 128u, r0 * 1280u + c0 * 32u,
        sOst, sSrc + (hh * 6u + (w >> 1)) * 40960u + (w & 1) * 5120u, wi < 80 ? WS_YP + t3 * 2560u + nb3 * 64u : 0x70000000u, wi < 80 ? WS_FM2 + (nb3 >> 2) * 12u * FS : WS_FLN,
        tab[0], wi < 80 ? WS_U + t3 * 2048u + nb3 * 32u : 0x70000000u, sM5, tab[1],
        0u, w == 0 ? (FWA ? 15u : 1u) : 0u, (WS_XS + (rb * 16 + w * 4) * 20480u) | xq | TIDX(0) << 2, w * 6144u,
        fs * 40960u + cbc * 128u, par, 147456u + w * 4096u, WS_EP + b * FS,
        wi < 960 ? WS_WOT + (wi / 5u) * 40960u : 0x70000000u, WS_XS + (kq1c * 2 + (w & 1)) * 1024u,
        kq1 < 10 ? WS_GP + (((kq1 * 2u + (w & 1)) * 2u + (w >> 1)) * 1536u + fc * 64u) * 8u : 0u, WS_GP + fs * 256u,
        cb < 10 ? WS_YP + fs * 5120u + cb * 256u : 0u, WS_FWO + (kqb * 60u + 0 * w * 15u) * FS, WS_FM1 + (fs >> 1) * 10u * FS, WS_FLN + (rb * 32 + w * 8) * FS,
        WS_FM1 + (kq1 < 10 ? fc * 10u + kq1 : b) * FS, WS_FLN + (rb * 32 + h) * FS, ROTW(rb) * 2048u, WS_STAT + r0 * (16u << STW)};
      for (int i = 0; i < 32; ++i) t.v[wi][i] = e[i];
    }
  return t;
}
#define TABSEC 0
static __device__ const __attribute__((aligned(128))) Tab TAB = make_tab();

__global__ __launch_bounds__(256, 1) __attribute__((aligned(4096))) void kf_fused(const u16* __restrict__ x_, char* ws_, const u16* __restrict__ wq_, const u16* __restrict__ wr_,
                                                   const u16* __restrict__ kc_, const u16* __restrict__ vc_, const u16* __restrict__ wo_, const u16* __restrict__ wg_,
                                                   const u16* __restrict__ wu_, const u16* __restrict__ wd_, u16* __restrict__ out_) {
  extern __shared__ __attribute__((aligned(16))) char lds[];
  u32x8 ka, kb; u32x4 kd; u32x2 ke;
  asm volatile("s_load_dwordx8 %0, %4, 0x0\n\ts_load_dwordx8 %1, %4, 0x20\n\ts_load_dwordx4 %2, %4, 0x40\n\ts_load_dwordx2 %3, %4, 0x50"
               : "=&{s[8:15]}"(ka), "=&{s[16:23]}"(kb), "=&{s[24:27]}"(kd), "=&{s[38:39]}"(ke) : "s"(__builtin_amdgcn_kernarg_segment_ptr()));
#define ARG(v, i) ((const char*)(((unsigned long long)v[i + 1] << 32) | v[i]))
#define x ARG(ka, 0)
#define ws ARG(ka, 2)
#define wq ARG(ka, 4)
#define wr ARG(ka, 6)
  const unsigned bp = __builtin_amdgcn_workgroup_id_x();
  unsigned b = bp;
  constexpr unsigned ldsb = 0;
  unsigned tid = __builtin_amdgcn_workitem_id_x();
  unsigned w = __builtin_amdgcn_readfirstlane(tid >> 6);
  unsigned T; rsrc_t rws;

  {
    const unsigned h = (b & 7) * 4 + ((b >> 3) & 3), rb = b >> 5, l = tid & 63;
    unsigned sM0 = w * 6144u;
    unsigned long long pc; unsigned len;
    asm volatile("s_getpc_b64 %0\nKF_START:\n\ts_mov_b32 %1, KF_END-KF_START" : "=s"(pc), "=s"(len));
#define CPFX(A) { \
      const rsrc_t rc = mk((const void*)pc, len); \
      asm volatile("s_add_u32 m0, %2, %3\n\ts_nop 0\n\tbuffer_load_dword %0, %1, 0 offen" NTC " lds" :: "v"(A), "s"(rc), "s"(sM0), "i"(KSCR + 1024) : "scc", "memory"); \
    }
#define CPF() CPFX(CPFADDR)
    CPF();
    FENCE();
    const unsigned row = rb * 256 + h * 8 + w * 2 + (l & 1), sub = l >> 1;
    u32x4 xv[3], wv[3]; unsigned ep;
#define XLOAD() { \
      const u32x4 rx = mk4(x, S_ * D_ * 2); \
      asm volatile( \
        "buffer_load_dwordx4 %0, %4, %6, 0 offen" NTX "\n\tbuffer_load_dwordx4 %1, %4, %6, 0 offen offset:512" NTX "\n\tbuffer_load_dwordx4 %2, %5, %6, 0 offen offset:1024" NTX "\n\t" \
        EPLOAD \
        : "=&v"(xv[0]), "=&v"(xv[1]), "=&v"(xv[2]), "=&v"(ep) : "v"(vx), "v"(vx2), "s"(rx), "{s[40:43]}"(r0_), EPADDR : "memory"); \
    }
#define EPLOAD "buffer_load_dword %3, off, %7, %8 sc1"
#define EPADDR "s"(WS_EP)
    unsigned vx = __umul24(row, 1280u) + sub * 16u, vx2 = sub < 16 ? vx : OOB;
    unsigned vXr[2];
    unsigned sWb = h * 384u + w * 16u * 12288u, sMx = XST + w * 8192u, xro = (rb * 256 + w * 64) * 1280u;
    unsigned xq = (b & 1) * 2 + ((b >> 1) & 1);
#pragma unroll
    for (unsigned e = 0; e < 2; ++e) vXr[e] = (e * 8 + (l >> 3)) * 1280u + (((l & 7) ^ (e * 4 + (l >> 4))) << 4) - 1024u * e;
    unsigned vWd[3], vKd[3];
#pragma unroll
    for (unsigned j = 0; j < 3; ++j) {
      const unsigned P = 64 * j + l, r = __umul24(P, 171u) >> 12, c = P - __umul24(r, 24u);
      vWd[j] = __umul24(r, 12288u) + ((c ^ (((r >> 1) & 1u) << 2)) << 4) - 1024u * j;
      vKd[j] = __umul24(r, 384u) + ((c ^ (r & 6u)) << 4) - 1024u * j;
    }
    asm volatile("" : "+v"(vWd[0]), "+v"(vWd[1]), "+v"(vWd[2]), "+v"(vx), "+v"(vx2), "+v"(vXr[0]), "+v"(vXr[1]));
    const unsigned* ptab = TAB.v[b * 4 + w];
    asm volatile("" : "+s"(sWb), "+s"(sM0), "+s"(sMx), "+s"(xro), "+s"(xq), "+s"(ptab));
    FENCE();
    asm volatile("s_waitcnt lgkmcnt(0)" : "+{s[8:15]}"(ka), "+{s[16:23]}"(kb), "+{s[24:27]}"(kd), "+{s[38:39]}"(ke));

    u32x4 r0_ = mk4(ws, WS_SRD);
    XLOAD();
    u32x16 qa, qb;
    asm volatile("s_load_dwordx16 %0, %2, 0x0\n\ts_load_dwordx16 %1, %2, 0x40" : "=&{s[68:83]}"(qa), "=&{s[84:99]}"(qb) : "s"(ptab));
    FENCE();
#define CHAIN() asm volatile("s_mov_b64 exec, 0\n\ts_cmp_eq_u32 %0, 0\n\ts_cbranch_scc1 KFP1\n\ts_cmp_eq_u32 %0, 1\n\ts_cbranch_scc1 KFP2\n\ts_cmp_eq_u32 %0, 2\n\ts_cbranch_scc1 KFP3\n\ts_cmp_eq_u32 %0, 3\n\ts_cbranch_scc1 KFP4\n\tKFR1:\n\ts_mov_b64 exec, -1" :: "s"(w) : "scc")
    u32x4 rwq = mk4(wq + sWb, D_ * H_ * DH * 2 - sWb);
    DMA_W(0);
    FENCE();
    u32x4 rxr = mk4(x + xro, S_ * D_ * 2 - xro);
    FENCE();

    rws = mk(ws, WS_SRD);
    const unsigned g = l >> 4, qq = (l >> 2) & 3, p = l & 3, r16 = l & 15;
    const unsigned vXs = sMx + r16 * 128u + ((g ^ (r16 >> 1)) << 4);
    unsigned vWe[2], vKe[2], vVe[4];
#pragma unroll
    for (unsigned e = 0; e < 2; ++e) {
      vWe[e] = ldsb + (8 * g + qq) * 384u + (((e ^ (qq >> 1)) * 4 + p) << 4);
      vKe[e] = ldsb + r16 * 384u + (((4 * e + g) ^ (r16 & 6u)) << 4);
    }
#pragma unroll
    for (unsigned e = 0; e < 4; ++e) vVe[e] = ldsb + (4 * g + qq) * 384u + (((2 * e + (p >> 1)) ^ ((4 * g + qq) & 6u)) << 4) + (p & 1) * 8;
    unsigned und; asm volatile("" : "=v"(und));
    u32x16 vi = {l * 16u, vWd[0], vWd[1], vWd[2], vKd[0], vKd[1], vKd[2], vWe[0], vWe[1], vKe[0], vKe[1], vVe[0], vVe[1], vVe[2], vVe[3], und};
    u32x4 vx4 = {vXr[0], vXr[1], vXs, vXs ^ 64u};
    unsigned vso = WS_XS + (((row >> 4) * 20 + (sub >> 2)) * 64 + XSU(row, sub & 3)) * 16, vso1 = vso + 8192u, vso2 = sub < 16 ? vso + 16384u : OOB, vst = STW ? (sub < 2 ? WS_STAT + row * 32 + sub * 16 : OOB - 0x10000u) : sub == 0 ? WS_STAT + row * 16 : OOB;
    unsigned vkey = KSCR + 4 * (sub == 0 ? w * 2 + l : 64 + l), vrow = 0xffffu - row;
    unsigned vfl = 2 == 2 ? (tid < 16 ? tid * 4u : OOB) : tid < 4 ? tid * 16u : OOB;
#define CARRYV "+{v[4:19]}"(vi), "+{v[20:23]}"(vx4), "+{v2}"(vfl)
#define CARRY CARRYV, "+{s[56:59]}"(rxr), "+{s[38:39]}"(ke), "+{s[40:43]}"(r0_), "+{s[44:47]}"(rwq), "+{s[68:83]}"(qa), "+{s[84:99]}"(qb)
    asm volatile("" : CARRYV, "+v"(vso), "+v"(vso1), "+v"(vso2), "+v"(vst), "+v"(vkey), "+v"(vrow));
    vWd[0] = vi[1]; vWd[1] = vi[2]; vWd[2] = vi[3]; vXr[0] = vx4[0]; vXr[1] = vx4[1];
    FENCE();
    CHAIN();

    LOAD_WR();
    DMA_X(0);
    asm volatile(XWAIT : "+v"(xv[0]), "+v"(xv[1]), "+v"(xv[2]), "+v"(ep) :: "memory");

    T = __builtin_amdgcn_readfirstlane(ep) * 0x9E3779B1u + 0x7F4A7C15u;
    {
      f32x2 xf[12];
      float s = 0.f, s2 = 0.f;
#pragma unroll
      for (int i = 0; i < 12; ++i) {
        const unsigned u = xv[i >> 2][i & 3];
        if (i & 1) s2 = dot2(u, 0x3f803f80u, s2); else s = dot2(u, 0x3f803f80u, s);
        xf[i] = f32x2{lo16(u), hi16(u)};
        if (i == 4) PAD(0, 4, s);
        if (i == 10) PAD(1, 5, s);
      }
      s = sum32(s + s2);
      PAD(2, 6, s);
      const float mean = s * (1.f / 640.f);
      f32x2 q = {0.f, 0.f}, q2 = {0.f, 0.f}, q3_ = {0.f, 0.f};
#pragma unroll
      for (int e = 0; e < 4; ++e) {
        const f32x2 a = xf[e] - mean, c = xf[4 + e] - mean, d = xf[8 + e] - mean;
        q += a * a; q2 += c * c; q3_ += d * d;
        if (e == 1) PAD(3, 7, q);
        if (e == 3) PAD(4, 8, q2);
      }
      q += q2;
      const float mb = rbf(mean);
      unsigned cc[12];
#pragma unroll
      for (int i = 0; i < 12; ++i) { const f32x2 a = xf[i] - mb; cc[i] = pk2(a[0], a[1]);
        if (i == 3) PAD(5, 9, cc[i]);
        if (i == 7) PAD(6, 10, cc[i]);
        if (i == 11) PAD(7, 11, cc[i]);
        xf[i] = f32x2{lo16(cc[i]), hi16(cc[i])}; }
      const float qs = sum32(q[0] + q[1] + (sub < 16 ? q3_[0] + q3_[1] : 0.f));
      float vb = rbf(qs * (1.f / 640.f));
      PAD(8, 12, vb);
      const float den = rbf(__builtin_amdgcn_sqrtf(rbf(vb + 1e-5f))), rd = __builtin_amdgcn_rcpf(den), rq = rd * QSCALE;
      u32x4 o[3];
#pragma unroll
      for (int i = 0; i < 12; ++i) { const f32x2 a = xf[i] * rq; unsigned t = pk2(a[0], a[1]);
        if (i == 2) PAD(9, 13, t);
        if (i == 9) PAD(10, 14, t);
        o[i >> 2][i & 3] = t; }
      st4(rws, vso, o[0]); st4(rws, vso1, o[1]); st4(rws, vso2, o[2]);
      st4(rws, vst, u32x4{__float_as_uint(rq), __float_as_uint(-mb * rq), und, und});

      asm volatile(WWAIT : "+v"(wv[0]), "+v"(wv[1]), "+v"(wv[2]), "+v"(ep) :: "memory");
      float r = 0.f, r2 = 0.f;
#pragma unroll
      for (int i = 0; i < 12; ++i) {
        const f32x2 a = xf[i] * rd; const unsigned nn = pk2(a[0], a[1]);
        if (i & 1) r2 = dot2(nn, wv[i >> 2][i & 3], r2); else r = dot2(nn, wv[i >> 2][i & 3], r);
        if (i == 0) PAD(11, 15, r);
        if (i == 5) PAD(12, R, r2);
        if (i == 10) PAD(13, R, r);
      }
      r = sum32(r + r2);
      PAD(14, R, r);
      const unsigned rbits = pk2(r, 0.f) & 0xffffu;
      st4(rws, vst + 0x10000u, u32x4{rbits << 16, pk2(mb, den), und, und});
      unsigned key = (((rbits & 0x8000u) ? (~rbits & 0xffffu) : (rbits | 0x8000u)) << 16) | vrow;
      PAD(15, R, key);
      *(unsigned*)(lds + vkey) = key;
    }
    DMA_W(1);

    unsigned res; u32x2 t0_; u32x8 t3_; u32x4 t1_, t2_; u32x32 k0, p0, p1, p2, p3, a0, a1, a2, a3, a4, a5, a6; u32x8 k1; u32x16 a7, xa;
#define KEPT(C) C "{s35}"(res), C "{s[36:37]}"(t0_), C "{s[48:55]}"(t3_), C "{s[60:63]}"(t1_), C "{s[64:67]}"(t2_), C "{v[56:63]}"(k1), C "{a[240:255]}"(xa)
#define REDO(C) C "{v[128:159]}"(p0), C "{v[160:191]}"(p1), C "{v[192:223]}"(p2), C "{v[224:255]}"(p3), C "{a[0:31]}"(a0), C "{a[32:63]}"(a1), C "{a[64:95]}"(a2), \
      C "{a[96:127]}"(a3), C "{a[128:159]}"(a4), C "{a[160:191]}"(a5), C "{a[192:223]}"(a6), C "{a[224:239]}"(a7)
    asm volatile(
    "KFA0:\n\t"
    "s_waitcnt lgkmcnt(0)\n\t"
    "s_mov_b32 s80, s28\n\t"
    "s_waitcnt vmcnt(17)\n\t"
    "ds_read_b128 a[0:3], v22 offset:0\n\t"
    "ds_read_b128 a[4:7], v23 offset:0\n\t"
    "s_waitcnt vmcnt(15)\n\t"
    "ds_read_b128 a[8:11], v22 offset:2048\n\t"
    "ds_read_b128 a[12:15], v23 offset:2048\n\t"
    "s_waitcnt vmcnt(13)\n\t"
    "ds_read_b128 a[16:19], v22 offset:4096\n\t"
    "ds_read_b128 a[20:23], v23 offset:4096\n\t"
    "s_waitcnt vmcnt(11)\n\t"
    "ds_read_b128 a[24:27], v22 offset:6144\n\t"
    "ds_read_b128 a[28:31], v23 offset:6144\n\t"
    "s_barrier\n\t"
    "ds_read_b64_tr_b16 a[96:97], v11 offset:0\n\t"
    "ds_read_b64_tr_b16 a[98:99], v11 offset:1536\n\t"
    "ds_read_b64_tr_b16 a[100:101], v11 offset:12288\n\t"
    "ds_read_b64_tr_b16 a[102:103], v11 offset:13824\n\t"
    "ds_read_b64_tr_b16 a[104:105], v11 offset:8\n\t"
    "ds_read_b64_tr_b16 a[106:107], v11 offset:1544\n\t"
    "ds_read_b64_tr_b16 a[108:109], v11 offset:12296\n\t"
    "ds_read_b64_tr_b16 a[110:111], v11 offset:13832\n\t"
    "ds_read_b64_tr_b16 a[112:113], v12 offset:0\n\t"
    "ds_read_b64_tr_b16 a[114:115], v12 offset:1536\n\t"
    "ds_read_b64_tr_b16 a[116:117], v12 offset:12288\n\t"
    "ds_read_b64_tr_b16 a[118:119], v12 offset:13824\n\t"
    "s_waitcnt lgkmcnt(10)\n\t"
    "v_mfma_f32_16x16x32_bf16 v[64:67], a[96:99], a[0:3], 0\n\t"
    "s_waitcnt lgkmcnt(8)\n\t"
    "s_add_u32 s61, s82, 4\n\t"
    "v_mov_b32 v60, s80\n\t"
    "v_mfma_f32_16x16x32_bf16 v[64:67], a[100:103], a[4:7], v[64:67]\n\t"
    "s_bfe_u32 s61, s61, 0x20002\n\t"
    "ds_read_b64_tr_b16 a[120:121], v12 offset:8\n\t"
    "v_mov_b32 v44, 0x3f803f80\n\t"
    "v_mfma_f32_16x16x32_bf16 v[72:75], a[96:99], a[8:11], 0\n\t"
    "s_mul_i32 s61, s61, 128\n\t"
    "ds_read_b64_tr_b16 a[122:123], v12 offset:1544\n\t"
    "v_accvgpr_write_b32 a164, v44\n\t"
    "v_mfma_f32_16x16x32_bf16 v[72:75], a[100:103], a[12:15], v[72:75]\n\t"
    "s_xor_b32 s60, s82, 0\n\t"
    "ds_read_b64_tr_b16 a[124:125], v12 offset:12296\n\t"
    "v_accvgpr_write_b32 a165, v44\n\t"
    "v_mfma_f32_16x16x32_bf16 v[80:83], a[96:99], a[16:19], 0\n\t"
    "s_and_b32 s60, s60, 3\n\t"
    "ds_read_b64_tr_b16 a[126:127], v12 offset:13832\n\t"
    "v_accvgpr_write_b32 a166, v44\n\t"
    "v_mfma_f32_16x16x32_bf16 v[80:83], a[100:103], a[20:23], v[80:83]\n\t"
    "s_mulk_i32 s60, 20480\n\t"
    "v_accvgpr_write_b32 a167, v44\n\t"
    "v_mov_b32 v52, 73728\n\t"
    "v_mfma_f32_16x16x32_bf16 v[88:91], a[96:99], a[24:27], 0\n\t"
    "s_add_u32 s60, s60, s61\n\t"
    "ds_read_b128 v[44:47], v52 offset:0\n\t"
    "v_lshlrev_b32 v61, 3, v4\n\t"
    "v_mfma_f32_16x16x32_bf16 v[88:91], a[100:103], a[28:31], v[88:91]\n\t"
    "s_waitcnt lgkmcnt(5)\n\t"
    "s_lshl1_add_u32 m0, s86, 4294803456\n\t"
    "ds_read_b128 v[48:51], v52 offset:16\n\t"
    "v_mfma_f32_16x16x32_bf16 v[68:71], a[104:107], a[0:3], 0\n\t"
    "buffer_load_dwordx4 v20, s[56:59], s60 offen offset:0 lds\n\t"
    "v_mfma_f32_16x16x32_bf16 v[68:71], a[108:111], a[4:7], v[68:71]\n\t"
    "s_bfe_u32 s67, s86, 0x2000c\n\t"
    "ds_read_b64_tr_b16 a[96:97], v11 offset:128\n\t"
    "v_add_u32 v24, 49152, v11\n\t"
    "v_mfma_f32_16x16x32_bf16 v[76:79], a[104:107], a[8:11], 0\n\t"
    "s_mul_i32 s67, s67, 3\n\t"
    "ds_read_b64_tr_b16 a[98:99], v11 offset:1664\n\t"
    "v_add_u32 v25, 49152, v12\n\t"
    "v_mfma_f32_16x16x32_bf16 v[76:79], a[108:111], a[12:15], v[76:79]\n\t"
    "s_add_u32 s66, s67, 0\n\t"
    "ds_read_b64_tr_b16 a[100:101], v11 offset:12416\n\t"
    "v_add_u32 v26, 98304, v11\n\t"
    "v_mfma_f32_16x16x32_bf16 v[84:87], a[104:107], a[16:19], 0\n\t"
    "s_lshl_b32 s63, s66, 5\n\t"
    "buffer_load_dwordx4 v21, s[56:59], s60 offen offset:1024 lds\n\t"
    "v_mfma_f32_16x16x32_bf16 v[84:87], a[108:111], a[20:23], v[84:87]\n\t"
    "s_xor_b32 s60, s82, 1\n\t"
    "ds_read_b64_tr_b16 a[102:103], v11 offset:13952\n\t"
    "v_add_u32 v27, 98304, v12\n\t"
    "v_mfma_f32_16x16x32_bf16 v[92:95], a[104:107], a[24:27], 0\n\t"
    "s_and_b32 s60, s60, 3\n\t"
    "ds_read_b64_tr_b16 a[104:105], v11 offset:136\n\t"
    "v_lshlrev_b32 v56, 1, v2\n\t"
    "v_mfma_f32_16x16x32_bf16 v[92:95], a[108:111], a[28:31], v[92:95]\n\t"
    "s_mulk_i32 s60, 20480\n\t"
    "ds_read_b64_tr_b16 a[106:107], v11 offset:1672\n\t"
    "v_mfma_f32_16x16x32_bf16 v[96:99], a[112:115], a[0:3], 0\n\t"
    "s_add_u32 s60, s60, s61\n\t"
    "ds_read_b64_tr_b16 a[108:109], v11 offset:12424\n\t"
    "v_mfma_f32_16x16x32_bf16 v[96:99], a[116:119], a[4:7], v[96:99]\n\t"
    "KFP1:\n\t"
    "s_cbranch_execz KFP5\n\t"
    "s_lshl1_add_u32 m0, s86, 4294805504\n\t"
    "s_nop 0\n\t"
    "buffer_load_dwordx4 v20, s[56:59], s60 offen offset:0 lds\n\t"
    "v_mfma_f32_16x16x32_bf16 v[104:107], a[112:115], a[8:11], 0\n\t"
    "s_and_b32 s65, s63, 64\n\t"
    "ds_read_b64_tr_b16 a[110:111], v11 offset:13960\n\t"
    "v_xor_b32 v37, s65, v11\n\t"
    "v_mfma_f32_16x16x32_bf16 v[104:107], a[116:119], a[12:15], v[104:107]\n\t"
    "s_and_b32 s63, s63, 0x180\n\t"
    "v_cmp_gt_u32 vcc, 8, v2\n\t"
    "s_nop 1\n\t"
    "v_cndmask_b32 v56, -1, v56, vcc\n\t"
    "v_mfma_f32_16x16x32_bf16 v[112:115], a[112:115], a[16:19], 0\n\t"
    "s_and_b32 s66, s66, 1\n\t"
    "v_cmp_eq_u32 vcc, 4, v2\n\t"
    "v_and_b32 v36, 0xf0, v4\n\t"
    "v_mfma_f32_16x16x32_bf16 v[112:115], a[116:119], a[20:23], v[112:115]\n\t"
    "s_lshl3_add_u32 s63, s66, s63\n\t"
    "buffer_load_dwordx4 v21, s[56:59], s60 offen offset:1024 lds\n\t"
    "v_mfma_f32_16x16x32_bf16 v[120:123], a[112:115], a[24:27], 0\n\t"
    "s_xor_b32 s60, s82, 2\n\t"
    "ds_read_b64_tr_b16 a[112:113], v12 offset:128\n\t"
    "v_add_u32 v37, s63, v37\n\t"
    "v_mfma_f32_16x16x32_bf16 v[120:123], a[116:119], a[28:31], v[120:123]\n\t"
    "s_waitcnt lgkmcnt(4)\n\t"
    "s_and_b32 s60, s60, 3\n\t"
    "ds_read_b64_tr_b16 a[180:181], v37 offset:0\n\t"
    "v_mfma_f32_16x16x32_bf16 v[100:103], a[120:123], a[0:3], 0\n\t"
    "s_mulk_i32 s60, 20480\n\t"
    "ds_read_b64_tr_b16 a[182:183], v37 offset:1536\n\t"
    "v_mfma_f32_16x16x32_bf16 v[100:103], a[124:127], a[4:7], v[100:103]\n\t"
    "s_add_u32 s60, s60, s61\n\t"
    "ds_read_b64_tr_b16 a[184:185], v37 offset:12288\n\t"
    "v_lshlrev_b32 v36, 1, v36\n\t"
    "v_mfma_f32_16x16x32_bf16 v[108:111], a[120:123], a[8:11], 0\n\t"
    "s_lshl1_add_u32 m0, s86, 4294807552\n\t"
    "s_nop 0\n\t"
    "buffer_load_dwordx4 v20, s[56:59], s60 offen offset:0 lds\n\t"
    "v_mfma_f32_16x16x32_bf16 v[108:111], a[124:127], a[12:15], v[108:111]\n\t"
    "s_add_u32 s66, s67, 1\n\t"
    "s_add_u32 s48, s16, s69\n\t"
    "s_addc_u32 s49, s17, 0\n\t"
    "v_mfma_f32_16x16x32_bf16 v[116:119], a[120:123], a[16:19], 0\n\t"
    "s_lshl_b32 s63, s66, 5\n\t"
    "s_add_u32 s52, s18, s69\n\t"
    "s_addc_u32 s53, s19, 0\n\t"
    "v_mfma_f32_16x16x32_bf16 v[116:119], a[124:127], a[20:23], v[116:119]\n\t"
    "s_and_b32 s65, s63, 64\n\t"
    "ds_read_b64_tr_b16 a[186:187], v37 offset:13824\n\t"
    "v_xor_b32 v38, s65, v11\n\t"
    "v_mfma_f32_16x16x32_bf16 v[124:127], a[120:123], a[24:27], 0\n\t"
    "s_waitcnt vmcnt(12)\n\t"
    "s_barrier\n\t"
    "v_max_u32 v52, v44, v45\n\t"
    "v_mfma_f32_16x16x32_bf16 v[124:127], a[124:127], a[28:31], v[124:127]\n\t"
    "buffer_store_dword v60, v2, s[40:43], s97 offen sc1\n\t"
    "v_mfma_f32_16x16x32_bf16 v[128:131], a[96:99], a[0:3], 0\n\t"
    "ds_read_b64_tr_b16 a[114:115], v12 offset:1664\n\t"
    "v_min_u32 v45, v44, v45\n\t"
    "v_mfma_f32_16x16x32_bf16 v[128:131], a[100:103], a[4:7], v[128:131]\n\t"
    "s_and_b32 s63, s63, 0x180\n\t"
    "buffer_load_dwordx4 v21, s[56:59], s60 offen offset:1024 lds\n\t"
    "v_mfma_f32_16x16x32_bf16 v[136:139], a[96:99], a[8:11], 0\n\t"
    "s_xor_b32 s60, s82, 3\n\t"
    "ds_read_b64_tr_b16 a[116:117], v12 offset:12416\n\t"
    "v_max_u32 v53, v46, v47\n\t"
    "v_mfma_f32_16x16x32_bf16 v[136:139], a[100:103], a[12:15], v[136:139]\n\t"
    "s_and_b32 s60, s60, 3\n\t"
    "ds_read_b64_tr_b16 a[118:119], v12 offset:13952\n\t"
    "v_min_u32 v47, v46, v47\n\t"
    "v_mfma_f32_16x16x32_bf16 v[144:147], a[96:99], a[16:19], 0\n\t"
    "s_mulk_i32 s60, 20480\n\t"
    "ds_read_b64_tr_b16 a[120:121], v12 offset:136\n\t"
    "v_max_u32 v54, v48, v49\n\t"
    "v_mfma_f32_16x16x32_bf16 v[144:147], a[100:103], a[20:23], v[144:147]\n\t"
    "s_add_u32 s60, s60, s61\n\t"
    "ds_read_b64_tr_b16 a[122:123], v12 offset:1672\n\t"
    "v_min_u32 v49, v48, v49\n\t"
    "v_mfma_f32_16x16x32_bf16 v[152:155], a[96:99], a[24:27], 0\n\t"
    "s_lshl1_add_u32 m0, s86, 4294809600\n\t"
    "s_nop 0\n\t"
    "buffer_load_dwordx4 v20, s[56:59], s60 offen offset:0 lds\n\t"
    "v_mfma_f32_16x16x32_bf16 v[152:155], a[100:103], a[28:31], v[152:155]\n\t"
    "s_waitcnt lgkmcnt(5)\n\t"
    "s_add_u32 s61, s82, 8\n\t"
    "v_max_u32 v55, v50, v51\n\t"
    "v_mfma_f32_16x16x32_bf16 v[132:135], a[104:107], a[0:3], 0\n\t"
    "s_bfe_u32 s61, s61, 0x20002\n\t"
    "v_min_u32 v51, v50, v51\n\t"
    "v_mfma_f32_16x16x32_bf16 v[132:135], a[108:111], a[4:7], v[132:135]\n\t"
    "s_mul_i32 s61, s61, 128\n\t"
    "ds_read_b64_tr_b16 a[124:125], v12 offset:12424\n\t"
    "v_max_u32 v45, v45, v47\n\t"
    "v_mfma_f32_16x16x32_bf16 v[140:143], a[104:107], a[8:11], 0\n\t"
    "s_and_b32 s66, s66, 1\n\t"
    "buffer_load_dwordx4 v21, s[56:59], s60 offen offset:1024 lds\n\t"
    "v_mfma_f32_16x16x32_bf16 v[140:143], a[108:111], a[12:15], v[140:143]\n\t"
    "s_add_u32 m0, s83, 49152\n\t"
    "ds_read_b64_tr_b16 a[126:127], v12 offset:13960\n\t"
    "v_med3_u32 v45, v52, v53, v45\n\t"
    "v_mfma_f32_16x16x32_bf16 v[148:151], a[104:107], a[16:19], 0\n\t"
    "s_add_u32 s60, s82, 8\n\t"
    "ds_read_b64_tr_b16 a[96:97], v11 offset:256\n\t"
    "v_max_u32 v52, v52, v53\n\t"
    "v_mfma_f32_16x16x32_bf16 v[148:151], a[108:111], a[20:23], v[148:151]\n\t"
    "s_bfe_u32 s60, s60, 0x20002\n\t"
    "ds_read_b64_tr_b16 a[98:99], v11 offset:1792\n\t"
    "v_max_u32 v49, v49, v51\n\t"
    "v_mfma_f32_16x16x32_bf16 v[156:159], a[104:107], a[24:27], 0\n\t"
    "s_mul_i32 s60, s60, 786432\n\t"
    "buffer_load_dwordx4 v5, s[44:47], s60 offen offset:0 lds\n\t"
    "v_mfma_f32_16x16x32_bf16 v[156:159], a[108:111], a[28:31], v[156:159]\n\t"
    "s_lshl3_add_u32 s63, s66, s63\n\t"
    "v_add_u32 v38, s63, v38\n\t"
    "v_mfma_f32_16x16x32_bf16 a[168:171], a[180:183], a[164:167], 0\n\t"
    "s_add_u32 s66, s67, 2\n\t"
    "v_med3_u32 v49, v54, v55, v49\n\t"
    "v_mfma_f32_16x16x32_bf16 a[168:171], a[184:187], a[164:167], a[168:171]\n\t"
    "s_waitcnt lgkmcnt(4)\n\t"
    "s_lshl_b32 s63, s66, 5\n\t"
    "v_max_u32 v54, v54, v55\n\t"
    "v_mfma_f32_16x16x32_bf16 v[160:163], a[112:115], a[0:3], 0\n\t"
    "buffer_load_dwordx4 v6, s[44:47], s60 offen offset:1024 lds\n\t"
    "v_mfma_f32_16x16x32_bf16 v[160:163], a[116:119], a[4:7], v[160:163]\n\t"
    "s_and_b32 s65, s63, 64\n\t"
    "ds_read_b64_tr_b16 a[100:101], v11 offset:12544\n\t"
    "v_max_u32 v45, v45, v49\n\t"
    "v_mfma_f32_16x16x32_bf16 v[168:171], a[112:115], a[8:11], 0\n\t"
    "s_and_b32 s63, s63, 0x180\n\t"
    "ds_read_b64_tr_b16 a[102:103], v11 offset:14080\n\t"
    "v_med3_u32 v45, v52, v54, v45\n\t"
    "v_mfma_f32_16x16x32_bf16 v[168:171], a[116:119], a[12:15], v[168:171]\n\t"
    "s_and_b32 s66, s66, 1\n\t"
    "ds_read_b64_tr_b16 a[188:189], v38 offset:0\n\t"
    "v_xor_b32 v39, s65, v11\n\t"
    "v_mfma_f32_16x16x32_bf16 v[176:179], a[112:115], a[16:19], 0\n\t"
    "s_lshl3_add_u32 s63, s66, s63\n\t"
    "buffer_load_dwordx4 v7, s[44:47], s60 offen offset:2048 lds\n\t"
    "v_mfma_f32_16x16x32_bf16 v[176:179], a[116:119], a[20:23], v[176:179]\n\t"
    "s_add_u32 m0, s83, 52224\n\t"
    "ds_read_b64_tr_b16 a[190:191], v38 offset:1536\n\t"
    "v_max_u32 v52, v52, v54\n\t"
    "v_mfma_f32_16x16x32_bf16 v[184:187], a[112:115], a[24:27], 0\n\t"
    "s_add_u32 s60, s60, 98304\n\t"
    "ds_read_b64_tr_b16 a[192:193], v38 offset:12288\n\t"
    "v_add_u32 v39, s63, v39\n\t"
    "v_mfma_f32_16x16x32_bf16 v[184:187], a[116:119], a[28:31], v[184:187]\n\t"
    "ds_read_b64_tr_b16 a[194:195], v38 offset:13824\n\t"
    "v_mov_b32 v53, s80\n\t"
    "v_mfma_f32_16x16x32_bf16 v[164:167], a[120:123], a[0:3], 0\n\t"
    "s_waitcnt lgkmcnt(6)\n\t"
    "buffer_load_dwordx4 v5, s[44:47], s60 offen offset:0 lds\n\t"
    "v_mfma_f32_16x16x32_bf16 v[164:167], a[124:127], a[4:7], v[164:167]\n\t"
    "s_sub_u32 s63, s87, 3949723648\n\t"
    "ds_read_b64_tr_b16 a[104:105], v11 offset:264\n\t"
    "v_cndmask_b32 v52, v52, v45, vcc\n\t"
    "v_mfma_f32_16x16x32_bf16 v[172:175], a[120:123], a[8:11], 0\n\t"
    "s_lshr_b32 s63, s63, 3\n\t"
    "ds_read_b64_tr_b16 a[106:107], v11 offset:1800\n\t"
    "v_and_b32 v44, 0x3c0, v4\n\t"
    "v_mfma_f32_16x16x32_bf16 v[172:175], a[124:127], a[12:15], v[172:175]\n\t"
    "s_sub_u32 s50, 25165824, s69\n\t"
    "ds_read_b64_tr_b16 a[108:109], v11 offset:12552\n\t"
    "v_lshrrev_b32 v45, 2, v4\n\t"
    "v_mfma_f32_16x16x32_bf16 v[180:183], a[120:123], a[16:19], 0\n\t"
    "s_mov_b32 s54, s50\n\t"
    "buffer_load_dwordx4 v6, s[44:47], s60 offen offset:1024 lds\n\t"
    "v_mfma_f32_16x16x32_bf16 v[180:183], a[124:127], a[20:23], v[180:183]\n\t"
    "s_mov_b32 s51, 0x20000\n\t"
    "ds_read_b64_tr_b16 a[110:111], v11 offset:14088\n\t"
    "v_lshrrev_b32 v46, 1, v4\n\t"
    "v_mfma_f32_16x16x32_bf16 v[188:191], a[120:123], a[24:27], 0\n\t"
    "s_mov_b32 s55, 0x20000\n\t"
    "ds_read_b64_tr_b16 a[112:113], v12 offset:256\n\t"
    "v_lshlrev_b32 v44, 1, v44\n\t"
    "v_mfma_f32_16x16x32_bf16 v[188:191], a[124:127], a[28:31], v[188:191]\n\t"
    "ds_read_b64_tr_b16 a[114:115], v12 offset:1792\n\t"
    "v_and_b32 v45, 0x60, v45\n\t"
    "v_mfma_f32_16x16x32_bf16 v[192:195], a[96:99], a[0:3], 0\n\t"
    "s_waitcnt lgkmcnt(6)\n\t"
    "buffer_load_dwordx4 v7, s[44:47], s60 offen offset:2048 lds\n\t"
    "v_mfma_f32_16x16x32_bf16 v[192:195], a[100:103], a[4:7], v[192:195]\n\t"
    "s_xor_b32 s60, s82, 0\n\t"
    "ds_read_b64_tr_b16 a[116:117], v12 offset:12544\n\t"
    "v_and_b32 v46, 24, v46\n\t"
    "v_mfma_f32_16x16x32_bf16 v[200:203], a[96:99], a[8:11], 0\n\t"
    "s_and_b32 s60, s60, 3\n\t"
    "ds_read_b64_tr_b16 a[118:119], v12 offset:14080\n\t"
    "v_add3_u32 v63, v44, v45, v46\n\t"
    "v_mfma_f32_16x16x32_bf16 v[200:203], a[100:103], a[12:15], v[200:203]\n\t"
    "s_mulk_i32 s60, 20480\n\t"
    "ds_read_b64_tr_b16 a[120:121], v12 offset:264\n\t"
    "v_lshrrev_b32 v44, 3, v4\n\t"
    "v_mfma_f32_16x16x32_bf16 v[208:211], a[96:99], a[16:19], 0\n\t"
    "s_add_u32 s60, s60, s61\n\t"
    "ds_read_b64_tr_b16 a[122:123], v12 offset:1800\n\t"
    "v_and_b32 v44, 0x60, v44\n\t"
    "v_mfma_f32_16x16x32_bf16 v[208:211], a[100:103], a[20:23], v[208:211]\n\t"
    "s_lshl1_add_u32 m0, s86, 4294770688\n\t"
    "s_nop 0\n\t"
    "buffer_load_dwordx4 v20, s[56:59], s60 offen offset:0 lds\n\t"
    "v_mfma_f32_16x16x32_bf16 v[216:219], a[96:99], a[24:27], 0\n\t"
    "s_waitcnt vmcnt(16)\n\t"
    "s_barrier\n\t"
    "ds_read_b64_tr_b16 a[124:125], v12 offset:12552\n\t"
    "v_mfma_f32_16x16x32_bf16 v[216:219], a[100:103], a[28:31], v[216:219]\n\t"
    "buffer_store_dwordx2 v[52:53], v56, s[40:43], s63 offen sc1\n\t"
    "v_mfma_f32_16x16x32_bf16 a[172:175], a[188:191], a[164:167], 0\n\t"
    "ds_read_b64_tr_b16 a[126:127], v12 offset:14088\n\t"
    "v_and_b32 v45, 0x70, v4\n\t"
    "v_mfma_f32_16x16x32_bf16 a[172:175], a[192:195], a[164:167], a[172:175]\n\t"
    "s_waitcnt lgkmcnt(6)\n\t"
    "buffer_load_dwordx4 v21, s[56:59], s60 offen offset:1024 lds\n\t"
    "v_mfma_f32_16x16x32_bf16 v[196:199], a[104:107], a[0:3], 0\n\t"
    "s_xor_b32 s60, s82, 1\n\t"
    "ds_read_b64_tr_b16 a[196:197], v39 offset:0\n\t"
    "v_mfma_f32_16x16x32_bf16 v[196:199], a[108:111], a[4:7], v[196:199]\n\t"
    "s_and_b32 s60, s60, 3\n\t"
    "ds_read_b64_tr_b16 a[198:199], v39 offset:1536\n\t"
    "v_xor_b32 v45, v45, v44\n\t"
    "v_mfma_f32_16x16x32_bf16 v[204:207], a[104:107], a[8:11], 0\n\t"
    "s_mulk_i32 s60, 20480\n\t"
    "ds_read_b64_tr_b16 a[200:201], v39 offset:12288\n\t"
    "v_lshrrev_b32 v46, 7, v4\n\t"
    "v_mfma_f32_16x16x32_bf16 v[204:207], a[108:111], a[12:15], v[204:207]\n\t"
    "s_add_u32 s60, s60, s61\n\t"
    "ds_read_b64_tr_b16 a[202:203], v39 offset:13824\n\t"
    "v_mov_b32 v47, 0x500\n\t"
    "v_mfma_f32_16x16x32_bf16 v[212:215], a[104:107], a[16:19], 0\n\t"
    "s_lshl1_add_u32 m0, s86, 4294772736\n\t"
    "s_nop 0\n\t"
    "buffer_load_dwordx4 v20, s[56:59], s60 offen offset:0 lds\n\t"
    "v_mfma_f32_16x16x32_bf16 v[212:215], a[108:111], a[20:23], v[212:215]\n\t"
    "s_xor_b32 s63, s82, 0\n\t"
    "ds_read_b64_tr_b16 a[96:97], v11 offset:24576\n\t"
    "v_add_u32 v63, s86, v63\n\t"
    "v_mfma_f32_16x16x32_bf16 v[220:223], a[104:107], a[24:27], 0\n\t"
    "s_and_b32 s63, s63, 3\n\t"
    "ds_read_b64_tr_b16 a[98:99], v11 offset:26112\n\t"
    "v_mad_u32_u24 v62, v46, v47, v45\n\t"
    "v_mfma_f32_16x16x32_bf16 v[220:223], a[108:111], a[28:31], v[220:223]\n\t"
    "ds_read_b64_tr_b16 a[100:101], v11 offset:36864\n\t"
    "v_mov_b32 v45, s80\n\t"
    "v_mfma_f32_16x16x32_bf16 v[224:227], a[112:115], a[0:3], 0\n\t"
    "s_waitcnt lgkmcnt(7)\n\t"
    "buffer_load_dwordx4 v21, s[56:59], s60 offen offset:1024 lds\n\t"
    "v_mfma_f32_16x16x32_bf16 v[224:227], a[116:119], a[4:7], v[224:227]\n\t"
    "s_xor_b32 s60, s82, 2\n\t"
    "ds_read_b64_tr_b16 a[102:103], v11 offset:38400\n\t"
    "v_and_b32 v47, 0xc0, v4\n\t"
    "v_mfma_f32_16x16x32_bf16 v[232:235], a[112:115], a[8:11], 0\n\t"
    "s_and_b32 s60, s60, 3\n\t"
    "s_waitcnt vmcnt(18)\n\t"
    "ds_read_b128 a[32:35], v22 offset:32768\n\t"
    "v_and_b32 v46, 16, v4\n\t"
    "v_mfma_f32_16x16x32_bf16 v[232:235], a[116:119], a[12:15], v[232:235]\n\t"
    "s_mulk_i32 s60, 20480\n\t"
    "ds_read_b128 a[36:39], v23 offset:32768\n\t"
    "v_lshl_add_u32 v47, v47, 2, v46\n\t"
    "v_mfma_f32_16x16x32_bf16 v[240:243], a[112:115], a[16:19], 0\n\t"
    "s_add_u32 s60, s60, s61\n\t"
    "s_waitcnt vmcnt(16)\n\t"
    "ds_read_b128 a[40:43], v22 offset:34816\n\t"
    "v_and_b32 v46, 32, v4\n\t"
    "v_mfma_f32_16x16x32_bf16 v[240:243], a[116:119], a[20:23], v[240:243]\n\t"
    "s_lshl1_add_u32 m0, s86, 4294774784\n\t"
    "s_nop 0\n\t"
    "buffer_load_dwordx4 v20, s[56:59], s60 offen offset:0 lds\n\t"
    "v_mfma_f32_16x16x32_bf16 v[248:251], a[112:115], a[24:27], 0\n\t"
    "ds_read_b128 a[44:47], v23 offset:34816\n\t"
    "v_lshl_add_u32 v47, v46, 1, v47\n\t"
    "v_mfma_f32_16x16x32_bf16 v[248:251], a[116:119], a[28:31], v[248:251]\n\t"
    "s_waitcnt vmcnt(14)\n\t"
    "ds_read_b128 a[48:51], v22 offset:36864\n\t"
    "v_bfe_u32 v46, v4, 8, 1\n\t"
    "v_mfma_f32_16x16x32_bf16 v[228:231], a[120:123], a[0:3], 0\n\t"
    "ds_read_b128 a[52:55], v23 offset:36864\n\t"
    "v_lshl_add_u32 v47, v46, 5, v47\n\t"
    "v_mfma_f32_16x16x32_bf16 v[228:231], a[124:127], a[4:7], v[228:231]\n\t"
    "buffer_load_dwordx4 v21, s[56:59], s60 offen offset:1024 lds\n\t"
    "v_mfma_f32_16x16x32_bf16 v[236:239], a[120:123], a[8:11], 0\n\t"
    "s_xor_b32 s60, s82, 3\n\t"
    "s_waitcnt vmcnt(13)\n\t"
    "ds_read_b128 a[56:59], v22 offset:38912\n\t"
    "v_lshl_add_u32 v28, s63, 9, v36\n\t"
    "v_mfma_f32_16x16x32_bf16 v[236:239], a[124:127], a[12:15], v[236:239]\n\t"
    "s_and_b32 s60, s60, 3\n\t"
    "ds_read_b128 a[60:63], v23 offset:38912\n\t"
    "v_lshrrev_b32 v46, 9, v4\n\t"
    "v_mfma_f32_16x16x32_bf16 v[244:247], a[120:123], a[16:19], 0\n\t"
    "s_mulk_i32 s60, 20480\n\t"
    "ds_read_b64_tr_b16 a[104:105], v11 offset:24584\n\t"
    "v_lshl_add_u32 v47, v46, 7, v47\n\t"
    "v_mfma_f32_16x16x32_bf16 v[244:247], a[124:127], a[20:23], v[244:247]\n\t"
    "s_add_u32 s60, s60, s61\n\t"
    "ds_read_b64_tr_b16 a[106:107], v11 offset:26120\n\t"
    "v_lshrrev_b32 v54, 7, v4\n\t"
    "v_mfma_f32_16x16x32_bf16 v[252:255], a[120:123], a[24:27], 0\n\t"
    "s_lshl1_add_u32 m0, s86, 4294776832\n\t"
    "s_nop 0\n\t"
    "buffer_load_dwordx4 v20, s[56:59], s60 offen offset:0 lds\n\t"
    "v_mfma_f32_16x16x32_bf16 v[252:255], a[124:127], a[28:31], v[252:255]\n\t"
    "s_waitcnt lgkmcnt(6)\n\t"
    "s_add_u32 s61, s82, 12\n\t"
    "ds_read_b64_tr_b16 a[108:109], v11 offset:36872\n\t"
    "v_mfma_f32_16x16x32_bf16 a[176:179], a[196:199], a[164:167], 0\n\t"
    "s_bfe_u32 s61, s61, 0x20002\n\t"
    "ds_read_b64_tr_b16 a[110:111], v11 offset:38408\n\t"
    "v_mfma_f32_16x16x32_bf16 a[176:179], a[200:203], a[164:167], a[176:179]\n\t"
    "s_mul_i32 s61, s61, 128\n\t"
    "ds_read_b64_tr_b16 a[112:113], v12 offset:24576\n\t"
    "v_mfma_f32_16x16x32_bf16 v[64:67], a[96:99], a[32:35], v[64:67]\n\t"
    "buffer_load_dwordx4 v21, s[56:59], s60 offen offset:1024 lds\n\t"
    "v_mfma_f32_16x16x32_bf16 v[64:67], a[100:103], a[36:39], v[64:67]\n\t"
    "s_xor_b32 s60, s82, 0\n\t"
    "ds_read_b64_tr_b16 a[114:115], v12 offset:26112\n\t"
    "v_mfma_f32_16x16x32_bf16 v[72:75], a[96:99], a[40:43], v[72:75]\n\t"
    "s_and_b32 s60, s60, 3\n\t"
    "ds_read_b64_tr_b16 a[116:117], v12 offset:36864\n\t"
    "v_mfma_f32_16x16x32_bf16 v[72:75], a[100:103], a[44:47], v[72:75]\n\t"
    "s_waitcnt lgkmcnt(7)\n\t"
    "s_mulk_i32 s60, 20480\n\t"
    "ds_read_b64_tr_b16 a[118:119], v12 offset:38400\n\t"
    "v_mfma_f32_16x16x32_bf16 v[80:83], a[96:99], a[48:51], v[80:83]\n\t"
    "s_add_u32 s60, s60, s61\n\t"
    "ds_read_b64_tr_b16 a[120:121], v12 offset:24584\n\t"
    "v_mfma_f32_16x16x32_bf16 v[80:83], a[100:103], a[52:55], v[80:83]\n\t"
    "s_lshl1_add_u32 m0, s86, 4294803456\n\t"
    "ds_read_b64_tr_b16 a[122:123], v12 offset:26120\n\t"
    "v_mfma_f32_16x16x32_bf16 v[88:91], a[96:99], a[56:59], v[88:91]\n\t"
    "buffer_load_dwordx4 v20, s[56:59], s60 offen offset:0 lds\n\t"
    "v_mfma_f32_16x16x32_bf16 v[88:91], a[100:103], a[60:63], v[88:91]\n\t"
    "s_waitcnt lgkmcnt(8)\n\t"
    "ds_read_b64_tr_b16 a[124:125], v12 offset:36872\n\t"
    "v_mul_u32_u24 v54, 0x700, v54\n\t"
    "v_mfma_f32_16x16x32_bf16 v[68:71], a[104:107], a[32:35], v[68:71]\n\t"
    "s_waitcnt lgkmcnt(7)\n\t"
    "ds_read_b64_tr_b16 a[126:127], v12 offset:38408\n\t"
    "v_add_u32 v54, v54, v62\n\t"
    "v_mfma_f32_16x16x32_bf16 v[68:71], a[108:111], a[36:39], v[68:71]\n\t"
    "s_mulk_i32 s63, 20480\n\t"
    "s_add_u32 s36, s8, s71\n\t"
    "s_addc_u32 s37, s9, 0\n\t"
    "v_mfma_f32_16x16x32_bf16 v[76:79], a[104:107], a[40:43], v[76:79]\n\t"
    "buffer_load_dwordx4 v21, s[56:59], s60 offen offset:1024 lds\n\t"
    "v_mfma_f32_16x16x32_bf16 v[76:79], a[108:111], a[44:47], v[76:79]\n\t"
    "s_xor_b32 s60, s82, 1\n\t"
    "s_bitcmp1_b32 s86, 13\n\t"
    "s_cselect_b64 s[66:67], s[24:25], s[22:23]\n\t"
    "v_mfma_f32_16x16x32_bf16 v[84:87], a[104:107], a[48:51], v[84:87]\n\t"
    "s_and_b32 s60, s60, 3\n\t"
    "s_add_u32 s68, s20, s68\n\t"
    "s_addc_u32 s69, s21, 0\n\t"
    "v_mfma_f32_16x16x32_bf16 v[84:87], a[108:111], a[52:55], v[84:87]\n\t"
    "s_mulk_i32 s60, 20480\n\t"
    "s_add_u32 s84, s26, s84\n\t"
    "s_addc_u32 s85, s27, 0\n\t"
    "v_mfma_f32_16x16x32_bf16 v[92:95], a[104:107], a[56:59], v[92:95]\n\t"
    "s_add_u32 s60, s60, s61\n\t"
    "s_add_u32 s70, s66, s70\n\t"
    "s_addc_u32 s71, s67, 0\n\t"
    "v_mfma_f32_16x16x32_bf16 v[92:95], a[108:111], a[60:63], v[92:95]\n\t"
    "s_waitcnt lgkmcnt(2)\n\t"
    "s_lshl1_add_u32 m0, s86, 4294805504\n\t"
    "ds_read_b64_tr_b16 a[96:97], v11 offset:24704\n\t"
    "v_mfma_f32_16x16x32_bf16 v[96:99], a[112:115], a[32:35], v[96:99]\n\t"
    "buffer_load_dwordx4 v20, s[56:59], s60 offen offset:0 lds\n\t"
    "v_mfma_f32_16x16x32_bf16 v[96:99], a[116:119], a[36:39], v[96:99]\n\t"
    "s_andn2_b32 s66, s82, 15\n\t"
    "ds_read_b64_tr_b16 a[98:99], v11 offset:26240\n\t"
    "v_mfma_f32_16x16x32_bf16 v[104:107], a[112:115], a[40:43], v[104:107]\n\t"
    "s_add_u32 s63, s63, s66\n\t"
    "ds_read_b64_tr_b16 a[100:101], v11 offset:36992\n\t"
    "v_mfma_f32_16x16x32_bf16 v[104:107], a[116:119], a[44:47], v[104:107]\n\t"
    "s_andn2_b32 s67, s99, 1024\n\t"
    "ds_read_b64_tr_b16 a[102:103], v11 offset:38528\n\t"
    "v_mfma_f32_16x16x32_bf16 v[112:115], a[112:115], a[48:51], v[112:115]\n\t"
    "buffer_load_dwordx4 v21, s[56:59], s60 offen offset:1024 lds\n\t"
    "v_mfma_f32_16x16x32_bf16 v[112:115], a[116:119], a[52:55], v[112:115]\n\t"
    "s_xor_b32 s60, s82, 2\n\t"
    "ds_read_b64_tr_b16 a[104:105], v11 offset:24712\n\t"
    "v_mfma_f32_16x16x32_bf16 v[120:123], a[112:115], a[56:59], v[120:123]\n\t"
    "s_and_b32 s60, s60, 3\n\t"
    "ds_read_b64_tr_b16 a[106:107], v11 offset:26248\n\t"
    "v_mfma_f32_16x16x32_bf16 v[120:123], a[116:119], a[60:63], v[120:123]\n\t"
    "s_mulk_i32 s60, 20480\n\t"
    "ds_read_b64_tr_b16 a[108:109], v11 offset:37000\n\t"
    "v_mfma_f32_16x16x32_bf16 v[100:103], a[120:123], a[32:35], v[100:103]\n\t"
    "s_waitcnt lgkmcnt(7)\n\t"
    "s_add_u32 s60, s60, s61\n\t"
    "ds_read_b64_tr_b16 a[110:111], v11 offset:38536\n\t"
    "v_mfma_f32_16x16x32_bf16 v[100:103], a[124:127], a[36:39], v[100:103]\n\t"
    "s_lshl1_add_u32 m0, s86, 4294807552\n\t"
    "s_nop 0\n\t"
    "buffer_load_dwordx4 v20, s[56:59], s60 offen offset:0 lds\n\t"
    "v_mfma_f32_16x16x32_bf16 v[108:111], a[120:123], a[40:43], v[108:111]\n\t"
    "ds_read_b64_tr_b16 a[180:181], v37 offset:24576\n\t"
    "v_mfma_f32_16x16x32_bf16 v[108:111], a[124:127], a[44:47], v[108:111]\n\t"
    "ds_read_b64_tr_b16 a[182:183], v37 offset:26112\n\t"
    "v_mfma_f32_16x16x32_bf16 v[116:119], a[120:123], a[48:51], v[116:119]\n\t"
    "ds_read_b64_tr_b16 a[184:185], v37 offset:36864\n\t"
    "v_mfma_f32_16x16x32_bf16 v[116:119], a[124:127], a[52:55], v[116:119]\n\t"
    "buffer_load_dwordx4 v21, s[56:59], s60 offen offset:1024 lds\n\t"
    "v_mfma_f32_16x16x32_bf16 v[124:127], a[120:123], a[56:59], v[124:127]\n\t"
    "s_xor_b32 s60, s82, 3\n\t"
    "ds_read_b64_tr_b16 a[186:187], v37 offset:38400\n\t"
    "v_add_u32 v37, 49152, v37\n\t"
    "v_mfma_f32_16x16x32_bf16 v[124:127], a[124:127], a[60:63], v[124:127]\n\t"
    "s_waitcnt lgkmcnt(8)\n\t"
    "s_and_b32 s60, s60, 3\n\t"
    "ds_read_b64_tr_b16 a[112:113], v12 offset:24704\n\t"
    "v_mfma_f32_16x16x32_bf16 v[128:131], a[96:99], a[32:35], v[128:131]\n\t"
    "s_mulk_i32 s60, 20480\n\t"
    "ds_read_b64_tr_b16 a[114:115], v12 offset:26240\n\t"
    "v_mfma_f32_16x16x32_bf16 v[128:131], a[100:103], a[36:39], v[128:131]\n\t"
    "s_add_u32 s60, s60, s61\n\t"
    "ds_read_b64_tr_b16 a[116:117], v12 offset:36992\n\t"
    "v_mfma_f32_16x16x32_bf16 v[136:139], a[96:99], a[40:43], v[136:139]\n\t"
    "s_lshl1_add_u32 m0, s86, 4294809600\n\t"
    "s_nop 0\n\t"
    "buffer_load_dwordx4 v20, s[56:59], s60 offen offset:0 lds\n\t"
    "v_mfma_f32_16x16x32_bf16 v[136:139], a[100:103], a[44:47], v[136:139]\n\t"
    "s_mul_i32 s61, s98, 1\n\t"
    "ds_read_b64_tr_b16 a[118:119], v12 offset:38528\n\t"
    "v_add_u32 v20, s63, v47\n\t"
    "v_mfma_f32_16x16x32_bf16 v[144:147], a[96:99], a[48:51], v[144:147]\n\t"
    "s_xor_b32 s63, s82, 1\n\t"
    "ds_read_b64_tr_b16 a[120:121], v12 offset:24712\n\t"
    "v_mfma_f32_16x16x32_bf16 v[144:147], a[100:103], a[52:55], v[144:147]\n\t"
    "s_and_b32 s63, s63, 3\n\t"
    "ds_read_b64_tr_b16 a[122:123], v12 offset:26248\n\t"
    "v_lshl_add_u32 v30, s63, 9, v36\n\t"
    "v_mfma_f32_16x16x32_bf16 v[152:155], a[96:99], a[56:59], v[152:155]\n\t"
    "s_mulk_i32 s63, 20480\n\t"
    "buffer_load_dwordx4 v21, s[56:59], s60 offen offset:1024 lds\n\t"
    "v_mfma_f32_16x16x32_bf16 v[152:155], a[100:103], a[60:63], v[152:155]\n\t"
    "s_waitcnt lgkmcnt(7)\n\t"
    "s_add_u32 m0, s83, 73728\n\t"
    "ds_read_b64_tr_b16 a[124:125], v12 offset:37000\n\t"
    "v_mfma_f32_16x16x32_bf16 v[132:135], a[104:107], a[32:35], v[132:135]\n\t"
    "s_add_u32 s60, s82, 12\n\t"
    "s_waitcnt vmcnt(14)\n\t"
    "ds_read_b128 a[64:67], v22 offset:0\n\t"
    "v_mfma_f32_16x16x32_bf16 v[132:135], a[108:111], a[36:39], v[132:135]\n\t"
    "s_bfe_u32 s60, s60, 0x20002\n\t"
    "ds_read_b128 a[68:71], v23 offset:0\n\t"
    "v_mfma_f32_16x16x32_bf16 v[140:143], a[104:107], a[40:43], v[140:143]\n\t"
    "s_mul_i32 s60, s60, 786432\n\t"
    "buffer_load_dwordx4 v5, s[44:47], s60 offen offset:0 lds\n\t"
    "v_mfma_f32_16x16x32_bf16 v[140:143], a[108:111], a[44:47], v[140:143]\n\t"
    "s_add_u32 s63, s63, s66\n\t"
    "s_waitcnt vmcnt(13)\n\t"
    "ds_read_b128 a[72:75], v22 offset:2048\n\t"
    "v_add_u32 v21, s63, v47\n\t"
    "v_mfma_f32_16x16x32_bf16 v[148:151], a[104:107], a[48:51], v[148:151]\n\t"
    "s_xor_b32 s63, s82, 2\n\t"
    "ds_read_b64_tr_b16 a[126:127], v12 offset:38536\n\t"
    "v_mfma_f32_16x16x32_bf16 v[148:151], a[108:111], a[52:55], v[148:151]\n\t"
    "s_and_b32 s63, s63, 3\n\t"
    "ds_read_b128 a[76:79], v23 offset:2048\n\t"
    "v_lshl_add_u32 v32, s63, 9, v36\n\t"
    "v_mfma_f32_16x16x32_bf16 v[156:159], a[104:107], a[56:59], v[156:159]\n\t"
    "s_mulk_i32 s63, 20480\n\t"
    "buffer_load_dwordx4 v6, s[44:47], s60 offen offset:1024 lds\n\t"
    "v_mfma_f32_16x16x32_bf16 v[156:159], a[108:111], a[60:63], v[156:159]\n\t"
    "s_waitcnt vmcnt(12)\n\t"
    "ds_read_b128 a[80:83], v22 offset:4096\n\t"
    "v_mfma_f32_16x16x32_bf16 a[168:171], a[180:183], a[164:167], a[168:171]\n\t"
    "s_waitcnt lgkmcnt(6)\n\t"
    "ds_read_b128 a[84:87], v23 offset:4096\n\t"
    "v_mfma_f32_16x16x32_bf16 a[168:171], a[184:187], a[164:167], a[168:171]\n\t"
    "s_waitcnt vmcnt(10)\n\t"
    "ds_read_b128 a[88:91], v22 offset:6144\n\t"
    "v_mfma_f32_16x16x32_bf16 v[160:163], a[112:115], a[32:35], v[160:163]\n\t"
    "buffer_load_dwordx4 v7, s[44:47], s60 offen offset:2048 lds\n\t"
    "v_mfma_f32_16x16x32_bf16 v[160:163], a[116:119], a[36:39], v[160:163]\n\t"
    "s_add_u32 m0, s83, 76800\n\t"
    "ds_read_b64_tr_b16 a[96:97], v11 offset:24832\n\t"
    "v_mfma_f32_16x16x32_bf16 v[168:171], a[112:115], a[40:43], v[168:171]\n\t"
    "s_add_u32 s60, s60, 98304\n\t"
    "ds_read_b64_tr_b16 a[98:99], v11 offset:26368\n\t"
    "v_mfma_f32_16x16x32_bf16 v[168:171], a[116:119], a[44:47], v[168:171]\n\t"
    "s_add_u32 s63, s63, s66\n\t"
    "ds_read_b64_tr_b16 a[100:101], v11 offset:37120\n\t"
    "v_mfma_f32_16x16x32_bf16 v[176:179], a[112:115], a[48:51], v[176:179]\n\t"
    "s_add_u32 s61, s61, 0\n\t"
    "buffer_load_dwordx4 v5, s[44:47], s60 offen offset:0 lds\n\t"
    "v_mfma_f32_16x16x32_bf16 v[176:179], a[116:119], a[52:55], v[176:179]\n\t"
    "s_sub_u32 s64, s61, 12288\n\t"
    "ds_read_b64_tr_b16 a[102:103], v11 offset:38656\n\t"
    "v_mfma_f32_16x16x32_bf16 v[184:187], a[112:115], a[56:59], v[184:187]\n\t"
    "s_min_u32 s61, s61, s64\n\t"
    "ds_read_b128 a[92:95], v23 offset:6144\n\t"
    "v_mfma_f32_16x16x32_bf16 v[184:187], a[116:119], a[60:63], v[184:187]\n\t"
    "ds_read_b64_tr_b16 a[188:189], v38 offset:24576\n\t"
    "v_mfma_f32_16x16x32_bf16 v[164:167], a[120:123], a[32:35], v[164:167]\n\t"
    "s_waitcnt lgkmcnt(9)\n\t"
    "buffer_load_dwordx4 v6, s[44:47], s60 offen offset:1024 lds\n\t"
    "v_mfma_f32_16x16x32_bf16 v[164:167], a[124:127], a[36:39], v[164:167]\n\t"
    "s_add_u32 s61, s61, 8192\n\t"
    "ds_read_b64_tr_b16 a[190:191], v38 offset:26112\n\t"
    "v_mfma_f32_16x16x32_bf16 v[172:175], a[120:123], a[40:43], v[172:175]\n\t"
    "s_waitcnt lgkmcnt(2)\n\t"
    "s_barrier\n\t"
    "ds_read_b64_tr_b16 a[192:193], v38 offset:36864\n\t"
    "v_mfma_f32_16x16x32_bf16 v[172:175], a[124:127], a[44:47], v[172:175]\n\t"
    "ds_read_b64_tr_b16 a[194:195], v38 offset:38400\n\t"
    "v_add_u32 v38, 49152, v38\n\t"
    "v_mfma_f32_16x16x32_bf16 v[180:183], a[120:123], a[48:51], v[180:183]\n\t"
    "buffer_load_dwordx4 v7, s[44:47], s60 offen offset:2048 lds\n\t"
    "v_mfma_f32_16x16x32_bf16 v[180:183], a[124:127], a[52:55], v[180:183]\n\t"
    "s_add_u32 m0, s83, 98304\n\t"
    "ds_read_b64_tr_b16 a[104:105], v11 offset:24840\n\t"
    "v_mfma_f32_16x16x32_bf16 v[188:191], a[120:123], a[56:59], v[188:191]\n\t"
    "s_mul_i32 s60, s98, 384\n\t"
    "ds_read_b64_tr_b16 a[106:107], v11 offset:26376\n\t"
    "v_mfma_f32_16x16x32_bf16 v[188:191], a[124:127], a[60:63], v[188:191]\n\t"
    "ds_read_b64_tr_b16 a[108:109], v11 offset:37128\n\t"
    "v_mfma_f32_16x16x32_bf16 v[192:195], a[96:99], a[32:35], v[192:195]\n\t"
    "ds_read_b64_tr_b16 a[110:111], v11 offset:38664\n\t"
    "v_mfma_f32_16x16x32_bf16 v[192:195], a[100:103], a[36:39], v[192:195]\n\t"
    "s_add_u32 s60, s60, 0\n\t"
    "ds_read_b64_tr_b16 a[112:113], v12 offset:24832\n\t"
    "v_mfma_f32_16x16x32_bf16 v[200:203], a[96:99], a[40:43], v[200:203]\n\t"
    "s_sub_u32 s62, s60, 4718592\n\t"
    "ds_read_b64_tr_b16 a[114:115], v12 offset:26368\n\t"
    "v_mfma_f32_16x16x32_bf16 v[200:203], a[100:103], a[44:47], v[200:203]\n\t"
    "s_min_u32 s60, s60, s62\n\t"
    "ds_read_b64_tr_b16 a[116:117], v12 offset:37120\n\t"
    "v_mfma_f32_16x16x32_bf16 v[208:211], a[96:99], a[48:51], v[208:211]\n\t"
    "s_add_u32 s60, s60, 3145728\n\t"
    "buffer_load_dwordx4 v5, s[44:47], s60 offen offset:0 lds\n\t"
    "v_mfma_f32_16x16x32_bf16 v[208:211], a[100:103], a[52:55], v[208:211]\n\t"
    "ds_read_b64_tr_b16 a[118:119], v12 offset:38656\n\t"
    "v_mfma_f32_16x16x32_bf16 v[216:219], a[96:99], a[56:59], v[216:219]\n\t"
    "ds_read_b64_tr_b16 a[120:121], v12 offset:24840\n\t"
    "v_mfma_f32_16x16x32_bf16 v[216:219], a[100:103], a[60:63], v[216:219]\n\t"
    "s_waitcnt lgkmcnt(9)\n\t"
    "ds_read_b64_tr_b16 a[122:123], v12 offset:26376\n\t"
    "v_mfma_f32_16x16x32_bf16 a[172:175], a[188:191], a[164:167], a[172:175]\n\t"
    "buffer_load_dwordx4 v6, s[44:47], s60 offen offset:1024 lds\n\t"
    "v_mfma_f32_16x16x32_bf16 a[172:175], a[192:195], a[164:167], a[172:175]\n\t"
    "s_waitcnt lgkmcnt(8)\n\t"
    "ds_read_b64_tr_b16 a[124:125], v12 offset:37128\n\t"
    "v_mfma_f32_16x16x32_bf16 v[196:199], a[104:107], a[32:35], v[196:199]\n\t"
    "s_waitcnt lgkmcnt(7)\n\t"
    "ds_read_b64_tr_b16 a[126:127], v12 offset:38664\n\t"
    "v_mfma_f32_16x16x32_bf16 v[196:199], a[108:111], a[36:39], v[196:199]\n\t"
    "ds_read_b64_tr_b16 a[196:197], v39 offset:24576\n\t"
    "v_mfma_f32_16x16x32_bf16 v[204:207], a[104:107], a[40:43], v[204:207]\n\t"
    "buffer_load_dwordx4 v7, s[44:47], s60 offen offset:2048 lds\n\t"
    "v_mfma_f32_16x16x32_bf16 v[204:207], a[108:111], a[44:47], v[204:207]\n\t"
    "s_add_u32 m0, s83, 101376\n\t"
    "ds_read_b64_tr_b16 a[198:199], v39 offset:26112\n\t"
    "v_mfma_f32_16x16x32_bf16 v[212:215], a[104:107], a[48:51], v[212:215]\n\t"
    "s_add_u32 s60, s60, 98304\n\t"
    "ds_read_b64_tr_b16 a[200:201], v39 offset:36864\n\t"
    "v_mfma_f32_16x16x32_bf16 v[212:215], a[108:111], a[52:55], v[212:215]\n\t"
    "ds_read_b64_tr_b16 a[202:203], v39 offset:38400\n\t"
    "v_add_u32 v39, 49152, v39\n\t"
    "v_mfma_f32_16x16x32_bf16 v[220:223], a[104:107], a[56:59], v[220:223]\n\t"
    "buffer_load_dwordx4 v5, s[44:47], s60 offen offset:0 lds\n\t"
    "v_mfma_f32_16x16x32_bf16 v[220:223], a[108:111], a[60:63], v[220:223]\n\t"
    "s_waitcnt lgkmcnt(9)\n\t"
    "ds_read_b64_tr_b16 a[96:97], v24 offset:0\n\t"
    "v_mfma_f32_16x16x32_bf16 v[224:227], a[112:115], a[32:35], v[224:227]\n\t"
    "s_waitcnt lgkmcnt(8)\n\t"
    "ds_read_b64_tr_b16 a[98:99], v24 offset:1536\n\t"
    "v_mfma_f32_16x16x32_bf16 v[224:227], a[116:119], a[36:39], v[224:227]\n\t"
    "ds_read_b64_tr_b16 a[100:101], v24 offset:12288\n\t"
    "v_mfma_f32_16x16x32_bf16 v[232:235], a[112:115], a[40:43], v[232:235]\n\t"
    "buffer_load_dwordx4 v6, s[44:47], s60 offen offset:1024 lds\n\t"
    "v_mfma_f32_16x16x32_bf16 v[232:235], a[116:119], a[44:47], v[232:235]\n\t"
    "ds_read_b64_tr_b16 a[102:103], v24 offset:13824\n\t"
    "v_mfma_f32_16x16x32_bf16 v[240:243], a[112:115], a[48:51], v[240:243]\n\t"
    "ds_read_b64_tr_b16 a[104:105], v24 offset:8\n\t"
    "v_mfma_f32_16x16x32_bf16 v[240:243], a[116:119], a[52:55], v[240:243]\n\t"
    "ds_read_b64_tr_b16 a[106:107], v24 offset:1544\n\t"
    "v_mfma_f32_16x16x32_bf16 v[248:251], a[112:115], a[56:59], v[248:251]\n\t"
    "s_bfm_b64 exec, 8, 0\n\t"
    "buffer_load_dword v45, v61, s[40:43], s95 offen sc1\n\t"
    "s_mov_b64 exec, -1\n\t"
    "v_mfma_f32_16x16x32_bf16 v[248:251], a[116:119], a[60:63], v[248:251]\n\t"
    "s_waitcnt lgkmcnt(9)\n\t"
    "ds_read_b64_tr_b16 a[108:109], v24 offset:12296\n\t"
    "v_mfma_f32_16x16x32_bf16 v[228:231], a[120:123], a[32:35], v[228:231]\n\t"
    "buffer_load_dwordx4 v7, s[44:47], s60 offen offset:2048 lds\n\t"
    "v_mfma_f32_16x16x32_bf16 v[228:231], a[124:127], a[36:39], v[228:231]\n\t"
    "s_add_u32 m0, s83, 0\n\t"
    "ds_read_b64_tr_b16 a[110:111], v24 offset:13832\n\t"
    "v_mfma_f32_16x16x32_bf16 v[236:239], a[120:123], a[40:43], v[236:239]\n\t"
    "s_mul_i32 s60, s98, 384\n\t"
    "ds_read_b64_tr_b16 a[112:113], v25 offset:0\n\t"
    "v_mfma_f32_16x16x32_bf16 v[236:239], a[124:127], a[44:47], v[236:239]\n\t"
    "s_add_u32 s60, s60, 1572864\n\t"
    "ds_read_b64_tr_b16 a[114:115], v25 offset:1536\n\t"
    "v_mfma_f32_16x16x32_bf16 v[244:247], a[120:123], a[48:51], v[244:247]\n\t"
    "s_sub_u32 s62, s60, 4718592\n\t"
    "ds_read_b64_tr_b16 a[116:117], v25 offset:12288\n\t"
    "v_mfma_f32_16x16x32_bf16 v[244:247], a[124:127], a[52:55], v[244:247]\n\t"
    "s_min_u32 s60, s60, s62\n\t"
    "ds_read_b64_tr_b16 a[118:119], v25 offset:13824\n\t"
    "v_mfma_f32_16x16x32_bf16 v[252:255], a[120:123], a[56:59], v[252:255]\n\t"
    "s_add_u32 s60, s60, 3145728\n\t"
    "buffer_load_dwordx4 v5, s[44:47], s60 offen offset:0 lds\n\t"
    "v_mfma_f32_16x16x32_bf16 v[252:255], a[124:127], a[60:63], v[252:255]\n\t"
    "KFP2:\n\t"
    "s_cbranch_execz KFP6\n\t"
    "s_waitcnt lgkmcnt(7)\n\t"
    "ds_read_b64_tr_b16 a[120:121], v25 offset:8\n\t"
    "v_mfma_f32_16x16x32_bf16 a[176:179], a[196:199], a[164:167], a[176:179]\n\t"
    "ds_read_b64_tr_b16 a[122:123], v25 offset:1544\n\t"
    "v_mfma_f32_16x16x32_bf16 a[176:179], a[200:203], a[164:167], a[176:179]\n\t"
    "ds_read_b64_tr_b16 a[124:125], v25 offset:12296\n\t"
    "v_mfma_f32_16x16x32_bf16 v[64:67], a[96:99], a[64:67], v[64:67]\n\t"
    "buffer_load_dwordx4 v6, s[44:47], s60 offen offset:1024 lds\n\t"
    "v_mfma_f32_16x16x32_bf16 v[64:67], a[100:103], a[68:71], v[64:67]\n\t"
    "s_waitcnt vmcnt(21)\n\t"
    "ds_read_b128 a[0:3], v22 offset:32768\n\t"
    "v_mfma_f32_16x16x32_bf16 v[72:75], a[96:99], a[72:75], v[72:75]\n\t"
    "ds_read_b128 a[4:7], v23 offset:32768\n\t"
    "v_mfma_f32_16x16x32_bf16 v[72:75], a[100:103], a[76:79], v[72:75]\n\t"
    "s_waitcnt vmcnt(19)\n\t"
    "ds_read_b128 a[8:11], v22 offset:34816\n\t"
    "v_mfma_f32_16x16x32_bf16 v[80:83], a[96:99], a[80:83], v[80:83]\n\t"
    "buffer_load_dwordx4 v7, s[44:47], s60 offen offset:2048 lds\n\t"
    "v_mfma_f32_16x16x32_bf16 v[80:83], a[100:103], a[84:87], v[80:83]\n\t"
    "ds_read_b128 a[12:15], v23 offset:34816\n\t"
    "v_mfma_f32_16x16x32_bf16 v[88:91], a[96:99], a[88:91], v[88:91]\n\t"
    "s_waitcnt vmcnt(18)\n\t"
    "ds_read_b128 a[16:19], v22 offset:36864\n\t"
    "v_mfma_f32_16x16x32_bf16 v[88:91], a[100:103], a[92:95], v[88:91]\n\t"
    "s_waitcnt lgkmcnt(9)\n\t"
    "ds_read_b128 a[20:23], v23 offset:36864\n\t"
    "v_mfma_f32_16x16x32_bf16 v[68:71], a[104:107], a[64:67], v[68:71]\n\t"
    "ds_read_b64_tr_b16 a[126:127], v25 offset:13832\n\t"
    "v_mfma_f32_16x16x32_bf16 v[68:71], a[108:111], a[68:71], v[68:71]\n\t"
    "s_add_u32 m0, s83, 3072\n\t"
    "s_waitcnt vmcnt(16)\n\t"
    "ds_read_b128 a[24:27], v22 offset:38912\n\t"
    "v_add_u32 v22, s63, v47\n\t"
    "v_mfma_f32_16x16x32_bf16 v[76:79], a[104:107], a[72:75], v[76:79]\n\t"
    "s_add_u32 s60, s60, 98304\n\t"
    "buffer_load_dwordx4 v5, s[44:47], s60 offen offset:0 lds\n\t"
    "v_mfma_f32_16x16x32_bf16 v[76:79], a[108:111], a[76:79], v[76:79]\n\t"
    "s_xor_b32 s63, s82, 3\n\t"
    "ds_read_b128 a[28:31], v23 offset:38912\n\t"
    "v_mfma_f32_16x16x32_bf16 v[84:87], a[104:107], a[80:83], v[84:87]\n\t"
    "s_and_b32 s63, s63, 3\n\t"
    "ds_read_b64_tr_b16 a[96:97], v24 offset:128\n\t"
    "v_lshl_add_u32 v34, s63, 9, v36\n\t"
    "v_mfma_f32_16x16x32_bf16 v[84:87], a[108:111], a[84:87], v[84:87]\n\t"
    "s_mulk_i32 s63, 20480\n\t"
    "ds_read_b64_tr_b16 a[98:99], v24 offset:1664\n\t"
    "v_mfma_f32_16x16x32_bf16 v[92:95], a[104:107], a[88:91], v[92:95]\n\t"
    "s_add_u32 s63, s63, s66\n\t"
    "ds_read_b64_tr_b16 a[100:101], v24 offset:12416\n\t"
    "v_add_u32 v23, s63, v47\n\t"
    "v_mfma_f32_16x16x32_bf16 v[92:95], a[108:111], a[92:95], v[92:95]\n\t"
    "buffer_load_dwordx4 v6, s[44:47], s60 offen offset:1024 lds\n\t"
    "v_mfma_f32_16x16x32_bf16 v[96:99], a[112:115], a[64:67], v[96:99]\n\t"
    "s_waitcnt lgkmcnt(7)\n\t"
    "ds_read_b64_tr_b16 a[102:103], v24 offset:13952\n\t"
    "v_mfma_f32_16x16x32_bf16 v[96:99], a[116:119], a[68:71], v[96:99]\n\t"
    "ds_read_b64_tr_b16 a[104:105], v24 offset:136\n\t"
    "v_mfma_f32_16x16x32_bf16 v[104:107], a[112:115], a[72:75], v[104:107]\n\t"
    "ds_read_b64_tr_b16 a[106:107], v24 offset:1672\n\t"
    "v_mfma_f32_16x16x32_bf16 v[104:107], a[116:119], a[76:79], v[104:107]\n\t"
    "buffer_load_dwordx4 v7, s[44:47], s60 offen offset:2048 lds\n\t"
    "v_mfma_f32_16x16x32_bf16 v[112:115], a[112:115], a[80:83], v[112:115]\n\t"
    "s_add_u32 m0, s83, 122880\n\t"
    "ds_read_b64_tr_b16 a[108:109], v24 offset:12424\n\t"
    "v_mfma_f32_16x16x32_bf16 v[112:115], a[116:119], a[84:87], v[112:115]\n\t"
    "s_mul_i32 s60, s98, 384\n\t"
    "ds_read_b64_tr_b16 a[110:111], v24 offset:13960\n\t"
    "v_mfma_f32_16x16x32_bf16 v[120:123], a[112:115], a[88:91], v[120:123]\n\t"
    "s_add_u32 s60, s60, 786432\n\t"
    "ds_read_b64_tr_b16 a[180:181], v37 offset:0\n\t"
    "v_mfma_f32_16x16x32_bf16 v[120:123], a[116:119], a[92:95], v[120:123]\n\t"
    "ds_read_b64_tr_b16 a[182:183], v37 offset:1536\n\t"
    "v_mfma_f32_16x16x32_bf16 v[100:103], a[120:123], a[64:67], v[100:103]\n\t"
    "s_waitcnt lgkmcnt(9)\n\t"
    "ds_read_b64_tr_b16 a[184:185], v37 offset:12288\n\t"
    "v_mfma_f32_16x16x32_bf16 v[100:103], a[124:127], a[68:71], v[100:103]\n\t"
    "s_sub_u32 s62, s60, 4718592\n\t"
    "ds_read_b64_tr_b16 a[186:187], v37 offset:13824\n\t"
    "v_mfma_f32_16x16x32_bf16 v[108:111], a[120:123], a[72:75], v[108:111]\n\t"
    "s_min_u32 s60, s60, s62\n\t"
    "ds_read_b64_tr_b16 a[112:113], v25 offset:128\n\t"
    "v_mfma_f32_16x16x32_bf16 v[108:111], a[124:127], a[76:79], v[108:111]\n\t"
    "s_add_u32 s60, s60, 3145728\n\t"
    "ds_read_b64_tr_b16 a[114:115], v25 offset:1664\n\t"
    "v_mfma_f32_16x16x32_bf16 v[116:119], a[120:123], a[80:83], v[116:119]\n\t"
    "ds_read_b64_tr_b16 a[116:117], v25 offset:12416\n\t"
    "v_mfma_f32_16x16x32_bf16 v[116:119], a[124:127], a[84:87], v[116:119]\n\t"
    "ds_read_b64_tr_b16 a[118:119], v25 offset:13952\n\t"
    "v_mfma_f32_16x16x32_bf16 v[124:127], a[120:123], a[88:91], v[124:127]\n\t"
    "ds_read_b64_tr_b16 a[120:121], v25 offset:136\n\t"
    "v_mfma_f32_16x16x32_bf16 v[124:127], a[124:127], a[92:95], v[124:127]\n\t"
    "s_waitcnt lgkmcnt(11)\n\t"
    "ds_read_b64_tr_b16 a[122:123], v25 offset:1672\n\t"
    "v_mfma_f32_16x16x32_bf16 v[128:131], a[96:99], a[64:67], v[128:131]\n\t"
    "ds_read_b64_tr_b16 a[124:125], v25 offset:12424\n\t"
    "v_mfma_f32_16x16x32_bf16 v[128:131], a[100:103], a[68:71], v[128:131]\n\t"
    "ds_read_b64_tr_b16 a[126:127], v25 offset:13960\n\t"
    "v_mfma_f32_16x16x32_bf16 v[136:139], a[96:99], a[72:75], v[136:139]\n\t"
    "ds_read_b64_tr_b16 a[188:189], v38 offset:0\n\t"
    "v_mfma_f32_16x16x32_bf16 v[136:139], a[100:103], a[76:79], v[136:139]\n\t"
    "ds_read_b64_tr_b16 a[190:191], v38 offset:1536\n\t"
    "v_mfma_f32_16x16x32_bf16 v[144:147], a[96:99], a[80:83], v[144:147]\n\t"
    "ds_read_b64_tr_b16 a[192:193], v38 offset:12288\n\t"
    "v_mfma_f32_16x16x32_bf16 v[144:147], a[100:103], a[84:87], v[144:147]\n\t"
    "ds_read_b64_tr_b16 a[194:195], v38 offset:13824\n\t"
    "v_mfma_f32_16x16x32_bf16 v[152:155], a[96:99], a[88:91], v[152:155]\n\t"
    "ds_read_b64_tr_b16 a[96:97], v24 offset:256\n\t"
    "v_mfma_f32_16x16x32_bf16 v[152:155], a[100:103], a[92:95], v[152:155]\n\t"
    "ds_read_b64_tr_b16 a[98:99], v24 offset:1792\n\t"
    "v_mfma_f32_16x16x32_bf16 v[132:135], a[104:107], a[64:67], v[132:135]\n\t"
    "s_waitcnt lgkmcnt(14)\n\t"
    "ds_read_b64_tr_b16 a[100:101], v24 offset:12544\n\t"
    "v_mfma_f32_16x16x32_bf16 v[132:135], a[108:111], a[68:71], v[132:135]\n\t"
    "ds_read_b64_tr_b16 a[102:103], v24 offset:14080\n\t"
    "v_mfma_f32_16x16x32_bf16 v[140:143], a[104:107], a[72:75], v[140:143]\n\t"
    "ds_read_b64_tr_b16 a[196:197], v39 offset:0\n\t"
    "v_mfma_f32_16x16x32_bf16 v[140:143], a[108:111], a[76:79], v[140:143]\n\t"
    "ds_read_b64_tr_b16 a[198:199], v39 offset:1536\n\t"
    "v_mfma_f32_16x16x32_bf16 v[148:151], a[104:107], a[80:83], v[148:151]\n\t"
    "ds_read_b64_tr_b16 a[200:201], v39 offset:12288\n\t"
    "v_mfma_f32_16x16x32_bf16 v[148:151], a[108:111], a[84:87], v[148:151]\n\t"
    "ds_read_b64_tr_b16 a[202:203], v39 offset:13824\n\t"
    "v_mfma_f32_16x16x32_bf16 v[156:159], a[104:107], a[88:91], v[156:159]\n\t"
    "ds_read_b64_tr_b16 a[104:105], v24 offset:264\n\t"
    "v_mfma_f32_16x16x32_bf16 v[156:159], a[108:111], a[92:95], v[156:159]\n\t"
    "ds_read_b64_tr_b16 a[106:107], v24 offset:1800\n\t"
    "v_mfma_f32_16x16x32_bf16 a[168:171], a[180:183], a[164:167], a[168:171]\n\t"
    "ds_read_b64_tr_b16 a[108:109], v24 offset:12552\n\t"
    "v_mfma_f32_16x16x32_bf16 a[168:171], a[184:187], a[164:167], a[168:171]\n\t"
    "s_waitcnt lgkmcnt(14)\n\t"
    "ds_read_b64_tr_b16 a[110:111], v24 offset:14088\n\t"
    "v_mfma_f32_16x16x32_bf16 v[160:163], a[112:115], a[64:67], v[160:163]\n\t"
    "v_mfma_f32_16x16x32_bf16 v[160:163], a[116:119], a[68:71], v[160:163]\n\t"
    "v_mfma_f32_16x16x32_bf16 v[168:171], a[112:115], a[72:75], v[168:171]\n\t"
    "s_waitcnt vmcnt(13)\n\t"
    "s_barrier\n\t"
    "ds_read_b64_tr_b16 a[180:181], v37 offset:24576\n\t"
    "v_mfma_f32_16x16x32_bf16 v[168:171], a[116:119], a[76:79], v[168:171]\n\t"
    "buffer_load_dwordx4 v5, s[44:47], s60 offen offset:0 lds\n\t"
    "v_mfma_f32_16x16x32_bf16 v[176:179], a[112:115], a[80:83], v[176:179]\n\t"
    "ds_read_b64_tr_b16 a[182:183], v37 offset:26112\n\t"
    "v_mfma_f32_16x16x32_bf16 v[176:179], a[116:119], a[84:87], v[176:179]\n\t"
    "ds_read_b64_tr_b16 a[184:185], v37 offset:36864\n\t"
    "v_mfma_f32_16x16x32_bf16 v[184:187], a[112:115], a[88:91], v[184:187]\n\t"
    "s_waitcnt vmcnt(8)\n\t"
    "v_cmp_ne_u32 vcc, s80, v45\n\t"
    "s_cbranch_vccnz KFASL\n\t"
    "KFARL:\n\t"
    "v_mfma_f32_16x16x32_bf16 v[184:187], a[116:119], a[92:95], v[184:187]\n\t"
    "buffer_load_dwordx2 v[28:29], v28, s[40:43], s67 offen offset:0 sc1\n\t"
    "v_mfma_f32_16x16x32_bf16 v[164:167], a[120:123], a[64:67], v[164:167]\n\t"
    "ds_read_b64_tr_b16 a[112:113], v25 offset:256\n\t"
    "v_mfma_f32_16x16x32_bf16 v[164:167], a[124:127], a[68:71], v[164:167]\n\t"
    "s_lshr_b32 s65, s83, 1\n\t"
    "ds_read_b64_tr_b16 a[114:115], v25 offset:1792\n\t"
    "v_add_u32 v41, s65, v4\n\t"
    "v_mfma_f32_16x16x32_bf16 v[172:175], a[120:123], a[72:75], v[172:175]\n\t"
    "ds_read_b64_tr_b16 a[116:117], v25 offset:12544\n\t"
    "v_mfma_f32_16x16x32_bf16 v[172:175], a[124:127], a[76:79], v[172:175]\n\t"
    "buffer_load_dwordx2 v[30:31], v30, s[40:43], s67 offen offset:0 sc1\n\t"
    "v_mfma_f32_16x16x32_bf16 v[180:183], a[120:123], a[80:83], v[180:183]\n\t"
    "ds_read_b64_tr_b16 a[118:119], v25 offset:14080\n\t"
    "v_mfma_f32_16x16x32_bf16 v[180:183], a[124:127], a[84:87], v[180:183]\n\t"
    "ds_read_b64_tr_b16 a[186:187], v37 offset:38400\n\t"
    "v_mfma_f32_16x16x32_bf16 v[188:191], a[120:123], a[88:91], v[188:191]\n\t"
    "ds_read_b64_tr_b16 a[120:121], v25 offset:264\n\t"
    "v_mfma_f32_16x16x32_bf16 v[188:191], a[124:127], a[92:95], v[188:191]\n\t"
    "s_waitcnt lgkmcnt(8)\n\t"
    "buffer_load_dwordx2 v[32:33], v32, s[40:43], s67 offen offset:0 sc1\n\t"
    "v_mfma_f32_16x16x32_bf16 v[192:195], a[96:99], a[64:67], v[192:195]\n\t"
    "ds_read_b64_tr_b16 a[122:123], v25 offset:1800\n\t"
    "v_mfma_f32_16x16x32_bf16 v[192:195], a[100:103], a[68:71], v[192:195]\n\t"
    "ds_read_b64_tr_b16 a[124:125], v25 offset:12552\n\t"
    "v_mfma_f32_16x16x32_bf16 v[200:203], a[96:99], a[72:75], v[200:203]\n\t"
    "ds_read_b64_tr_b16 a[126:127], v25 offset:14088\n\t"
    "v_mfma_f32_16x16x32_bf16 v[200:203], a[100:103], a[76:79], v[200:203]\n\t"
    "buffer_load_dwordx2 v[34:35], v34, s[40:43], s67 offen offset:0 sc1\n\t"
    "v_mfma_f32_16x16x32_bf16 v[208:211], a[96:99], a[80:83], v[208:211]\n\t"
    "v_mfma_f32_16x16x32_bf16 v[208:211], a[100:103], a[84:87], v[208:211]\n\t"
    "v_mfma_f32_16x16x32_bf16 v[216:219], a[96:99], a[88:91], v[216:219]\n\t"
    "ds_read_b64_tr_b16 a[96:97], v24 offset:24576\n\t"
    "v_mfma_f32_16x16x32_bf16 v[216:219], a[100:103], a[92:95], v[216:219]\n\t"
    "buffer_load_dwordx4 a[32:35], v20, s[40:43], s61 offen offset:0 sc1\n\t"
    "v_mfma_f32_16x16x32_bf16 a[172:175], a[188:191], a[164:167], a[172:175]\n\t"
    "ds_read_b64_tr_b16 a[98:99], v24 offset:26112\n\t"
    "v_mfma_f32_16x16x32_bf16 a[172:175], a[192:195], a[164:167], a[172:175]\n\t"
    "ds_read_b64_tr_b16 a[100:101], v24 offset:36864\n\t"
    "v_mfma_f32_16x16x32_bf16 v[196:199], a[104:107], a[64:67], v[196:199]\n\t"
    "ds_read_b64_tr_b16 a[102:103], v24 offset:38400\n\t"
    "v_mfma_f32_16x16x32_bf16 v[196:199], a[108:111], a[68:71], v[196:199]\n\t"
    "buffer_load_dwordx4 a[36:39], v20, s[40:43], s61 offen offset:1024 sc1\n\t"
    "v_mfma_f32_16x16x32_bf16 v[204:207], a[104:107], a[72:75], v[204:207]\n\t"
    "ds_read_b64_tr_b16 a[188:189], v38 offset:24576\n\t"
    "v_mfma_f32_16x16x32_bf16 v[204:207], a[108:111], a[76:79], v[204:207]\n\t"
    "ds_read_b64_tr_b16 a[190:191], v38 offset:26112\n\t"
    "v_mfma_f32_16x16x32_bf16 v[212:215], a[104:107], a[80:83], v[212:215]\n\t"
    "ds_read_b64_tr_b16 a[192:193], v38 offset:36864\n\t"
    "v_mfma_f32_16x16x32_bf16 v[212:215], a[108:111], a[84:87], v[212:215]\n\t"
    "buffer_load_dwordx4 a[40:43], v21, s[40:43], s61 offen offset:0 sc1\n\t"
    "v_mfma_f32_16x16x32_bf16 v[220:223], a[104:107], a[88:91], v[220:223]\n\t"
    "ds_read_b64_tr_b16 a[104:105], v24 offset:24584\n\t"
    "v_mfma_f32_16x16x32_bf16 v[220:223], a[108:111], a[92:95], v[220:223]\n\t"
    "s_waitcnt lgkmcnt(10)\n\t"
    "ds_read_b64_tr_b16 a[106:107], v24 offset:26120\n\t"
    "v_mfma_f32_16x16x32_bf16 v[224:227], a[112:115], a[64:67], v[224:227]\n\t"
    "ds_read_b64_tr_b16 a[108:109], v24 offset:36872\n\t"
    "v_mfma_f32_16x16x32_bf16 v[224:227], a[116:119], a[68:71], v[224:227]\n\t"
    "buffer_load_dwordx4 a[44:47], v21, s[40:43], s61 offen offset:1024 sc1\n\t"
    "v_mfma_f32_16x16x32_bf16 v[232:235], a[112:115], a[72:75], v[232:235]\n\t"
    "ds_read_b64_tr_b16 a[110:111], v24 offset:38408\n\t"
    "v_mfma_f32_16x16x32_bf16 v[232:235], a[116:119], a[76:79], v[232:235]\n\t"
    "ds_read_b64_tr_b16 a[194:195], v38 offset:38400\n\t"
    "v_mfma_f32_16x16x32_bf16 v[240:243], a[112:115], a[80:83], v[240:243]\n\t"
    "v_mfma_f32_16x16x32_bf16 v[240:243], a[116:119], a[84:87], v[240:243]\n\t"
    "buffer_load_dwordx4 a[48:51], v22, s[40:43], s61 offen offset:0 sc1\n\t"
    "v_mfma_f32_16x16x32_bf16 v[248:251], a[112:115], a[88:91], v[248:251]\n\t"
    "ds_read_b64_tr_b16 a[112:113], v25 offset:24576\n\t"
    "v_mfma_f32_16x16x32_bf16 v[248:251], a[116:119], a[92:95], v[248:251]\n\t"
    "ds_read_b64_tr_b16 a[114:115], v25 offset:26112\n\t"
    "v_mfma_f32_16x16x32_bf16 v[228:231], a[120:123], a[64:67], v[228:231]\n\t"
    "s_waitcnt lgkmcnt(10)\n\t"
    "ds_read_b64_tr_b16 a[116:117], v25 offset:36864\n\t"
    "v_mfma_f32_16x16x32_bf16 v[228:231], a[124:127], a[68:71], v[228:231]\n\t"
    "buffer_load_dwordx4 a[52:55], v22, s[40:43], s61 offen offset:1024 sc1\n\t"
    "v_mfma_f32_16x16x32_bf16 v[236:239], a[120:123], a[72:75], v[236:239]\n\t"
    "ds_read_b64_tr_b16 a[118:119], v25 offset:38400\n\t"
    "v_mfma_f32_16x16x32_bf16 v[236:239], a[124:127], a[76:79], v[236:239]\n\t"
    "v_mfma_f32_16x16x32_bf16 v[244:247], a[120:123], a[80:83], v[244:247]\n\t"
    "v_mfma_f32_16x16x32_bf16 v[244:247], a[124:127], a[84:87], v[244:247]\n\t"
    "buffer_load_dwordx4 a[56:59], v23, s[40:43], s61 offen offset:0 sc1\n\t"
    "v_mfma_f32_16x16x32_bf16 v[252:255], a[120:123], a[88:91], v[252:255]\n\t"
    "ds_read_b64_tr_b16 a[120:121], v25 offset:24584\n\t"
    "v_mfma_f32_16x16x32_bf16 v[252:255], a[124:127], a[92:95], v[252:255]\n\t"
    "ds_read_b64_tr_b16 a[122:123], v25 offset:26120\n\t"
    "v_mfma_f32_16x16x32_bf16 a[176:179], a[196:199], a[164:167], a[176:179]\n\t"
    "ds_read_b64_tr_b16 a[124:125], v25 offset:36872\n\t"
    "v_mfma_f32_16x16x32_bf16 a[176:179], a[200:203], a[164:167], a[176:179]\n\t"
    "buffer_load_dwordx4 a[60:63], v23, s[40:43], s61 offen offset:1024 sc1\n\t"
    "v_mfma_f32_16x16x32_bf16 v[64:67], a[96:99], a[0:3], v[64:67]\n\t"
    "s_mul_i32 s61, s98, 1\n\t"
    "ds_read_b64_tr_b16 a[126:127], v25 offset:38408\n\t"
    "v_mfma_f32_16x16x32_bf16 v[64:67], a[100:103], a[4:7], v[64:67]\n\t"
    "s_add_u32 s61, s61, 2048\n\t"
    "ds_read_b64_tr_b16 a[196:197], v39 offset:24576\n\t"
    "v_mfma_f32_16x16x32_bf16 v[72:75], a[96:99], a[8:11], v[72:75]\n\t"
    "s_sub_u32 s64, s61, 12288\n\t"
    "ds_read_b64_tr_b16 a[198:199], v39 offset:26112\n\t"
    "v_mfma_f32_16x16x32_bf16 v[72:75], a[100:103], a[12:15], v[72:75]\n\t"
    "buffer_load_dwordx4 v6, s[44:47], s60 offen offset:1024 lds\n\t"
    "v_mfma_f32_16x16x32_bf16 v[80:83], a[96:99], a[16:19], v[80:83]\n\t"
    "s_min_u32 s61, s61, s64\n\t"
    "ds_read_b64_tr_b16 a[200:201], v39 offset:36864\n\t"
    "v_mfma_f32_16x16x32_bf16 v[80:83], a[100:103], a[20:23], v[80:83]\n\t"
    "s_add_u32 s61, s61, 8192\n\t"
    "ds_read_b64_tr_b16 a[202:203], v39 offset:38400\n\t"
    "v_mfma_f32_16x16x32_bf16 v[88:91], a[96:99], a[24:27], v[88:91]\n\t"
    "ds_read_b64_tr_b16 a[96:97], v24 offset:24704\n\t"
    "v_mfma_f32_16x16x32_bf16 v[88:91], a[100:103], a[28:31], v[88:91]\n\t"
    "s_waitcnt lgkmcnt(9)\n\t"
    "buffer_load_dwordx4 v7, s[44:47], s60 offen offset:2048 lds\n\t"
    "v_mfma_f32_16x16x32_bf16 v[68:71], a[104:107], a[0:3], v[68:71]\n\t"
    "s_add_u32 m0, s83, 125952\n\t"
    "ds_read_b64_tr_b16 a[98:99], v24 offset:26240\n\t"
    "v_mfma_f32_16x16x32_bf16 v[68:71], a[108:111], a[4:7], v[68:71]\n\t"
    "s_add_u32 s60, s60, 98304\n\t"
    "ds_read_b64_tr_b16 a[100:101], v24 offset:36992\n\t"
    "v_mfma_f32_16x16x32_bf16 v[76:79], a[104:107], a[8:11], v[76:79]\n\t"
    "ds_read_b64_tr_b16 a[102:103], v24 offset:38528\n\t"
    "v_mfma_f32_16x16x32_bf16 v[76:79], a[108:111], a[12:15], v[76:79]\n\t"
    "buffer_load_dwordx4 v5, s[44:47], s60 offen offset:0 lds\n\t"
    "v_mfma_f32_16x16x32_bf16 v[84:87], a[104:107], a[16:19], v[84:87]\n\t"
    "v_mfma_f32_16x16x32_bf16 v[84:87], a[108:111], a[20:23], v[84:87]\n\t"
    "v_mfma_f32_16x16x32_bf16 v[92:95], a[104:107], a[24:27], v[92:95]\n\t"
    "ds_read_b64_tr_b16 a[104:105], v24 offset:24712\n\t"
    "v_mfma_f32_16x16x32_bf16 v[92:95], a[108:111], a[28:31], v[92:95]\n\t"
    "buffer_load_dwordx4 v6, s[44:47], s60 offen offset:1024 lds\n\t"
    "v_mfma_f32_16x16x32_bf16 v[96:99], a[112:115], a[0:3], v[96:99]\n\t"
    "ds_read_b64_tr_b16 a[106:107], v24 offset:26248\n\t"
    "v_mfma_f32_16x16x32_bf16 v[96:99], a[116:119], a[4:7], v[96:99]\n\t"
    "s_mov_b64 s[56:57], s[70:71]\n\t"
    "ds_read_b64_tr_b16 a[108:109], v24 offset:37000\n\t"
    "v_mfma_f32_16x16x32_bf16 v[104:107], a[112:115], a[8:11], v[104:107]\n\t"
    "s_cmp_eq_u32 s90, 0\n\t"
    "s_cselect_b32 s58, 0, 98304\n\t"
    "ds_read_b64_tr_b16 a[110:111], v24 offset:38536\n\t"
    "v_mfma_f32_16x16x32_bf16 v[104:107], a[116:119], a[12:15], v[104:107]\n\t"
    "buffer_load_dwordx4 v7, s[44:47], s60 offen offset:2048 lds\n\t"
    "v_mfma_f32_16x16x32_bf16 v[112:115], a[112:115], a[16:19], v[112:115]\n\t"
    "s_mov_b32 m0, s86\n\t"
    "s_waitcnt vmcnt(16)\n\t"
    "v_mul_f32 v64, v28, v64\n\t"
    "v_mul_f32 v65, v28, v65\n\t"
    "v_mfma_f32_16x16x32_bf16 v[112:115], a[116:119], a[20:23], v[112:115]\n\t"
    "s_mov_b32 s60, 0\n\t"
    "v_mul_f32 v66, v28, v66\n\t"
    "v_mul_f32 v67, v28, v67\n\t"
    "v_mfma_f32_16x16x32_bf16 v[120:123], a[112:115], a[24:27], v[120:123]\n\t"
    "ds_read_b64_tr_b16 a[112:113], v25 offset:24704\n\t"
    "s_waitcnt vmcnt(15)\n\t"
    "v_mul_f32 v72, v30, v72\n\t"
    "v_mfma_f32_16x16x32_bf16 v[120:123], a[116:119], a[28:31], v[120:123]\n\t"
    "s_waitcnt lgkmcnt(5)\n\t"
    "buffer_load_dwordx4 a[64:67], v20, s[40:43], s61 offen offset:0 sc1\n\t"
    "v_mfma_f32_16x16x32_bf16 v[100:103], a[120:123], a[0:3], v[100:103]\n\t"
    "ds_read_b64_tr_b16 a[114:115], v25 offset:26240\n\t"
    "v_mul_f32 v73, v30, v73\n\t"
    "v_mfma_f32_16x16x32_bf16 v[100:103], a[124:127], a[4:7], v[100:103]\n\t"
    "ds_read_b64_tr_b16 a[116:117], v25 offset:36992\n\t"
    "v_mul_f32 v74, v30, v74\n\t"
    "v_mfma_f32_16x16x32_bf16 v[108:111], a[120:123], a[8:11], v[108:111]\n\t"
    "ds_read_b64_tr_b16 a[118:119], v25 offset:38528\n\t"
    "v_mul_f32 v75, v30, v75\n\t"
    "v_mfma_f32_16x16x32_bf16 v[108:111], a[124:127], a[12:15], v[108:111]\n\t"
    "buffer_load_dwordx4 a[68:71], v20, s[40:43], s61 offen offset:1024 sc1\n\t"
    "v_mfma_f32_16x16x32_bf16 v[116:119], a[120:123], a[16:19], v[116:119]\n\t"
    "s_waitcnt vmcnt(16)\n\t"
    "v_mul_f32 v80, v32, v80\n\t"
    "v_mul_f32 v81, v32, v81\n\t"
    "v_mfma_f32_16x16x32_bf16 v[116:119], a[124:127], a[20:23], v[116:119]\n\t"
    "v_mul_f32 v82, v32, v82\n\t"
    "v_mul_f32 v83, v32, v83\n\t"
    "v_mfma_f32_16x16x32_bf16 v[124:127], a[120:123], a[24:27], v[124:127]\n\t"
    "ds_read_b64_tr_b16 a[120:121], v25 offset:24712\n\t"
    "s_waitcnt vmcnt(15)\n\t"
    "v_mul_f32 v88, v34, v88\n\t"
    "v_mfma_f32_16x16x32_bf16 v[124:127], a[124:127], a[28:31], v[124:127]\n\t"
    "buffer_load_dwordx4 a[72:75], v21, s[40:43], s61 offen offset:0 sc1\n\t"
    "v_mfma_f32_16x16x32_bf16 v[128:131], a[96:99], a[0:3], v[128:131]\n\t"
    "ds_read_b64_tr_b16 a[122:123], v25 offset:26248\n\t"
    "v_mul_f32 v89, v34, v89\n\t"
    "v_mfma_f32_16x16x32_bf16 v[128:131], a[100:103], a[4:7], v[128:131]\n\t"
    "ds_read_b64_tr_b16 a[124:125], v25 offset:37000\n\t"
    "v_mul_f32 v90, v34, v90\n\t"
    "v_mfma_f32_16x16x32_bf16 v[136:139], a[96:99], a[8:11], v[136:139]\n\t"
    "ds_read_b64_tr_b16 a[126:127], v25 offset:38536\n\t"
    "v_mul_f32 v91, v34, v91\n\t"
    "v_mfma_f32_16x16x32_bf16 v[136:139], a[100:103], a[12:15], v[136:139]\n\t"
    "buffer_load_dwordx4 a[76:79], v21, s[40:43], s61 offen offset:1024 sc1\n\t"
    "v_mfma_f32_16x16x32_bf16 v[144:147], a[96:99], a[16:19], v[144:147]\n\t"
    "v_mul_f32 v68, v28, v68\n\t"
    "v_mul_f32 v69, v28, v69\n\t"
    "v_mfma_f32_16x16x32_bf16 v[144:147], a[100:103], a[20:23], v[144:147]\n\t"
    "v_mul_f32 v70, v28, v70\n\t"
    "v_mul_f32 v71, v28, v71\n\t"
    "v_mfma_f32_16x16x32_bf16 v[152:155], a[96:99], a[24:27], v[152:155]\n\t"
    "ds_read_b64_tr_b16 a[96:97], v24 offset:24832\n\t"
    "v_mul_f32 v76, v30, v76\n\t"
    "v_mfma_f32_16x16x32_bf16 v[152:155], a[100:103], a[28:31], v[152:155]\n\t"
    "s_waitcnt lgkmcnt(5)\n\t"
    "buffer_load_dwordx4 a[80:83], v22, s[40:43], s61 offen offset:0 sc1\n\t"
    "v_mfma_f32_16x16x32_bf16 v[132:135], a[104:107], a[0:3], v[132:135]\n\t"
    "ds_read_b64_tr_b16 a[98:99], v24 offset:26368\n\t"
    "v_mul_f32 v77, v30, v77\n\t"
    "v_mfma_f32_16x16x32_bf16 v[132:135], a[108:111], a[4:7], v[132:135]\n\t"
    "ds_read_b64_tr_b16 a[100:101], v24 offset:37120\n\t"
    "v_mul_f32 v78, v30, v78\n\t"
    "v_mfma_f32_16x16x32_bf16 v[140:143], a[104:107], a[8:11], v[140:143]\n\t"
    "ds_read_b64_tr_b16 a[102:103], v24 offset:38656\n\t"
    "v_mul_f32 v79, v30, v79\n\t"
    "v_mfma_f32_16x16x32_bf16 v[140:143], a[108:111], a[12:15], v[140:143]\n\t"
    "buffer_load_dwordx4 a[84:87], v22, s[40:43], s61 offen offset:1024 sc1\n\t"
    "v_mfma_f32_16x16x32_bf16 v[148:151], a[104:107], a[16:19], v[148:151]\n\t"
    "v_mul_f32 v84, v32, v84\n\t"
    "v_mul_f32 v85, v32, v85\n\t"
    "v_mfma_f32_16x16x32_bf16 v[148:151], a[108:111], a[20:23], v[148:151]\n\t"
    "v_mul_f32 v86, v32, v86\n\t"
    "v_mul_f32 v87, v32, v87\n\t"
    "v_mfma_f32_16x16x32_bf16 v[156:159], a[104:107], a[24:27], v[156:159]\n\t"
    "ds_read_b64_tr_b16 a[104:105], v24 offset:24840\n\t"
    "v_mul_f32 v92, v34, v92\n\t"
    "v_mfma_f32_16x16x32_bf16 v[156:159], a[108:111], a[28:31], v[156:159]\n\t"
    "buffer_load_dwordx4 a[88:91], v23, s[40:43], s61 offen offset:0 sc1\n\t"
    "v_mfma_f32_16x16x32_bf16 a[168:171], a[180:183], a[164:167], a[168:171]\n\t"
    "ds_read_b64_tr_b16 a[106:107], v24 offset:26376\n\t"
    "v_mul_f32 v93, v34, v93\n\t"
    "v_mfma_f32_16x16x32_bf16 a[168:171], a[184:187], a[164:167], a[168:171]\n\t"
    "ds_read_b64_tr_b16 a[108:109], v24 offset:37128\n\t"
    "v_mul_f32 v94, v34, v94\n\t"
    "v_mfma_f32_16x16x32_bf16 v[160:163], a[112:115], a[0:3], v[160:163]\n\t"
    "ds_read_b64_tr_b16 a[110:111], v24 offset:38664\n\t"
    "v_mul_f32 v95, v34, v95\n\t"
    "v_mfma_f32_16x16x32_bf16 v[160:163], a[116:119], a[4:7], v[160:163]\n\t"
    "buffer_load_dwordx4 a[92:95], v23, s[40:43], s61 offen offset:1024 sc1\n\t"
    "v_mfma_f32_16x16x32_bf16 v[168:171], a[112:115], a[8:11], v[168:171]\n\t"
    "s_barrier\n\t"
    "v_mul_f32 v96, v28, v96\n\t"
    "v_mfma_f32_16x16x32_bf16 v[168:171], a[116:119], a[12:15], v[168:171]\n\t"
    "v_mul_f32 v97, v28, v97\n\t"
    "v_mul_f32 v98, v28, v98\n\t"
    "v_mfma_f32_16x16x32_bf16 v[176:179], a[112:115], a[16:19], v[176:179]\n\t"
    "v_mul_f32 v99, v28, v99\n\t"
    "v_mul_f32 v104, v30, v104\n\t"
    "v_mfma_f32_16x16x32_bf16 v[176:179], a[116:119], a[20:23], v[176:179]\n\t"
    "buffer_load_dwordx4 v54, s[56:59], s60 offen offset:0 nt lds\n\t"
    "v_mfma_f32_16x16x32_bf16 v[184:187], a[112:115], a[24:27], v[184:187]\n\t"
    "s_mov_b32 s60, 23552\n\t"
    "ds_read_b64_tr_b16 a[112:113], v25 offset:24832\n\t"
    "v_mul_f32 v105, v30, v105\n\t"
    "v_mfma_f32_16x16x32_bf16 v[184:187], a[116:119], a[28:31], v[184:187]\n\t"
    "s_waitcnt lgkmcnt(4)\n\t"
    "ds_read_b64_tr_b16 a[114:115], v25 offset:26368\n\t"
    "v_mul_f32 v106, v30, v106\n\t"
    "v_mfma_f32_16x16x32_bf16 v[164:167], a[120:123], a[0:3], v[164:167]\n\t"
    "ds_read_b64_tr_b16 a[116:117], v25 offset:37120\n\t"
    "v_mul_f32 v107, v30, v107\n\t"
    "v_mfma_f32_16x16x32_bf16 v[164:167], a[124:127], a[4:7], v[164:167]\n\t"
    "buffer_load_dwordx4 v54, s[56:59], s60 offen offset:1024 nt lds\n\t"
    "v_mfma_f32_16x16x32_bf16 v[172:175], a[120:123], a[8:11], v[172:175]\n\t"
    "s_mov_b32 s60, 47104\n\t"
    "ds_read_b64_tr_b16 a[118:119], v25 offset:38656\n\t"
    "v_mul_f32 v112, v32, v112\n\t"
    "v_mfma_f32_16x16x32_bf16 v[172:175], a[124:127], a[12:15], v[172:175]\n\t"
    "v_mul_f32 v113, v32, v113\n\t"
    "v_mul_f32 v114, v32, v114\n\t"
    "v_mfma_f32_16x16x32_bf16 v[180:183], a[120:123], a[16:19], v[180:183]\n\t"
    "v_mul_f32 v115, v32, v115\n\t"
    "v_mul_f32 v120, v34, v120\n\t"
    "v_mfma_f32_16x16x32_bf16 v[180:183], a[124:127], a[20:23], v[180:183]\n\t"
    "buffer_load_dwordx4 v54, s[56:59], s60 offen offset:2048 nt lds\n\t"
    "v_mfma_f32_16x16x32_bf16 v[188:191], a[120:123], a[24:27], v[188:191]\n\t"
    "s_mov_b32 s60, 70656\n\t"
    "ds_read_b64_tr_b16 a[120:121], v25 offset:24840\n\t"
    "v_mul_f32 v121, v34, v121\n\t"
    "v_mfma_f32_16x16x32_bf16 v[188:191], a[124:127], a[28:31], v[188:191]\n\t"
    "ds_read_b64_tr_b16 a[122:123], v25 offset:26376\n\t"
    "v_mul_f32 v122, v34, v122\n\t"
    "v_mfma_f32_16x16x32_bf16 v[192:195], a[96:99], a[0:3], v[192:195]\n\t"
    "ds_read_b64_tr_b16 a[124:125], v25 offset:37128\n\t"
    "v_mul_f32 v123, v34, v123\n\t"
    "v_mfma_f32_16x16x32_bf16 v[192:195], a[100:103], a[4:7], v[192:195]\n\t"
    "buffer_load_dwordx4 v54, s[56:59], s60 offen offset:3072 nt lds\n\t"
    "v_mfma_f32_16x16x32_bf16 v[200:203], a[96:99], a[8:11], v[200:203]\n\t"
    "s_add_u32 m0, s83, 24576\n\t"
    "ds_read_b64_tr_b16 a[126:127], v25 offset:38664\n\t"
    "v_mul_f32 v100, v28, v100\n\t"
    "v_mfma_f32_16x16x32_bf16 v[200:203], a[100:103], a[12:15], v[200:203]\n\t"
    "s_mul_i32 s60, s98, 384\n\t"
    "ds_write_b128 v41, a[168:171] offset:49152\n\t"
    "v_mfma_f32_16x16x32_bf16 v[208:211], a[96:99], a[16:19], v[208:211]\n\t"
    "s_add_u32 s60, s60, 2359296\n\t"
    "v_mul_f32 v101, v28, v101\n\t"
    "v_mul_f32 v102, v28, v102\n\t"
    "v_mfma_f32_16x16x32_bf16 v[208:211], a[100:103], a[20:23], v[208:211]\n\t"
    "s_sub_u32 s62, s60, 4718592\n\t"
    "v_mul_f32 v103, v28, v103\n\t"
    "v_mul_f32 v108, v30, v108\n\t"
    "v_mfma_f32_16x16x32_bf16 v[216:219], a[96:99], a[24:27], v[216:219]\n\t"
    "s_min_u32 s60, s60, s62\n\t"
    "ds_read_b64_tr_b16 a[96:97], v26 offset:0\n\t"
    "v_mul_f32 v109, v30, v109\n\t"
    "v_mfma_f32_16x16x32_bf16 v[216:219], a[100:103], a[28:31], v[216:219]\n\t"
    "ds_read_b64_tr_b16 a[98:99], v26 offset:1536\n\t"
    "v_mul_f32 v110, v30, v110\n\t"
    "v_mfma_f32_16x16x32_bf16 a[172:175], a[188:191], a[164:167], a[172:175]\n\t"
    "ds_read_b64_tr_b16 a[100:101], v26 offset:12288\n\t"
    "v_mul_f32 v111, v30, v111\n\t"
    "v_mfma_f32_16x16x32_bf16 a[172:175], a[192:195], a[164:167], a[172:175]\n\t"
    "s_waitcnt lgkmcnt(6)\n\t"
    "ds_read_b64_tr_b16 a[102:103], v26 offset:13824\n\t"
    "v_mul_f32 v116, v32, v116\n\t"
    "v_mfma_f32_16x16x32_bf16 v[196:199], a[104:107], a[0:3], v[196:199]\n\t"
    "v_mul_f32 v117, v32, v117\n\t"
    "v_mul_f32 v118, v32, v118\n\t"
    "v_mfma_f32_16x16x32_bf16 v[196:199], a[108:111], a[4:7], v[196:199]\n\t"
    "s_add_u32 s60, s60, 3145728\n\t"
    "v_mul_f32 v119, v32, v119\n\t"
    "v_mul_f32 v124, v34, v124\n\t"
    "v_mfma_f32_16x16x32_bf16 v[204:207], a[104:107], a[8:11], v[204:207]\n\t"
    "ds_write_b128 v41, a[172:175] offset:50176\n\t"
    "v_mfma_f32_16x16x32_bf16 v[204:207], a[108:111], a[12:15], v[204:207]\n\t"
    "v_mul_f32 v125, v34, v125\n\t"
    "v_mul_f32 v126, v34, v126\n\t"
    "v_mfma_f32_16x16x32_bf16 v[212:215], a[104:107], a[16:19], v[212:215]\n\t"
    "v_mul_f32 v127, v34, v127\n\t"
    "v_mul_f32 v128, v28, v128\n\t"
    "v_mfma_f32_16x16x32_bf16 v[212:215], a[108:111], a[20:23], v[212:215]\n\t"
    "v_mul_f32 v129, v28, v129\n\t"
    "v_mul_f32 v130, v28, v130\n\t"
    "v_mfma_f32_16x16x32_bf16 v[220:223], a[104:107], a[24:27], v[220:223]\n\t"
    "ds_read_b64_tr_b16 a[104:105], v26 offset:8\n\t"
    "v_mul_f32 v131, v28, v131\n\t"
    "v_mfma_f32_16x16x32_bf16 v[220:223], a[108:111], a[28:31], v[220:223]\n\t"
    "ds_read_b64_tr_b16 a[106:107], v26 offset:1544\n\t"
    "v_mul_f32 v136, v30, v136\n\t"
    "v_mfma_f32_16x16x32_bf16 v[224:227], a[112:115], a[0:3], v[224:227]\n\t"
    "ds_read_b64_tr_b16 a[108:109], v26 offset:12296\n\t"
    "v_mul_f32 v137, v30, v137\n\t"
    "v_mfma_f32_16x16x32_bf16 v[224:227], a[116:119], a[4:7], v[224:227]\n\t"
    "ds_read_b64_tr_b16 a[110:111], v26 offset:13832\n\t"
    "v_mul_f32 v138, v30, v138\n\t"
    "v_mfma_f32_16x16x32_bf16 v[232:235], a[112:115], a[8:11], v[232:235]\n\t"
    "v_mul_f32 v139, v30, v139\n\t"
    "v_mul_f32 v144, v32, v144\n\t"
    "v_mfma_f32_16x16x32_bf16 v[232:235], a[116:119], a[12:15], v[232:235]\n\t"
    "v_mul_f32 v145, v32, v145\n\t"
    "v_mul_f32 v146, v32, v146\n\t"
    "v_mfma_f32_16x16x32_bf16 v[240:243], a[112:115], a[16:19], v[240:243]\n\t"
    "v_mul_f32 v147, v32, v147\n\t"
    "v_mul_f32 v152, v34, v152\n\t"
    "v_mfma_f32_16x16x32_bf16 v[240:243], a[116:119], a[20:23], v[240:243]\n\t"
    "v_mul_f32 v153, v34, v153\n\t"
    "v_mul_f32 v154, v34, v154\n\t"
    "v_mfma_f32_16x16x32_bf16 v[248:251], a[112:115], a[24:27], v[248:251]\n\t"
    "s_mov_b32 s63, 49315840\n\t"
    "buffer_load_dwordx4 v[44:47], v4, s[40:43], s63 offen offset:0 sc1\n\t"
    "v_mfma_f32_16x16x32_bf16 v[248:251], a[116:119], a[28:31], v[248:251]\n\t"
    "ds_read_b64_tr_b16 a[112:113], v27 offset:0\n\t"
    "v_mul_f32 v155, v34, v155\n\t"
    "v_mfma_f32_16x16x32_bf16 v[228:231], a[120:123], a[0:3], v[228:231]\n\t"
    "s_waitcnt lgkmcnt(5)\n\t"
    "ds_read_b64_tr_b16 a[114:115], v27 offset:1536\n\t"
    "v_mul_f32 v132, v28, v132\n\t"
    "v_mfma_f32_16x16x32_bf16 v[228:231], a[124:127], a[4:7], v[228:231]\n\t"
    "ds_read_b64_tr_b16 a[116:117], v27 offset:12288\n\t"
    "v_mul_f32 v133, v28, v133\n\t"
    "v_mfma_f32_16x16x32_bf16 v[236:239], a[120:123], a[8:11], v[236:239]\n\t"
    "buffer_load_dwordx4 v[48:51], v4, s[40:43], s63 offen offset:1024 sc1\n\t"
    "v_mfma_f32_16x16x32_bf16 v[236:239], a[124:127], a[12:15], v[236:239]\n\t"
    "ds_read_b64_tr_b16 a[118:119], v27 offset:13824\n\t"
    "v_mul_f32 v134, v28, v134\n\t"
    "v_mfma_f32_16x16x32_bf16 v[244:247], a[120:123], a[16:19], v[244:247]\n\t"
    "v_mul_f32 v135, v28, v135\n\t"
    "v_mul_f32 v140, v30, v140\n\t"
    "v_mfma_f32_16x16x32_bf16 v[244:247], a[124:127], a[20:23], v[244:247]\n\t"
    "v_mul_f32 v141, v30, v141\n\t"
    "v_mul_f32 v142, v30, v142\n\t"
    "v_mfma_f32_16x16x32_bf16 v[252:255], a[120:123], a[24:27], v[252:255]\n\t"
    "buffer_load_dwordx4 v[52:55], v4, s[40:43], s63 offen offset:2048 sc1\n\t"
    "v_mfma_f32_16x16x32_bf16 v[252:255], a[124:127], a[28:31], v[252:255]\n\t"
    "ds_read_b64_tr_b16 a[120:121], v27 offset:8\n\t"
    "v_mul_f32 v143, v30, v143\n\t"
    "v_mfma_f32_16x16x32_bf16 a[176:179], a[196:199], a[164:167], a[176:179]\n\t"
    "ds_read_b64_tr_b16 a[122:123], v27 offset:1544\n\t"
    "v_mul_f32 v148, v32, v148\n\t"
    "v_mfma_f32_16x16x32_bf16 a[176:179], a[200:203], a[164:167], a[176:179]\n\t"
    "s_waitcnt vmcnt(20)\n\t"
    "ds_read_b64_tr_b16 a[124:125], v27 offset:12296\n\t"
    "v_mul_f32 v149, v32, v149\n\t"
    "v_mfma_f32_16x16x32_bf16 v[64:67], a[96:99], a[32:35], v[64:67]\n\t"
    "buffer_load_dwordx4 v[56:59], v4, s[40:43], s63 offen offset:3072 sc1\n\t"
    "v_mfma_f32_16x16x32_bf16 v[64:67], a[100:103], a[36:39], v[64:67]\n\t"
    "ds_read_b64_tr_b16 a[126:127], v27 offset:13832\n\t"
    "v_mul_f32 v150, v32, v150\n\t"
    "v_mfma_f32_16x16x32_bf16 v[72:75], a[96:99], a[40:43], v[72:75]\n\t"
    "ds_write_b128 v41, a[176:179] offset:51200\n\t"
    "v_mfma_f32_16x16x32_bf16 v[72:75], a[100:103], a[44:47], v[72:75]\n\t"
    "v_mul_f32 v151, v32, v151\n\t"
    "v_mul_f32 v156, v34, v156\n\t"
    "v_mfma_f32_16x16x32_bf16 v[80:83], a[96:99], a[48:51], v[80:83]\n\t"
    "v_mul_f32 v157, v34, v157\n\t"
    "v_mul_f32 v158, v34, v158\n\t"
    "v_mfma_f32_16x16x32_bf16 v[80:83], a[100:103], a[52:55], v[80:83]\n\t"
    "v_mul_f32 v159, v34, v159\n\t"
    "v_mul_f32 v160, v28, v160\n\t"
    "v_mfma_f32_16x16x32_bf16 v[88:91], a[96:99], a[56:59], v[88:91]\n\t"
    "ds_read_b64_tr_b16 a[96:97], v26 offset:128\n\t"
    "v_mul_f32 v161, v28, v161\n\t"
    "v_mfma_f32_16x16x32_bf16 v[88:91], a[100:103], a[60:63], v[88:91]\n\t"
    "s_waitcnt lgkmcnt(6)\n\t"
    "ds_read_b64_tr_b16 a[98:99], v26 offset:1664\n\t"
    "v_mul_f32 v162, v28, v162\n\t"
    "v_mfma_f32_16x16x32_bf16 v[68:71], a[104:107], a[32:35], v[68:71]\n\t"
    "ds_read_b64_tr_b16 a[100:101], v26 offset:12416\n\t"
    "v_mul_f32 v163, v28, v163\n\t"
    "v_mfma_f32_16x16x32_bf16 v[68:71], a[108:111], a[36:39], v[68:71]\n\t"
    "ds_read_b64_tr_b16 a[102:103], v26 offset:13952\n\t"
    "v_mul_f32 v168, v30, v168\n\t"
    "v_mfma_f32_16x16x32_bf16 v[76:79], a[104:107], a[40:43], v[76:79]\n\t"
    "v_mul_f32 v169, v30, v169\n\t"
    "v_mul_f32 v170, v30, v170\n\t"
    "v_mfma_f32_16x16x32_bf16 v[76:79], a[108:111], a[44:47], v[76:79]\n\t"
    "v_mul_f32 v171, v30, v171\n\t"
    "v_mul_f32 v176, v32, v176\n\t"
    "v_mfma_f32_16x16x32_bf16 v[84:87], a[104:107], a[48:51], v[84:87]\n\t"
    "v_mul_f32 v177, v32, v177\n\t"
    "v_mul_f32 v178, v32, v178\n\t"
    "v_mfma_f32_16x16x32_bf16 v[84:87], a[108:111], a[52:55], v[84:87]\n\t"
    "v_mul_f32 v179, v32, v179\n\t"
    "v_mul_f32 v184, v34, v184\n\t"
    "v_mfma_f32_16x16x32_bf16 v[92:95], a[104:107], a[56:59], v[92:95]\n\t"
    "ds_read_b64_tr_b16 a[104:105], v26 offset:136\n\t"
    "v_mul_f32 v185, v34, v185\n\t"
    "v_mfma_f32_16x16x32_bf16 v[92:95], a[108:111], a[60:63], v[92:95]\n\t"
    "ds_read_b64_tr_b16 a[106:107], v26 offset:1672\n\t"
    "v_mul_f32 v186, v34, v186\n\t"
    "v_mfma_f32_16x16x32_bf16 v[96:99], a[112:115], a[32:35], v[96:99]\n\t"
    "ds_read_b64_tr_b16 a[108:109], v26 offset:12424\n\t"
    "v_mul_f32 v187, v34, v187\n\t"
    "v_mfma_f32_16x16x32_bf16 v[96:99], a[116:119], a[36:39], v[96:99]\n\t"
    "ds_read_b64_tr_b16 a[110:111], v26 offset:13960\n\t"
    "v_mul_f32 v164, v28, v164\n\t"
    "v_mfma_f32_16x16x32_bf16 v[104:107], a[112:115], a[40:43], v[104:107]\n\t"
    "KFP3:\n\t"
    "s_cbranch_execz KFP7\n\t"
    "v_mul_f32 v165, v28, v165\n\t"
    "v_mul_f32 v166, v28, v166\n\t"
    "v_mfma_f32_16x16x32_bf16 v[104:107], a[116:119], a[44:47], v[104:107]\n\t"
    "v_mul_f32 v167, v28, v167\n\t"
    "v_mul_f32 v172, v30, v172\n\t"
    "v_mfma_f32_16x16x32_bf16 v[112:115], a[112:115], a[48:51], v[112:115]\n\t"
    "v_mul_f32 v173, v30, v173\n\t"
    "v_mul_f32 v174, v30, v174\n\t"
    "v_mfma_f32_16x16x32_bf16 v[112:115], a[116:119], a[52:55], v[112:115]\n\t"
    "v_mul_f32 v175, v30, v175\n\t"
    "v_mul_f32 v180, v32, v180\n\t"
    "v_mfma_f32_16x16x32_bf16 v[120:123], a[112:115], a[56:59], v[120:123]\n\t"
    "s_mul_i32 s61, s98, 1\n\t"
    "ds_read_b64_tr_b16 a[112:113], v27 offset:128\n\t"
    "v_mul_f32 v181, v32, v181\n\t"
    "v_mfma_f32_16x16x32_bf16 v[120:123], a[116:119], a[60:63], v[120:123]\n\t"
    "s_waitcnt lgkmcnt(5)\n\t"
    "s_add_u32 s61, s61, 4096\n\t"
    "ds_read_b64_tr_b16 a[114:115], v27 offset:1664\n\t"
    "v_mfma_f32_16x16x32_bf16 v[100:103], a[120:123], a[32:35], v[100:103]\n\t"
    "s_sub_u32 s64, s61, 12288\n\t"
    "ds_read_b64_tr_b16 a[116:117], v27 offset:12416\n\t"
    "v_mfma_f32_16x16x32_bf16 v[100:103], a[124:127], a[36:39], v[100:103]\n\t"
    "s_min_u32 s61, s61, s64\n\t"
    "ds_read_b64_tr_b16 a[118:119], v27 offset:13952\n\t"
    "v_mul_f32 v182, v32, v182\n\t"
    "v_mfma_f32_16x16x32_bf16 v[108:111], a[120:123], a[40:43], v[108:111]\n\t"
    "s_add_u32 s61, s61, 8192\n\t"
    "buffer_load_dwordx4 a[0:3], v20, s[40:43], s61 offen offset:0 sc1\n\t"
    "v_mfma_f32_16x16x32_bf16 v[108:111], a[124:127], a[44:47], v[108:111]\n\t"
    "v_mul_f32 v183, v32, v183\n\t"
    "v_mul_f32 v188, v34, v188\n\t"
    "v_mfma_f32_16x16x32_bf16 v[116:119], a[120:123], a[48:51], v[116:119]\n\t"
    "v_mul_f32 v189, v34, v189\n\t"
    "v_mul_f32 v190, v34, v190\n\t"
    "v_mfma_f32_16x16x32_bf16 v[116:119], a[124:127], a[52:55], v[116:119]\n\t"
    "v_mul_f32 v191, v34, v191\n\t"
    "v_mul_f32 v192, v28, v192\n\t"
    "v_mfma_f32_16x16x32_bf16 v[124:127], a[120:123], a[56:59], v[124:127]\n\t"
    "buffer_load_dwordx4 a[4:7], v20, s[40:43], s61 offen offset:1024 sc1\n\t"
    "v_mfma_f32_16x16x32_bf16 v[124:127], a[124:127], a[60:63], v[124:127]\n\t"
    "ds_read_b64_tr_b16 a[120:121], v27 offset:136\n\t"
    "v_mul_f32 v193, v28, v193\n\t"
    "v_mfma_f32_16x16x32_bf16 v[128:131], a[96:99], a[32:35], v[128:131]\n\t"
    "ds_read_b64_tr_b16 a[122:123], v27 offset:1672\n\t"
    "v_mul_f32 v194, v28, v194\n\t"
    "v_mfma_f32_16x16x32_bf16 v[128:131], a[100:103], a[36:39], v[128:131]\n\t"
    "ds_read_b64_tr_b16 a[124:125], v27 offset:12424\n\t"
    "v_mul_f32 v195, v28, v195\n\t"
    "v_mfma_f32_16x16x32_bf16 v[136:139], a[96:99], a[40:43], v[136:139]\n\t"
    "buffer_load_dwordx4 a[8:11], v21, s[40:43], s61 offen offset:0 sc1\n\t"
    "v_mfma_f32_16x16x32_bf16 v[136:139], a[100:103], a[44:47], v[136:139]\n\t"
    "ds_read_b64_tr_b16 a[126:127], v27 offset:13960\n\t"
    "v_mul_f32 v200, v30, v200\n\t"
    "v_mfma_f32_16x16x32_bf16 v[144:147], a[96:99], a[48:51], v[144:147]\n\t"
    "v_mul_f32 v201, v30, v201\n\t"
    "v_mul_f32 v202, v30, v202\n\t"
    "v_mfma_f32_16x16x32_bf16 v[144:147], a[100:103], a[52:55], v[144:147]\n\t"
    "v_mul_f32 v203, v30, v203\n\t"
    "v_mul_f32 v208, v32, v208\n\t"
    "v_mfma_f32_16x16x32_bf16 v[152:155], a[96:99], a[56:59], v[152:155]\n\t"
    "buffer_load_dwordx4 a[12:15], v21, s[40:43], s61 offen offset:1024 sc1\n\t"
    "v_mfma_f32_16x16x32_bf16 v[152:155], a[100:103], a[60:63], v[152:155]\n\t"
    "s_waitcnt lgkmcnt(4)\n\t"
    "ds_read_b64_tr_b16 a[96:97], v26 offset:256\n\t"
    "v_mul_f32 v209, v32, v209\n\t"
    "v_mfma_f32_16x16x32_bf16 v[132:135], a[104:107], a[32:35], v[132:135]\n\t"
    "ds_read_b64_tr_b16 a[98:99], v26 offset:1792\n\t"
    "v_mul_f32 v210, v32, v210\n\t"
    "v_mfma_f32_16x16x32_bf16 v[132:135], a[108:111], a[36:39], v[132:135]\n\t"
    "ds_read_b64_tr_b16 a[100:101], v26 offset:12544\n\t"
    "v_mul_f32 v211, v32, v211\n\t"
    "v_mfma_f32_16x16x32_bf16 v[140:143], a[104:107], a[40:43], v[140:143]\n\t"
    "buffer_load_dwordx4 a[16:19], v22, s[40:43], s61 offen offset:0 sc1\n\t"
    "v_mfma_f32_16x16x32_bf16 v[140:143], a[108:111], a[44:47], v[140:143]\n\t"
    "ds_read_b64_tr_b16 a[102:103], v26 offset:14080\n\t"
    "v_mul_f32 v216, v34, v216\n\t"
    "v_mfma_f32_16x16x32_bf16 v[148:151], a[104:107], a[48:51], v[148:151]\n\t"
    "v_mul_f32 v217, v34, v217\n\t"
    "v_mul_f32 v218, v34, v218\n\t"
    "v_mfma_f32_16x16x32_bf16 v[148:151], a[108:111], a[52:55], v[148:151]\n\t"
    "v_mul_f32 v219, v34, v219\n\t"
    "v_mul_f32 v196, v28, v196\n\t"
    "v_mfma_f32_16x16x32_bf16 v[156:159], a[104:107], a[56:59], v[156:159]\n\t"
    "buffer_load_dwordx4 a[20:23], v22, s[40:43], s61 offen offset:1024 sc1\n\t"
    "v_mfma_f32_16x16x32_bf16 v[156:159], a[108:111], a[60:63], v[156:159]\n\t"
    "ds_read_b64_tr_b16 a[104:105], v26 offset:264\n\t"
    "v_mul_f32 v197, v28, v197\n\t"
    "v_mfma_f32_16x16x32_bf16 v[160:163], a[112:115], a[32:35], v[160:163]\n\t"
    "ds_read_b64_tr_b16 a[106:107], v26 offset:1800\n\t"
    "v_mul_f32 v198, v28, v198\n\t"
    "v_mfma_f32_16x16x32_bf16 v[160:163], a[116:119], a[36:39], v[160:163]\n\t"
    "ds_read_b64_tr_b16 a[108:109], v26 offset:12552\n\t"
    "v_mul_f32 v199, v28, v199\n\t"
    "v_mfma_f32_16x16x32_bf16 v[168:171], a[112:115], a[40:43], v[168:171]\n\t"
    "s_waitcnt vmcnt(14)\n\t"
    "s_barrier\n\t"
    "ds_read_b64_tr_b16 a[110:111], v26 offset:14088\n\t"
    "v_mfma_f32_16x16x32_bf16 v[168:171], a[116:119], a[44:47], v[168:171]\n\t"
    "buffer_load_dwordx4 a[24:27], v23, s[40:43], s61 offen offset:0 sc1\n\t"
    "v_mfma_f32_16x16x32_bf16 v[176:179], a[112:115], a[48:51], v[176:179]\n\t"
    "v_mul_f32 v204, v30, v204\n\t"
    "v_mul_f32 v205, v30, v205\n\t"
    "v_mfma_f32_16x16x32_bf16 v[176:179], a[116:119], a[52:55], v[176:179]\n\t"
    "v_mul_f32 v206, v30, v206\n\t"
    "v_mul_f32 v207, v30, v207\n\t"
    "v_mfma_f32_16x16x32_bf16 v[184:187], a[112:115], a[56:59], v[184:187]\n\t"
    "ds_read_b64_tr_b16 a[112:113], v27 offset:256\n\t"
    "v_mul_f32 v212, v32, v212\n\t"
    "v_mfma_f32_16x16x32_bf16 v[184:187], a[116:119], a[60:63], v[184:187]\n\t"
    "s_waitcnt lgkmcnt(5)\n\t"
    "buffer_load_dwordx4 a[28:31], v23, s[40:43], s61 offen offset:1024 sc1\n\t"
    "v_mfma_f32_16x16x32_bf16 v[164:167], a[120:123], a[32:35], v[164:167]\n\t"
    "ds_read_b64_tr_b16 a[114:115], v27 offset:1792\n\t"
    "v_mul_f32 v213, v32, v213\n\t"
    "v_mfma_f32_16x16x32_bf16 v[164:167], a[124:127], a[36:39], v[164:167]\n\t"
    "s_mul_i32 s61, s98, 1\n\t"
    "ds_read_b64_tr_b16 a[116:117], v27 offset:12544\n\t"
    "v_mul_f32 v214, v32, v214\n\t"
    "v_mfma_f32_16x16x32_bf16 v[172:175], a[120:123], a[40:43], v[172:175]\n\t"
    "s_add_u32 s61, s61, 6144\n\t"
    "ds_read_b64_tr_b16 a[118:119], v27 offset:14080\n\t"
    "v_mul_f32 v215, v32, v215\n\t"
    "v_mfma_f32_16x16x32_bf16 v[172:175], a[124:127], a[44:47], v[172:175]\n\t"
    "s_sub_u32 s64, s61, 12288\n\t"
    "v_mul_f32 v220, v34, v220\n\t"
    "v_mul_f32 v221, v34, v221\n\t"
    "v_mfma_f32_16x16x32_bf16 v[180:183], a[120:123], a[48:51], v[180:183]\n\t"
    "s_min_u32 s61, s61, s64\n\t"
    "v_mul_f32 v222, v34, v222\n\t"
    "v_mul_f32 v223, v34, v223\n\t"
    "v_mfma_f32_16x16x32_bf16 v[180:183], a[124:127], a[52:55], v[180:183]\n\t"
    "s_add_u32 s61, s61, 8192\n\t"
    "v_mul_f32 v224, v28, v224\n\t"
    "v_mul_f32 v225, v28, v225\n\t"
    "v_mfma_f32_16x16x32_bf16 v[188:191], a[120:123], a[56:59], v[188:191]\n\t"
    "ds_read_b64_tr_b16 a[120:121], v27 offset:264\n\t"
    "v_mul_f32 v226, v28, v226\n\t"
    "v_mfma_f32_16x16x32_bf16 v[188:191], a[124:127], a[60:63], v[188:191]\n\t"
    "ds_read_b64_tr_b16 a[122:123], v27 offset:1800\n\t"
    "v_mul_f32 v227, v28, v227\n\t"
    "v_mfma_f32_16x16x32_bf16 v[192:195], a[96:99], a[32:35], v[192:195]\n\t"
    "buffer_load_dwordx4 v5, s[44:47], s60 offen offset:0 lds\n\t"
    "v_mfma_f32_16x16x32_bf16 v[192:195], a[100:103], a[36:39], v[192:195]\n\t"
    "ds_read_b64_tr_b16 a[124:125], v27 offset:12552\n\t"
    "v_mul_f32 v232, v30, v232\n\t"
    "v_mfma_f32_16x16x32_bf16 v[200:203], a[96:99], a[40:43], v[200:203]\n\t"
    "ds_read_b64_tr_b16 a[126:127], v27 offset:14088\n\t"
    "v_mul_f32 v233, v30, v233\n\t"
    "v_mfma_f32_16x16x32_bf16 v[200:203], a[100:103], a[44:47], v[200:203]\n\t"
    "v_mul_f32 v234, v30, v234\n\t"
    "v_mul_f32 v235, v30, v235\n\t"
    "v_mfma_f32_16x16x32_bf16 v[208:211], a[96:99], a[48:51], v[208:211]\n\t"
    "buffer_load_dwordx4 v6, s[44:47], s60 offen offset:1024 lds\n\t"
    "v_mfma_f32_16x16x32_bf16 v[208:211], a[100:103], a[52:55], v[208:211]\n\t"
    "v_mul_f32 v240, v32, v240\n\t"
    "v_mul_f32 v241, v32, v241\n\t"
    "v_mfma_f32_16x16x32_bf16 v[216:219], a[96:99], a[56:59], v[216:219]\n\t"
    "ds_read_b64_tr_b16 a[96:97], v26 offset:24576\n\t"
    "v_mul_f32 v242, v32, v242\n\t"
    "v_mfma_f32_16x16x32_bf16 v[216:219], a[100:103], a[60:63], v[216:219]\n\t"
    "s_waitcnt lgkmcnt(5)\n\t"
    "ds_read_b64_tr_b16 a[98:99], v26 offset:26112\n\t"
    "v_mul_f32 v243, v32, v243\n\t"
    "v_mfma_f32_16x16x32_bf16 v[196:199], a[104:107], a[32:35], v[196:199]\n\t"
    "ds_read_b64_tr_b16 a[100:101], v26 offset:36864\n\t"
    "v_mul_f32 v248, v34, v248\n\t"
    "v_mfma_f32_16x16x32_bf16 v[196:199], a[108:111], a[36:39], v[196:199]\n\t"
    "ds_read_b64_tr_b16 a[102:103], v26 offset:38400\n\t"
    "v_mul_f32 v249, v34, v249\n\t"
    "v_mfma_f32_16x16x32_bf16 v[204:207], a[104:107], a[40:43], v[204:207]\n\t"
    "v_mul_f32 v250, v34, v250\n\t"
    "v_mul_f32 v251, v34, v251\n\t"
    "v_mfma_f32_16x16x32_bf16 v[204:207], a[108:111], a[44:47], v[204:207]\n\t"
    "buffer_load_dwordx4 v7, s[44:47], s60 offen offset:2048 lds\n\t"
    "v_mfma_f32_16x16x32_bf16 v[212:215], a[104:107], a[48:51], v[212:215]\n\t"
    "s_add_u32 m0, s83, 27648\n\t"
    "ds_read_b128 a[204:207], v4 offset:49152\n\t"
    "v_mul_f32 v228, v28, v228\n\t"
    "v_mfma_f32_16x16x32_bf16 v[212:215], a[108:111], a[52:55], v[212:215]\n\t"
    "s_add_u32 s60, s60, 98304\n\t"
    "v_mul_f32 v229, v28, v229\n\t"
    "v_mul_f32 v230, v28, v230\n\t"
    "v_mfma_f32_16x16x32_bf16 v[220:223], a[104:107], a[56:59], v[220:223]\n\t"
    "ds_read_b64_tr_b16 a[104:105], v26 offset:24584\n\t"
    "v_mul_f32 v231, v28, v231\n\t"
    "v_mfma_f32_16x16x32_bf16 v[220:223], a[108:111], a[60:63], v[220:223]\n\t"
    "ds_read_b64_tr_b16 a[106:107], v26 offset:26120\n\t"
    "v_mul_f32 v236, v30, v236\n\t"
    "v_mfma_f32_16x16x32_bf16 v[224:227], a[112:115], a[32:35], v[224:227]\n\t"
    "ds_read_b64_tr_b16 a[108:109], v26 offset:36872\n\t"
    "v_mul_f32 v237, v30, v237\n\t"
    "v_mfma_f32_16x16x32_bf16 v[224:227], a[116:119], a[36:39], v[224:227]\n\t"
    "ds_read_b64_tr_b16 a[110:111], v26 offset:38408\n\t"
    "v_mul_f32 v238, v30, v238\n\t"
    "v_mfma_f32_16x16x32_bf16 v[232:235], a[112:115], a[40:43], v[232:235]\n\t"
    "ds_read_b128 a[208:211], v4 offset:50176\n\t"
    "v_mul_f32 v239, v30, v239\n\t"
    "v_mfma_f32_16x16x32_bf16 v[232:235], a[116:119], a[44:47], v[232:235]\n\t"
    "v_mul_f32 v244, v32, v244\n\t"
    "v_mul_f32 v245, v32, v245\n\t"
    "v_mfma_f32_16x16x32_bf16 v[240:243], a[112:115], a[48:51], v[240:243]\n\t"
    "ds_read_b128 a[212:215], v4 offset:51200\n\t"
    "v_mul_f32 v246, v32, v246\n\t"
    "v_mfma_f32_16x16x32_bf16 v[240:243], a[116:119], a[52:55], v[240:243]\n\t"
    "v_mul_f32 v247, v32, v247\n\t"
    "v_mul_f32 v252, v34, v252\n\t"
    "v_mfma_f32_16x16x32_bf16 v[248:251], a[112:115], a[56:59], v[248:251]\n\t"
    "ds_read_b64_tr_b16 a[112:113], v27 offset:24576\n\t"
    "v_mul_f32 v253, v34, v253\n\t"
    "v_mfma_f32_16x16x32_bf16 v[248:251], a[116:119], a[60:63], v[248:251]\n\t"
    "s_waitcnt lgkmcnt(7)\n\t"
    "ds_read_b64_tr_b16 a[114:115], v27 offset:26112\n\t"
    "v_mul_f32 v254, v34, v254\n\t"
    "v_mfma_f32_16x16x32_bf16 v[228:231], a[120:123], a[32:35], v[228:231]\n\t"
    "ds_read_b64_tr_b16 a[116:117], v27 offset:36864\n\t"
    "v_mul_f32 v255, v34, v255\n\t"
    "v_mfma_f32_16x16x32_bf16 v[228:231], a[124:127], a[36:39], v[228:231]\n\t"
    "buffer_load_dwordx4 v5, s[44:47], s60 offen offset:0 lds\n\t"
    "v_mfma_f32_16x16x32_bf16 v[236:239], a[120:123], a[40:43], v[236:239]\n\t"
    "ds_read_b64_tr_b16 a[118:119], v27 offset:38400\n\t"
    "v_add_u32 v30, 98304, v13\n\t"
    "v_mfma_f32_16x16x32_bf16 v[236:239], a[124:127], a[44:47], v[236:239]\n\t"
    "ds_read_b128 a[216:219], v4 offset:52224\n\t"
    "v_add_u32 v28, 49152, v13\n\t"
    "v_mfma_f32_16x16x32_bf16 v[244:247], a[120:123], a[48:51], v[244:247]\n\t"
    "ds_read_b128 a[220:223], v4 offset:53248\n\t"
    "v_add_u32 v32, 49152, v15\n\t"
    "v_mfma_f32_16x16x32_bf16 v[244:247], a[124:127], a[52:55], v[244:247]\n\t"
    "buffer_load_dwordx4 v6, s[44:47], s60 offen offset:1024 lds\n\t"
    "v_mfma_f32_16x16x32_bf16 v[252:255], a[120:123], a[56:59], v[252:255]\n\t"
    "ds_read_b64_tr_b16 a[120:121], v27 offset:24584\n\t"
    "v_add_u32 v34, 49152, v17\n\t"
    "v_mfma_f32_16x16x32_bf16 v[252:255], a[124:127], a[60:63], v[252:255]\n\t"
    "ds_read_b64_tr_b16 a[122:123], v27 offset:26120\n\t"
    "v_mfma_f32_16x16x32_bf16 v[64:67], a[96:99], a[64:67], v[64:67]\n\t"
    "ds_read_b64_tr_b16 a[124:125], v27 offset:36872\n\t"
    "s_waitcnt vmcnt(12)\n\t"
    "v_xor_b32 v45, s80, v45\n\t"
    "v_mfma_f32_16x16x32_bf16 v[64:67], a[100:103], a[68:71], v[64:67]\n\t"
    "buffer_load_dwordx4 v7, s[44:47], s60 offen offset:2048 lds\n\t"
    "v_mfma_f32_16x16x32_bf16 v[72:75], a[96:99], a[72:75], v[72:75]\n\t"
    "ds_read_b64_tr_b16 a[126:127], v27 offset:38408\n\t"
    "v_xor_b32 v47, s80, v47\n\t"
    "v_mfma_f32_16x16x32_bf16 v[72:75], a[100:103], a[76:79], v[72:75]\n\t"
    "ds_read_b128 a[224:227], v4 offset:54272\n\t"
    "v_xor_b32 v49, s80, v49\n\t"
    "v_mfma_f32_16x16x32_bf16 v[80:83], a[96:99], a[80:83], v[80:83]\n\t"
    "ds_read_b128 a[228:231], v4 offset:55296\n\t"
    "v_xor_b32 v51, s80, v51\n\t"
    "v_mfma_f32_16x16x32_bf16 v[80:83], a[100:103], a[84:87], v[80:83]\n\t"
    "buffer_load_dwordx4 a[32:35], v20, s[40:43], s61 offen offset:0 sc1\n\t"
    "v_mfma_f32_16x16x32_bf16 v[88:91], a[96:99], a[88:91], v[88:91]\n\t"
    "ds_read_b64_tr_b16 a[96:97], v26 offset:24704\n\t"
    "v_xor_b32 v53, s80, v53\n\t"
    "v_mfma_f32_16x16x32_bf16 v[88:91], a[100:103], a[92:95], v[88:91]\n\t"
    "s_waitcnt lgkmcnt(8)\n\t"
    "ds_read_b64_tr_b16 a[98:99], v26 offset:26240\n\t"
    "v_xor_b32 v55, s80, v55\n\t"
    "v_mfma_f32_16x16x32_bf16 v[68:71], a[104:107], a[64:67], v[68:71]\n\t"
    "ds_read_b64_tr_b16 a[100:101], v26 offset:36992\n\t"
    "v_xor_b32 v57, s80, v57\n\t"
    "v_mfma_f32_16x16x32_bf16 v[68:71], a[108:111], a[68:71], v[68:71]\n\t"
    "s_add_u32 m0, s83, 49152\n\t"
    "ds_read_b64_tr_b16 a[102:103], v26 offset:38528\n\t"
    "v_xor_b32 v59, s80, v59\n\t"
    "v_mfma_f32_16x16x32_bf16 v[76:79], a[104:107], a[72:75], v[76:79]\n\t"
    "s_mul_i32 s60, s98, 384\n\t"
    "ds_read_b128 a[232:235], v4 offset:56320\n\t"
    "v_or3_b32 v45, v45, v47, v49\n\t"
    "v_mfma_f32_16x16x32_bf16 v[76:79], a[108:111], a[76:79], v[76:79]\n\t"
    "s_add_u32 s60, s60, 3145728\n\t"
    "buffer_load_dwordx4 a[36:39], v20, s[40:43], s61 offen offset:1024 sc1\n\t"
    "v_mfma_f32_16x16x32_bf16 v[84:87], a[104:107], a[80:83], v[84:87]\n\t"
    "s_sub_u32 s62, s60, 4718592\n\t"
    "ds_read_b128 a[236:239], v4 offset:57344\n\t"
    "v_or3_b32 v51, v51, v53, v55\n\t"
    "v_mfma_f32_16x16x32_bf16 v[84:87], a[108:111], a[84:87], v[84:87]\n\t"
    "s_min_u32 s60, s60, s62\n\t"
    "v_or3_b32 v45, v45, v57, v59\n\t"
    "v_or_b32 v45, v45, v51\n\t"
    "v_mfma_f32_16x16x32_bf16 v[92:95], a[104:107], a[88:91], v[92:95]\n\t"
    "s_add_u32 s60, s60, 3145728\n\t"
    "v_cmp_ne_u32 vcc, 0, v45\n\t"
    "s_cbranch_vccnz KFASK\n\t"
    "KFARK:\n\t"
    "v_mfma_f32_16x16x32_bf16 v[92:95], a[108:111], a[92:95], v[92:95]\n\t"
    "buffer_load_dwordx4 a[40:43], v21, s[40:43], s61 offen offset:0 sc1\n\t"
    "v_mfma_f32_16x16x32_bf16 v[96:99], a[112:115], a[64:67], v[96:99]\n\t"
    "ds_read_b64_tr_b16 a[104:105], v26 offset:24712\n\t"
    "v_max_u32 v46, v46, v50\n\t"
    "v_mfma_f32_16x16x32_bf16 v[96:99], a[116:119], a[68:71], v[96:99]\n\t"
    "ds_read_b64_tr_b16 a[106:107], v26 offset:26248\n\t"
    "v_med3_u32 v46, v44, v48, v46\n\t"
    "v_mfma_f32_16x16x32_bf16 v[104:107], a[112:115], a[72:75], v[104:107]\n\t"
    "ds_read_b64_tr_b16 a[108:109], v26 offset:37000\n\t"
    "v_max_u32 v44, v44, v48\n\t"
    "v_mfma_f32_16x16x32_bf16 v[104:107], a[116:119], a[76:79], v[104:107]\n\t"
    "buffer_load_dwordx4 a[44:47], v21, s[40:43], s61 offen offset:1024 sc1\n\t"
    "v_mfma_f32_16x16x32_bf16 v[112:115], a[112:115], a[80:83], v[112:115]\n\t"
    "ds_read_b64_tr_b16 a[110:111], v26 offset:38536\n\t"
    "v_max_u32 v54, v54, v58\n\t"
    "v_mfma_f32_16x16x32_bf16 v[112:115], a[116:119], a[84:87], v[112:115]\n\t"
    "ds_read_b128 a[240:243], v4 offset:58368\n\t"
    "v_med3_u32 v54, v52, v56, v54\n\t"
    "v_mfma_f32_16x16x32_bf16 v[120:123], a[112:115], a[88:91], v[120:123]\n\t"
    "ds_read_b64_tr_b16 a[112:113], v27 offset:24704\n\t"
    "v_max_u32 v52, v52, v56\n\t"
    "v_mfma_f32_16x16x32_bf16 v[120:123], a[116:119], a[92:95], v[120:123]\n\t"
    "s_waitcnt lgkmcnt(6)\n\t"
    "buffer_load_dwordx4 a[48:51], v22, s[40:43], s61 offen offset:0 sc1\n\t"
    "v_mfma_f32_16x16x32_bf16 v[100:103], a[120:123], a[64:67], v[100:103]\n\t"
    "ds_read_b64_tr_b16 a[114:115], v27 offset:26240\n\t"
    "v_max_u32 v46, v46, v54\n\t"
    "v_mfma_f32_16x16x32_bf16 v[100:103], a[124:127], a[68:71], v[100:103]\n\t"
    "ds_read_b64_tr_b16 a[116:117], v27 offset:36992\n\t"
    "v_med3_u32 v46, v44, v52, v46\n\t"
    "v_mfma_f32_16x16x32_bf16 v[108:111], a[120:123], a[72:75], v[108:111]\n\t"
    "ds_read_b64_tr_b16 a[118:119], v27 offset:38528\n\t"
    "v_max_u32 v44, v44, v52\n\t"
    "v_mfma_f32_16x16x32_bf16 v[108:111], a[124:127], a[76:79], v[108:111]\n\t"
    "buffer_load_dwordx4 a[52:55], v22, s[40:43], s61 offen offset:1024 sc1\n\t"
    "v_mfma_f32_16x16x32_bf16 v[116:119], a[120:123], a[80:83], v[116:119]\n\t"
    "ds_read_b128 a[244:247], v4 offset:59392\n\t"
    "v_mov_b32_dpp v45, v44 quad_perm:[1,0,3,2] row_mask:0xf bank_mask:0xf\n\t"
    "v_mfma_f32_16x16x32_bf16 v[116:119], a[124:127], a[84:87], v[116:119]\n\t"
    "ds_read_b128 a[248:251], v4 offset:60416\n\t"
    "v_max_u32_dpp v46, v46, v46 quad_perm:[1,0,3,2] row_mask:0xf bank_mask:0xf\n\t"
    "v_mfma_f32_16x16x32_bf16 v[124:127], a[120:123], a[88:91], v[124:127]\n\t"
    "ds_read_b64_tr_b16 a[120:121], v27 offset:24712\n\t"
    "v_med3_u32 v46, v44, v45, v46\n\t"
    "v_mfma_f32_16x16x32_bf16 v[124:127], a[124:127], a[92:95], v[124:127]\n\t"
    "buffer_load_dwordx4 a[56:59], v23, s[40:43], s61 offen offset:0 sc1\n\t"
    "v_mfma_f32_16x16x32_bf16 v[128:131], a[96:99], a[64:67], v[128:131]\n\t"
    "ds_read_b64_tr_b16 a[122:123], v27 offset:26248\n\t"
    "v_max_u32 v44, v44, v45\n\t"
    "v_mfma_f32_16x16x32_bf16 v[128:131], a[100:103], a[68:71], v[128:131]\n\t"
    "ds_read_b64_tr_b16 a[124:125], v27 offset:37000\n\t"
    "v_mov_b32_dpp v45, v44 quad_perm:[2,3,0,1] row_mask:0xf bank_mask:0xf\n\t"
    "v_mfma_f32_16x16x32_bf16 v[136:139], a[96:99], a[72:75], v[136:139]\n\t"
    "ds_read_b64_tr_b16 a[126:127], v27 offset:38536\n\t"
    "v_max_u32_dpp v46, v46, v46 quad_perm:[2,3,0,1] row_mask:0xf bank_mask:0xf\n\t"
    "v_mfma_f32_16x16x32_bf16 v[136:139], a[100:103], a[76:79], v[136:139]\n\t"
    "buffer_load_dwordx4 a[60:63], v23, s[40:43], s61 offen offset:1024 sc1\n\t"
    "v_mfma_f32_16x16x32_bf16 v[144:147], a[96:99], a[80:83], v[144:147]\n\t"
    "s_mul_i32 s61, s98, 1\n\t"
    "v_med3_u32 v46, v44, v45, v46\n\t"
    "v_max_u32 v44, v44, v45\n\t"
    "v_mfma_f32_16x16x32_bf16 v[144:147], a[100:103], a[84:87], v[144:147]\n\t"
    "s_add_u32 s61, s61, 8192\n\t"
    "v_mov_b32_dpp v45, v44 row_half_mirror row_mask:0xf bank_mask:0xf\n\t"
    "v_max_u32_dpp v46, v46, v46 row_half_mirror row_mask:0xf bank_mask:0xf\n\t"
    "v_mfma_f32_16x16x32_bf16 v[152:155], a[96:99], a[88:91], v[152:155]\n\t"
    "s_sub_u32 s64, s61, 12288\n\t"
    "ds_read_b64_tr_b16 a[96:97], v26 offset:24832\n\t"
    "v_med3_u32 v46, v44, v45, v46\n\t"
    "v_mfma_f32_16x16x32_bf16 v[152:155], a[100:103], a[92:95], v[152:155]\n\t"
    "s_waitcnt lgkmcnt(5)\n\t"
    "ds_read_b64_tr_b16 a[98:99], v26 offset:26368\n\t"
    "v_max_u32 v44, v44, v45\n\t"
    "v_mfma_f32_16x16x32_bf16 v[132:135], a[104:107], a[64:67], v[132:135]\n\t"
    "ds_read_b64_tr_b16 a[100:101], v26 offset:37120\n\t"
    "v_mov_b32_dpp v45, v44 row_mirror row_mask:0xf bank_mask:0xf\n\t"
    "v_mfma_f32_16x16x32_bf16 v[132:135], a[108:111], a[68:71], v[132:135]\n\t"
    "s_min_u32 s61, s61, s64\n\t"
    "ds_read_b64_tr_b16 a[102:103], v26 offset:38656\n\t"
    "v_max_u32_dpp v46, v46, v46 row_mirror row_mask:0xf bank_mask:0xf\n\t"
    "v_mfma_f32_16x16x32_bf16 v[140:143], a[104:107], a[72:75], v[140:143]\n\t"
    "s_add_u32 s61, s61, 8192\n\t"
    "v_med3_u32 v46, v44, v45, v46\n\t"
    "v_max_u32 v44, v44, v45\n\t"
    "v_mfma_f32_16x16x32_bf16 v[140:143], a[108:111], a[76:79], v[140:143]\n\t"
    "v_mov_b32 v45, v44\n\t"
    "v_mov_b32 v47, v46\n\t"
    "v_mfma_f32_16x16x32_bf16 v[148:151], a[104:107], a[80:83], v[148:151]\n\t"
    "v_permlane16_swap_b32 v44, v45\n\t"
    "v_permlane16_swap_b32 v46, v47\n\t"
    "v_mfma_f32_16x16x32_bf16 v[148:151], a[108:111], a[84:87], v[148:151]\n\t"
    "v_max_u32 v46, v46, v47\n\t"
    "v_med3_u32 v46, v44, v45, v46\n\t"
    "v_mfma_f32_16x16x32_bf16 v[156:159], a[104:107], a[88:91], v[156:159]\n\t"
    "ds_read_b64_tr_b16 a[104:105], v26 offset:24840\n\t"
    "v_max_u32 v44, v44, v45\n\t"
    "v_mfma_f32_16x16x32_bf16 v[156:159], a[108:111], a[92:95], v[156:159]\n\t"
    "ds_read_b64_tr_b16 a[106:107], v26 offset:26376\n\t"
    "v_mov_b32 v45, v44\n\t"
    "v_mfma_f32_16x16x32_bf16 v[160:163], a[112:115], a[64:67], v[160:163]\n\t"
    "ds_read_b64_tr_b16 a[108:109], v26 offset:37128\n\t"
    "v_mov_b32 v47, v46\n\t"
    "v_mfma_f32_16x16x32_bf16 v[160:163], a[116:119], a[68:71], v[160:163]\n\t"
    "ds_read_b64_tr_b16 a[110:111], v26 offset:38664\n\t"
    "v_permlane32_swap_b32 v44, v45\n\t"
    "v_mfma_f32_16x16x32_bf16 v[168:171], a[112:115], a[72:75], v[168:171]\n\t"
    "s_barrier\n\t"
    "v_permlane32_swap_b32 v46, v47\n\t"
    "v_mfma_f32_16x16x32_bf16 v[168:171], a[116:119], a[76:79], v[168:171]\n\t"
    "v_max_u32 v46, v46, v47\n\t"
    "v_med3_u32 v46, v44, v45, v46\n\t"
    "v_mfma_f32_16x16x32_bf16 v[176:179], a[112:115], a[80:83], v[176:179]\n\t"
    "v_max_u32 v44, v44, v45\n\t"
    "s_nop 0\n\t"
    "v_readfirstlane_b32 s66, v44\n\t"
    "v_mfma_f32_16x16x32_bf16 v[176:179], a[116:119], a[84:87], v[176:179]\n\t"
    "s_andn2_b32 s66, 0x7ff, s66\n\t"
    "v_readfirstlane_b32 s67, v46\n\t"
    "v_and_b32 v50, 16, v4\n\t"
    "v_mfma_f32_16x16x32_bf16 v[184:187], a[112:115], a[88:91], v[184:187]\n\t"
    "s_andn2_b32 s67, 0x7ff, s67\n\t"
    "ds_read_b64_tr_b16 a[112:113], v27 offset:24832\n\t"
    "v_cmp_ne_u32 vcc, 0, v50\n\t"
    "v_mfma_f32_16x16x32_bf16 v[184:187], a[116:119], a[92:95], v[184:187]\n\t"
    "s_waitcnt lgkmcnt(5)\n\t"
    "ds_read_b64_tr_b16 a[114:115], v27 offset:26368\n\t"
    "v_lshrrev_b32 v51, 9, v4\n\t"
    "v_mfma_f32_16x16x32_bf16 v[164:167], a[120:123], a[64:67], v[164:167]\n\t"
    "ds_read_b64_tr_b16 a[116:117], v27 offset:37120\n\t"
    "v_xor_b32 v57, 32, v63\n\t"
    "v_mfma_f32_16x16x32_bf16 v[164:167], a[124:127], a[68:71], v[164:167]\n\t"
    "s_lshl_b32 s35, s67, 11\n\t"
    "ds_read_b64_tr_b16 a[118:119], v27 offset:38656\n\t"
    "v_xor_b32 v58, 64, v63\n\t"
    "v_mfma_f32_16x16x32_bf16 v[172:175], a[120:123], a[72:75], v[172:175]\n\t"
    "s_or_b32 s35, s35, s66\n\t"
    "buffer_load_dwordx4 v5, s[44:47], s60 offen offset:0 lds\n\t"
    "v_mfma_f32_16x16x32_bf16 v[172:175], a[124:127], a[76:79], v[172:175]\n\t"
    "s_lshr_b32 s63, s66, 4\n\t"
    "v_xor_b32 v59, 96, v63\n\t"
    "v_lshrrev_b32 v52, 1, v4\n\t"
    "v_mfma_f32_16x16x32_bf16 v[180:183], a[120:123], a[80:83], v[180:183]\n\t"
    "s_mul_i32 s63, s63, 1264\n\t"
    "v_mov_b32 v53, 0x7ffffff0\n\t"
    "v_mfma_f32_16x16x32_bf16 v[180:183], a[124:127], a[84:87], v[180:183]\n\t"
    "s_add_u32 s63, s63, s66\n\t"
    "v_mfma_f32_16x16x32_bf16 v[188:191], a[120:123], a[88:91], v[188:191]\n\t"
    "s_and_b32 s65, s66, 2\n\t"
    "buffer_load_dwordx4 v6, s[44:47], s60 offen offset:1024 lds\n\t"
    "v_mfma_f32_16x16x32_bf16 v[188:191], a[124:127], a[92:95], v[188:191]\n\t"
    "ds_read_b64_tr_b16 a[120:121], v27 offset:24840\n\t"
    "v_mfma_f32_16x16x32_bf16 v[192:195], a[96:99], a[64:67], v[192:195]\n\t"
    "ds_read_b64_tr_b16 a[122:123], v27 offset:26376\n\t"
    "v_mfma_f32_16x16x32_bf16 v[192:195], a[100:103], a[68:71], v[192:195]\n\t"
    "s_add_u32 s63, s63, s65\n\t"
    "ds_read_b64_tr_b16 a[124:125], v27 offset:37128\n\t"
    "v_mfma_f32_16x16x32_bf16 v[200:203], a[96:99], a[72:75], v[200:203]\n\t"
    "s_and_b32 s66, s66, 12\n\t"
    "buffer_load_dwordx4 v7, s[44:47], s60 offen offset:2048 lds\n\t"
    "v_mfma_f32_16x16x32_bf16 v[200:203], a[100:103], a[76:79], v[200:203]\n\t"
    "s_mul_i32 s66, s66, 3\n\t"
    "ds_read_b64_tr_b16 a[126:127], v27 offset:38664\n\t"
    "v_mov_b32 v27, s80\n\t"
    "v_mfma_f32_16x16x32_bf16 v[208:211], a[96:99], a[80:83], v[208:211]\n\t"
    "s_add_u32 s66, s66, s63\n\t"
    "v_mfma_f32_16x16x32_bf16 v[208:211], a[100:103], a[84:87], v[208:211]\n\t"
    "s_lshl_b32 s66, s66, 4\n\t"
    "v_mov_b32 v48, s66\n\t"
    "v_mfma_f32_16x16x32_bf16 v[216:219], a[96:99], a[88:91], v[216:219]\n\t"
    "s_lshr_b32 s63, s67, 4\n\t"
    "ds_read_b64_tr_b16 a[96:97], v11 offset:0\n\t"
    "v_mfma_f32_16x16x32_bf16 v[216:219], a[100:103], a[92:95], v[216:219]\n\t"
    "s_waitcnt lgkmcnt(5)\n\t"
    "ds_read_b64_tr_b16 a[98:99], v11 offset:1536\n\t"
    "v_mfma_f32_16x16x32_bf16 v[196:199], a[104:107], a[64:67], v[196:199]\n\t"
    "ds_read_b64_tr_b16 a[100:101], v11 offset:12288\n\t"
    "v_mfma_f32_16x16x32_bf16 v[196:199], a[108:111], a[68:71], v[196:199]\n\t"
    "s_mul_i32 s63, s63, 1264\n\t"
    "ds_read_b64_tr_b16 a[102:103], v11 offset:13824\n\t"
    "v_mfma_f32_16x16x32_bf16 v[204:207], a[104:107], a[72:75], v[204:207]\n\t"
    "s_add_u32 s63, s63, s67\n\t"
    "v_mfma_f32_16x16x32_bf16 v[204:207], a[108:111], a[76:79], v[204:207]\n\t"
    "s_and_b32 s65, s67, 2\n\t"
    "v_mfma_f32_16x16x32_bf16 v[212:215], a[104:107], a[80:83], v[212:215]\n\t"
    "s_add_u32 s63, s63, s65\n\t"
    "v_mfma_f32_16x16x32_bf16 v[212:215], a[108:111], a[84:87], v[212:215]\n\t"
    "s_and_b32 s67, s67, 12\n\t"
    "v_mfma_f32_16x16x32_bf16 v[220:223], a[104:107], a[88:91], v[220:223]\n\t"
    "s_mul_i32 s67, s67, 3\n\t"
    "ds_read_b64_tr_b16 a[104:105], v11 offset:8\n\t"
    "v_mfma_f32_16x16x32_bf16 v[220:223], a[108:111], a[92:95], v[220:223]\n\t"
    "s_add_u32 s67, s67, s63\n\t"
    "ds_read_b64_tr_b16 a[106:107], v11 offset:1544\n\t"
    "v_mfma_f32_16x16x32_bf16 v[224:227], a[112:115], a[64:67], v[224:227]\n\t"
    "s_lshl_b32 s67, s67, 4\n\t"
    "v_mov_b32 v49, s67\n\t"
    "v_mfma_f32_16x16x32_bf16 v[224:227], a[116:119], a[68:71], v[224:227]\n\t"
    "s_add_u32 m0, s83, 52224\n\t"
    "ds_read_b64_tr_b16 a[108:109], v11 offset:12296\n\t"
    "v_cndmask_b32 v48, v48, v49, vcc\n\t"
    "v_mfma_f32_16x16x32_bf16 v[232:235], a[112:115], a[72:75], v[232:235]\n\t"
    "s_add_u32 s60, s60, 98304\n\t"
    "ds_read_b64_tr_b16 a[110:111], v11 offset:13832\n\t"
    "v_lshl_add_u32 v48, v51, 5, v48\n\t"
    "v_mfma_f32_16x16x32_bf16 v[232:235], a[116:119], a[76:79], v[232:235]\n\t"
    "s_cmp_eq_u32 s90, 0\n\t"
    "s_cselect_b32 s67, 0, 0x100\n\t"
    "v_bfe_u32 v51, v4, 8, 1\n\t"
    "v_lshl_add_u32 v48, v51, 3, v48\n\t"
    "v_mfma_f32_16x16x32_bf16 v[240:243], a[112:115], a[80:83], v[240:243]\n\t"
    "s_cmp_eq_u32 s92, 0\n\t"
    "s_cselect_b32 s66, 0x70000000, 0\n\t"
    "buffer_load_dwordx4 v5, s[44:47], s60 offen offset:0 lds\n\t"
    "v_mfma_f32_16x16x32_bf16 v[240:243], a[116:119], a[84:87], v[240:243]\n\t"
    "s_bfe_u32 s63, s76, 0x80000\n\t"
    "v_cmp_gt_u32 vcc, s67, v4\n\t"
    "s_nop 1\n\t"
    "v_cndmask_b32 v52, v53, v52, vcc\n\t"
    "v_mfma_f32_16x16x32_bf16 v[248:251], a[112:115], a[88:91], v[248:251]\n\t"
    "s_lshl_b32 s63, s63, 10\n\t"
    "ds_read_b64_tr_b16 a[112:113], v12 offset:0\n\t"
    "v_mfma_f32_16x16x32_bf16 v[248:251], a[116:119], a[92:95], v[248:251]\n\t"
    "s_waitcnt lgkmcnt(4)\n\t"
    "ds_read_b64_tr_b16 a[114:115], v12 offset:1536\n\t"
    "v_mfma_f32_16x16x32_bf16 v[228:231], a[120:123], a[64:67], v[228:231]\n\t"
    "buffer_load_dwordx4 v6, s[44:47], s60 offen offset:1024 lds\n\t"
    "v_mfma_f32_16x16x32_bf16 v[228:231], a[124:127], a[68:71], v[228:231]\n\t"
    "s_add_u32 s63, s63, s88\n\t"
    "ds_read_b64_tr_b16 a[116:117], v12 offset:12288\n\t"
    "v_mfma_f32_16x16x32_bf16 v[236:239], a[120:123], a[72:75], v[236:239]\n\t"
    "ds_read_b64_tr_b16 a[118:119], v12 offset:13824\n\t"
    "v_mfma_f32_16x16x32_bf16 v[236:239], a[124:127], a[76:79], v[236:239]\n\t"
    "v_mfma_f32_16x16x32_bf16 v[244:247], a[120:123], a[80:83], v[244:247]\n\t"
    "buffer_load_dwordx4 v7, s[44:47], s60 offen offset:2048 lds\n\t"
    "v_mfma_f32_16x16x32_bf16 v[244:247], a[124:127], a[84:87], v[244:247]\n\t"
    "v_mfma_f32_16x16x32_bf16 v[252:255], a[120:123], a[88:91], v[252:255]\n\t"
    "ds_read_b64_tr_b16 a[120:121], v12 offset:8\n\t"
    "v_mfma_f32_16x16x32_bf16 v[252:255], a[124:127], a[92:95], v[252:255]\n\t"
    "ds_read_b64_tr_b16 a[122:123], v12 offset:1544\n\t"
    "v_mfma_f32_16x16x32_bf16 v[64:67], a[96:99], a[0:3], v[64:67]\n\t"
    "s_waitcnt vmcnt(20)\n\t"
    "buffer_load_dwordx4 a[64:67], v20, s[40:43], s61 offen offset:0 sc1\n\t"
    "v_mfma_f32_16x16x32_bf16 v[64:67], a[100:103], a[4:7], v[64:67]\n\t"
    "ds_read_b64_tr_b16 a[124:125], v12 offset:12296\n\t"
    "v_accvgpr_read_b32 v36, a204\n\t"
    "v_mfma_f32_16x16x32_bf16 v[72:75], a[96:99], a[8:11], v[72:75]\n\t"
    "ds_read_b64_tr_b16 a[126:127], v12 offset:13832\n\t"
    "v_accvgpr_read_b32 v37, a205\n\t"
    "v_mfma_f32_16x16x32_bf16 v[72:75], a[100:103], a[12:15], v[72:75]\n\t"
    "v_accvgpr_read_b32 v38, a206\n\t"
    "v_accvgpr_read_b32 v39, a207\n\t"
    "v_mfma_f32_16x16x32_bf16 v[80:83], a[96:99], a[16:19], v[80:83]\n\t"
    "buffer_load_dwordx4 a[68:71], v20, s[40:43], s61 offen offset:1024 sc1\n\t"
    "v_mfma_f32_16x16x32_bf16 v[80:83], a[100:103], a[20:23], v[80:83]\n\t"
    "v_fmac_f32 v64, v36, v29\n\t"
    "v_fmac_f32 v65, v37, v29\n\t"
    "v_mfma_f32_16x16x32_bf16 v[88:91], a[96:99], a[24:27], v[88:91]\n\t"
    "ds_read_b64_tr_b16 a[96:97], v11 offset:128\n\t"
    "v_fmac_f32 v66, v38, v29\n\t"
    "v_mfma_f32_16x16x32_bf16 v[88:91], a[100:103], a[28:31], v[88:91]\n\t"
    "s_waitcnt lgkmcnt(6)\n\t"
    "ds_read_b64_tr_b16 a[98:99], v11 offset:1664\n\t"
    "v_fmac_f32 v67, v39, v29\n\t"
    "v_mfma_f32_16x16x32_bf16 v[68:71], a[104:107], a[0:3], v[68:71]\n\t"
    "buffer_load_dwordx4 a[72:75], v21, s[40:43], s61 offen offset:0 sc1\n\t"
    "v_mfma_f32_16x16x32_bf16 v[68:71], a[108:111], a[4:7], v[68:71]\n\t"
    "ds_read_b64_tr_b16 a[100:101], v11 offset:12416\n\t"
    "v_fmac_f32 v72, v36, v31\n\t"
    "v_mfma_f32_16x16x32_bf16 v[76:79], a[104:107], a[8:11], v[76:79]\n\t"
    "ds_read_b64_tr_b16 a[102:103], v11 offset:13952\n\t"
    "v_fmac_f32 v73, v37, v31\n\t"
    "v_mfma_f32_16x16x32_bf16 v[76:79], a[108:111], a[12:15], v[76:79]\n\t"
    "v_fmac_f32 v74, v38, v31\n\t"
    "v_fmac_f32 v75, v39, v31\n\t"
    "v_mfma_f32_16x16x32_bf16 v[84:87], a[104:107], a[16:19], v[84:87]\n\t"
    "buffer_load_dwordx4 a[76:79], v21, s[40:43], s61 offen offset:1024 sc1\n\t"
    "v_mfma_f32_16x16x32_bf16 v[84:87], a[108:111], a[20:23], v[84:87]\n\t"
    "v_fmac_f32 v80, v36, v33\n\t"
    "v_fmac_f32 v81, v37, v33\n\t"
    "v_mfma_f32_16x16x32_bf16 v[92:95], a[104:107], a[24:27], v[92:95]\n\t"
    "ds_read_b64_tr_b16 a[104:105], v11 offset:136\n\t"
    "v_fmac_f32 v82, v38, v33\n\t"
    "v_mfma_f32_16x16x32_bf16 v[92:95], a[108:111], a[28:31], v[92:95]\n\t"
    "ds_read_b64_tr_b16 a[106:107], v11 offset:1672\n\t"
    "v_fmac_f32 v83, v39, v33\n\t"
    "v_mfma_f32_16x16x32_bf16 v[96:99], a[112:115], a[0:3], v[96:99]\n\t"
    "s_waitcnt lgkmcnt(6)\n\t"
    "buffer_load_dwordx4 a[80:83], v22, s[40:43], s61 offen offset:0 sc1\n\t"
    "v_mfma_f32_16x16x32_bf16 v[96:99], a[116:119], a[4:7], v[96:99]\n\t"
    "ds_read_b64_tr_b16 a[108:109], v11 offset:12424\n\t"
    "v_fmac_f32 v88, v36, v35\n\t"
    "v_mfma_f32_16x16x32_bf16 v[104:107], a[112:115], a[8:11], v[104:107]\n\t"
    "ds_read_b64_tr_b16 a[110:111], v11 offset:13960\n\t"
    "v_fmac_f32 v89, v37, v35\n\t"
    "v_mfma_f32_16x16x32_bf16 v[104:107], a[116:119], a[12:15], v[104:107]\n\t"
    "v_fmac_f32 v90, v38, v35\n\t"
    "v_fmac_f32 v91, v39, v35\n\t"
    "v_mfma_f32_16x16x32_bf16 v[112:115], a[112:115], a[16:19], v[112:115]\n\t"
    "buffer_load_dwordx4 a[84:87], v22, s[40:43], s61 offen offset:1024 sc1\n\t"
    "v_mfma_f32_16x16x32_bf16 v[112:115], a[116:119], a[20:23], v[112:115]\n\t"
    "v_mfma_f32_16x16x32_bf16 v[120:123], a[112:115], a[24:27], v[120:123]\n\t"
    "ds_read_b64_tr_b16 a[112:113], v12 offset:128\n\t"
    "v_mfma_f32_16x16x32_bf16 v[120:123], a[116:119], a[28:31], v[120:123]\n\t"
    "ds_read_b64_tr_b16 a[114:115], v12 offset:1664\n\t"
    "v_mfma_f32_16x16x32_bf16 v[100:103], a[120:123], a[0:3], v[100:103]\n\t"
    "buffer_load_dwordx4 a[88:91], v23, s[40:43], s61 offen offset:0 sc1\n\t"
    "v_mfma_f32_16x16x32_bf16 v[100:103], a[124:127], a[4:7], v[100:103]\n\t"
    "ds_read_b64_tr_b16 a[116:117], v12 offset:12416\n\t"
    "v_accvgpr_read_b32 v40, a216\n\t"
    "v_mfma_f32_16x16x32_bf16 v[108:111], a[120:123], a[8:11], v[108:111]\n\t"
    "ds_read_b64_tr_b16 a[118:119], v12 offset:13952\n\t"
    "v_accvgpr_read_b32 v41, a217\n\t"
    "v_mfma_f32_16x16x32_bf16 v[108:111], a[124:127], a[12:15], v[108:111]\n\t"
    "v_accvgpr_read_b32 v42, a218\n\t"
    "v_accvgpr_read_b32 v43, a219\n\t"
    "v_mfma_f32_16x16x32_bf16 v[116:119], a[120:123], a[16:19], v[116:119]\n\t"
    "s_add_u32 m0, s83, 98304\n\t"
    "buffer_load_dwordx4 a[92:95], v23, s[40:43], s61 offen offset:1024 sc1\n\t"
    "v_mfma_f32_16x16x32_bf16 v[116:119], a[124:127], a[20:23], v[116:119]\n\t"
    "s_add_u32 s60, s87, 0\n\t"
    "v_fmac_f32 v100, v40, v29\n\t"
    "v_fmac_f32 v101, v41, v29\n\t"
    "v_mfma_f32_16x16x32_bf16 v[124:127], a[120:123], a[24:27], v[124:127]\n\t"
    "KFP4:\n\t"
    "s_cbranch_execz KFP8\n\t"
    "s_bfe_u32 s60, s60, 0x2000c\n\t"
    "ds_read_b64_tr_b16 a[120:121], v12 offset:136\n\t"
    "v_fmac_f32 v102, v42, v29\n\t"
    "v_mfma_f32_16x16x32_bf16 v[124:127], a[124:127], a[28:31], v[124:127]\n\t"
    "s_waitcnt lgkmcnt(5)\n\t"
    "ds_read_b64_tr_b16 a[122:123], v12 offset:1672\n\t"
    "v_fmac_f32 v103, v43, v29\n\t"
    "v_mfma_f32_16x16x32_bf16 v[128:131], a[96:99], a[0:3], v[128:131]\n\t"
    "buffer_load_dwordx2 a[128:129], v48, s[40:43], s89 offen offset:0 sc1\n\t"
    "v_mfma_f32_16x16x32_bf16 v[128:131], a[100:103], a[4:7], v[128:131]\n\t"
    "s_mulk_i32 s60, 24576\n\t"
    "ds_read_b64_tr_b16 a[124:125], v12 offset:12424\n\t"
    "v_fmac_f32 v108, v40, v31\n\t"
    "v_mfma_f32_16x16x32_bf16 v[136:139], a[96:99], a[8:11], v[136:139]\n\t"
    "ds_read_b64_tr_b16 a[126:127], v12 offset:13960\n\t"
    "v_fmac_f32 v109, v41, v31\n\t"
    "v_mfma_f32_16x16x32_bf16 v[136:139], a[100:103], a[12:15], v[136:139]\n\t"
    "v_fmac_f32 v110, v42, v31\n\t"
    "v_fmac_f32 v111, v43, v31\n\t"
    "v_mfma_f32_16x16x32_bf16 v[144:147], a[96:99], a[16:19], v[144:147]\n\t"
    "buffer_load_dwordx2 a[130:131], v48, s[40:43], s89 offen offset:128 sc1\n\t"
    "v_mfma_f32_16x16x32_bf16 v[144:147], a[100:103], a[20:23], v[144:147]\n\t"
    "v_fmac_f32 v116, v40, v33\n\t"
    "v_fmac_f32 v117, v41, v33\n\t"
    "v_mfma_f32_16x16x32_bf16 v[152:155], a[96:99], a[24:27], v[152:155]\n\t"
    "ds_read_b64_tr_b16 a[96:97], v11 offset:256\n\t"
    "v_fmac_f32 v118, v42, v33\n\t"
    "v_mfma_f32_16x16x32_bf16 v[152:155], a[100:103], a[28:31], v[152:155]\n\t"
    "ds_read_b64_tr_b16 a[98:99], v11 offset:1792\n\t"
    "v_fmac_f32 v119, v43, v33\n\t"
    "v_mfma_f32_16x16x32_bf16 v[132:135], a[104:107], a[0:3], v[132:135]\n\t"
    "buffer_load_dwordx4 v8, s[48:51], s60 offen offset:0 lds\n\t"
    "v_mfma_f32_16x16x32_bf16 v[132:135], a[108:111], a[4:7], v[132:135]\n\t"
    "ds_read_b64_tr_b16 a[100:101], v11 offset:12544\n\t"
    "v_fmac_f32 v124, v40, v35\n\t"
    "v_mfma_f32_16x16x32_bf16 v[140:143], a[104:107], a[8:11], v[140:143]\n\t"
    "ds_read_b64_tr_b16 a[102:103], v11 offset:14080\n\t"
    "v_fmac_f32 v125, v41, v35\n\t"
    "v_mfma_f32_16x16x32_bf16 v[140:143], a[108:111], a[12:15], v[140:143]\n\t"
    "v_fmac_f32 v126, v42, v35\n\t"
    "v_fmac_f32 v127, v43, v35\n\t"
    "v_mfma_f32_16x16x32_bf16 v[148:151], a[104:107], a[16:19], v[148:151]\n\t"
    "buffer_load_dwordx4 v9, s[48:51], s60 offen offset:1024 lds\n\t"
    "v_mfma_f32_16x16x32_bf16 v[148:151], a[108:111], a[20:23], v[148:151]\n\t"
    "v_mfma_f32_16x16x32_bf16 v[156:159], a[104:107], a[24:27], v[156:159]\n\t"
    "ds_read_b64_tr_b16 a[104:105], v11 offset:264\n\t"
    "v_mfma_f32_16x16x32_bf16 v[156:159], a[108:111], a[28:31], v[156:159]\n\t"
    "s_waitcnt lgkmcnt(5)\n\t"
    "ds_read_b64_tr_b16 a[106:107], v11 offset:1800\n\t"
    "v_mfma_f32_16x16x32_bf16 v[160:163], a[112:115], a[0:3], v[160:163]\n\t"
    "buffer_load_dwordx4 v10, s[48:51], s60 offen offset:2048 lds\n\t"
    "v_mfma_f32_16x16x32_bf16 v[160:163], a[116:119], a[4:7], v[160:163]\n\t"
    "s_add_u32 m0, s83, 101376\n\t"
    "ds_read_b64_tr_b16 a[108:109], v11 offset:12552\n\t"
    "v_accvgpr_read_b32 v36, a228\n\t"
    "v_mfma_f32_16x16x32_bf16 v[168:171], a[112:115], a[8:11], v[168:171]\n\t"
    "s_waitcnt vmcnt(13)\n\t"
    "s_barrier\n\t"
    "ds_read_b64_tr_b16 a[110:111], v11 offset:14088\n\t"
    "v_mfma_f32_16x16x32_bf16 v[168:171], a[116:119], a[12:15], v[168:171]\n\t"
    "s_add_u32 s60, s60, 3072\n\t"
    "v_accvgpr_read_b32 v37, a229\n\t"
    "v_accvgpr_read_b32 v38, a230\n\t"
    "v_mfma_f32_16x16x32_bf16 v[176:179], a[112:115], a[16:19], v[176:179]\n\t"
    "buffer_load_dwordx4 v8, s[48:51], s60 offen offset:0 lds\n\t"
    "v_mfma_f32_16x16x32_bf16 v[176:179], a[116:119], a[20:23], v[176:179]\n\t"
    "v_accvgpr_read_b32 v39, a231\n\t"
    "v_fmac_f32 v160, v36, v29\n\t"
    "v_mfma_f32_16x16x32_bf16 v[184:187], a[112:115], a[24:27], v[184:187]\n\t"
    "ds_read_b64_tr_b16 a[112:113], v12 offset:256\n\t"
    "v_fmac_f32 v161, v37, v29\n\t"
    "v_mfma_f32_16x16x32_bf16 v[184:187], a[116:119], a[28:31], v[184:187]\n\t"
    "ds_read_b64_tr_b16 a[114:115], v12 offset:1792\n\t"
    "v_fmac_f32 v162, v38, v29\n\t"
    "v_mfma_f32_16x16x32_bf16 v[164:167], a[120:123], a[0:3], v[164:167]\n\t"
    "buffer_load_dwordx4 v9, s[48:51], s60 offen offset:1024 lds\n\t"
    "v_mfma_f32_16x16x32_bf16 v[164:167], a[124:127], a[4:7], v[164:167]\n\t"
    "ds_read_b64_tr_b16 a[116:117], v12 offset:12544\n\t"
    "v_fmac_f32 v163, v39, v29\n\t"
    "v_mfma_f32_16x16x32_bf16 v[172:175], a[120:123], a[8:11], v[172:175]\n\t"
    "ds_read_b64_tr_b16 a[118:119], v12 offset:14080\n\t"
    "v_fmac_f32 v168, v36, v31\n\t"
    "v_mfma_f32_16x16x32_bf16 v[172:175], a[124:127], a[12:15], v[172:175]\n\t"
    "v_fmac_f32 v169, v37, v31\n\t"
    "v_fmac_f32 v170, v38, v31\n\t"
    "v_mfma_f32_16x16x32_bf16 v[180:183], a[120:123], a[16:19], v[180:183]\n\t"
    "buffer_load_dwordx4 v10, s[48:51], s60 offen offset:2048 lds\n\t"
    "v_mfma_f32_16x16x32_bf16 v[180:183], a[124:127], a[20:23], v[180:183]\n\t"
    "v_fmac_f32 v171, v39, v31\n\t"
    "v_fmac_f32 v176, v36, v33\n\t"
    "v_mfma_f32_16x16x32_bf16 v[188:191], a[120:123], a[24:27], v[188:191]\n\t"
    "ds_read_b64_tr_b16 a[120:121], v12 offset:264\n\t"
    "v_fmac_f32 v177, v37, v33\n\t"
    "v_mfma_f32_16x16x32_bf16 v[188:191], a[124:127], a[28:31], v[188:191]\n\t"
    "s_waitcnt lgkmcnt(5)\n\t"
    "ds_read_b64_tr_b16 a[122:123], v12 offset:1800\n\t"
    "v_fmac_f32 v178, v38, v33\n\t"
    "v_mfma_f32_16x16x32_bf16 v[192:195], a[96:99], a[0:3], v[192:195]\n\t"
    "ds_read_b64_tr_b16 a[124:125], v12 offset:12552\n\t"
    "v_fmac_f32 v179, v39, v33\n\t"
    "v_mfma_f32_16x16x32_bf16 v[192:195], a[100:103], a[4:7], v[192:195]\n\t"
    "ds_read_b64_tr_b16 a[126:127], v12 offset:14088\n\t"
    "v_fmac_f32 v184, v36, v35\n\t"
    "v_mfma_f32_16x16x32_bf16 v[200:203], a[96:99], a[8:11], v[200:203]\n\t"
    "v_fmac_f32 v185, v37, v35\n\t"
    "v_fmac_f32 v186, v38, v35\n\t"
    "v_mfma_f32_16x16x32_bf16 v[200:203], a[100:103], a[12:15], v[200:203]\n\t"
    "v_fmac_f32 v187, v39, v35\n\t"
    "v_mfma_f32_16x16x32_bf16 v[208:211], a[96:99], a[16:19], v[208:211]\n\t"
    "v_mfma_f32_16x16x32_bf16 v[208:211], a[100:103], a[20:23], v[208:211]\n\t"
    "v_mfma_f32_16x16x32_bf16 v[216:219], a[96:99], a[24:27], v[216:219]\n\t"
    "ds_read_b64_tr_b16 a[96:97], v11 offset:24576\n\t"
    "v_mfma_f32_16x16x32_bf16 v[216:219], a[100:103], a[28:31], v[216:219]\n\t"
    "ds_read_b64_tr_b16 a[98:99], v11 offset:26112\n\t"
    "v_mfma_f32_16x16x32_bf16 v[196:199], a[104:107], a[0:3], v[196:199]\n\t"
    "ds_read_b64_tr_b16 a[100:101], v11 offset:36864\n\t"
    "v_accvgpr_read_b32 v40, a240\n\t"
    "v_mfma_f32_16x16x32_bf16 v[196:199], a[108:111], a[4:7], v[196:199]\n\t"
    "ds_read_b64_tr_b16 a[102:103], v11 offset:38400\n\t"
    "v_accvgpr_read_b32 v41, a241\n\t"
    "v_mfma_f32_16x16x32_bf16 v[204:207], a[104:107], a[8:11], v[204:207]\n\t"
    "v_accvgpr_read_b32 v42, a242\n\t"
    "v_accvgpr_read_b32 v43, a243\n\t"
    "v_mfma_f32_16x16x32_bf16 v[204:207], a[108:111], a[12:15], v[204:207]\n\t"
    "v_mfma_f32_16x16x32_bf16 v[212:215], a[104:107], a[16:19], v[212:215]\n\t"
    "s_nop 0\n\t"
    "v_fmac_f32 v196, v40, v29\n\t"
    "v_fmac_f32 v197, v41, v29\n\t"
    "v_mfma_f32_16x16x32_bf16 v[212:215], a[108:111], a[20:23], v[212:215]\n\t"
    "v_fmac_f32 v198, v42, v29\n\t"
    "v_fmac_f32 v199, v43, v29\n\t"
    "v_mfma_f32_16x16x32_bf16 v[220:223], a[104:107], a[24:27], v[220:223]\n\t"
    "ds_read_b64_tr_b16 a[104:105], v11 offset:24584\n\t"
    "v_fmac_f32 v204, v40, v31\n\t"
    "v_mfma_f32_16x16x32_bf16 v[220:223], a[108:111], a[28:31], v[220:223]\n\t"
    "s_waitcnt lgkmcnt(5)\n\t"
    "ds_read_b64_tr_b16 a[106:107], v11 offset:26120\n\t"
    "v_fmac_f32 v205, v41, v31\n\t"
    "v_mfma_f32_16x16x32_bf16 v[224:227], a[112:115], a[0:3], v[224:227]\n\t"
    "ds_read_b64_tr_b16 a[108:109], v11 offset:36872\n\t"
    "v_fmac_f32 v206, v42, v31\n\t"
    "v_mfma_f32_16x16x32_bf16 v[224:227], a[116:119], a[4:7], v[224:227]\n\t"
    "ds_read_b64_tr_b16 a[110:111], v11 offset:38408\n\t"
    "v_fmac_f32 v207, v43, v31\n\t"
    "v_mfma_f32_16x16x32_bf16 v[232:235], a[112:115], a[8:11], v[232:235]\n\t"
    "v_fmac_f32 v212, v40, v33\n\t"
    "v_fmac_f32 v213, v41, v33\n\t"
    "v_mfma_f32_16x16x32_bf16 v[232:235], a[116:119], a[12:15], v[232:235]\n\t"
    "v_fmac_f32 v214, v42, v33\n\t"
    "v_fmac_f32 v215, v43, v33\n\t"
    "v_mfma_f32_16x16x32_bf16 v[240:243], a[112:115], a[16:19], v[240:243]\n\t"
    "v_fmac_f32 v220, v40, v35\n\t"
    "v_fmac_f32 v221, v41, v35\n\t"
    "v_mfma_f32_16x16x32_bf16 v[240:243], a[116:119], a[20:23], v[240:243]\n\t"
    "v_fmac_f32 v222, v42, v35\n\t"
    "v_fmac_f32 v223, v43, v35\n\t"
    "v_mfma_f32_16x16x32_bf16 v[248:251], a[112:115], a[24:27], v[248:251]\n\t"
    "ds_read_b64_tr_b16 a[112:113], v12 offset:24576\n\t"
    "v_mfma_f32_16x16x32_bf16 v[248:251], a[116:119], a[28:31], v[248:251]\n\t"
    "ds_read_b64_tr_b16 a[114:115], v12 offset:26112\n\t"
    "v_mfma_f32_16x16x32_bf16 v[228:231], a[120:123], a[0:3], v[228:231]\n\t"
    "ds_read_b64_tr_b16 a[116:117], v12 offset:36864\n\t"
    "v_mfma_f32_16x16x32_bf16 v[228:231], a[124:127], a[4:7], v[228:231]\n\t"
    "ds_read_b64_tr_b16 a[118:119], v12 offset:38400\n\t"
    "v_mfma_f32_16x16x32_bf16 v[236:239], a[120:123], a[8:11], v[236:239]\n\t"
    "v_mfma_f32_16x16x32_bf16 v[236:239], a[124:127], a[12:15], v[236:239]\n\t"
    "v_mfma_f32_16x16x32_bf16 v[244:247], a[120:123], a[16:19], v[244:247]\n\t"
    "v_mfma_f32_16x16x32_bf16 v[244:247], a[124:127], a[20:23], v[244:247]\n\t"
    "v_mfma_f32_16x16x32_bf16 v[252:255], a[120:123], a[24:27], v[252:255]\n\t"
    "ds_read_b64_tr_b16 a[120:121], v12 offset:24584\n\t"
    "v_mfma_f32_16x16x32_bf16 v[252:255], a[124:127], a[28:31], v[252:255]\n\t"
    "s_waitcnt lgkmcnt(6)\n\t"
    "ds_read_b64_tr_b16 a[122:123], v12 offset:26120\n\t"
    "v_mfma_f32_16x16x32_bf16 v[64:67], a[96:99], a[32:35], v[64:67]\n\t"
    "ds_read_b64_tr_b16 a[132:133], v63 offset:0\n\t"
    "v_mfma_f32_16x16x32_bf16 v[64:67], a[100:103], a[36:39], v[64:67]\n\t"
    "ds_read_b64_tr_b16 a[134:135], v63 offset:2048\n\t"
    "v_and_b32 v63, 0xffffffe7, v63\n\t"
    "v_mfma_f32_16x16x32_bf16 v[72:75], a[96:99], a[40:43], v[72:75]\n\t"
    "ds_read_b64_tr_b16 a[136:137], v57 offset:0\n\t"
    "v_mfma_f32_16x16x32_bf16 v[72:75], a[100:103], a[44:47], v[72:75]\n\t"
    "ds_read_b64_tr_b16 a[124:125], v12 offset:36872\n\t"
    "v_mfma_f32_16x16x32_bf16 v[80:83], a[96:99], a[48:51], v[80:83]\n\t"
    "s_mul_i32 s61, s98, 1\n\t"
    "ds_read_b64_tr_b16 a[126:127], v12 offset:38408\n\t"
    "v_mfma_f32_16x16x32_bf16 v[80:83], a[100:103], a[52:55], v[80:83]\n\t"
    "s_add_u32 s61, s61, 10240\n\t"
    "ds_read_b64_tr_b16 a[138:139], v57 offset:2048\n\t"
    "v_mfma_f32_16x16x32_bf16 v[88:91], a[96:99], a[56:59], v[88:91]\n\t"
    "s_sub_u32 s64, s61, 12288\n\t"
    "ds_read_b64_tr_b16 a[140:141], v58 offset:0\n\t"
    "v_mfma_f32_16x16x32_bf16 v[88:91], a[100:103], a[60:63], v[88:91]\n\t"
    "s_min_u32 s61, s61, s64\n\t"
    "ds_read_b64_tr_b16 a[142:143], v58 offset:2048\n\t"
    "v_mfma_f32_16x16x32_bf16 v[68:71], a[104:107], a[32:35], v[68:71]\n\t"
    "s_waitcnt lgkmcnt(10)\n\t"
    "s_add_u32 s61, s61, 8192\n\t"
    "ds_read_b64_tr_b16 a[144:145], v59 offset:0\n\t"
    "v_mfma_f32_16x16x32_bf16 v[68:71], a[108:111], a[36:39], v[68:71]\n\t"
    "buffer_load_dwordx4 a[0:3], v20, s[40:43], s61 offen offset:0 sc1\n\t"
    "v_mfma_f32_16x16x32_bf16 v[76:79], a[104:107], a[40:43], v[76:79]\n\t"
    "ds_read_b64_tr_b16 a[96:97], v11 offset:24704\n\t"
    "v_accvgpr_read_b32 v36, a208\n\t"
    "v_mfma_f32_16x16x32_bf16 v[76:79], a[108:111], a[44:47], v[76:79]\n\t"
    "ds_read_b64_tr_b16 a[98:99], v11 offset:26240\n\t"
    "v_accvgpr_read_b32 v37, a209\n\t"
    "v_mfma_f32_16x16x32_bf16 v[84:87], a[104:107], a[48:51], v[84:87]\n\t"
    "ds_read_b64_tr_b16 a[100:101], v11 offset:36992\n\t"
    "v_accvgpr_read_b32 v38, a210\n\t"
    "v_mfma_f32_16x16x32_bf16 v[84:87], a[108:111], a[52:55], v[84:87]\n\t"
    "buffer_load_dwordx4 a[4:7], v20, s[40:43], s61 offen offset:1024 sc1\n\t"
    "v_mfma_f32_16x16x32_bf16 v[92:95], a[104:107], a[56:59], v[92:95]\n\t"
    "ds_read_b64_tr_b16 a[102:103], v11 offset:38528\n\t"
    "v_accvgpr_read_b32 v39, a211\n\t"
    "v_mfma_f32_16x16x32_bf16 v[92:95], a[108:111], a[60:63], v[92:95]\n\t"
    "ds_read_b64_tr_b16 a[146:147], v59 offset:2048\n\t"
    "v_fmac_f32 v68, v36, v29\n\t"
    "v_mfma_f32_16x16x32_bf16 v[96:99], a[112:115], a[32:35], v[96:99]\n\t"
    "ds_read_b64_tr_b16 a[104:105], v11 offset:24712\n\t"
    "v_fmac_f32 v69, v37, v29\n\t"
    "v_mfma_f32_16x16x32_bf16 v[96:99], a[116:119], a[36:39], v[96:99]\n\t"
    "buffer_load_dwordx4 a[8:11], v21, s[40:43], s61 offen offset:0 sc1\n\t"
    "v_mfma_f32_16x16x32_bf16 v[104:107], a[112:115], a[40:43], v[104:107]\n\t"
    "ds_read_b64_tr_b16 a[106:107], v11 offset:26248\n\t"
    "v_fmac_f32 v70, v38, v29\n\t"
    "v_mfma_f32_16x16x32_bf16 v[104:107], a[116:119], a[44:47], v[104:107]\n\t"
    "ds_read_b64_tr_b16 a[108:109], v11 offset:37000\n\t"
    "v_fmac_f32 v71, v39, v29\n\t"
    "v_mfma_f32_16x16x32_bf16 v[112:115], a[112:115], a[48:51], v[112:115]\n\t"
    "ds_read_b64_tr_b16 a[110:111], v11 offset:38536\n\t"
    "v_fmac_f32 v76, v36, v31\n\t"
    "v_mfma_f32_16x16x32_bf16 v[112:115], a[116:119], a[52:55], v[112:115]\n\t"
    "buffer_load_dwordx4 a[12:15], v21, s[40:43], s61 offen offset:1024 sc1\n\t"
    "v_mfma_f32_16x16x32_bf16 v[120:123], a[112:115], a[56:59], v[120:123]\n\t"
    "ds_read_b64_tr_b16 a[112:113], v12 offset:24704\n\t"
    "v_fmac_f32 v77, v37, v31\n\t"
    "v_mfma_f32_16x16x32_bf16 v[120:123], a[116:119], a[60:63], v[120:123]\n\t"
    "s_waitcnt lgkmcnt(8)\n\t"
    "ds_read_b64_tr_b16 a[114:115], v12 offset:26240\n\t"
    "v_fmac_f32 v78, v38, v31\n\t"
    "v_mfma_f32_16x16x32_bf16 v[100:103], a[120:123], a[32:35], v[100:103]\n\t"
    "ds_read_b64_tr_b16 a[116:117], v12 offset:36992\n\t"
    "v_fmac_f32 v79, v39, v31\n\t"
    "v_mfma_f32_16x16x32_bf16 v[100:103], a[124:127], a[36:39], v[100:103]\n\t"
    "buffer_load_dwordx4 a[16:19], v22, s[40:43], s61 offen offset:0 sc1\n\t"
    "v_mfma_f32_16x16x32_bf16 v[108:111], a[120:123], a[40:43], v[108:111]\n\t"
    "ds_read_b64_tr_b16 a[118:119], v12 offset:38528\n\t"
    "v_fmac_f32 v84, v36, v33\n\t"
    "v_mfma_f32_16x16x32_bf16 v[108:111], a[124:127], a[44:47], v[108:111]\n\t"
    "v_fmac_f32 v85, v37, v33\n\t"
    "v_fmac_f32 v86, v38, v33\n\t"
    "v_mfma_f32_16x16x32_bf16 v[116:119], a[120:123], a[48:51], v[116:119]\n\t"
    "v_fmac_f32 v87, v39, v33\n\t"
    "v_fmac_f32 v92, v36, v35\n\t"
    "v_mfma_f32_16x16x32_bf16 v[116:119], a[124:127], a[52:55], v[116:119]\n\t"
    "buffer_load_dwordx4 a[20:23], v22, s[40:43], s61 offen offset:1024 sc1\n\t"
    "v_mfma_f32_16x16x32_bf16 v[124:127], a[120:123], a[56:59], v[124:127]\n\t"
    "ds_read_b64_tr_b16 a[120:121], v12 offset:24712\n\t"
    "v_fmac_f32 v93, v37, v35\n\t"
    "v_mfma_f32_16x16x32_bf16 v[124:127], a[124:127], a[60:63], v[124:127]\n\t"
    "ds_read_b64_tr_b16 a[122:123], v12 offset:26248\n\t"
    "v_fmac_f32 v94, v38, v35\n\t"
    "v_mfma_f32_16x16x32_bf16 v[128:131], a[96:99], a[32:35], v[128:131]\n\t"
    "s_waitcnt lgkmcnt(6)\n\t"
    "ds_read_b64_tr_b16 a[124:125], v12 offset:37000\n\t"
    "v_fmac_f32 v95, v39, v35\n\t"
    "v_mfma_f32_16x16x32_bf16 v[128:131], a[100:103], a[36:39], v[128:131]\n\t"
    "buffer_load_dwordx4 a[24:27], v23, s[40:43], s61 offen offset:0 sc1\n\t"
    "v_mfma_f32_16x16x32_bf16 v[136:139], a[96:99], a[40:43], v[136:139]\n\t"
    "ds_read_b64_tr_b16 a[126:127], v12 offset:38536\n\t"
    "v_accvgpr_read_b32 v40, a220\n\t"
    "v_mfma_f32_16x16x32_bf16 v[136:139], a[100:103], a[44:47], v[136:139]\n\t"
    "v_accvgpr_read_b32 v41, a221\n\t"
    "v_accvgpr_read_b32 v42, a222\n\t"
    "v_mfma_f32_16x16x32_bf16 v[144:147], a[96:99], a[48:51], v[144:147]\n\t"
    "v_accvgpr_read_b32 v43, a223\n\t"
    "v_fmac_f32 v128, v40, v29\n\t"
    "v_mfma_f32_16x16x32_bf16 v[144:147], a[100:103], a[52:55], v[144:147]\n\t"
    "buffer_load_dwordx4 a[28:31], v23, s[40:43], s61 offen offset:1024 sc1\n\t"
    "v_mfma_f32_16x16x32_bf16 v[152:155], a[96:99], a[56:59], v[152:155]\n\t"
    "ds_read_b64_tr_b16 a[96:97], v11 offset:24832\n\t"
    "v_fmac_f32 v129, v41, v29\n\t"
    "v_mfma_f32_16x16x32_bf16 v[152:155], a[100:103], a[60:63], v[152:155]\n\t"
    "s_waitcnt vmcnt(14)\n\t"
    "ds_read_b64_tr_b16 a[98:99], v11 offset:26368\n\t"
    "v_fmac_f32 v130, v42, v29\n\t"
    "v_mfma_f32_16x16x32_bf16 a[148:151], a[128:131], a[132:135], 0\n\t"
    "ds_read_b64_tr_b16 a[100:101], v11 offset:37120\n\t"
    "v_fmac_f32 v131, v43, v29\n\t"
    "v_mfma_f32_16x16x32_bf16 a[152:155], a[128:131], a[136:139], 0\n\t"
    "ds_read_b64_tr_b16 a[102:103], v11 offset:38656\n\t"
    "v_fmac_f32 v136, v40, v31\n\t"
    "v_mfma_f32_16x16x32_bf16 a[156:159], a[128:131], a[140:143], 0\n\t"
    "v_fmac_f32 v137, v41, v31\n\t"
    "v_fmac_f32 v138, v42, v31\n\t"
    "v_mfma_f32_16x16x32_bf16 a[160:163], a[128:131], a[144:147], 0\n\t"
    "buffer_store_dwordx2 a[148:149], v52, s[40:43], s90 offen offset:0 sc1\n\t"
    "v_mfma_f32_16x16x32_bf16 v[132:135], a[104:107], a[32:35], v[132:135]\n\t"
    "v_fmac_f32 v139, v43, v31\n\t"
    "v_fmac_f32 v144, v40, v33\n\t"
    "v_mfma_f32_16x16x32_bf16 v[132:135], a[108:111], a[36:39], v[132:135]\n\t"
    "v_fmac_f32 v145, v41, v33\n\t"
    "v_fmac_f32 v146, v42, v33\n\t"
    "v_mfma_f32_16x16x32_bf16 v[140:143], a[104:107], a[40:43], v[140:143]\n\t"
    "v_fmac_f32 v147, v43, v33\n\t"
    "v_fmac_f32 v152, v40, v35\n\t"
    "v_mfma_f32_16x16x32_bf16 v[140:143], a[108:111], a[44:47], v[140:143]\n\t"
    "buffer_store_dwordx2 a[152:153], v52, s[40:43], s90 offen offset:128 sc1\n\t"
    "v_mfma_f32_16x16x32_bf16 v[148:151], a[104:107], a[48:51], v[148:151]\n\t"
    "v_fmac_f32 v153, v41, v35\n\t"
    "v_fmac_f32 v154, v42, v35\n\t"
    "v_mfma_f32_16x16x32_bf16 v[148:151], a[108:111], a[52:55], v[148:151]\n\t"
    "v_fmac_f32 v155, v43, v35\n\t"
    "v_lshrrev_b32 v21, 8, v4\n\t"
    "v_mfma_f32_16x16x32_bf16 v[156:159], a[104:107], a[56:59], v[156:159]\n\t"
    "ds_read_b64_tr_b16 a[104:105], v11 offset:24840\n\t"
    "v_mul_u32_u24 v21, 0x2f00, v21\n\t"
    "v_mfma_f32_16x16x32_bf16 v[156:159], a[108:111], a[60:63], v[156:159]\n\t"
    "s_waitcnt lgkmcnt(2)\n\t"
    "buffer_store_dwordx2 a[156:157], v52, s[40:43], s90 offen offset:256 sc1\n\t"
    "v_mfma_f32_16x16x32_bf16 v[160:163], a[112:115], a[32:35], v[160:163]\n\t"
    "ds_read_b64_tr_b16 a[106:107], v11 offset:26376\n\t"
    "v_mfma_f32_16x16x32_bf16 v[160:163], a[116:119], a[36:39], v[160:163]\n\t"
    "ds_read_b64_tr_b16 a[108:109], v11 offset:37128\n\t"
    "v_mfma_f32_16x16x32_bf16 v[168:171], a[112:115], a[40:43], v[168:171]\n\t"
    "ds_read_b64_tr_b16 a[110:111], v11 offset:38664\n\t"
    "v_mfma_f32_16x16x32_bf16 v[168:171], a[116:119], a[44:47], v[168:171]\n\t"
    "buffer_store_dwordx2 a[160:161], v52, s[40:43], s90 offen offset:384 sc1\n\t"
    "v_mfma_f32_16x16x32_bf16 v[176:179], a[112:115], a[48:51], v[176:179]\n\t"
    "v_mfma_f32_16x16x32_bf16 v[176:179], a[116:119], a[52:55], v[176:179]\n\t"
    "v_mfma_f32_16x16x32_bf16 v[184:187], a[112:115], a[56:59], v[184:187]\n\t"
    "s_add_u32 m0, s83, 73728\n\t"
    "ds_read_b64_tr_b16 a[112:113], v12 offset:24832\n\t"
    "v_mfma_f32_16x16x32_bf16 v[184:187], a[116:119], a[60:63], v[184:187]\n\t"
    "ds_read_b64_tr_b16 a[114:115], v12 offset:26368\n\t"
    "v_mfma_f32_16x16x32_bf16 v[164:167], a[120:123], a[32:35], v[164:167]\n\t"
    "ds_read_b64_tr_b16 a[116:117], v12 offset:37120\n\t"
    "v_accvgpr_read_b32 v36, a232\n\t"
    "v_mfma_f32_16x16x32_bf16 v[164:167], a[124:127], a[36:39], v[164:167]\n\t"
    "s_mul_i32 s60, s98, 384\n\t"
    "ds_read_b64_tr_b16 a[118:119], v12 offset:38656\n\t"
    "v_accvgpr_read_b32 v37, a233\n\t"
    "v_mfma_f32_16x16x32_bf16 v[172:175], a[120:123], a[40:43], v[172:175]\n\t"
    "s_add_u32 s60, s60, 3932160\n\t"
    "v_accvgpr_read_b32 v38, a234\n\t"
    "v_accvgpr_read_b32 v39, a235\n\t"
    "v_mfma_f32_16x16x32_bf16 v[172:175], a[124:127], a[44:47], v[172:175]\n\t"
    "s_sub_u32 s62, s60, 4718592\n\t"
    "v_mfma_f32_16x16x32_bf16 v[180:183], a[120:123], a[48:51], v[180:183]\n\t"
    "s_min_u32 s60, s60, s62\n\t"
    "v_fmac_f32 v164, v36, v29\n\t"
    "v_fmac_f32 v165, v37, v29\n\t"
    "v_mfma_f32_16x16x32_bf16 v[180:183], a[124:127], a[52:55], v[180:183]\n\t"
    "s_add_u32 s60, s60, 3145728\n\t"
    "buffer_load_dwordx4 v5, s[44:47], s60 offen offset:0 lds\n\t"
    "v_mfma_f32_16x16x32_bf16 v[188:191], a[120:123], a[56:59], v[188:191]\n\t"
    "ds_read_b64_tr_b16 a[120:121], v12 offset:24840\n\t"
    "v_fmac_f32 v166, v38, v29\n\t"
    "v_mfma_f32_16x16x32_bf16 v[188:191], a[124:127], a[60:63], v[188:191]\n\t"
    "ds_read_b64_tr_b16 a[122:123], v12 offset:26376\n\t"
    "v_fmac_f32 v167, v39, v29\n\t"
    "v_mfma_f32_16x16x32_bf16 v[192:195], a[96:99], a[32:35], v[192:195]\n\t"
    "s_waitcnt lgkmcnt(6)\n\t"
    "ds_read_b64_tr_b16 a[124:125], v12 offset:37128\n\t"
    "v_fmac_f32 v172, v36, v31\n\t"
    "v_mfma_f32_16x16x32_bf16 v[192:195], a[100:103], a[36:39], v[192:195]\n\t"
    "ds_read_b64_tr_b16 a[126:127], v12 offset:38664\n\t"
    "v_fmac_f32 v173, v37, v31\n\t"
    "v_mfma_f32_16x16x32_bf16 v[200:203], a[96:99], a[40:43], v[200:203]\n\t"
    "buffer_load_dwordx4 v6, s[44:47], s60 offen offset:1024 lds\n\t"
    "v_mfma_f32_16x16x32_bf16 v[200:203], a[100:103], a[44:47], v[200:203]\n\t"
    "v_fmac_f32 v174, v38, v31\n\t"
    "v_fmac_f32 v175, v39, v31\n\t"
    "v_mfma_f32_16x16x32_bf16 v[208:211], a[96:99], a[48:51], v[208:211]\n\t"
    "v_fmac_f32 v180, v36, v33\n\t"
    "v_fmac_f32 v181, v37, v33\n\t"
    "v_mfma_f32_16x16x32_bf16 v[208:211], a[100:103], a[52:55], v[208:211]\n\t"
    "v_fmac_f32 v182, v38, v33\n\t"
    "v_fmac_f32 v183, v39, v33\n\t"
    "v_mfma_f32_16x16x32_bf16 v[216:219], a[96:99], a[56:59], v[216:219]\n\t"
    "buffer_load_dwordx4 v7, s[44:47], s60 offen offset:2048 lds\n\t"
    "v_mfma_f32_16x16x32_bf16 v[216:219], a[100:103], a[60:63], v[216:219]\n\t"
    "ds_read_b64_tr_b16 a[96:97], v24 offset:0\n\t"
    "v_fmac_f32 v188, v36, v35\n\t"
    "v_mfma_f32_16x16x32_bf16 v[196:199], a[104:107], a[32:35], v[196:199]\n\t"
    "ds_read_b64_tr_b16 a[98:99], v24 offset:1536\n\t"
    "v_fmac_f32 v189, v37, v35\n\t"
    "v_mfma_f32_16x16x32_bf16 v[196:199], a[108:111], a[36:39], v[196:199]\n\t"
    "s_add_u32 m0, s83, 76800\n\t"
    "ds_read_b64_tr_b16 a[100:101], v24 offset:12288\n\t"
    "v_fmac_f32 v190, v38, v35\n\t"
    "v_mfma_f32_16x16x32_bf16 v[204:207], a[104:107], a[40:43], v[204:207]\n\t"
    "s_add_u32 s60, s60, 98304\n\t"
    "buffer_load_dwordx4 v5, s[44:47], s60 offen offset:0 lds\n\t"
    "v_mfma_f32_16x16x32_bf16 v[204:207], a[108:111], a[44:47], v[204:207]\n\t"
    "ds_read_b64_tr_b16 a[102:103], v24 offset:13824\n\t"
    "v_fmac_f32 v191, v39, v35\n\t"
    "v_mfma_f32_16x16x32_bf16 v[212:215], a[104:107], a[48:51], v[212:215]\n\t"
    "v_mfma_f32_16x16x32_bf16 v[212:215], a[108:111], a[52:55], v[212:215]\n\t"
    "v_mfma_f32_16x16x32_bf16 v[220:223], a[104:107], a[56:59], v[220:223]\n\t"
    "buffer_load_dwordx4 v6, s[44:47], s60 offen offset:1024 lds\n\t"
    "v_mfma_f32_16x16x32_bf16 v[220:223], a[108:111], a[60:63], v[220:223]\n\t"
    "s_waitcnt lgkmcnt(4)\n\t"
    "ds_read_b64_tr_b16 a[104:105], v24 offset:8\n\t"
    "v_mfma_f32_16x16x32_bf16 v[224:227], a[112:115], a[32:35], v[224:227]\n\t"
    "ds_read_b64_tr_b16 a[106:107], v24 offset:1544\n\t"
    "v_accvgpr_read_b32 v40, a244\n\t"
    "v_mfma_f32_16x16x32_bf16 v[224:227], a[116:119], a[36:39], v[224:227]\n\t"
    "ds_read_b64_tr_b16 a[108:109], v24 offset:12296\n\t"
    "v_accvgpr_read_b32 v41, a245\n\t"
    "v_mfma_f32_16x16x32_bf16 v[232:235], a[112:115], a[40:43], v[232:235]\n\t"
    "buffer_load_dwordx4 v7, s[44:47], s60 offen offset:2048 lds\n\t"
    "v_mfma_f32_16x16x32_bf16 v[232:235], a[116:119], a[44:47], v[232:235]\n\t"
    "s_add_u32 m0, s83, 122880\n\t"
    "ds_read_b64_tr_b16 a[110:111], v24 offset:13832\n\t"
    "v_accvgpr_read_b32 v42, a246\n\t"
    "v_mfma_f32_16x16x32_bf16 v[240:243], a[112:115], a[48:51], v[240:243]\n\t"
    "s_add_u32 s60, s87, 4096\n\t"
    "v_accvgpr_read_b32 v43, a247\n\t"
    "v_fmac_f32 v224, v40, v29\n\t"
    "v_mfma_f32_16x16x32_bf16 v[240:243], a[116:119], a[52:55], v[240:243]\n\t"
    "s_bfe_u32 s60, s60, 0x2000c\n\t"
    "v_fmac_f32 v225, v41, v29\n\t"
    "v_fmac_f32 v226, v42, v29\n\t"
    "v_mfma_f32_16x16x32_bf16 v[248:251], a[112:115], a[56:59], v[248:251]\n\t"
    "s_mulk_i32 s60, 24576\n\t"
    "buffer_load_dwordx4 v8, s[48:51], s60 offen offset:0 lds\n\t"
    "v_mfma_f32_16x16x32_bf16 v[248:251], a[116:119], a[60:63], v[248:251]\n\t"
    "ds_read_b64_tr_b16 a[112:113], v25 offset:0\n\t"
    "v_fmac_f32 v227, v43, v29\n\t"
    "v_mfma_f32_16x16x32_bf16 v[228:231], a[120:123], a[32:35], v[228:231]\n\t"
    "ds_read_b64_tr_b16 a[114:115], v25 offset:1536\n\t"
    "v_fmac_f32 v232, v40, v31\n\t"
    "v_mfma_f32_16x16x32_bf16 v[228:231], a[124:127], a[36:39], v[228:231]\n\t"
    "ds_read_b64_tr_b16 a[116:117], v25 offset:12288\n\t"
    "v_fmac_f32 v233, v41, v31\n\t"
    "v_mfma_f32_16x16x32_bf16 v[236:239], a[120:123], a[40:43], v[236:239]\n\t"
    "buffer_load_dwordx4 v9, s[48:51], s60 offen offset:1024 lds\n\t"
    "v_mfma_f32_16x16x32_bf16 v[236:239], a[124:127], a[44:47], v[236:239]\n\t"
    "ds_read_b64_tr_b16 a[118:119], v25 offset:13824\n\t"
    "v_fmac_f32 v234, v42, v31\n\t"
    "v_mfma_f32_16x16x32_bf16 v[244:247], a[120:123], a[48:51], v[244:247]\n\t"
    "v_fmac_f32 v235, v43, v31\n\t"
    "v_fmac_f32 v240, v40, v33\n\t"
    "v_mfma_f32_16x16x32_bf16 v[244:247], a[124:127], a[52:55], v[244:247]\n\t"
    "v_fmac_f32 v241, v41, v33\n\t"
    "v_fmac_f32 v242, v42, v33\n\t"
    "v_mfma_f32_16x16x32_bf16 v[252:255], a[120:123], a[56:59], v[252:255]\n\t"
    "s_mov_b64 s[56:57], s[68:69]\n\t"
    "buffer_load_dwordx4 v10, s[48:51], s60 offen offset:2048 lds\n\t"
    "v_mfma_f32_16x16x32_bf16 v[252:255], a[124:127], a[60:63], v[252:255]\n\t"
    "s_waitcnt lgkmcnt(4)\n\t"
    "ds_read_b64_tr_b16 a[120:121], v25 offset:8\n\t"
    "v_fmac_f32 v243, v43, v33\n\t"
    "v_mfma_f32_16x16x32_bf16 v[64:67], a[96:99], a[64:67], v[64:67]\n\t"
    "ds_read_b64_tr_b16 a[122:123], v25 offset:1544\n\t"
    "v_fmac_f32 v248, v40, v35\n\t"
    "v_mfma_f32_16x16x32_bf16 v[64:67], a[100:103], a[68:71], v[64:67]\n\t"
    "ds_read_b64_tr_b16 a[124:125], v25 offset:12296\n\t"
    "v_fmac_f32 v249, v41, v35\n\t"
    "v_mfma_f32_16x16x32_bf16 v[72:75], a[96:99], a[72:75], v[72:75]\n\t"
    "ds_read_b64_tr_b16 a[126:127], v25 offset:13832\n\t"
    "v_fmac_f32 v250, v42, v35\n\t"
    "v_mfma_f32_16x16x32_bf16 v[72:75], a[100:103], a[76:79], v[72:75]\n\t"
    "v_fmac_f32 v251, v43, v35\n\t"
    "v_mfma_f32_16x16x32_bf16 v[80:83], a[96:99], a[80:83], v[80:83]\n\t"
    "s_add_u32 m0, s83, 125952\n\t"
    "v_mfma_f32_16x16x32_bf16 v[80:83], a[100:103], a[84:87], v[80:83]\n\t"
    "s_add_u32 s60, s60, 3072\n\t"
    "v_mfma_f32_16x16x32_bf16 v[88:91], a[96:99], a[88:91], v[88:91]\n\t"
    "s_cmp_eq_u32 s88, 1879048192\n\t"
    "s_cselect_b32 s58, 0, 40960\n\t"
    "ds_read_b64_tr_b16 a[96:97], v24 offset:128\n\t"
    "v_mfma_f32_16x16x32_bf16 v[88:91], a[100:103], a[92:95], v[88:91]\n\t"
    "buffer_load_dwordx4 v8, s[48:51], s60 offen offset:0 lds\n\t"
    "v_mfma_f32_16x16x32_bf16 v[68:71], a[104:107], a[64:67], v[68:71]\n\t"
    "ds_read_b64_tr_b16 a[98:99], v24 offset:1664\n\t"
    "v_mfma_f32_16x16x32_bf16 v[68:71], a[108:111], a[68:71], v[68:71]\n\t"
    "ds_read_b64_tr_b16 a[100:101], v24 offset:12416\n\t"
    "v_mfma_f32_16x16x32_bf16 v[76:79], a[104:107], a[72:75], v[76:79]\n\t"
    "ds_read_b64_tr_b16 a[102:103], v24 offset:13952\n\t"
    "v_mfma_f32_16x16x32_bf16 v[76:79], a[108:111], a[76:79], v[76:79]\n\t"
    "v_mfma_f32_16x16x32_bf16 v[84:87], a[104:107], a[80:83], v[84:87]\n\t"
    "buffer_load_dwordx4 v9, s[48:51], s60 offen offset:1024 lds\n\t"
    "v_mfma_f32_16x16x32_bf16 v[84:87], a[108:111], a[84:87], v[84:87]\n\t"
    "v_mfma_f32_16x16x32_bf16 v[92:95], a[104:107], a[88:91], v[92:95]\n\t"
    "ds_read_b64_tr_b16 a[104:105], v24 offset:136\n\t"
    "v_mfma_f32_16x16x32_bf16 v[92:95], a[108:111], a[92:95], v[92:95]\n\t"
    "s_waitcnt lgkmcnt(7)\n\t"
    "ds_read_b64_tr_b16 a[106:107], v24 offset:1672\n\t"
    "v_mfma_f32_16x16x32_bf16 v[96:99], a[112:115], a[64:67], v[96:99]\n\t"
    "ds_read_b64_tr_b16 a[108:109], v24 offset:12424\n\t"
    "v_accvgpr_read_b32 v36, a212\n\t"
    "v_mfma_f32_16x16x32_bf16 v[96:99], a[116:119], a[68:71], v[96:99]\n\t"
    "buffer_load_dwordx4 v10, s[48:51], s60 offen offset:2048 lds\n\t"
    "v_mfma_f32_16x16x32_bf16 v[104:107], a[112:115], a[72:75], v[104:107]\n\t"
    "s_mov_b32 m0, s86\n\t"
    "ds_read_b64_tr_b16 a[110:111], v24 offset:13960\n\t"
    "v_accvgpr_read_b32 v37, a213\n\t"
    "v_mfma_f32_16x16x32_bf16 v[104:107], a[116:119], a[76:79], v[104:107]\n\t"
    "s_mov_b32 s60, 0\n\t"
    "v_accvgpr_read_b32 v38, a214\n\t"
    "v_accvgpr_read_b32 v39, a215\n\t"
    "v_mfma_f32_16x16x32_bf16 v[112:115], a[112:115], a[80:83], v[112:115]\n\t"
    "v_fmac_f32 v96, v36, v29\n\t"
    "v_fmac_f32 v97, v37, v29\n\t"
    "v_mfma_f32_16x16x32_bf16 v[112:115], a[116:119], a[84:87], v[112:115]\n\t"
    "v_fmac_f32 v98, v38, v29\n\t"
    "v_fmac_f32 v99, v39, v29\n\t"
    "v_mfma_f32_16x16x32_bf16 v[120:123], a[112:115], a[88:91], v[120:123]\n\t"
    "buffer_load_dwordx4 v62, s[56:59], s60 offen offset:0 nt lds\n\t"
    "v_mfma_f32_16x16x32_bf16 v[120:123], a[116:119], a[92:95], v[120:123]\n\t"
    "s_mov_b32 s60, 9216\n\t"
    "ds_read_b64_tr_b16 a[112:113], v25 offset:128\n\t"
    "v_mfma_f32_16x16x32_bf16 v[100:103], a[120:123], a[64:67], v[100:103]\n\t"
    "s_waitcnt lgkmcnt(5)\n\t"
    "ds_read_b64_tr_b16 a[114:115], v25 offset:1664\n\t"
    "v_fmac_f32 v104, v36, v31\n\t"
    "v_mfma_f32_16x16x32_bf16 v[100:103], a[124:127], a[68:71], v[100:103]\n\t"
    "ds_read_b64_tr_b16 a[116:117], v25 offset:12416\n\t"
    "v_fmac_f32 v105, v37, v31\n\t"
    "v_mfma_f32_16x16x32_bf16 v[108:111], a[120:123], a[72:75], v[108:111]\n\t"
    "ds_read_b64_tr_b16 a[118:119], v25 offset:13952\n\t"
    "v_fmac_f32 v106, v38, v31\n\t"
    "v_mfma_f32_16x16x32_bf16 v[108:111], a[124:127], a[76:79], v[108:111]\n\t"
    "buffer_load_dwordx4 v62, s[56:59], s60 offen offset:1024 nt lds\n\t"
    "v_mfma_f32_16x16x32_bf16 v[116:119], a[120:123], a[80:83], v[116:119]\n\t"
    "s_mov_b32 s60, 18432\n\t"
    "v_fmac_f32 v107, v39, v31\n\t"
    "v_fmac_f32 v112, v36, v33\n\t"
    "v_mfma_f32_16x16x32_bf16 v[116:119], a[124:127], a[84:87], v[116:119]\n\t"
    "v_fmac_f32 v113, v37, v33\n\t"
    "v_fmac_f32 v114, v38, v33\n\t"
    "v_mfma_f32_16x16x32_bf16 v[124:127], a[120:123], a[88:91], v[124:127]\n\t"
    "ds_read_b64_tr_b16 a[120:121], v25 offset:136\n\t"
    "v_fmac_f32 v115, v39, v33\n\t"
    "v_mfma_f32_16x16x32_bf16 v[124:127], a[124:127], a[92:95], v[124:127]\n\t"
    "ds_read_b64_tr_b16 a[122:123], v25 offset:1672\n\t"
    "v_fmac_f32 v120, v36, v35\n\t"
    "v_mfma_f32_16x16x32_bf16 v[128:131], a[96:99], a[64:67], v[128:131]\n\t"
    "buffer_load_dwordx4 v62, s[56:59], s60 offen offset:2048 nt lds\n\t"
    "v_mfma_f32_16x16x32_bf16 v[128:131], a[100:103], a[68:71], v[128:131]\n\t"
    "s_mov_b32 s60, 27648\n\t"
    "ds_read_b64_tr_b16 a[124:125], v25 offset:12424\n\t"
    "v_fmac_f32 v121, v37, v35\n\t"
    "v_mfma_f32_16x16x32_bf16 v[136:139], a[96:99], a[72:75], v[136:139]\n\t"
    "ds_read_b64_tr_b16 a[126:127], v25 offset:13960\n\t"
    "v_fmac_f32 v122, v38, v35\n\t"
    "v_mfma_f32_16x16x32_bf16 v[136:139], a[100:103], a[76:79], v[136:139]\n\t"
    "v_fmac_f32 v123, v39, v35\n\t"
    "v_mfma_f32_16x16x32_bf16 v[144:147], a[96:99], a[80:83], v[144:147]\n\t"
    "v_mfma_f32_16x16x32_bf16 v[144:147], a[100:103], a[84:87], v[144:147]\n\t"
    "buffer_load_dwordx4 v62, s[56:59], s60 offen offset:3072 nt lds\n\t"
    "v_mfma_f32_16x16x32_bf16 v[152:155], a[96:99], a[88:91], v[152:155]\n\t"
    "s_add_u32 m0, s83, 0\n\t"
    "ds_read_b64_tr_b16 a[96:97], v24 offset:256\n\t"
    "v_mfma_f32_16x16x32_bf16 v[152:155], a[100:103], a[92:95], v[152:155]\n\t"
    "s_waitcnt lgkmcnt(5)\n\t"
    "ds_read_b64_tr_b16 a[98:99], v24 offset:1792\n\t"
    "v_mfma_f32_16x16x32_bf16 v[132:135], a[104:107], a[64:67], v[132:135]\n\t"
    "ds_read_b64_tr_b16 a[100:101], v24 offset:12544\n\t"
    "v_accvgpr_read_b32 v40, a224\n\t"
    "v_mfma_f32_16x16x32_bf16 v[132:135], a[108:111], a[68:71], v[132:135]\n\t"
    "s_add_u32 s60, s87, 0\n\t"
    "ds_read_b64_tr_b16 a[102:103], v24 offset:14080\n\t"
    "v_accvgpr_read_b32 v41, a225\n\t"
    "v_mfma_f32_16x16x32_bf16 v[140:143], a[104:107], a[72:75], v[140:143]\n\t"
    "s_bfe_u32 s60, s60, 0x2000c\n\t"
    "v_accvgpr_read_b32 v42, a226\n\t"
    "v_accvgpr_read_b32 v43, a227\n\t"
    "v_mfma_f32_16x16x32_bf16 v[140:143], a[108:111], a[76:79], v[140:143]\n\t"
    "s_mulk_i32 s60, 24576\n\t"
    "v_mov_b32 v59, 0x3e803e80\n\t"
    "v_accvgpr_write_b32 a188, v59\n\t"
    "v_mfma_f32_16x16x32_bf16 v[148:151], a[104:107], a[80:83], v[148:151]\n\t"
    "v_fmac_f32 v132, v40, v29\n\t"
    "v_fmac_f32 v133, v41, v29\n\t"
    "v_mfma_f32_16x16x32_bf16 v[148:151], a[108:111], a[84:87], v[148:151]\n\t"
    "v_fmac_f32 v134, v42, v29\n\t"
    "v_fmac_f32 v135, v43, v29\n\t"
    "v_mfma_f32_16x16x32_bf16 v[156:159], a[104:107], a[88:91], v[156:159]\n\t"
    "ds_read_b64_tr_b16 a[104:105], v24 offset:264\n\t"
    "v_fmac_f32 v140, v40, v31\n\t"
    "v_mfma_f32_16x16x32_bf16 v[156:159], a[108:111], a[92:95], v[156:159]\n\t"
    "ds_read_b64_tr_b16 a[106:107], v24 offset:1800\n\t"
    "v_fmac_f32 v141, v41, v31\n\t"
    "v_mfma_f32_16x16x32_bf16 v[160:163], a[112:115], a[64:67], v[160:163]\n\t"
    "ds_read_b64_tr_b16 a[108:109], v24 offset:12552\n\t"
    "v_fmac_f32 v142, v42, v31\n\t"
    "v_mfma_f32_16x16x32_bf16 v[160:163], a[116:119], a[68:71], v[160:163]\n\t"
    "ds_read_b64_tr_b16 a[110:111], v24 offset:14088\n\t"
    "v_fmac_f32 v143, v43, v31\n\t"
    "v_mfma_f32_16x16x32_bf16 v[168:171], a[112:115], a[72:75], v[168:171]\n\t"
    "KFP5:\n\t"
    "s_cbranch_execz KFP9\n\t"
    "s_waitcnt vmcnt(10)\n\t"
    "s_barrier\n\t"
    "v_fmac_f32 v148, v40, v33\n\t"
    "v_mfma_f32_16x16x32_bf16 v[168:171], a[116:119], a[76:79], v[168:171]\n\t"
    "s_mov_b32 s65, s96\n\t"
    "buffer_store_dword v60, v2, s[40:43], s65 offen sc1\n\t"
    "v_mfma_f32_16x16x32_bf16 v[176:179], a[112:115], a[80:83], v[176:179]\n\t"
    "v_fmac_f32 v149, v41, v33\n\t"
    "v_fmac_f32 v150, v42, v33\n\t"
    "v_mfma_f32_16x16x32_bf16 v[176:179], a[116:119], a[84:87], v[176:179]\n\t"
    "v_fmac_f32 v151, v43, v33\n\t"
    "v_fmac_f32 v156, v40, v35\n\t"
    "v_mfma_f32_16x16x32_bf16 v[184:187], a[112:115], a[88:91], v[184:187]\n\t"
    "ds_read_b64_tr_b16 a[112:113], v25 offset:256\n\t"
    "v_fmac_f32 v157, v41, v35\n\t"
    "v_mfma_f32_16x16x32_bf16 v[184:187], a[116:119], a[92:95], v[184:187]\n\t"
    "s_waitcnt lgkmcnt(5)\n\t"
    "ds_read_b64_tr_b16 a[114:115], v25 offset:1792\n\t"
    "v_fmac_f32 v158, v42, v35\n\t"
    "v_mfma_f32_16x16x32_bf16 v[164:167], a[120:123], a[64:67], v[164:167]\n\t"
    "ds_read_b64_tr_b16 a[116:117], v25 offset:12544\n\t"
    "v_fmac_f32 v159, v43, v35\n\t"
    "v_mfma_f32_16x16x32_bf16 v[164:167], a[124:127], a[68:71], v[164:167]\n\t"
    "ds_read_b64_tr_b16 a[118:119], v25 offset:14080\n\t"
    "v_accvgpr_write_b32 a189, v59\n\t"
    "v_mfma_f32_16x16x32_bf16 v[172:175], a[120:123], a[72:75], v[172:175]\n\t"
    "buffer_load_dwordx4 v8, s[52:55], s60 offen offset:0 lds\n\t"
    "v_mfma_f32_16x16x32_bf16 v[172:175], a[124:127], a[76:79], v[172:175]\n\t"
    "v_accvgpr_write_b32 a190, v59\n\t"
    "v_accvgpr_write_b32 a191, v59\n\t"
    "v_mfma_f32_16x16x32_bf16 v[180:183], a[120:123], a[80:83], v[180:183]\n\t"
    "v_mfma_f32_16x16x32_bf16 v[180:183], a[124:127], a[84:87], v[180:183]\n\t"
    "v_mfma_f32_16x16x32_bf16 v[188:191], a[120:123], a[88:91], v[188:191]\n\t"
    "ds_read_b64_tr_b16 a[120:121], v25 offset:264\n\t"
    "v_mfma_f32_16x16x32_bf16 v[188:191], a[124:127], a[92:95], v[188:191]\n\t"
    "buffer_load_dwordx4 v9, s[52:55], s60 offen offset:1024 lds\n\t"
    "v_mfma_f32_16x16x32_bf16 v[192:195], a[96:99], a[64:67], v[192:195]\n\t"
    "ds_read_b64_tr_b16 a[122:123], v25 offset:1800\n\t"
    "v_accvgpr_read_b32 v36, a236\n\t"
    "v_mfma_f32_16x16x32_bf16 v[192:195], a[100:103], a[68:71], v[192:195]\n\t"
    "ds_read_b64_tr_b16 a[124:125], v25 offset:12552\n\t"
    "v_accvgpr_read_b32 v37, a237\n\t"
    "v_mfma_f32_16x16x32_bf16 v[200:203], a[96:99], a[72:75], v[200:203]\n\t"
    "ds_read_b64_tr_b16 a[126:127], v25 offset:14088\n\t"
    "v_accvgpr_read_b32 v38, a238\n\t"
    "v_mfma_f32_16x16x32_bf16 v[200:203], a[100:103], a[76:79], v[200:203]\n\t"
    "v_accvgpr_read_b32 v39, a239\n\t"
    "v_mfma_f32_16x16x32_bf16 v[208:211], a[96:99], a[80:83], v[208:211]\n\t"
    "buffer_load_dwordx4 v10, s[52:55], s60 offen offset:2048 lds\n\t"
    "v_mfma_f32_16x16x32_bf16 v[208:211], a[100:103], a[84:87], v[208:211]\n\t"
    "s_add_u32 m0, s83, 3072\n\t"
    "v_fmac_f32 v192, v36, v29\n\t"
    "v_fmac_f32 v193, v37, v29\n\t"
    "v_mfma_f32_16x16x32_bf16 v[216:219], a[96:99], a[88:91], v[216:219]\n\t"
    "s_add_u32 s60, s60, 3072\n\t"
    "ds_read_b64_tr_b16 a[96:97], v24 offset:24576\n\t"
    "v_fmac_f32 v194, v38, v29\n\t"
    "v_mfma_f32_16x16x32_bf16 v[216:219], a[100:103], a[92:95], v[216:219]\n\t"
    "s_waitcnt lgkmcnt(5)\n\t"
    "ds_read_b64_tr_b16 a[98:99], v24 offset:26112\n\t"
    "v_fmac_f32 v195, v39, v29\n\t"
    "v_mfma_f32_16x16x32_bf16 v[196:199], a[104:107], a[64:67], v[196:199]\n\t"
    "ds_read_b64_tr_b16 a[100:101], v24 offset:36864\n\t"
    "v_fmac_f32 v200, v36, v31\n\t"
    "v_mfma_f32_16x16x32_bf16 v[196:199], a[108:111], a[68:71], v[196:199]\n\t"
    "buffer_load_dwordx4 v8, s[52:55], s60 offen offset:0 lds\n\t"
    "v_mfma_f32_16x16x32_bf16 v[204:207], a[104:107], a[72:75], v[204:207]\n\t"
    "ds_read_b64_tr_b16 a[102:103], v24 offset:38400\n\t"
    "v_fmac_f32 v201, v37, v31\n\t"
    "v_mfma_f32_16x16x32_bf16 v[204:207], a[108:111], a[76:79], v[204:207]\n\t"
    "v_fmac_f32 v202, v38, v31\n\t"
    "v_fmac_f32 v203, v39, v31\n\t"
    "v_mfma_f32_16x16x32_bf16 v[212:215], a[104:107], a[80:83], v[212:215]\n\t"
    "v_fmac_f32 v208, v36, v33\n\t"
    "v_fmac_f32 v209, v37, v33\n\t"
    "v_mfma_f32_16x16x32_bf16 v[212:215], a[108:111], a[84:87], v[212:215]\n\t"
    "v_fmac_f32 v210, v38, v33\n\t"
    "v_fmac_f32 v211, v39, v33\n\t"
    "v_mfma_f32_16x16x32_bf16 v[220:223], a[104:107], a[88:91], v[220:223]\n\t"
    "buffer_load_dwordx4 v9, s[52:55], s60 offen offset:1024 lds\n\t"
    "v_mfma_f32_16x16x32_bf16 v[220:223], a[108:111], a[92:95], v[220:223]\n\t"
    "ds_read_b64_tr_b16 a[104:105], v24 offset:24584\n\t"
    "v_fmac_f32 v216, v36, v35\n\t"
    "v_mfma_f32_16x16x32_bf16 v[224:227], a[112:115], a[64:67], v[224:227]\n\t"
    "ds_read_b64_tr_b16 a[106:107], v24 offset:26120\n\t"
    "v_fmac_f32 v217, v37, v35\n\t"
    "v_mfma_f32_16x16x32_bf16 v[224:227], a[116:119], a[68:71], v[224:227]\n\t"
    "ds_read_b64_tr_b16 a[108:109], v24 offset:36872\n\t"
    "v_fmac_f32 v218, v38, v35\n\t"
    "v_mfma_f32_16x16x32_bf16 v[232:235], a[112:115], a[72:75], v[232:235]\n\t"
    "ds_read_b64_tr_b16 a[110:111], v24 offset:38408\n\t"
    "v_fmac_f32 v219, v39, v35\n\t"
    "v_mfma_f32_16x16x32_bf16 v[232:235], a[116:119], a[76:79], v[232:235]\n\t"
    "buffer_load_dwordx4 v10, s[52:55], s60 offen offset:2048 lds\n\t"
    "v_mfma_f32_16x16x32_bf16 v[240:243], a[112:115], a[80:83], v[240:243]\n\t"
    "v_add_u32 v36, 98304, v15\n\t"
    "v_add_u32 v37, 98304, v16\n\t"
    "v_mfma_f32_16x16x32_bf16 v[240:243], a[116:119], a[84:87], v[240:243]\n\t"
    "v_add_u32 v38, 98304, v17\n\t"
    "v_add_u32 v39, 98304, v18\n\t"
    "v_mfma_f32_16x16x32_bf16 v[248:251], a[112:115], a[88:91], v[248:251]\n\t"
    "ds_read_b64_tr_b16 a[112:113], v25 offset:24576\n\t"
    "v_mfma_f32_16x16x32_bf16 v[248:251], a[116:119], a[92:95], v[248:251]\n\t"
    "s_waitcnt lgkmcnt(5)\n\t"
    "ds_read_b64_tr_b16 a[114:115], v25 offset:26112\n\t"
    "v_mfma_f32_16x16x32_bf16 v[228:231], a[120:123], a[64:67], v[228:231]\n\t"
    "ds_read_b64_tr_b16 a[116:117], v25 offset:36864\n\t"
    "v_accvgpr_read_b32 v40, a248\n\t"
    "v_mfma_f32_16x16x32_bf16 v[228:231], a[124:127], a[68:71], v[228:231]\n\t"
    "ds_read_b64_tr_b16 a[118:119], v25 offset:38400\n\t"
    "v_accvgpr_read_b32 v41, a249\n\t"
    "v_mfma_f32_16x16x32_bf16 v[236:239], a[120:123], a[72:75], v[236:239]\n\t"
    "v_accvgpr_read_b32 v42, a250\n\t"
    "v_accvgpr_read_b32 v43, a251\n\t"
    "v_mfma_f32_16x16x32_bf16 v[236:239], a[124:127], a[76:79], v[236:239]\n\t"
    "v_mfma_f32_16x16x32_bf16 v[244:247], a[120:123], a[80:83], v[244:247]\n\t"
    "s_nop 0\n\t"
    "v_fmac_f32 v228, v40, v29\n\t"
    "v_fmac_f32 v229, v41, v29\n\t"
    "v_mfma_f32_16x16x32_bf16 v[244:247], a[124:127], a[84:87], v[244:247]\n\t"
    "v_fmac_f32 v230, v42, v29\n\t"
    "v_fmac_f32 v231, v43, v29\n\t"
    "v_mfma_f32_16x16x32_bf16 v[252:255], a[120:123], a[88:91], v[252:255]\n\t"
    "ds_read_b64_tr_b16 a[120:121], v25 offset:24584\n\t"
    "v_fmac_f32 v236, v40, v31\n\t"
    "v_mfma_f32_16x16x32_bf16 v[252:255], a[124:127], a[92:95], v[252:255]\n\t"
    "ds_read_b64_tr_b16 a[122:123], v25 offset:26120\n\t"
    "v_fmac_f32 v237, v41, v31\n\t"
    "v_mfma_f32_16x16x32_bf16 v[64:67], a[96:99], a[0:3], v[64:67]\n\t"
    "ds_read_b64_tr_b16 a[124:125], v25 offset:36872\n\t"
    "v_fmac_f32 v238, v42, v31\n\t"
    "v_mfma_f32_16x16x32_bf16 v[64:67], a[100:103], a[4:7], v[64:67]\n\t"
    "ds_read_b64_tr_b16 a[126:127], v25 offset:38408\n\t"
    "v_fmac_f32 v239, v43, v31\n\t"
    "v_mfma_f32_16x16x32_bf16 v[72:75], a[96:99], a[8:11], v[72:75]\n\t"
    "v_fmac_f32 v244, v40, v33\n\t"
    "v_fmac_f32 v245, v41, v33\n\t"
    "v_mfma_f32_16x16x32_bf16 v[72:75], a[100:103], a[12:15], v[72:75]\n\t"
    "v_fmac_f32 v246, v42, v33\n\t"
    "v_fmac_f32 v247, v43, v33\n\t"
    "v_mfma_f32_16x16x32_bf16 v[80:83], a[96:99], a[16:19], v[80:83]\n\t"
    "v_fmac_f32 v252, v40, v35\n\t"
    "v_fmac_f32 v253, v41, v35\n\t"
    "v_mfma_f32_16x16x32_bf16 v[80:83], a[100:103], a[20:23], v[80:83]\n\t"
    "v_fmac_f32 v254, v42, v35\n\t"
    "v_fmac_f32 v255, v43, v35\n\t"
    "v_mfma_f32_16x16x32_bf16 v[88:91], a[96:99], a[24:27], v[88:91]\n\t"
    "s_add_u32 m0, s83, 24576\n\t"
    "ds_read_b64_tr_b16 a[96:97], v24 offset:24704\n\t"
    "v_mfma_f32_16x16x32_bf16 v[88:91], a[100:103], a[28:31], v[88:91]\n\t"
    "s_waitcnt lgkmcnt(5)\n\t"
    "ds_read_b64_tr_b16 a[98:99], v24 offset:26240\n\t"
    "v_add_u32 v31, 98304, v14\n\t"
    "v_mfma_f32_16x16x32_bf16 v[68:71], a[104:107], a[0:3], v[68:71]\n\t"
    "ds_read_b64_tr_b16 a[100:101], v24 offset:36992\n\t"
    "v_add_u32 v29, 49152, v14\n\t"
    "v_mfma_f32_16x16x32_bf16 v[68:71], a[108:111], a[4:7], v[68:71]\n\t"
    "s_add_u32 s60, s87, 8192\n\t"
    "ds_read_b64_tr_b16 a[102:103], v24 offset:38528\n\t"
    "v_mov_b32 v40, 0\n\t"
    "v_mfma_f32_16x16x32_bf16 v[76:79], a[104:107], a[8:11], v[76:79]\n\t"
    "s_bfe_u32 s60, s60, 0x2000c\n\t"
    "v_mov_b32 v41, 0\n\t"
    "v_mov_b32 v42, 0\n\t"
    "v_mfma_f32_16x16x32_bf16 v[76:79], a[108:111], a[12:15], v[76:79]\n\t"
    "s_mulk_i32 s60, 24576\n\t"
    "buffer_load_dwordx4 v8, s[48:51], s60 offen offset:0 lds\n\t"
    "v_mfma_f32_16x16x32_bf16 v[84:87], a[104:107], a[16:19], v[84:87]\n\t"
    "v_mov_b32 v43, 0\n\t"
    "v_add_u32 v33, 49152, v16\n\t"
    "v_mfma_f32_16x16x32_bf16 v[84:87], a[108:111], a[20:23], v[84:87]\n\t"
    "v_add_u32 v35, 49152, v18\n\t"
    "v_cvt_pk_bf16_f32 v64, v64, v65\n\t"
    "v_mfma_f32_16x16x32_bf16 v[92:95], a[104:107], a[24:27], v[92:95]\n\t"
    "ds_read_b64_tr_b16 a[104:105], v24 offset:24712\n\t"
    "v_cvt_pk_bf16_f32 v65, v66, v67\n\t"
    "v_mfma_f32_16x16x32_bf16 v[92:95], a[108:111], a[28:31], v[92:95]\n\t"
    "ds_read_b64_tr_b16 a[106:107], v24 offset:26248\n\t"
    "v_cvt_pk_bf16_f32 v66, v68, v69\n\t"
    "v_mfma_f32_16x16x32_bf16 v[96:99], a[112:115], a[0:3], v[96:99]\n\t"
    "buffer_load_dwordx4 v9, s[48:51], s60 offen offset:1024 lds\n\t"
    "v_mfma_f32_16x16x32_bf16 v[96:99], a[116:119], a[4:7], v[96:99]\n\t"
    "ds_read_b64_tr_b16 a[108:109], v24 offset:37000\n\t"
    "v_cvt_pk_bf16_f32 v67, v70, v71\n\t"
    "v_mfma_f32_16x16x32_bf16 v[104:107], a[112:115], a[8:11], v[104:107]\n\t"
    "ds_read_b64_tr_b16 a[110:111], v24 offset:38536\n\t"
    "v_cvt_pk_bf16_f32 v72, v72, v73\n\t"
    "v_mfma_f32_16x16x32_bf16 v[104:107], a[116:119], a[12:15], v[104:107]\n\t"
    "v_cvt_pk_bf16_f32 v73, v74, v75\n\t"
    "v_cvt_pk_bf16_f32 v74, v76, v77\n\t"
    "v_mfma_f32_16x16x32_bf16 v[112:115], a[112:115], a[16:19], v[112:115]\n\t"
    "v_cvt_pk_bf16_f32 v96, v96, v97\n\t"
    "v_cvt_pk_bf16_f32 v97, v98, v99\n\t"
    "v_mfma_f32_16x16x32_bf16 v[112:115], a[116:119], a[20:23], v[112:115]\n\t"
    "buffer_load_dwordx4 v10, s[48:51], s60 offen offset:2048 lds\n\t"
    "v_mfma_f32_16x16x32_bf16 v[120:123], a[112:115], a[24:27], v[120:123]\n\t"
    "s_add_u32 m0, s83, 27648\n\t"
    "ds_read_b64_tr_b16 a[112:113], v25 offset:24704\n\t"
    "v_cvt_pk_bf16_f32 v104, v104, v105\n\t"
    "v_mfma_f32_16x16x32_bf16 v[120:123], a[116:119], a[28:31], v[120:123]\n\t"
    "s_waitcnt lgkmcnt(5)\n\t"
    "ds_read_b64_tr_b16 a[114:115], v25 offset:26240\n\t"
    "v_cvt_pk_bf16_f32 v75, v78, v79\n\t"
    "v_mfma_f32_16x16x32_bf16 v[100:103], a[120:123], a[0:3], v[100:103]\n\t"
    "ds_read_b64_tr_b16 a[116:117], v25 offset:36992\n\t"
    "v_cvt_pk_bf16_f32 v105, v106, v107\n\t"
    "v_mfma_f32_16x16x32_bf16 v[100:103], a[124:127], a[4:7], v[100:103]\n\t"
    "s_add_u32 s60, s60, 3072\n\t"
    "ds_read_b64_tr_b16 a[118:119], v25 offset:38528\n\t"
    "v_cvt_pk_bf16_f32 v80, v80, v81\n\t"
    "v_mfma_f32_16x16x32_bf16 v[108:111], a[120:123], a[8:11], v[108:111]\n\t"
    "buffer_load_dwordx4 v8, s[48:51], s60 offen offset:0 lds\n\t"
    "v_mfma_f32_16x16x32_bf16 v[108:111], a[124:127], a[12:15], v[108:111]\n\t"
    "v_cvt_pk_bf16_f32 v81, v82, v83\n\t"
    "v_cvt_pk_bf16_f32 v112, v112, v113\n\t"
    "v_mfma_f32_16x16x32_bf16 v[116:119], a[120:123], a[16:19], v[116:119]\n\t"
    "v_cvt_pk_bf16_f32 v98, v100, v101\n\t"
    "v_cvt_pk_bf16_f32 v99, v102, v103\n\t"
    "v_mfma_f32_16x16x32_bf16 v[116:119], a[124:127], a[20:23], v[116:119]\n\t"
    "v_cvt_pk_bf16_f32 v82, v84, v85\n\t"
    "v_cvt_pk_bf16_f32 v83, v86, v87\n\t"
    "v_mfma_f32_16x16x32_bf16 v[124:127], a[120:123], a[24:27], v[124:127]\n\t"
    "ds_read_b64_tr_b16 a[120:121], v25 offset:24712\n\t"
    "v_cvt_pk_bf16_f32 v106, v108, v109\n\t"
    "v_mfma_f32_16x16x32_bf16 v[124:127], a[124:127], a[28:31], v[124:127]\n\t"
    "buffer_load_dwordx4 v9, s[48:51], s60 offen offset:1024 lds\n\t"
    "v_mfma_f32_16x16x32_bf16 v[128:131], a[96:99], a[0:3], v[128:131]\n\t"
    "ds_read_b64_tr_b16 a[122:123], v25 offset:26248\n\t"
    "v_cvt_pk_bf16_f32 v107, v110, v111\n\t"
    "v_mfma_f32_16x16x32_bf16 v[128:131], a[100:103], a[4:7], v[128:131]\n\t"
    "ds_read_b64_tr_b16 a[124:125], v25 offset:37000\n\t"
    "v_cvt_pk_bf16_f32 v113, v114, v115\n\t"
    "v_mfma_f32_16x16x32_bf16 v[136:139], a[96:99], a[8:11], v[136:139]\n\t"
    "ds_read_b64_tr_b16 a[126:127], v25 offset:38536\n\t"
    "v_cvt_pk_bf16_f32 v114, v116, v117\n\t"
    "v_mfma_f32_16x16x32_bf16 v[136:139], a[100:103], a[12:15], v[136:139]\n\t"
    "v_cvt_pk_bf16_f32 v115, v118, v119\n\t"
    "v_cvt_pk_bf16_f32 v88, v88, v89\n\t"
    "v_mfma_f32_16x16x32_bf16 v[144:147], a[96:99], a[16:19], v[144:147]\n\t"
    "buffer_load_dwordx4 v10, s[48:51], s60 offen offset:2048 lds\n\t"
    "v_mfma_f32_16x16x32_bf16 v[144:147], a[100:103], a[20:23], v[144:147]\n\t"
    "s_add_u32 m0, s83, 49152\n\t"
    "v_cvt_pk_bf16_f32 v128, v128, v129\n\t"
    "v_cvt_pk_bf16_f32 v129, v130, v131\n\t"
    "v_mfma_f32_16x16x32_bf16 v[152:155], a[96:99], a[24:27], v[152:155]\n\t"
    "s_add_u32 s60, s87, 4096\n\t"
    "ds_read_b64_tr_b16 a[96:97], v24 offset:24832\n\t"
    "v_cvt_pk_bf16_f32 v136, v136, v137\n\t"
    "v_mfma_f32_16x16x32_bf16 v[152:155], a[100:103], a[28:31], v[152:155]\n\t"
    "s_waitcnt lgkmcnt(5)\n\t"
    "ds_read_b64_tr_b16 a[98:99], v24 offset:26368\n\t"
    "v_cvt_pk_bf16_f32 v137, v138, v139\n\t"
    "v_mfma_f32_16x16x32_bf16 v[132:135], a[104:107], a[0:3], v[132:135]\n\t"
    "ds_read_b64_tr_b16 a[100:101], v24 offset:37120\n\t"
    "v_cvt_pk_bf16_f32 v144, v144, v145\n\t"
    "v_mfma_f32_16x16x32_bf16 v[132:135], a[108:111], a[4:7], v[132:135]\n\t"
    "s_bfe_u32 s60, s60, 0x2000c\n\t"
    "ds_read_b64_tr_b16 a[102:103], v24 offset:38656\n\t"
    "v_cvt_pk_bf16_f32 v145, v146, v147\n\t"
    "v_mfma_f32_16x16x32_bf16 v[140:143], a[104:107], a[8:11], v[140:143]\n\t"
    "s_mulk_i32 s60, 24576\n\t"
    "v_cvt_pk_bf16_f32 v89, v90, v91\n\t"
    "v_cvt_pk_bf16_f32 v120, v120, v121\n\t"
    "v_mfma_f32_16x16x32_bf16 v[140:143], a[108:111], a[12:15], v[140:143]\n\t"
    "v_cvt_pk_bf16_f32 v90, v92, v93\n\t"
    "v_cvt_pk_bf16_f32 v91, v94, v95\n\t"
    "v_mfma_f32_16x16x32_bf16 v[148:151], a[104:107], a[16:19], v[148:151]\n\t"
    "v_cvt_pk_bf16_f32 v130, v132, v133\n\t"
    "v_cvt_pk_bf16_f32 v131, v134, v135\n\t"
    "v_mfma_f32_16x16x32_bf16 v[148:151], a[108:111], a[20:23], v[148:151]\n\t"
    "v_cvt_pk_bf16_f32 v121, v122, v123\n\t"
    "v_cvt_pk_bf16_f32 v152, v152, v153\n\t"
    "v_mfma_f32_16x16x32_bf16 v[156:159], a[104:107], a[24:27], v[156:159]\n\t"
    "ds_read_b64_tr_b16 a[104:105], v24 offset:24840\n\t"
    "v_cvt_pk_bf16_f32 v138, v140, v141\n\t"
    "v_mfma_f32_16x16x32_bf16 v[156:159], a[108:111], a[28:31], v[156:159]\n\t"
    "ds_read_b64_tr_b16 a[106:107], v24 offset:26376\n\t"
    "v_cvt_pk_bf16_f32 v139, v142, v143\n\t"
    "v_mfma_f32_16x16x32_bf16 v[160:163], a[112:115], a[0:3], v[160:163]\n\t"
    "ds_read_b64_tr_b16 a[108:109], v24 offset:37128\n\t"
    "v_cvt_pk_bf16_f32 v146, v148, v149\n\t"
    "v_mfma_f32_16x16x32_bf16 v[160:163], a[116:119], a[4:7], v[160:163]\n\t"
    "ds_read_b64_tr_b16 a[110:111], v24 offset:38664\n\t"
    "v_cvt_pk_bf16_f32 v147, v150, v151\n\t"
    "v_mfma_f32_16x16x32_bf16 v[168:171], a[112:115], a[8:11], v[168:171]\n\t"
    "s_barrier\n\t"
    "ds_read_b128 a[192:195], v30 offset:0\n\t"
    "v_mfma_f32_16x16x32_bf16 v[168:171], a[116:119], a[12:15], v[168:171]\n\t"
    "buffer_load_dwordx4 v8, s[52:55], s60 offen offset:0 lds\n\t"
    "v_mfma_f32_16x16x32_bf16 v[176:179], a[112:115], a[16:19], v[176:179]\n\t"
    "ds_read_b128 a[196:199], v31 offset:0\n\t"
    "v_cvt_pk_bf16_f32 v160, v160, v161\n\t"
    "v_mfma_f32_16x16x32_bf16 v[176:179], a[116:119], a[20:23], v[176:179]\n\t"
    "ds_read_b128 a[200:203], v30 offset:128\n\t"
    "v_cvt_pk_bf16_f32 v161, v162, v163\n\t"
    "v_mfma_f32_16x16x32_bf16 v[184:187], a[112:115], a[24:27], v[184:187]\n\t"
    "ds_read_b64_tr_b16 a[112:113], v25 offset:24832\n\t"
    "v_cvt_pk_bf16_f32 v168, v168, v169\n\t"
    "v_mfma_f32_16x16x32_bf16 v[184:187], a[116:119], a[28:31], v[184:187]\n\t"
    "s_waitcnt lgkmcnt(8)\n\t"
    "ds_read_b64_tr_b16 a[114:115], v25 offset:26368\n\t"
    "v_cvt_pk_bf16_f32 v169, v170, v171\n\t"
    "v_mfma_f32_16x16x32_bf16 v[164:167], a[120:123], a[0:3], v[164:167]\n\t"
    "ds_read_b64_tr_b16 a[116:117], v25 offset:37120\n\t"
    "v_cvt_pk_bf16_f32 v176, v176, v177\n\t"
    "v_mfma_f32_16x16x32_bf16 v[164:167], a[124:127], a[4:7], v[164:167]\n\t"
    "ds_read_b64_tr_b16 a[118:119], v25 offset:38656\n\t"
    "v_cvt_pk_bf16_f32 v177, v178, v179\n\t"
    "v_mfma_f32_16x16x32_bf16 v[172:175], a[120:123], a[8:11], v[172:175]\n\t"
    "buffer_load_dwordx4 v9, s[52:55], s60 offen offset:1024 lds\n\t"
    "v_mfma_f32_16x16x32_bf16 v[172:175], a[124:127], a[12:15], v[172:175]\n\t"
    "ds_read_b128 a[204:207], v31 offset:128\n\t"
    "v_cvt_pk_bf16_f32 v122, v124, v125\n\t"
    "v_mfma_f32_16x16x32_bf16 v[180:183], a[120:123], a[16:19], v[180:183]\n\t"
    "ds_read_b128 a[208:211], v30 offset:256\n\t"
    "v_cvt_pk_bf16_f32 v162, v164, v165\n\t"
    "v_mfma_f32_16x16x32_bf16 v[180:183], a[124:127], a[20:23], v[180:183]\n\t"
    "ds_read_b128 a[212:215], v31 offset:256\n\t"
    "v_cvt_pk_bf16_f32 v163, v166, v167\n\t"
    "v_mfma_f32_16x16x32_bf16 v[188:191], a[120:123], a[24:27], v[188:191]\n\t"
    "ds_read_b64_tr_b16 a[120:121], v25 offset:24840\n\t"
    "v_cvt_pk_bf16_f32 v170, v172, v173\n\t"
    "v_mfma_f32_16x16x32_bf16 v[188:191], a[124:127], a[28:31], v[188:191]\n\t"
    "buffer_load_dwordx4 v10, s[52:55], s60 offen offset:2048 lds\n\t"
    "v_mfma_f32_16x16x32_bf16 v[192:195], a[96:99], a[0:3], v[192:195]\n\t"
    "ds_read_b64_tr_b16 a[122:123], v25 offset:26376\n\t"
    "v_cvt_pk_bf16_f32 v171, v174, v175\n\t"
    "v_mfma_f32_16x16x32_bf16 v[192:195], a[100:103], a[4:7], v[192:195]\n\t"
    "s_add_u32 m0, s83, 52224\n\t"
    "ds_read_b64_tr_b16 a[124:125], v25 offset:37128\n\t"
    "v_cvt_pk_bf16_f32 v178, v180, v181\n\t"
    "v_mfma_f32_16x16x32_bf16 v[200:203], a[96:99], a[8:11], v[200:203]\n\t"
    "s_add_u32 s60, s60, 3072\n\t"
    "ds_read_b64_tr_b16 a[126:127], v25 offset:38664\n\t"
    "v_cvt_pk_bf16_f32 v179, v182, v183\n\t"
    "v_mfma_f32_16x16x32_bf16 v[200:203], a[100:103], a[12:15], v[200:203]\n\t"
    "ds_read_b128 a[216:219], v30 offset:6144\n\t"
    "v_cvt_pk_bf16_f32 v123, v126, v127\n\t"
    "v_mfma_f32_16x16x32_bf16 v[208:211], a[96:99], a[16:19], v[208:211]\n\t"
    "buffer_load_dwordx4 v8, s[52:55], s60 offen offset:0 lds\n\t"
    "v_mfma_f32_16x16x32_bf16 v[208:211], a[100:103], a[20:23], v[208:211]\n\t"
    "ds_read_b128 a[220:223], v31 offset:6144\n\t"
    "v_cvt_pk_bf16_f32 v192, v192, v193\n\t"
    "v_mfma_f32_16x16x32_bf16 v[216:219], a[96:99], a[24:27], v[216:219]\n\t"
    "ds_read_b128 a[224:227], v30 offset:6272\n\t"
    "v_cvt_pk_bf16_f32 v193, v194, v195\n\t"
    "v_mfma_f32_16x16x32_bf16 v[216:219], a[100:103], a[28:31], v[216:219]\n\t"
    "s_waitcnt lgkmcnt(8)\n\t"
    "ds_read_b128 a[228:231], v31 offset:6272\n\t"
    "v_cvt_pk_bf16_f32 v200, v200, v201\n\t"
    "v_mfma_f32_16x16x32_bf16 v[196:199], a[104:107], a[0:3], v[196:199]\n\t"
    "ds_read_b128 a[232:235], v30 offset:6400\n\t"
    "v_cvt_pk_bf16_f32 v201, v202, v203\n\t"
    "v_mfma_f32_16x16x32_bf16 v[196:199], a[108:111], a[4:7], v[196:199]\n\t"
    "buffer_load_dwordx4 v9, s[52:55], s60 offen offset:1024 lds\n\t"
    "v_mfma_f32_16x16x32_bf16 v[204:207], a[104:107], a[8:11], v[204:207]\n\t"
    "ds_read_b128 a[236:239], v31 offset:6400\n\t"
    "v_cvt_pk_bf16_f32 v208, v208, v209\n\t"
    "v_mfma_f32_16x16x32_bf16 v[204:207], a[108:111], a[12:15], v[204:207]\n\t"
    "v_cvt_pk_bf16_f32 v209, v210, v211\n\t"
    "v_cvt_pk_bf16_f32 v153, v154, v155\n\t"
    "v_mfma_f32_16x16x32_bf16 v[212:215], a[104:107], a[16:19], v[212:215]\n\t"
    "v_cvt_pk_bf16_f32 v194, v196, v197\n\t"
    "v_cvt_pk_bf16_f32 v195, v198, v199\n\t"
    "v_mfma_f32_16x16x32_bf16 v[212:215], a[108:111], a[20:23], v[212:215]\n\t"
    "v_cvt_pk_bf16_f32 v184, v184, v185\n\t"
    "v_cvt_pk_bf16_f32 v154, v156, v157\n\t"
    "v_mfma_f32_16x16x32_bf16 v[220:223], a[104:107], a[24:27], v[220:223]\n\t"
    "buffer_load_dwordx4 v10, s[52:55], s60 offen offset:2048 lds\n\t"
    "v_mfma_f32_16x16x32_bf16 v[220:223], a[108:111], a[28:31], v[220:223]\n\t"
    "s_add_u32 s60, s87, 16384\n\t"
    "v_cvt_pk_bf16_f32 v202, v204, v205\n\t"
    "v_mfma_f32_16x16x32_bf16 v[224:227], a[112:115], a[0:3], v[224:227]\n\t"
    "s_bfe_u32 s60, s60, 0x2000c\n\t"
    "v_cvt_pk_bf16_f32 v203, v206, v207\n\t"
    "v_mfma_f32_16x16x32_bf16 v[224:227], a[116:119], a[4:7], v[224:227]\n\t"
    "s_add_u32 s60, s60, 4\n\t"
    "v_cvt_pk_bf16_f32 v210, v212, v213\n\t"
    "v_cvt_pk_bf16_f32 v211, v214, v215\n\t"
    "v_mfma_f32_16x16x32_bf16 v[232:235], a[112:115], a[8:11], v[232:235]\n\t"
    "s_mulk_i32 s60, 24576\n\t"
    "v_cvt_pk_bf16_f32 v155, v158, v159\n\t"
    "v_cvt_pk_bf16_f32 v185, v186, v187\n\t"
    "v_mfma_f32_16x16x32_bf16 v[232:235], a[116:119], a[12:15], v[232:235]\n\t"
    "s_add_u32 m0, s83, 73728\n\t"
    "v_cvt_pk_bf16_f32 v216, v216, v217\n\t"
    "v_cvt_pk_bf16_f32 v186, v188, v189\n\t"
    "v_mfma_f32_16x16x32_bf16 v[240:243], a[112:115], a[16:19], v[240:243]\n\t"
    "buffer_load_dword v22, v61, s[48:51], s60 offen\n\t"
    "v_mfma_f32_16x16x32_bf16 v[240:243], a[116:119], a[20:23], v[240:243]\n\t"
    "s_add_u32 s60, s87, 12288\n\t"
    "v_cvt_pk_bf16_f32 v224, v224, v225\n\t"
    "v_cvt_pk_bf16_f32 v225, v226, v227\n\t"
    "v_mfma_f32_16x16x32_bf16 v[248:251], a[112:115], a[24:27], v[248:251]\n\t"
    "s_bfe_u32 s60, s60, 0x2000c\n\t"
    "v_cvt_pk_bf16_f32 v232, v232, v233\n\t"
    "v_cvt_pk_bf16_f32 v233, v234, v235\n\t"
    "v_mfma_f32_16x16x32_bf16 v[248:251], a[116:119], a[28:31], v[248:251]\n\t"
    "s_waitcnt lgkmcnt(0)\n\t"
    "v_cvt_pk_bf16_f32 v187, v190, v191\n\t"
    "v_cvt_pk_bf16_f32 v217, v218, v219\n\t"
    "v_mfma_f32_16x16x32_bf16 v[228:231], a[120:123], a[0:3], v[228:231]\n\t"
    "v_cvt_pk_bf16_f32 v240, v240, v241\n\t"
    "v_cvt_pk_bf16_f32 v241, v242, v243\n\t"
    "v_mfma_f32_16x16x32_bf16 v[228:231], a[124:127], a[4:7], v[228:231]\n\t"
    "s_mulk_i32 s60, 24576\n\t"
    "v_cvt_pk_bf16_f32 v218, v220, v221\n\t"
    "v_cvt_pk_bf16_f32 v219, v222, v223\n\t"
    "v_mfma_f32_16x16x32_bf16 v[236:239], a[120:123], a[8:11], v[236:239]\n\t"
    "v_cvt_pk_bf16_f32 v248, v248, v249\n\t"
    "v_cvt_pk_bf16_f32 v249, v250, v251\n\t"
    "v_mfma_f32_16x16x32_bf16 v[236:239], a[124:127], a[12:15], v[236:239]\n\t"
    "v_and_b32 v24, 48, v4\n\t"
    "v_xor_b32 v63, v63, v24\n\t"
    "v_mfma_f32_16x16x32_bf16 v[244:247], a[120:123], a[16:19], v[244:247]\n\t"
    "v_cvt_pk_bf16_f32 v226, v228, v229\n\t"
    "v_cvt_pk_bf16_f32 v227, v230, v231\n\t"
    "v_mfma_f32_16x16x32_bf16 v[244:247], a[124:127], a[20:23], v[244:247]\n\t"
    "v_xor_b32 v24, 8, v63\n\t"
    "v_xor_b32 v25, 64, v63\n\t"
    "v_mfma_f32_16x16x32_bf16 v[252:255], a[120:123], a[24:27], v[252:255]\n\t"
    "v_cvt_pk_bf16_f32 v234, v236, v237\n\t"
    "v_cvt_pk_bf16_f32 v235, v238, v239\n\t"
    "v_mfma_f32_16x16x32_bf16 v[252:255], a[124:127], a[28:31], v[252:255]\n\t"
    "v_xor_b32 v26, 72, v63\n\t"
    "v_mfma_f32_16x16x32_bf16 v[68:71], a[192:195], v[64:67], 0\n\t"
    "v_cvt_pk_bf16_f32 v242, v244, v245\n\t"
    "v_cvt_pk_bf16_f32 v243, v246, v247\n\t"
    "v_mfma_f32_16x16x32_bf16 v[68:71], a[196:199], v[96:99], v[68:71]\n\t"
    "v_mfma_f32_16x16x32_bf16 v[68:71], a[200:203], v[128:131], v[68:71]\n\t"
    "s_nop 1\n\t"
    "v_cvt_pk_bf16_f32 v250, v252, v253\n\t"
    "v_cvt_pk_bf16_f32 v251, v254, v255\n\t"
    "v_mfma_f32_16x16x32_bf16 v[68:71], a[204:207], v[160:163], v[68:71]\n\t"
    "v_mfma_f32_16x16x32_bf16 v[68:71], a[208:211], v[192:195], v[68:71]\n\t"
    "v_mfma_f32_16x16x32_bf16 v[68:71], a[212:215], v[224:227], v[68:71]\n\t"
    "v_mfma_f32_16x16x32_bf16 v[76:79], a[192:195], v[72:75], 0\n\t"
    "v_mfma_f32_16x16x32_bf16 v[76:79], a[196:199], v[104:107], v[76:79]\n\t"
    "v_mfma_f32_16x16x32_bf16 v[76:79], a[200:203], v[136:139], v[76:79]\n\t"
    "s_nop 4\n\t"
    "v_max3_f32 v44, v68, v69, v70\n\t"
    "v_max_f32 v44, v44, v71\n\t"
    "v_mfma_f32_16x16x32_bf16 v[76:79], a[204:207], v[168:171], v[76:79]\n\t"
    "v_mov_b32 v45, v44\n\t"
    "s_nop 1\n\t"
    "v_permlane16_swap_b32 v44, v45\n\t"
    "v_mfma_f32_16x16x32_bf16 v[76:79], a[208:211], v[200:203], v[76:79]\n\t"
    "v_max_f32 v44, v44, v45\n\t"
    "v_mov_b32 v45, v44\n\t"
    "v_mfma_f32_16x16x32_bf16 v[76:79], a[212:215], v[232:235], v[76:79]\n\t"
    "s_nop 0\n\t"
    "v_permlane32_swap_b32 v44, v45\n\t"
    "v_max_f32 v44, v44, v45\n\t"
    "v_mfma_f32_16x16x32_bf16 v[84:87], a[192:195], v[80:83], 0\n\t"
    "v_sub_f32 v100, 0, v44\n\t"
    "v_sub_f32 v101, 0, v44\n\t"
    "v_mfma_f32_16x16x32_bf16 v[84:87], a[196:199], v[112:115], v[84:87]\n\t"
    "v_sub_f32 v102, 0, v44\n\t"
    "v_sub_f32 v103, 0, v44\n\t"
    "v_mfma_f32_16x16x32_bf16 v[84:87], a[200:203], v[144:147], v[84:87]\n\t"
    "v_max3_f32 v46, v76, v77, v78\n\t"
    "v_max_f32 v46, v46, v79\n\t"
    "v_mfma_f32_16x16x32_bf16 v[84:87], a[204:207], v[176:179], v[84:87]\n\t"
    "v_mov_b32 v47, v46\n\t"
    "s_nop 1\n\t"
    "v_permlane16_swap_b32 v46, v47\n\t"
    "v_mfma_f32_16x16x32_bf16 v[84:87], a[208:211], v[208:211], v[84:87]\n\t"
    "v_max_f32 v46, v46, v47\n\t"
    "v_mov_b32 v47, v46\n\t"
    "v_mfma_f32_16x16x32_bf16 v[84:87], a[212:215], v[240:243], v[84:87]\n\t"
    "s_nop 0\n\t"
    "v_permlane32_swap_b32 v46, v47\n\t"
    "v_max_f32 v46, v46, v47\n\t"
    "v_mfma_f32_16x16x32_bf16 v[92:95], a[192:195], v[88:91], 0\n\t"
    "ds_read_b128 a[192:195], v30 offset:12288\n\t"
    "v_sub_f32 v108, 0, v46\n\t"
    "v_mfma_f32_16x16x32_bf16 v[92:95], a[196:199], v[120:123], v[92:95]\n\t"
    "ds_read_b128 a[196:199], v31 offset:12288\n\t"
    "v_sub_f32 v109, 0, v46\n\t"
    "v_mfma_f32_16x16x32_bf16 v[92:95], a[200:203], v[152:155], v[92:95]\n\t"
    "ds_read_b128 a[200:203], v30 offset:12416\n\t"
    "v_max3_f32 v48, v84, v85, v86\n\t"
    "v_mfma_f32_16x16x32_bf16 v[92:95], a[204:207], v[184:187], v[92:95]\n\t"
    "ds_read_b128 a[204:207], v31 offset:12416\n\t"
    "v_max_f32 v48, v48, v87\n\t"
    "v_mfma_f32_16x16x32_bf16 v[92:95], a[208:211], v[216:219], v[92:95]\n\t"
    "ds_read_b128 a[208:211], v30 offset:12544\n\t"
    "v_sub_f32 v110, 0, v46\n\t"
    "v_mfma_f32_16x16x32_bf16 v[92:95], a[212:215], v[248:251], v[92:95]\n\t"
    "ds_read_b128 a[212:215], v31 offset:12544\n\t"
    "v_sub_f32 v111, 0, v46\n\t"
    "v_mfma_f32_16x16x32_bf16 v[196:199], a[216:219], v[64:67], v[100:103]\n\t"
    "v_mov_b32 v49, v48\n\t"
    "s_nop 1\n\t"
    "v_permlane16_swap_b32 v48, v49\n\t"
    "v_mfma_f32_16x16x32_bf16 v[196:199], a[220:223], v[96:99], v[196:199]\n\t"
    "v_max_f32 v48, v48, v49\n\t"
    "v_mov_b32 v49, v48\n\t"
    "v_mfma_f32_16x16x32_bf16 v[196:199], a[224:227], v[128:131], v[196:199]\n\t"
    "s_nop 0\n\t"
    "v_permlane32_swap_b32 v48, v49\n\t"
    "v_max3_f32 v50, v92, v93, v94\n\t"
    "v_mfma_f32_16x16x32_bf16 v[196:199], a[228:231], v[160:163], v[196:199]\n\t"
    "v_max_f32 v48, v48, v49\n\t"
    "v_max_f32 v50, v50, v95\n\t"
    "v_mfma_f32_16x16x32_bf16 v[196:199], a[232:235], v[192:195], v[196:199]\n\t"
    "v_sub_f32 v116, 0, v48\n\t"
    "v_sub_f32 v117, 0, v48\n\t"
    "v_mfma_f32_16x16x32_bf16 v[196:199], a[236:239], v[224:227], v[196:199]\n\t"
    "v_sub_f32 v118, 0, v48\n\t"
    "v_sub_f32 v119, 0, v48\n\t"
    "v_mfma_f32_16x16x32_bf16 v[204:207], a[216:219], v[72:75], v[108:111]\n\t"
    "v_mov_b32 v51, v50\n\t"
    "s_nop 1\n\t"
    "v_permlane16_swap_b32 v50, v51\n\t"
    "v_mfma_f32_16x16x32_bf16 v[204:207], a[220:223], v[104:107], v[204:207]\n\t"
    "s_bfm_b64 exec, 10, 0\n\t"
    "buffer_load_dword v27, v61, s[40:43], s94 offen sc1\n\t"
    "s_mov_b64 exec, -1\n\t"
    "v_mfma_f32_16x16x32_bf16 v[204:207], a[224:227], v[136:139], v[204:207]\n\t"
    "v_max_f32 v50, v50, v51\n\t"
    "v_mov_b32 v51, v50\n\t"
    "v_mfma_f32_16x16x32_bf16 v[204:207], a[228:231], v[168:171], v[204:207]\n\t"
    "s_nop 0\n\t"
    "v_permlane32_swap_b32 v50, v51\n\t"
    "v_max_f32 v50, v50, v51\n\t"
    "v_mfma_f32_16x16x32_bf16 v[204:207], a[232:235], v[200:203], v[204:207]\n\t"
    "v_sub_f32 v124, 0, v50\n\t"
    "v_sub_f32 v125, 0, v50\n\t"
    "v_mfma_f32_16x16x32_bf16 v[204:207], a[236:239], v[232:235], v[204:207]\n\t"
    "v_sub_f32 v126, 0, v50\n\t"
    "v_sub_f32 v127, 0, v50\n\t"
    "v_mfma_f32_16x16x32_bf16 v[212:215], a[216:219], v[80:83], v[116:119]\n\t"
    "v_add_f32 v68, v100, v68\n\t"
    "v_add_f32 v69, v100, v69\n\t"
    "v_mfma_f32_16x16x32_bf16 v[212:215], a[220:223], v[112:115], v[212:215]\n\t"
    "v_add_f32 v70, v100, v70\n\t"
    "v_add_f32 v71, v100, v71\n\t"
    "v_mfma_f32_16x16x32_bf16 v[212:215], a[224:227], v[144:147], v[212:215]\n\t"
    "v_exp_f32 v68, v68\n\t"
    "v_mfma_f32_16x16x32_bf16 v[212:215], a[228:231], v[176:179], v[212:215]\n\t"
    "v_exp_f32 v69, v69\n\t"
    "v_mfma_f32_16x16x32_bf16 v[212:215], a[232:235], v[208:211], v[212:215]\n\t"
    "v_exp_f32 v70, v70\n\t"
    "v_mfma_f32_16x16x32_bf16 v[212:215], a[236:239], v[240:243], v[212:215]\n\t"
    "v_exp_f32 v71, v71\n\t"
    "v_mfma_f32_16x16x32_bf16 v[220:223], a[216:219], v[88:91], v[124:127]\n\t"
    "ds_read_b128 a[216:219], v30 offset:18432\n\t"
    "v_cvt_pk_bf16_f32 v132, v68, v69\n\t"
    "v_mfma_f32_16x16x32_bf16 v[220:223], a[220:223], v[120:123], v[220:223]\n\t"
    "ds_read_b128 a[220:223], v31 offset:18432\n\t"
    "v_cvt_pk_bf16_f32 v133, v70, v71\n\t"
    "v_mfma_f32_16x16x32_bf16 v[220:223], a[224:227], v[152:155], v[220:223]\n\t"
    "ds_read_b128 a[224:227], v30 offset:18560\n\t"
    "v_add_f32 v76, v108, v76\n\t"
    "v_mfma_f32_16x16x32_bf16 v[220:223], a[228:231], v[184:187], v[220:223]\n\t"
    "ds_read_b128 a[228:231], v31 offset:18560\n\t"
    "v_add_f32 v77, v108, v77\n\t"
    "v_mfma_f32_16x16x32_bf16 v[220:223], a[232:235], v[216:219], v[220:223]\n\t"
    "ds_read_b128 a[232:235], v30 offset:18688\n\t"
    "v_add_f32 v78, v108, v78\n\t"
    "v_mfma_f32_16x16x32_bf16 v[220:223], a[236:239], v[248:251], v[220:223]\n\t"
    "s_waitcnt lgkmcnt(5)\n\t"
    "ds_read_b128 a[236:239], v31 offset:18688\n\t"
    "v_add_f32 v79, v108, v79\n\t"
    "v_mfma_f32_16x16x32_bf16 v[228:231], a[192:195], v[64:67], v[100:103]\n\t"
    "v_exp_f32 v76, v76\n\t"
    "v_mfma_f32_16x16x32_bf16 v[228:231], a[196:199], v[96:99], v[228:231]\n\t"
    "v_exp_f32 v77, v77\n\t"
    "v_mfma_f32_16x16x32_bf16 v[228:231], a[200:203], v[128:131], v[228:231]\n\t"
    "v_exp_f32 v78, v78\n\t"
    "v_mfma_f32_16x16x32_bf16 v[228:231], a[204:207], v[160:163], v[228:231]\n\t"
    "v_exp_f32 v79, v79\n\t"
    "v_mfma_f32_16x16x32_bf16 v[228:231], a[208:211], v[192:195], v[228:231]\n\t"
    "v_cvt_pk_bf16_f32 v140, v76, v77\n\t"
    "v_cvt_pk_bf16_f32 v141, v78, v79\n\t"
    "v_mfma_f32_16x16x32_bf16 v[228:231], a[212:215], v[224:227], v[228:231]\n\t"
    "v_add_f32 v84, v116, v84\n\t"
    "v_add_f32 v85, v116, v85\n\t"
    "v_mfma_f32_16x16x32_bf16 v[236:239], a[192:195], v[72:75], v[108:111]\n\t"
    "v_add_f32 v86, v116, v86\n\t"
    "v_add_f32 v87, v116, v87\n\t"
    "v_mfma_f32_16x16x32_bf16 v[236:239], a[196:199], v[104:107], v[236:239]\n\t"
    "KFP6:\n\t"
    "s_cbranch_execz KFP10\n\t"
    "v_exp_f32 v84, v84\n\t"
    "v_mfma_f32_16x16x32_bf16 v[236:239], a[200:203], v[136:139], v[236:239]\n\t"
    "v_exp_f32 v85, v85\n\t"
    "v_mfma_f32_16x16x32_bf16 v[236:239], a[204:207], v[168:171], v[236:239]\n\t"
    "v_exp_f32 v86, v86\n\t"
    "v_mfma_f32_16x16x32_bf16 v[236:239], a[208:211], v[200:203], v[236:239]\n\t"
    "s_waitcnt vmcnt(14) lgkmcnt(0)\n\t"
    "s_barrier\n\t"
    "v_cvt_pk_bf16_f32 v148, v84, v85\n\t"
    "v_mfma_f32_16x16x32_bf16 v[236:239], a[212:215], v[232:235], v[236:239]\n\t"
    "v_exp_f32 v87, v87\n\t"
    "v_mfma_f32_16x16x32_bf16 v[244:247], a[192:195], v[80:83], v[116:119]\n\t"
    "v_cvt_pk_bf16_f32 v149, v86, v87\n\t"
    "v_add_f32 v92, v124, v92\n\t"
    "v_mfma_f32_16x16x32_bf16 v[244:247], a[196:199], v[112:115], v[244:247]\n\t"
    "v_add_f32 v93, v124, v93\n\t"
    "v_add_f32 v94, v124, v94\n\t"
    "v_mfma_f32_16x16x32_bf16 v[244:247], a[200:203], v[144:147], v[244:247]\n\t"
    "v_exp_f32 v92, v92\n\t"
    "v_mfma_f32_16x16x32_bf16 v[244:247], a[204:207], v[176:179], v[244:247]\n\t"
    "v_exp_f32 v93, v93\n\t"
    "v_mfma_f32_16x16x32_bf16 v[244:247], a[208:211], v[208:211], v[244:247]\n\t"
    "v_add_f32 v95, v124, v95\n\t"
    "v_cvt_pk_bf16_f32 v156, v92, v93\n\t"
    "v_mfma_f32_16x16x32_bf16 v[244:247], a[212:215], v[240:243], v[244:247]\n\t"
    "v_exp_f32 v94, v94\n\t"
    "v_mfma_f32_16x16x32_bf16 v[252:255], a[192:195], v[88:91], v[124:127]\n\t"
    "v_exp_f32 v95, v95\n\t"
    "v_mfma_f32_16x16x32_bf16 v[252:255], a[196:199], v[120:123], v[252:255]\n\t"
    "ds_read_b128 a[192:195], v30 offset:24576\n\t"
    "v_cvt_pk_bf16_f32 v157, v94, v95\n\t"
    "v_mfma_f32_16x16x32_bf16 v[252:255], a[200:203], v[152:155], v[252:255]\n\t"
    "v_exp_f32 v196, v196\n\t"
    "v_mfma_f32_16x16x32_bf16 v[252:255], a[204:207], v[184:187], v[252:255]\n\t"
    "v_exp_f32 v197, v197\n\t"
    "v_mfma_f32_16x16x32_bf16 v[252:255], a[208:211], v[216:219], v[252:255]\n\t"
    "ds_read_b128 a[196:199], v31 offset:24576\n\t"
    "v_cvt_pk_bf16_f32 v134, v196, v197\n\t"
    "v_mfma_f32_16x16x32_bf16 v[252:255], a[212:215], v[248:251], v[252:255]\n\t"
    "v_exp_f32 v198, v198\n\t"
    "v_mfma_f32_16x16x32_bf16 v[68:71], a[216:219], v[64:67], v[100:103]\n\t"
    "v_exp_f32 v199, v199\n\t"
    "v_mfma_f32_16x16x32_bf16 v[68:71], a[220:223], v[96:99], v[68:71]\n\t"
    "ds_read_b128 a[200:203], v30 offset:24704\n\t"
    "v_cvt_pk_bf16_f32 v135, v198, v199\n\t"
    "v_mfma_f32_16x16x32_bf16 v[68:71], a[224:227], v[128:131], v[68:71]\n\t"
    "buffer_load_dwordx4 v8, s[48:51], s60 offen offset:0 lds\n\t"
    "v_mfma_f32_16x16x32_bf16 v[68:71], a[228:231], v[160:163], v[68:71]\n\t"
    "v_exp_f32 v204, v204\n\t"
    "v_mfma_f32_16x16x32_bf16 v[68:71], a[232:235], v[192:195], v[68:71]\n\t"
    "v_exp_f32 v205, v205\n\t"
    "v_mfma_f32_16x16x32_bf16 v[68:71], a[236:239], v[224:227], v[68:71]\n\t"
    "ds_read_b128 a[204:207], v31 offset:24704\n\t"
    "v_cvt_pk_bf16_f32 v142, v204, v205\n\t"
    "v_mfma_f32_16x16x32_bf16 v[76:79], a[216:219], v[72:75], v[108:111]\n\t"
    "v_exp_f32 v206, v206\n\t"
    "v_mfma_f32_16x16x32_bf16 v[76:79], a[220:223], v[104:107], v[76:79]\n\t"
    "v_exp_f32 v207, v207\n\t"
    "v_mfma_f32_16x16x32_bf16 v[76:79], a[224:227], v[136:139], v[76:79]\n\t"
    "ds_read_b128 a[208:211], v30 offset:24832\n\t"
    "v_cvt_pk_bf16_f32 v143, v206, v207\n\t"
    "v_mfma_f32_16x16x32_bf16 v[76:79], a[228:231], v[168:171], v[76:79]\n\t"
    "v_exp_f32 v68, v68\n\t"
    "v_mfma_f32_16x16x32_bf16 v[76:79], a[232:235], v[200:203], v[76:79]\n\t"
    "v_exp_f32 v69, v69\n\t"
    "v_mfma_f32_16x16x32_bf16 v[76:79], a[236:239], v[232:235], v[76:79]\n\t"
    "ds_read_b128 a[212:215], v31 offset:24832\n\t"
    "v_cvt_pk_bf16_f32 v166, v68, v69\n\t"
    "v_mfma_f32_16x16x32_bf16 v[84:87], a[216:219], v[80:83], v[116:119]\n\t"
    "v_exp_f32 v70, v70\n\t"
    "v_mfma_f32_16x16x32_bf16 v[84:87], a[220:223], v[112:115], v[84:87]\n\t"
    "v_exp_f32 v71, v71\n\t"
    "v_mfma_f32_16x16x32_bf16 v[84:87], a[224:227], v[144:147], v[84:87]\n\t"
    "s_nop 0\n\t"
    "v_exp_f32 v76, v76\n\t"
    "v_mfma_f32_16x16x32_bf16 v[84:87], a[228:231], v[176:179], v[84:87]\n\t"
    "v_exp_f32 v77, v77\n\t"
    "v_mfma_f32_16x16x32_bf16 v[84:87], a[232:235], v[208:211], v[84:87]\n\t"
    "v_cvt_pk_bf16_f32 v167, v70, v71\n\t"
    "v_cvt_pk_bf16_f32 v174, v76, v77\n\t"
    "v_mfma_f32_16x16x32_bf16 v[84:87], a[236:239], v[240:243], v[84:87]\n\t"
    "v_exp_f32 v78, v78\n\t"
    "v_mfma_f32_16x16x32_bf16 v[92:95], a[216:219], v[88:91], v[124:127]\n\t"
    "v_exp_f32 v79, v79\n\t"
    "v_mfma_f32_16x16x32_bf16 v[92:95], a[220:223], v[120:123], v[92:95]\n\t"
    "ds_read_b64_tr_b16 a[216:217], v15 offset:0\n\t"
    "v_cvt_pk_bf16_f32 v175, v78, v79\n\t"
    "v_mfma_f32_16x16x32_bf16 v[92:95], a[224:227], v[152:155], v[92:95]\n\t"
    "s_nop 0\n\t"
    "v_exp_f32 v84, v84\n\t"
    "v_mfma_f32_16x16x32_bf16 v[92:95], a[228:231], v[184:187], v[92:95]\n\t"
    "v_exp_f32 v85, v85\n\t"
    "v_mfma_f32_16x16x32_bf16 v[92:95], a[232:235], v[216:219], v[92:95]\n\t"
    "ds_read_b64_tr_b16 a[218:219], v15 offset:6144\n\t"
    "v_cvt_pk_bf16_f32 v182, v84, v85\n\t"
    "v_mfma_f32_16x16x32_bf16 v[92:95], a[236:239], v[248:251], v[92:95]\n\t"
    "s_waitcnt lgkmcnt(3)\n\t"
    "v_exp_f32 v86, v86\n\t"
    "v_mfma_f32_16x16x32_bf16 v[68:71], a[192:195], v[64:67], v[100:103]\n\t"
    "v_exp_f32 v87, v87\n\t"
    "v_mfma_f32_16x16x32_bf16 v[68:71], a[196:199], v[96:99], v[68:71]\n\t"
    "ds_read_b64_tr_b16 a[220:221], v15 offset:12288\n\t"
    "v_cvt_pk_bf16_f32 v183, v86, v87\n\t"
    "v_mfma_f32_16x16x32_bf16 v[68:71], a[200:203], v[128:131], v[68:71]\n\t"
    "v_exp_f32 v92, v92\n\t"
    "v_mfma_f32_16x16x32_bf16 v[68:71], a[204:207], v[160:163], v[68:71]\n\t"
    "ds_read_b64_tr_b16 a[222:223], v15 offset:18432\n\t"
    "v_mfma_f32_16x16x32_bf16 v[68:71], a[208:211], v[192:195], v[68:71]\n\t"
    "s_waitcnt lgkmcnt(4)\n\t"
    "v_exp_f32 v93, v93\n\t"
    "v_mfma_f32_16x16x32_bf16 v[68:71], a[212:215], v[224:227], v[68:71]\n\t"
    "ds_read_b64_tr_b16 a[224:225], v16 offset:0\n\t"
    "v_cvt_pk_bf16_f32 v190, v92, v93\n\t"
    "v_mfma_f32_16x16x32_bf16 v[76:79], a[192:195], v[72:75], v[108:111]\n\t"
    "v_exp_f32 v94, v94\n\t"
    "v_mfma_f32_16x16x32_bf16 v[76:79], a[196:199], v[104:107], v[76:79]\n\t"
    "ds_read_b64_tr_b16 a[226:227], v16 offset:6144\n\t"
    "v_mfma_f32_16x16x32_bf16 v[76:79], a[200:203], v[136:139], v[76:79]\n\t"
    "ds_read_b64_tr_b16 a[228:229], v16 offset:12288\n\t"
    "v_mfma_f32_16x16x32_bf16 v[76:79], a[204:207], v[168:171], v[76:79]\n\t"
    "v_exp_f32 v68, v68\n\t"
    "v_mfma_f32_16x16x32_bf16 v[76:79], a[208:211], v[200:203], v[76:79]\n\t"
    "ds_read_b64_tr_b16 a[230:231], v16 offset:18432\n\t"
    "v_add_f32 v40, v68, v40\n\t"
    "v_mfma_f32_16x16x32_bf16 v[76:79], a[212:215], v[232:235], v[76:79]\n\t"
    "v_exp_f32 v69, v69\n\t"
    "v_mfma_f32_16x16x32_bf16 v[84:87], a[192:195], v[80:83], v[116:119]\n\t"
    "v_exp_f32 v95, v95\n\t"
    "v_mfma_f32_16x16x32_bf16 v[84:87], a[196:199], v[112:115], v[84:87]\n\t"
    "ds_read_b64_tr_b16 a[232:233], v17 offset:0\n\t"
    "v_add_f32 v40, v69, v40\n\t"
    "v_mfma_f32_16x16x32_bf16 v[84:87], a[200:203], v[144:147], v[84:87]\n\t"
    "ds_read_b64_tr_b16 a[234:235], v17 offset:6144\n\t"
    "v_cvt_pk_bf16_f32 v191, v94, v95\n\t"
    "v_mfma_f32_16x16x32_bf16 v[84:87], a[204:207], v[176:179], v[84:87]\n\t"
    "ds_read_b64_tr_b16 a[236:237], v17 offset:12288\n\t"
    "v_cvt_pk_bf16_f32 v196, v68, v69\n\t"
    "v_mfma_f32_16x16x32_bf16 v[84:87], a[208:211], v[208:211], v[84:87]\n\t"
    "v_exp_f32 v70, v70\n\t"
    "v_mfma_f32_16x16x32_bf16 v[84:87], a[212:215], v[240:243], v[84:87]\n\t"
    "ds_read_b64_tr_b16 a[238:239], v17 offset:18432\n\t"
    "v_add_f32 v40, v70, v40\n\t"
    "v_mfma_f32_16x16x32_bf16 v[92:95], a[192:195], v[88:91], v[124:127]\n\t"
    "v_exp_f32 v71, v71\n\t"
    "v_mfma_f32_16x16x32_bf16 v[92:95], a[196:199], v[120:123], v[92:95]\n\t"
    "ds_read_b128 a[192:195], v30 offset:30720\n\t"
    "v_add_f32 v40, v71, v40\n\t"
    "v_mfma_f32_16x16x32_bf16 v[92:95], a[200:203], v[152:155], v[92:95]\n\t"
    "v_exp_f32 v212, v212\n\t"
    "v_mfma_f32_16x16x32_bf16 v[92:95], a[204:207], v[184:187], v[92:95]\n\t"
    "v_exp_f32 v213, v213\n\t"
    "v_mfma_f32_16x16x32_bf16 v[92:95], a[208:211], v[216:219], v[92:95]\n\t"
    "v_exp_f32 v214, v214\n\t"
    "v_mfma_f32_16x16x32_bf16 v[92:95], a[212:215], v[248:251], v[92:95]\n\t"
    "v_exp_f32 v215, v215\n\t"
    "v_mfma_f32_16x16x32_bf16 v[44:47], a[188:191], v[132:135], 0\n\t"
    "ds_read_b128 a[196:199], v31 offset:30720\n\t"
    "v_cvt_pk_bf16_f32 v197, v70, v71\n\t"
    "v_mfma_f32_16x16x32_bf16 v[48:51], a[188:191], v[140:143], 0\n\t"
    "v_exp_f32 v76, v76\n\t"
    "v_cvt_pk_bf16_f32 v150, v212, v213\n\t"
    "v_cvt_pk_bf16_f32 v151, v214, v215\n\t"
    "s_nop 1\n\t"
    "v_mfma_f32_16x16x32_bf16 v[52:55], a[188:191], v[148:151], 0\n\t"
    "v_exp_f32 v220, v220\n\t"
    "v_exp_f32 v221, v221\n\t"
    "v_exp_f32 v222, v222\n\t"
    "v_exp_f32 v223, v223\n\t"
    "v_cvt_pk_bf16_f32 v158, v220, v221\n\t"
    "v_cvt_pk_bf16_f32 v159, v222, v223\n\t"
    "s_nop 1\n\t"
    "v_mfma_f32_16x16x32_bf16 v[56:59], a[188:191], v[156:159], 0\n\t"
    "s_waitcnt lgkmcnt(3)\n\t"
    "buffer_load_dwordx4 v9, s[48:51], s60 offen offset:1024 lds\n\t"
    "v_mfma_f32_16x16x32_bf16 a[0:3], a[216:219], v[132:135], 0\n\t"
    "ds_read_b128 a[200:203], v30 offset:30848\n\t"
    "v_add_f32 v41, v76, v41\n\t"
    "v_mfma_f32_16x16x32_bf16 a[4:7], a[216:219], v[140:143], 0\n\t"
    "v_exp_f32 v77, v77\n\t"
    "v_mfma_f32_16x16x32_bf16 a[8:11], a[216:219], v[148:151], 0\n\t"
    "ds_read_b128 a[204:207], v31 offset:30848\n\t"
    "v_add_f32 v41, v77, v41\n\t"
    "v_mfma_f32_16x16x32_bf16 a[12:15], a[216:219], v[156:159], 0\n\t"
    "v_exp_f32 v78, v78\n\t"
    "v_mfma_f32_16x16x32_bf16 a[16:19], a[224:227], v[132:135], 0\n\t"
    "buffer_load_dwordx4 v10, s[48:51], s60 offen offset:2048 lds\n\t"
    "v_mfma_f32_16x16x32_bf16 a[20:23], a[224:227], v[140:143], 0\n\t"
    "s_add_u32 m0, s83, 76800\n\t"
    "ds_read_b128 a[208:211], v30 offset:30976\n\t"
    "v_cvt_pk_bf16_f32 v204, v76, v77\n\t"
    "v_mfma_f32_16x16x32_bf16 a[24:27], a[224:227], v[148:151], 0\n\t"
    "s_add_u32 s60, s60, 3072\n\t"
    "ds_read_b128 a[212:215], v31 offset:30976\n\t"
    "v_add_f32 v41, v78, v41\n\t"
    "v_mfma_f32_16x16x32_bf16 a[28:31], a[224:227], v[156:159], 0\n\t"
    "v_exp_f32 v79, v79\n\t"
    "v_mfma_f32_16x16x32_bf16 a[32:35], a[232:235], v[132:135], 0\n\t"
    "ds_read_b64_tr_b16 a[216:217], v18 offset:0\n\t"
    "v_add_f32 v41, v79, v41\n\t"
    "v_mfma_f32_16x16x32_bf16 a[36:39], a[232:235], v[140:143], 0\n\t"
    "ds_read_b64_tr_b16 a[218:219], v18 offset:6144\n\t"
    "v_cvt_pk_bf16_f32 v205, v78, v79\n\t"
    "v_mfma_f32_16x16x32_bf16 a[40:43], a[232:235], v[148:151], 0\n\t"
    "v_exp_f32 v84, v84\n\t"
    "v_mfma_f32_16x16x32_bf16 a[44:47], a[232:235], v[156:159], 0\n\t"
    "v_exp_f32 v228, v228\n\t"
    "v_exp_f32 v229, v229\n\t"
    "v_exp_f32 v230, v230\n\t"
    "v_exp_f32 v231, v231\n\t"
    "v_cvt_pk_bf16_f32 v164, v228, v229\n\t"
    "v_cvt_pk_bf16_f32 v165, v230, v231\n\t"
    "s_nop 1\n\t"
    "v_mfma_f32_16x16x32_bf16 a[0:3], a[220:223], v[164:167], a[0:3]\n\t"
    "ds_read_b64_tr_b16 a[224:225], v15 offset:128\n\t"
    "v_add_f32 v42, v84, v42\n\t"
    "v_exp_f32 v236, v236\n\t"
    "v_exp_f32 v237, v237\n\t"
    "v_exp_f32 v238, v238\n\t"
    "v_exp_f32 v239, v239\n\t"
    "v_cvt_pk_bf16_f32 v172, v236, v237\n\t"
    "v_cvt_pk_bf16_f32 v173, v238, v239\n\t"
    "s_nop 1\n\t"
    "v_mfma_f32_16x16x32_bf16 a[4:7], a[220:223], v[172:175], a[4:7]\n\t"
    "v_exp_f32 v85, v85\n\t"
    "v_exp_f32 v244, v244\n\t"
    "v_exp_f32 v245, v245\n\t"
    "v_exp_f32 v246, v246\n\t"
    "v_exp_f32 v247, v247\n\t"
    "v_cvt_pk_bf16_f32 v180, v244, v245\n\t"
    "v_cvt_pk_bf16_f32 v181, v246, v247\n\t"
    "s_nop 1\n\t"
    "v_mfma_f32_16x16x32_bf16 a[8:11], a[220:223], v[180:183], a[8:11]\n\t"
    "v_exp_f32 v252, v252\n\t"
    "v_exp_f32 v253, v253\n\t"
    "v_exp_f32 v254, v254\n\t"
    "v_exp_f32 v255, v255\n\t"
    "v_cvt_pk_bf16_f32 v188, v252, v253\n\t"
    "v_cvt_pk_bf16_f32 v189, v254, v255\n\t"
    "s_nop 1\n\t"
    "v_mfma_f32_16x16x32_bf16 a[12:15], a[220:223], v[188:191], a[12:15]\n\t"
    "ds_read_b64_tr_b16 a[220:221], v18 offset:12288\n\t"
    "v_add_f32 v42, v85, v42\n\t"
    "v_mfma_f32_16x16x32_bf16 a[16:19], a[228:231], v[164:167], a[16:19]\n\t"
    "buffer_load_dwordx4 v8, s[48:51], s60 offen offset:0 lds\n\t"
    "v_mfma_f32_16x16x32_bf16 a[20:23], a[228:231], v[172:175], a[20:23]\n\t"
    "v_exp_f32 v86, v86\n\t"
    "v_mfma_f32_16x16x32_bf16 a[24:27], a[228:231], v[180:183], a[24:27]\n\t"
    "ds_read_b64_tr_b16 a[222:223], v18 offset:18432\n\t"
    "v_cvt_pk_bf16_f32 v212, v84, v85\n\t"
    "v_mfma_f32_16x16x32_bf16 a[28:31], a[228:231], v[188:191], a[28:31]\n\t"
    "s_waitcnt lgkmcnt(2)\n\t"
    "ds_read_b64_tr_b16 a[226:227], v15 offset:6272\n\t"
    "v_add_f32 v42, v86, v42\n\t"
    "v_mfma_f32_16x16x32_bf16 a[32:35], a[236:239], v[164:167], a[32:35]\n\t"
    "v_exp_f32 v87, v87\n\t"
    "v_mfma_f32_16x16x32_bf16 a[36:39], a[236:239], v[172:175], a[36:39]\n\t"
    "buffer_load_dwordx4 v9, s[48:51], s60 offen offset:1024 lds\n\t"
    "v_mfma_f32_16x16x32_bf16 a[40:43], a[236:239], v[180:183], a[40:43]\n\t"
    "ds_read_b64_tr_b16 a[240:241], v63 offset:0\n\t"
    "v_add_f32 v42, v87, v42\n\t"
    "v_mfma_f32_16x16x32_bf16 a[44:47], a[236:239], v[188:191], a[44:47]\n\t"
    "ds_read_b64_tr_b16 a[242:243], v63 offset:2048\n\t"
    "v_cvt_pk_bf16_f32 v213, v86, v87\n\t"
    "v_mfma_f32_16x16x32_bf16 v[44:47], a[188:191], v[164:167], v[44:47]\n\t"
    "v_exp_f32 v92, v92\n\t"
    "v_mfma_f32_16x16x32_bf16 v[48:51], a[188:191], v[172:175], v[48:51]\n\t"
    "ds_read_b64_tr_b16 a[244:245], v24 offset:0\n\t"
    "v_add_f32 v43, v92, v43\n\t"
    "v_mfma_f32_16x16x32_bf16 v[52:55], a[188:191], v[180:183], v[52:55]\n\t"
    "buffer_load_dwordx4 v10, s[48:51], s60 offen offset:2048 lds\n\t"
    "v_mfma_f32_16x16x32_bf16 v[56:59], a[188:191], v[188:191], v[56:59]\n\t"
    "v_exp_f32 v93, v93\n\t"
    "v_mfma_f32_16x16x32_bf16 v[68:71], a[192:195], v[64:67], v[100:103]\n\t"
    "s_add_u32 m0, s83, 98304\n\t"
    "v_add_f32 v43, v93, v43\n\t"
    "v_mfma_f32_16x16x32_bf16 v[68:71], a[196:199], v[96:99], v[68:71]\n\t"
    "v_exp_f32 v94, v94\n\t"
    "v_mfma_f32_16x16x32_bf16 v[68:71], a[200:203], v[128:131], v[68:71]\n\t"
    "ds_read_b64_tr_b16 a[246:247], v24 offset:2048\n\t"
    "v_cvt_pk_bf16_f32 v220, v92, v93\n\t"
    "v_mfma_f32_16x16x32_bf16 v[68:71], a[204:207], v[160:163], v[68:71]\n\t"
    "s_add_u32 s60, s87, 8192\n\t"
    "v_add_f32 v43, v94, v43\n\t"
    "v_mfma_f32_16x16x32_bf16 v[68:71], a[208:211], v[192:195], v[68:71]\n\t"
    "s_waitcnt vmcnt(6)\n\t"
    "v_cmp_ne_u32 vcc, s80, v27\n\t"
    "s_cbranch_vccnz KFASM\n\t"
    "KFARM:\n\t"
    "v_mfma_f32_16x16x32_bf16 v[68:71], a[212:215], v[224:227], v[68:71]\n\t"
    "s_bfe_u32 s60, s60, 0x2000c\n\t"
    "v_exp_f32 v95, v95\n\t"
    "v_mfma_f32_16x16x32_bf16 v[76:79], a[192:195], v[72:75], v[108:111]\n\t"
    "s_mulk_i32 s60, 24576\n\t"
    "ds_read_b64_tr_b16 a[228:229], v15 offset:12416\n\t"
    "v_add_f32 v43, v95, v43\n\t"
    "v_mfma_f32_16x16x32_bf16 v[76:79], a[196:199], v[104:107], v[76:79]\n\t"
    "ds_read_b64_tr_b16 a[230:231], v15 offset:18560\n\t"
    "v_cvt_pk_bf16_f32 v221, v94, v95\n\t"
    "v_mfma_f32_16x16x32_bf16 v[76:79], a[200:203], v[136:139], v[76:79]\n\t"
    "buffer_load_dwordx4 v8, s[52:55], s60 offen offset:0 lds\n\t"
    "v_mfma_f32_16x16x32_bf16 v[76:79], a[204:207], v[168:171], v[76:79]\n\t"
    "v_exp_f32 v68, v68\n\t"
    "v_mfma_f32_16x16x32_bf16 v[76:79], a[208:211], v[200:203], v[76:79]\n\t"
    "ds_read_b64_tr_b16 a[248:249], v25 offset:0\n\t"
    "v_add_f32 v40, v44, v40\n\t"
    "v_mfma_f32_16x16x32_bf16 v[76:79], a[212:215], v[232:235], v[76:79]\n\t"
    "ds_read_b64_tr_b16 a[250:251], v25 offset:2048\n\t"
    "v_add_f32 v40, v68, v40\n\t"
    "v_mfma_f32_16x16x32_bf16 v[84:87], a[192:195], v[80:83], v[116:119]\n\t"
    "v_exp_f32 v69, v69\n\t"
    "v_mfma_f32_16x16x32_bf16 v[84:87], a[196:199], v[112:115], v[84:87]\n\t"
    "buffer_load_dwordx4 v9, s[52:55], s60 offen offset:1024 lds\n\t"
    "v_mfma_f32_16x16x32_bf16 v[84:87], a[200:203], v[144:147], v[84:87]\n\t"
    "ds_read_b64_tr_b16 a[232:233], v16 offset:128\n\t"
    "v_add_f32 v40, v69, v40\n\t"
    "v_mfma_f32_16x16x32_bf16 v[84:87], a[204:207], v[176:179], v[84:87]\n\t"
    "ds_read_b64_tr_b16 a[234:235], v16 offset:6272\n\t"
    "v_cvt_pk_bf16_f32 v198, v68, v69\n\t"
    "v_mfma_f32_16x16x32_bf16 v[84:87], a[208:211], v[208:211], v[84:87]\n\t"
    "ds_read_b64_tr_b16 a[236:237], v16 offset:12416\n\t"
    "v_add_f32 v41, v48, v41\n\t"
    "v_mfma_f32_16x16x32_bf16 v[84:87], a[212:215], v[240:243], v[84:87]\n\t"
    "v_exp_f32 v70, v70\n\t"
    "v_mfma_f32_16x16x32_bf16 v[92:95], a[192:195], v[88:91], v[124:127]\n\t"
    "s_waitcnt lgkmcnt(6)\n\t"
    "buffer_store_dwordx4 a[240:243], v4, s[40:43], s63 offen offset:0 sc1\n\t"
    "v_mfma_f32_16x16x32_bf16 v[92:95], a[196:199], v[120:123], v[92:95]\n\t"
    "s_bfe_u32 s63, s76, 0x80008\n\t"
    "ds_read_b64_tr_b16 a[238:239], v16 offset:18560\n\t"
    "v_add_f32 v40, v70, v40\n\t"
    "v_mfma_f32_16x16x32_bf16 v[92:95], a[200:203], v[152:155], v[92:95]\n\t"
    "s_lshl_b32 s63, s63, 10\n\t"
    "ds_read_b64_tr_b16 a[252:253], v26 offset:0\n\t"
    "v_add3_u32 v27, v21, v4, s66\n\t"
    "v_mfma_f32_16x16x32_bf16 v[92:95], a[204:207], v[184:187], v[92:95]\n\t"
    "s_add_u32 s63, s63, s88\n\t"
    "ds_read_b64_tr_b16 a[254:255], v26 offset:2048\n\t"
    "v_add_f32 v42, v52, v42\n\t"
    "v_mfma_f32_16x16x32_bf16 v[92:95], a[208:211], v[216:219], v[92:95]\n\t"
    "v_exp_f32 v71, v71\n\t"
    "v_mfma_f32_16x16x32_bf16 v[92:95], a[212:215], v[248:251], v[92:95]\n\t"
    "buffer_store_dwordx4 a[244:247], v4, s[40:43], s63 offen offset:0 sc1\n\t"
    "v_mfma_f32_16x16x32_bf16 a[48:51], a[216:219], v[132:135], 0\n\t"
    "s_bfe_u32 s63, s76, 0x80010\n\t"
    "v_add_f32 v40, v71, v40\n\t"
    "v_mfma_f32_16x16x32_bf16 a[48:51], a[220:223], v[164:167], a[48:51]\n\t"
    "s_lshl_b32 s63, s63, 10\n\t"
    "ds_read_b128 a[192:195], v30 offset:36864\n\t"
    "v_cvt_pk_bf16_f32 v199, v70, v71\n\t"
    "v_mfma_f32_16x16x32_bf16 a[52:55], a[216:219], v[140:143], 0\n\t"
    "s_add_u32 s63, s63, s88\n\t"
    "v_exp_f32 v76, v76\n\t"
    "v_mfma_f32_16x16x32_bf16 a[52:55], a[220:223], v[172:175], a[52:55]\n\t"
    "ds_read_b128 a[196:199], v31 offset:36864\n\t"
    "v_add_f32 v41, v76, v41\n\t"
    "v_mfma_f32_16x16x32_bf16 a[56:59], a[216:219], v[148:151], 0\n\t"
    "s_waitcnt lgkmcnt(5)\n\t"
    "buffer_store_dwordx4 a[248:251], v4, s[40:43], s63 offen offset:0 sc1\n\t"
    "v_mfma_f32_16x16x32_bf16 a[56:59], a[220:223], v[180:183], a[56:59]\n\t"
    "s_bfe_u32 s63, s76, 0x80018\n\t"
    "v_exp_f32 v77, v77\n\t"
    "v_mfma_f32_16x16x32_bf16 a[60:63], a[216:219], v[156:159], 0\n\t"
    "s_lshl_b32 s63, s63, 10\n\t"
    "ds_read_b128 a[200:203], v30 offset:36992\n\t"
    "v_add_f32 v41, v77, v41\n\t"
    "v_mfma_f32_16x16x32_bf16 a[60:63], a[220:223], v[188:191], a[60:63]\n\t"
    "s_add_u32 s63, s63, s88\n\t"
    "v_cvt_pk_bf16_f32 v206, v76, v77\n\t"
    "v_mfma_f32_16x16x32_bf16 a[64:67], a[224:227], v[132:135], 0\n\t"
    "v_exp_f32 v78, v78\n\t"
    "v_mfma_f32_16x16x32_bf16 a[64:67], a[228:231], v[164:167], a[64:67]\n\t"
    "buffer_load_dwordx4 v10, s[52:55], s60 offen offset:2048 lds\n\t"
    "v_mfma_f32_16x16x32_bf16 a[68:71], a[224:227], v[140:143], 0\n\t"
    "s_add_u32 m0, s83, 101376\n\t"
    "ds_read_b128 a[204:207], v31 offset:36992\n\t"
    "v_add_f32 v41, v78, v41\n\t"
    "v_mfma_f32_16x16x32_bf16 a[68:71], a[228:231], v[172:175], a[68:71]\n\t"
    "s_add_u32 s60, s60, 3072\n\t"
    "v_exp_f32 v79, v79\n\t"
    "v_mfma_f32_16x16x32_bf16 a[72:75], a[224:227], v[148:151], 0\n\t"
    "ds_read_b128 a[208:211], v30 offset:37120\n\t"
    "v_add_f32 v41, v79, v41\n\t"
    "v_mfma_f32_16x16x32_bf16 a[72:75], a[228:231], v[180:183], a[72:75]\n\t"
    "ds_read_b128 a[212:215], v31 offset:37120\n\t"
    "v_cvt_pk_bf16_f32 v207, v78, v79\n\t"
    "v_mfma_f32_16x16x32_bf16 a[76:79], a[224:227], v[156:159], 0\n\t"
    "s_waitcnt lgkmcnt(4)\n\t"
    "buffer_store_dwordx4 a[252:255], v4, s[40:43], s63 offen offset:0 sc1\n\t"
    "v_mfma_f32_16x16x32_bf16 a[76:79], a[228:231], v[188:191], a[76:79]\n\t"
    "s_add_u32 s63, s91, 0\n\t"
    "v_add_f32 v43, v56, v43\n\t"
    "v_mfma_f32_16x16x32_bf16 a[80:83], a[232:235], v[132:135], 0\n\t"
    "v_exp_f32 v84, v84\n\t"
    "v_mfma_f32_16x16x32_bf16 a[80:83], a[236:239], v[164:167], a[80:83]\n\t"
    "ds_read_b64_tr_b16 a[216:217], v17 offset:128\n\t"
    "v_add_f32 v42, v84, v42\n\t"
    "v_mfma_f32_16x16x32_bf16 a[84:87], a[232:235], v[140:143], 0\n\t"
    "v_exp_f32 v85, v85\n\t"
    "v_mfma_f32_16x16x32_bf16 a[84:87], a[236:239], v[172:175], a[84:87]\n\t"
    "buffer_load_dwordx4 v8, s[52:55], s60 offen offset:0 lds\n\t"
    "v_mfma_f32_16x16x32_bf16 a[88:91], a[232:235], v[148:151], 0\n\t"
    "ds_read_b64_tr_b16 a[218:219], v17 offset:6272\n\t"
    "v_add_f32 v42, v85, v42\n\t"
    "v_mfma_f32_16x16x32_bf16 a[88:91], a[236:239], v[180:183], a[88:91]\n\t"
    "v_exp_f32 v86, v86\n\t"
    "v_mfma_f32_16x16x32_bf16 a[92:95], a[232:235], v[156:159], 0\n\t"
    "ds_read_b64_tr_b16 a[220:221], v17 offset:12416\n\t"
    "v_cvt_pk_bf16_f32 v214, v84, v85\n\t"
    "v_mfma_f32_16x16x32_bf16 a[92:95], a[236:239], v[188:191], a[92:95]\n\t"
    "ds_read_b64_tr_b16 a[222:223], v17 offset:18560\n\t"
    "v_add_f32 v42, v86, v42\n\t"
    "v_mfma_f32_16x16x32_bf16 v[68:71], a[192:195], v[64:67], v[100:103]\n\t"
    "buffer_load_dwordx4 v[44:47], v27, s[40:43], s63 offen offset:0 sc1\n\t"
    "v_mfma_f32_16x16x32_bf16 v[68:71], a[196:199], v[96:99], v[68:71]\n\t"
    "s_waitcnt lgkmcnt(4)\n\t"
    "s_add_u32 s63, s91, 49152\n\t"
    "ds_read_b64_tr_b16 a[224:225], v18 offset:128\n\t"
    "v_mfma_f32_16x16x32_bf16 v[68:71], a[200:203], v[128:131], v[68:71]\n\t"
    "v_exp_f32 v87, v87\n\t"
    "v_mfma_f32_16x16x32_bf16 v[68:71], a[204:207], v[160:163], v[68:71]\n\t"
    "ds_read_b64_tr_b16 a[226:227], v18 offset:6272\n\t"
    "v_add_f32 v42, v87, v42\n\t"
    "v_mfma_f32_16x16x32_bf16 v[68:71], a[208:211], v[192:195], v[68:71]\n\t"
    "ds_read_b64_tr_b16 a[228:229], v18 offset:12416\n\t"
    "v_cvt_pk_bf16_f32 v215, v86, v87\n\t"
    "v_mfma_f32_16x16x32_bf16 v[68:71], a[212:215], v[224:227], v[68:71]\n\t"
    "buffer_load_dwordx4 v9, s[52:55], s60 offen offset:1024 lds\n\t"
    "v_mfma_f32_16x16x32_bf16 v[76:79], a[192:195], v[72:75], v[108:111]\n\t"
    "v_exp_f32 v92, v92\n\t"
    "v_mfma_f32_16x16x32_bf16 v[76:79], a[196:199], v[104:107], v[76:79]\n\t"
    "ds_read_b64_tr_b16 a[230:231], v18 offset:18560\n\t"
    "v_add_f32 v43, v92, v43\n\t"
    "v_mfma_f32_16x16x32_bf16 v[76:79], a[200:203], v[136:139], v[76:79]\n\t"
    "v_exp_f32 v93, v93\n\t"
    "v_mfma_f32_16x16x32_bf16 v[76:79], a[204:207], v[168:171], v[76:79]\n\t"
    "ds_read_b64_tr_b16 a[232:233], v15 offset:256\n\t"
    "v_add_f32 v43, v93, v43\n\t"
    "v_mfma_f32_16x16x32_bf16 v[76:79], a[208:211], v[200:203], v[76:79]\n\t"
    "buffer_load_dwordx4 v[48:51], v27, s[40:43], s63 offen offset:0 sc1\n\t"
    "v_mfma_f32_16x16x32_bf16 v[76:79], a[212:215], v[232:235], v[76:79]\n\t"
    "s_add_u32 s63, s91, 98304\n\t"
    "v_exp_f32 v94, v94\n\t"
    "v_mfma_f32_16x16x32_bf16 v[84:87], a[192:195], v[80:83], v[116:119]\n\t"
    "ds_read_b64_tr_b16 a[234:235], v15 offset:6400\n\t"
    "v_cvt_pk_bf16_f32 v222, v92, v93\n\t"
    "v_mfma_f32_16x16x32_bf16 v[84:87], a[196:199], v[112:115], v[84:87]\n\t"
    "ds_read_b64_tr_b16 a[236:237], v15 offset:12544\n\t"
    "v_add_f32 v43, v94, v43\n\t"
    "v_mfma_f32_16x16x32_bf16 v[84:87], a[200:203], v[144:147], v[84:87]\n\t"
    "v_exp_f32 v95, v95\n\t"
    "v_mfma_f32_16x16x32_bf16 v[84:87], a[204:207], v[176:179], v[84:87]\n\t"
    "buffer_load_dwordx4 v10, s[52:55], s60 offen offset:2048 lds\n\t"
    "v_mfma_f32_16x16x32_bf16 v[84:87], a[208:211], v[208:211], v[84:87]\n\t"
    "ds_read_b64_tr_b16 a[238:239], v15 offset:18688\n\t"
    "v_add_f32 v43, v95, v43\n\t"
    "v_mfma_f32_16x16x32_bf16 v[84:87], a[212:215], v[240:243], v[84:87]\n\t"
    "v_cvt_pk_bf16_f32 v223, v94, v95\n\t"
    "v_mfma_f32_16x16x32_bf16 v[92:95], a[192:195], v[88:91], v[124:127]\n\t"
    "v_exp_f32 v68, v68\n\t"
    "v_mfma_f32_16x16x32_bf16 v[92:95], a[196:199], v[120:123], v[92:95]\n\t"
    "ds_read_b128 a[192:195], v30 offset:43008\n\t"
    "v_add_f32 v40, v68, v40\n\t"
    "v_mfma_f32_16x16x32_bf16 v[92:95], a[200:203], v[152:155], v[92:95]\n\t"
    "buffer_load_dwordx4 v[52:55], v27, s[40:43], s63 offen offset:0 sc1\n\t"
    "v_mfma_f32_16x16x32_bf16 v[92:95], a[204:207], v[184:187], v[92:95]\n\t"
    "s_add_u32 s63, s91, 147456\n\t"
    "v_exp_f32 v69, v69\n\t"
    "v_mfma_f32_16x16x32_bf16 v[92:95], a[208:211], v[216:219], v[92:95]\n\t"
    "ds_read_b128 a[196:199], v31 offset:43008\n\t"
    "v_add_f32 v40, v69, v40\n\t"
    "v_mfma_f32_16x16x32_bf16 v[92:95], a[212:215], v[248:251], v[92:95]\n\t"
    "s_waitcnt lgkmcnt(5)\n\t"
    "v_exp_f32 v70, v70\n\t"
    "v_mfma_f32_16x16x32_bf16 a[96:99], a[216:219], v[132:135], 0\n\t"
    "ds_read_b128 a[200:203], v30 offset:43136\n\t"
    "v_cvt_pk_bf16_f32 v228, v68, v69\n\t"
    "v_mfma_f32_16x16x32_bf16 a[96:99], a[220:223], v[164:167], a[96:99]\n\t"
    "buffer_load_dwordx4 v[56:59], v27, s[40:43], s63 offen offset:0 sc1\n\t"
    "v_mfma_f32_16x16x32_bf16 a[100:103], a[216:219], v[140:143], 0\n\t"
    "s_mov_b32 m0, s86\n\t"
    "ds_read_b128 a[204:207], v31 offset:43136\n\t"
    "v_add_f32 v40, v70, v40\n\t"
    "v_mfma_f32_16x16x32_bf16 a[100:103], a[220:223], v[172:175], a[100:103]\n\t"
    "s_mov_b32 s60, 128\n\t"
    "v_exp_f32 v71, v71\n\t"
    "v_mfma_f32_16x16x32_bf16 a[104:107], a[216:219], v[148:151], 0\n\t"
    "s_bfe_u32 s63, s79, 0x80000\n\t"
    "ds_read_b128 a[208:211], v30 offset:43264\n\t"
    "v_add_f32 v40, v71, v40\n\t"
    "v_mfma_f32_16x16x32_bf16 a[104:107], a[220:223], v[180:183], a[104:107]\n\t"
    "s_lshl_b32 s63, s63, 10\n\t"
    "ds_read_b128 a[212:215], v31 offset:43264\n\t"
    "v_cvt_pk_bf16_f32 v229, v70, v71\n\t"
    "v_mfma_f32_16x16x32_bf16 a[108:111], a[216:219], v[156:159], 0\n\t"
    "s_add_u32 s63, s63, s88\n\t"
    "v_exp_f32 v76, v76\n\t"
    "v_mfma_f32_16x16x32_bf16 a[108:111], a[220:223], v[188:191], a[108:111]\n\t"
    "ds_read_b64_tr_b16 a[216:217], v16 offset:256\n\t"
    "v_add_f32 v41, v76, v41\n\t"
    "v_mfma_f32_16x16x32_bf16 a[112:115], a[224:227], v[132:135], 0\n\t"
    "v_exp_f32 v77, v77\n\t"
    "v_mfma_f32_16x16x32_bf16 a[112:115], a[228:231], v[164:167], a[112:115]\n\t"
    "ds_read_b64_tr_b16 a[218:219], v16 offset:6400\n\t"
    "v_add_f32 v41, v77, v41\n\t"
    "v_mfma_f32_16x16x32_bf16 a[116:119], a[224:227], v[140:143], 0\n\t"
    "v_exp_f32 v78, v78\n\t"
    "v_mfma_f32_16x16x32_bf16 a[116:119], a[228:231], v[172:175], a[116:119]\n\t"
    "ds_read_b64_tr_b16 a[220:221], v16 offset:12544\n\t"
    "v_cvt_pk_bf16_f32 v236, v76, v77\n\t"
    "v_mfma_f32_16x16x32_bf16 a[120:123], a[224:227], v[148:151], 0\n\t"
    "ds_read_b64_tr_b16 a[222:223], v16 offset:18688\n\t"
    "v_add_f32 v41, v78, v41\n\t"
    "v_mfma_f32_16x16x32_bf16 a[120:123], a[228:231], v[180:183], a[120:123]\n\t"
    "v_exp_f32 v79, v79\n\t"
    "v_mfma_f32_16x16x32_bf16 a[124:127], a[224:227], v[156:159], 0\n\t"
    "ds_read_b64_tr_b16 a[224:225], v17 offset:256\n\t"
    "v_add_f32 v41, v79, v41\n\t"
    "v_mfma_f32_16x16x32_bf16 a[124:127], a[228:231], v[188:191], a[124:127]\n\t"
    "s_waitcnt lgkmcnt(6)\n\t"
    "ds_read_b64_tr_b16 a[226:227], v17 offset:6400\n\t"
    "v_cvt_pk_bf16_f32 v237, v78, v79\n\t"
    "v_mfma_f32_16x16x32_bf16 a[128:131], a[232:235], v[132:135], 0\n\t"
    "v_exp_f32 v84, v84\n\t"
    "v_mfma_f32_16x16x32_bf16 a[128:131], a[236:239], v[164:167], a[128:131]\n\t"
    "ds_read_b64_tr_b16 a[228:229], v17 offset:12544\n\t"
    "v_add_f32 v42, v84, v42\n\t"
    "v_mfma_f32_16x16x32_bf16 a[132:135], a[232:235], v[140:143], 0\n\t"
    "v_exp_f32 v85, v85\n\t"
    "v_mfma_f32_16x16x32_bf16 a[132:135], a[236:239], v[172:175], a[132:135]\n\t"
    "buffer_load_dwordx4 v62, s[56:59], s60 offen offset:0 nt lds\n\t"
    "v_mfma_f32_16x16x32_bf16 a[136:139], a[232:235], v[148:151], 0\n\t"
    "s_mov_b32 s60, 9344\n\t"
    "ds_read_b64_tr_b16 a[230:231], v17 offset:18688\n\t"
    "v_add_f32 v42, v85, v42\n\t"
    "v_mfma_f32_16x16x32_bf16 a[136:139], a[236:239], v[180:183], a[136:139]\n\t"
    "v_exp_f32 v86, v86\n\t"
    "v_mfma_f32_16x16x32_bf16 a[140:143], a[232:235], v[156:159], 0\n\t"
    "ds_read_b64_tr_b16 a[232:233], v18 offset:256\n\t"
    "v_cvt_pk_bf16_f32 v244, v84, v85\n\t"
    "v_mfma_f32_16x16x32_bf16 a[140:143], a[236:239], v[188:191], a[140:143]\n\t"
    "ds_read_b64_tr_b16 a[234:235], v18 offset:6400\n\t"
    "v_add_f32 v42, v86, v42\n\t"
    "v_mfma_f32_16x16x32_bf16 v[68:71], a[192:195], v[64:67], v[100:103]\n\t"
    "v_exp_f32 v87, v87\n\t"
    "v_mfma_f32_16x16x32_bf16 v[68:71], a[196:199], v[96:99], v[68:71]\n\t"
    "buffer_load_dwordx4 v62, s[56:59], s60 offen offset:1024 nt lds\n\t"
    "v_mfma_f32_16x16x32_bf16 v[68:71], a[200:203], v[128:131], v[68:71]\n\t"
    "ds_read_b64_tr_b16 a[236:237], v18 offset:12544\n\t"
    "v_add_f32 v42, v87, v42\n\t"
    "v_mfma_f32_16x16x32_bf16 v[68:71], a[204:207], v[160:163], v[68:71]\n\t"
    "ds_read_b64_tr_b16 a[238:239], v18 offset:18688\n\t"
    "v_cvt_pk_bf16_f32 v245, v86, v87\n\t"
    "v_mfma_f32_16x16x32_bf16 v[68:71], a[208:211], v[192:195], v[68:71]\n\t"
    "s_waitcnt lgkmcnt(6)\n\t"
    "v_exp_f32 v92, v92\n\t"
    "v_mfma_f32_16x16x32_bf16 v[68:71], a[212:215], v[224:227], v[68:71]\n\t"
    "s_mov_b32 s60, 18560\n\t"
    "v_add_f32 v43, v92, v43\n\t"
    "v_mfma_f32_16x16x32_bf16 v[76:79], a[192:195], v[72:75], v[108:111]\n\t"
    "v_exp_f32 v93, v93\n\t"
    "v_mfma_f32_16x16x32_bf16 v[76:79], a[196:199], v[104:107], v[76:79]\n\t"
    "buffer_load_dwordx4 v62, s[56:59], s60 offen offset:2048 nt lds\n\t"
    "v_mfma_f32_16x16x32_bf16 v[76:79], a[200:203], v[136:139], v[76:79]\n\t"
    "s_mov_b32 s60, 27776\n\t"
    "v_add_f32 v43, v93, v43\n\t"
    "v_cvt_pk_bf16_f32 v252, v92, v93\n\t"
    "v_mfma_f32_16x16x32_bf16 v[76:79], a[204:207], v[168:171], v[76:79]\n\t"
    "v_exp_f32 v94, v94\n\t"
    "v_mfma_f32_16x16x32_bf16 v[76:79], a[208:211], v[200:203], v[76:79]\n\t"
    "v_add_f32 v43, v94, v43\n\t"
    "v_mfma_f32_16x16x32_bf16 v[76:79], a[212:215], v[232:235], v[76:79]\n\t"
    "v_exp_f32 v95, v95\n\t"
    "v_mfma_f32_16x16x32_bf16 v[84:87], a[192:195], v[80:83], v[116:119]\n\t"
    "buffer_load_dwordx4 v62, s[56:59], s60 offen offset:3072 nt lds\n\t"
    "v_mfma_f32_16x16x32_bf16 v[84:87], a[196:199], v[112:115], v[84:87]\n\t"
    "s_add_u32 m0, s83, 122880\n\t"
    "v_add_f32 v43, v95, v43\n\t"
    "v_cvt_pk_bf16_f32 v253, v94, v95\n\t"
    "v_mfma_f32_16x16x32_bf16 v[84:87], a[200:203], v[144:147], v[84:87]\n\t"
    "s_add_u32 s60, s87, 16384\n\t"
    "v_exp_f32 v68, v68\n\t"
    "v_mfma_f32_16x16x32_bf16 v[84:87], a[204:207], v[176:179], v[84:87]\n\t"
    "s_bfe_u32 s60, s60, 0x2000c\n\t"
    "v_add_f32 v40, v68, v40\n\t"
    "v_mfma_f32_16x16x32_bf16 v[84:87], a[208:211], v[208:211], v[84:87]\n\t"
    "s_add_u32 s60, s60, 4\n\t"
    "v_exp_f32 v69, v69\n\t"
    "v_mfma_f32_16x16x32_bf16 v[84:87], a[212:215], v[240:243], v[84:87]\n\t"
    "s_mulk_i32 s60, 24576\n\t"
    "v_add_f32 v40, v69, v40\n\t"
    "v_cvt_pk_bf16_f32 v230, v68, v69\n\t"
    "v_mfma_f32_16x16x32_bf16 v[92:95], a[192:195], v[88:91], v[124:127]\n\t"
    "v_exp_f32 v70, v70\n\t"
    "v_mfma_f32_16x16x32_bf16 v[92:95], a[196:199], v[120:123], v[92:95]\n\t"
    "v_add_f32 v40, v70, v40\n\t"
    "v_mfma_f32_16x16x32_bf16 v[92:95], a[200:203], v[152:155], v[92:95]\n\t"
    "s_waitcnt lgkmcnt(0)\n\t"
    "s_barrier\n\t"
    "ds_read_b128 a[192:195], v13 offset:24576\n\t"
    "v_mfma_f32_16x16x32_bf16 v[92:95], a[204:207], v[184:187], v[92:95]\n\t"
    "v_exp_f32 v71, v71\n\t"
    "v_mfma_f32_16x16x32_bf16 v[92:95], a[208:211], v[216:219], v[92:95]\n\t"
    "ds_read_b128 a[196:199], v14 offset:24576\n\t"
    "v_add_f32 v40, v71, v40\n\t"
    "v_mfma_f32_16x16x32_bf16 v[92:95], a[212:215], v[248:251], v[92:95]\n\t"
    "ds_read_b128 a[200:203], v13 offset:24704\n\t"
    "v_cvt_pk_bf16_f32 v231, v70, v71\n\t"
    "v_mfma_f32_16x16x32_bf16 a[144:147], a[216:219], v[132:135], 0\n\t"
    "v_exp_f32 v76, v76\n\t"
    "v_mfma_f32_16x16x32_bf16 a[144:147], a[220:223], v[164:167], a[144:147]\n\t"
    "ds_read_b128 a[204:207], v14 offset:24704\n\t"
    "v_add_f32 v41, v76, v41\n\t"
    "v_mfma_f32_16x16x32_bf16 a[148:151], a[216:219], v[140:143], 0\n\t"
    "v_exp_f32 v77, v77\n\t"
    "v_mfma_f32_16x16x32_bf16 a[148:151], a[220:223], v[172:175], a[148:151]\n\t"
    "ds_read_b128 a[208:211], v13 offset:24832\n\t"
    "v_add_f32 v41, v77, v41\n\t"
    "v_mfma_f32_16x16x32_bf16 a[152:155], a[216:219], v[148:151], 0\n\t"
    "KFP7:\n\t"
    "s_cbranch_execz KFP11\n\t"
    "v_exp_f32 v78, v78\n\t"
    "v_mfma_f32_16x16x32_bf16 a[152:155], a[220:223], v[180:183], a[152:155]\n\t"
    "ds_read_b128 a[212:215], v14 offset:24832\n\t"
    "v_cvt_pk_bf16_f32 v238, v76, v77\n\t"
    "v_mfma_f32_16x16x32_bf16 a[156:159], a[216:219], v[156:159], 0\n\t"
    "ds_read_b64_tr_b16 a[216:217], v32 offset:0\n\t"
    "v_add_f32 v41, v78, v41\n\t"
    "v_mfma_f32_16x16x32_bf16 a[156:159], a[220:223], v[188:191], a[156:159]\n\t"
    "v_exp_f32 v79, v79\n\t"
    "v_mfma_f32_16x16x32_bf16 a[160:163], a[224:227], v[132:135], 0\n\t"
    "ds_read_b64_tr_b16 a[218:219], v32 offset:6144\n\t"
    "v_add_f32 v41, v79, v41\n\t"
    "v_mfma_f32_16x16x32_bf16 a[160:163], a[228:231], v[164:167], a[160:163]\n\t"
    "ds_read_b64_tr_b16 a[220:221], v32 offset:12288\n\t"
    "v_cvt_pk_bf16_f32 v239, v78, v79\n\t"
    "v_mfma_f32_16x16x32_bf16 a[164:167], a[224:227], v[140:143], 0\n\t"
    "v_exp_f32 v84, v84\n\t"
    "v_mfma_f32_16x16x32_bf16 a[164:167], a[228:231], v[172:175], a[164:167]\n\t"
    "ds_read_b64_tr_b16 a[222:223], v32 offset:18432\n\t"
    "v_add_f32 v42, v84, v42\n\t"
    "v_mfma_f32_16x16x32_bf16 a[168:171], a[224:227], v[148:151], 0\n\t"
    "v_exp_f32 v85, v85\n\t"
    "v_mfma_f32_16x16x32_bf16 a[168:171], a[228:231], v[180:183], a[168:171]\n\t"
    "v_add_f32 v42, v85, v42\n\t"
    "v_cvt_pk_bf16_f32 v246, v84, v85\n\t"
    "v_mfma_f32_16x16x32_bf16 a[172:175], a[224:227], v[156:159], 0\n\t"
    "v_exp_f32 v86, v86\n\t"
    "v_mfma_f32_16x16x32_bf16 a[172:175], a[228:231], v[188:191], a[172:175]\n\t"
    "ds_read_b64_tr_b16 a[224:225], v33 offset:0\n\t"
    "v_add_f32 v42, v86, v42\n\t"
    "v_mfma_f32_16x16x32_bf16 a[176:179], a[232:235], v[132:135], 0\n\t"
    "v_exp_f32 v87, v87\n\t"
    "v_mfma_f32_16x16x32_bf16 a[176:179], a[236:239], v[164:167], a[176:179]\n\t"
    "ds_read_b64_tr_b16 a[226:227], v33 offset:6144\n\t"
    "v_add_f32 v42, v87, v42\n\t"
    "v_mfma_f32_16x16x32_bf16 a[180:183], a[232:235], v[140:143], 0\n\t"
    "ds_read_b64_tr_b16 a[228:229], v33 offset:12288\n\t"
    "v_cvt_pk_bf16_f32 v247, v86, v87\n\t"
    "v_mfma_f32_16x16x32_bf16 a[180:183], a[236:239], v[172:175], a[180:183]\n\t"
    "v_exp_f32 v92, v92\n\t"
    "v_mfma_f32_16x16x32_bf16 a[184:187], a[232:235], v[148:151], 0\n\t"
    "ds_read_b64_tr_b16 a[230:231], v33 offset:18432\n\t"
    "v_add_f32 v43, v92, v43\n\t"
    "v_mfma_f32_16x16x32_bf16 a[184:187], a[236:239], v[180:183], a[184:187]\n\t"
    "v_exp_f32 v93, v93\n\t"
    "v_mfma_f32_16x16x32_bf16 a[188:191], a[232:235], v[156:159], 0\n\t"
    "ds_read_b64_tr_b16 a[232:233], v34 offset:0\n\t"
    "v_add_f32 v43, v93, v43\n\t"
    "v_mfma_f32_16x16x32_bf16 a[188:191], a[236:239], v[188:191], a[188:191]\n\t"
    "s_waitcnt lgkmcnt(5)\n\t"
    "v_exp_f32 v94, v94\n\t"
    "v_mfma_f32_16x16x32_bf16 v[68:71], a[192:195], v[64:67], v[100:103]\n\t"
    "ds_read_b64_tr_b16 a[234:235], v34 offset:6144\n\t"
    "v_cvt_pk_bf16_f32 v254, v92, v93\n\t"
    "v_mfma_f32_16x16x32_bf16 v[68:71], a[196:199], v[96:99], v[68:71]\n\t"
    "ds_read_b64_tr_b16 a[236:237], v34 offset:12288\n\t"
    "v_add_f32 v43, v94, v43\n\t"
    "v_mfma_f32_16x16x32_bf16 v[68:71], a[200:203], v[128:131], v[68:71]\n\t"
    "v_exp_f32 v95, v95\n\t"
    "v_mfma_f32_16x16x32_bf16 v[68:71], a[204:207], v[160:163], v[68:71]\n\t"
    "ds_read_b64_tr_b16 a[238:239], v34 offset:18432\n\t"
    "v_add_f32 v43, v95, v43\n\t"
    "v_mfma_f32_16x16x32_bf16 v[68:71], a[208:211], v[192:195], v[68:71]\n\t"
    "buffer_load_dwordx4 v8, s[48:51], s60 offen offset:0 lds\n\t"
    "v_mfma_f32_16x16x32_bf16 v[68:71], a[212:215], v[224:227], v[68:71]\n\t"
    "v_cvt_pk_bf16_f32 v255, v94, v95\n\t"
    "v_mfma_f32_16x16x32_bf16 v[76:79], a[192:195], v[72:75], v[108:111]\n\t"
    "v_mfma_f32_16x16x32_bf16 v[76:79], a[196:199], v[104:107], v[76:79]\n\t"
    "v_mfma_f32_16x16x32_bf16 v[76:79], a[200:203], v[136:139], v[76:79]\n\t"
    "s_nop 3\n\t"
    "v_exp_f32 v68, v68\n\t"
    "v_mfma_f32_16x16x32_bf16 v[76:79], a[204:207], v[168:171], v[76:79]\n\t"
    "v_exp_f32 v69, v69\n\t"
    "v_mfma_f32_16x16x32_bf16 v[76:79], a[208:211], v[200:203], v[76:79]\n\t"
    "v_add_f32 v40, v68, v40\n\t"
    "v_add_f32 v40, v69, v40\n\t"
    "v_mfma_f32_16x16x32_bf16 v[76:79], a[212:215], v[232:235], v[76:79]\n\t"
    "v_exp_f32 v70, v70\n\t"
    "v_mfma_f32_16x16x32_bf16 v[84:87], a[192:195], v[80:83], v[116:119]\n\t"
    "v_cvt_pk_bf16_f32 v132, v68, v69\n\t"
    "v_add_f32 v40, v70, v40\n\t"
    "v_mfma_f32_16x16x32_bf16 v[84:87], a[196:199], v[112:115], v[84:87]\n\t"
    "buffer_load_dwordx4 v9, s[48:51], s60 offen offset:1024 lds\n\t"
    "v_mfma_f32_16x16x32_bf16 v[84:87], a[200:203], v[144:147], v[84:87]\n\t"
    "v_exp_f32 v71, v71\n\t"
    "v_mfma_f32_16x16x32_bf16 v[84:87], a[204:207], v[176:179], v[84:87]\n\t"
    "v_add_f32 v40, v71, v40\n\t"
    "v_cvt_pk_bf16_f32 v133, v70, v71\n\t"
    "v_mfma_f32_16x16x32_bf16 v[84:87], a[208:211], v[208:211], v[84:87]\n\t"
    "v_exp_f32 v76, v76\n\t"
    "v_mfma_f32_16x16x32_bf16 v[84:87], a[212:215], v[240:243], v[84:87]\n\t"
    "v_exp_f32 v77, v77\n\t"
    "v_mfma_f32_16x16x32_bf16 v[92:95], a[192:195], v[88:91], v[124:127]\n\t"
    "buffer_load_dwordx4 v10, s[48:51], s60 offen offset:2048 lds\n\t"
    "v_mfma_f32_16x16x32_bf16 v[92:95], a[196:199], v[120:123], v[92:95]\n\t"
    "s_add_u32 m0, s83, 125952\n\t"
    "ds_read_b128 a[192:195], v13 offset:30720\n\t"
    "v_add_f32 v41, v76, v41\n\t"
    "v_mfma_f32_16x16x32_bf16 v[92:95], a[200:203], v[152:155], v[92:95]\n\t"
    "s_add_u32 s60, s60, 3072\n\t"
    "ds_read_b128 a[196:199], v14 offset:30720\n\t"
    "v_add_f32 v41, v77, v41\n\t"
    "v_mfma_f32_16x16x32_bf16 v[92:95], a[204:207], v[184:187], v[92:95]\n\t"
    "v_exp_f32 v78, v78\n\t"
    "v_mfma_f32_16x16x32_bf16 v[92:95], a[208:211], v[216:219], v[92:95]\n\t"
    "ds_read_b128 a[200:203], v13 offset:30848\n\t"
    "v_cvt_pk_bf16_f32 v140, v76, v77\n\t"
    "v_mfma_f32_16x16x32_bf16 v[92:95], a[212:215], v[248:251], v[92:95]\n\t"
    "ds_read_b128 a[204:207], v14 offset:30848\n\t"
    "v_add_f32 v41, v78, v41\n\t"
    "v_mfma_f32_16x16x32_bf16 a[0:3], a[216:219], v[196:199], a[0:3]\n\t"
    "v_exp_f32 v79, v79\n\t"
    "v_mfma_f32_16x16x32_bf16 a[0:3], a[220:223], v[228:231], a[0:3]\n\t"
    "ds_read_b128 a[208:211], v13 offset:30976\n\t"
    "v_add_f32 v41, v79, v41\n\t"
    "v_mfma_f32_16x16x32_bf16 a[4:7], a[216:219], v[204:207], a[4:7]\n\t"
    "ds_read_b128 a[212:215], v14 offset:30976\n\t"
    "v_cvt_pk_bf16_f32 v141, v78, v79\n\t"
    "v_mfma_f32_16x16x32_bf16 a[4:7], a[220:223], v[236:239], a[4:7]\n\t"
    "v_exp_f32 v84, v84\n\t"
    "v_mfma_f32_16x16x32_bf16 a[8:11], a[216:219], v[212:215], a[8:11]\n\t"
    "v_add_f32 v42, v84, v42\n\t"
    "s_waitcnt vmcnt(10)\n\t"
    "v_add_f32 v20, v44, v48\n\t"
    "v_mfma_f32_16x16x32_bf16 a[8:11], a[220:223], v[244:247], a[8:11]\n\t"
    "v_exp_f32 v85, v85\n\t"
    "v_mfma_f32_16x16x32_bf16 a[12:15], a[216:219], v[220:223], a[12:15]\n\t"
    "ds_read_b64_tr_b16 a[216:217], v35 offset:0\n\t"
    "v_add_f32 v42, v85, v42\n\t"
    "v_mfma_f32_16x16x32_bf16 a[12:15], a[220:223], v[252:255], a[12:15]\n\t"
    "s_waitcnt lgkmcnt(6)\n\t"
    "v_exp_f32 v86, v86\n\t"
    "v_mfma_f32_16x16x32_bf16 a[16:19], a[224:227], v[196:199], a[16:19]\n\t"
    "ds_read_b64_tr_b16 a[218:219], v35 offset:6144\n\t"
    "v_cvt_pk_bf16_f32 v148, v84, v85\n\t"
    "v_mfma_f32_16x16x32_bf16 a[16:19], a[228:231], v[228:231], a[16:19]\n\t"
    "buffer_load_dwordx4 v8, s[48:51], s60 offen offset:0 lds\n\t"
    "v_mfma_f32_16x16x32_bf16 a[20:23], a[224:227], v[204:207], a[20:23]\n\t"
    "ds_read_b64_tr_b16 a[220:221], v35 offset:12288\n\t"
    "v_add_f32 v42, v86, v42\n\t"
    "v_mfma_f32_16x16x32_bf16 a[20:23], a[228:231], v[236:239], a[20:23]\n\t"
    "v_exp_f32 v87, v87\n\t"
    "v_mfma_f32_16x16x32_bf16 a[24:27], a[224:227], v[212:215], a[24:27]\n\t"
    "ds_read_b64_tr_b16 a[222:223], v35 offset:18432\n\t"
    "v_add_f32 v42, v87, v42\n\t"
    "v_mfma_f32_16x16x32_bf16 a[24:27], a[228:231], v[244:247], a[24:27]\n\t"
    "v_cvt_pk_bf16_f32 v149, v86, v87\n\t"
    "v_add_f32 v21, v45, v49\n\t"
    "v_mfma_f32_16x16x32_bf16 a[28:31], a[224:227], v[220:223], a[28:31]\n\t"
    "buffer_load_dwordx4 v9, s[48:51], s60 offen offset:1024 lds\n\t"
    "v_mfma_f32_16x16x32_bf16 a[28:31], a[228:231], v[252:255], a[28:31]\n\t"
    "v_exp_f32 v92, v92\n\t"
    "v_mfma_f32_16x16x32_bf16 a[32:35], a[232:235], v[196:199], a[32:35]\n\t"
    "ds_read_b64_tr_b16 a[224:225], v32 offset:128\n\t"
    "v_add_f32 v43, v92, v43\n\t"
    "v_mfma_f32_16x16x32_bf16 a[32:35], a[236:239], v[228:231], a[32:35]\n\t"
    "v_exp_f32 v93, v93\n\t"
    "v_mfma_f32_16x16x32_bf16 a[36:39], a[232:235], v[204:207], a[36:39]\n\t"
    "ds_read_b64_tr_b16 a[226:227], v32 offset:6272\n\t"
    "v_add_f32 v43, v93, v43\n\t"
    "v_mfma_f32_16x16x32_bf16 a[36:39], a[236:239], v[236:239], a[36:39]\n\t"
    "buffer_load_dwordx4 v10, s[48:51], s60 offen offset:2048 lds\n\t"
    "v_mfma_f32_16x16x32_bf16 a[40:43], a[232:235], v[212:215], a[40:43]\n\t"
    "s_add_u32 m0, s83, 0\n\t"
    "v_exp_f32 v94, v94\n\t"
    "v_mfma_f32_16x16x32_bf16 a[40:43], a[236:239], v[244:247], a[40:43]\n\t"
    "s_add_u32 s60, s87, 12288\n\t"
    "ds_read_b64_tr_b16 a[228:229], v32 offset:12416\n\t"
    "v_cvt_pk_bf16_f32 v156, v92, v93\n\t"
    "v_mfma_f32_16x16x32_bf16 a[44:47], a[232:235], v[220:223], a[44:47]\n\t"
    "s_bfe_u32 s60, s60, 0x2000c\n\t"
    "ds_read_b64_tr_b16 a[230:231], v32 offset:18560\n\t"
    "v_add_f32 v43, v94, v43\n\t"
    "v_mfma_f32_16x16x32_bf16 a[44:47], a[236:239], v[252:255], a[44:47]\n\t"
    "v_exp_f32 v95, v95\n\t"
    "v_mfma_f32_16x16x32_bf16 v[68:71], a[192:195], v[64:67], v[100:103]\n\t"
    "s_waitcnt lgkmcnt(4)\n\t"
    "ds_read_b64_tr_b16 a[232:233], v33 offset:128\n\t"
    "v_add_f32 v43, v95, v43\n\t"
    "v_mfma_f32_16x16x32_bf16 v[68:71], a[196:199], v[96:99], v[68:71]\n\t"
    "ds_read_b64_tr_b16 a[234:235], v33 offset:6272\n\t"
    "v_add_f32 v22, v46, v50\n\t"
    "v_mfma_f32_16x16x32_bf16 v[68:71], a[200:203], v[128:131], v[68:71]\n\t"
    "ds_read_b64_tr_b16 a[236:237], v33 offset:12416\n\t"
    "v_add_f32 v23, v47, v51\n\t"
    "v_mfma_f32_16x16x32_bf16 v[68:71], a[204:207], v[160:163], v[68:71]\n\t"
    "ds_read_b64_tr_b16 a[238:239], v33 offset:18560\n\t"
    "v_cvt_pk_bf16_f32 v157, v94, v95\n\t"
    "v_mfma_f32_16x16x32_bf16 v[68:71], a[208:211], v[192:195], v[68:71]\n\t"
    "s_waitcnt vmcnt(10)\n\t"
    "v_add_f32 v20, v20, v52\n\t"
    "v_add_f32 v21, v21, v53\n\t"
    "v_mfma_f32_16x16x32_bf16 v[68:71], a[212:215], v[224:227], v[68:71]\n\t"
    "s_mulk_i32 s60, 24576\n\t"
    "v_add_f32 v22, v22, v54\n\t"
    "v_add_f32 v23, v23, v55\n\t"
    "v_mfma_f32_16x16x32_bf16 v[76:79], a[192:195], v[72:75], v[108:111]\n\t"
    "v_add_f32 v20, v20, v56\n\t"
    "v_add_f32 v21, v21, v57\n\t"
    "v_mfma_f32_16x16x32_bf16 v[76:79], a[196:199], v[104:107], v[76:79]\n\t"
    "v_add_f32 v22, v22, v58\n\t"
    "v_add_f32 v23, v23, v59\n\t"
    "v_mfma_f32_16x16x32_bf16 v[76:79], a[200:203], v[136:139], v[76:79]\n\t"
    "v_exp_f32 v68, v68\n\t"
    "v_mfma_f32_16x16x32_bf16 v[76:79], a[204:207], v[168:171], v[76:79]\n\t"
    "v_exp_f32 v69, v69\n\t"
    "v_mfma_f32_16x16x32_bf16 v[76:79], a[208:211], v[200:203], v[76:79]\n\t"
    "v_add_f32 v40, v68, v40\n\t"
    "v_add_f32 v40, v69, v40\n\t"
    "v_mfma_f32_16x16x32_bf16 v[76:79], a[212:215], v[232:235], v[76:79]\n\t"
    "v_exp_f32 v70, v70\n\t"
    "v_mfma_f32_16x16x32_bf16 v[84:87], a[192:195], v[80:83], v[116:119]\n\t"
    "s_waitcnt vmcnt(6)\n\t"
    "ds_read_b64_tr_b16 a[240:241], v63 offset:0\n\t"
    "v_cvt_pk_bf16_f32 v134, v68, v69\n\t"
    "v_mfma_f32_16x16x32_bf16 v[84:87], a[196:199], v[112:115], v[84:87]\n\t"
    "ds_read_b64_tr_b16 a[242:243], v63 offset:2048\n\t"
    "v_add_f32 v40, v70, v40\n\t"
    "v_mfma_f32_16x16x32_bf16 v[84:87], a[200:203], v[144:147], v[84:87]\n\t"
    "buffer_load_dwordx4 v8, s[52:55], s60 offen offset:0 lds\n\t"
    "v_mfma_f32_16x16x32_bf16 v[84:87], a[204:207], v[176:179], v[84:87]\n\t"
    "v_exp_f32 v71, v71\n\t"
    "v_mfma_f32_16x16x32_bf16 v[84:87], a[208:211], v[208:211], v[84:87]\n\t"
    "ds_read_b64_tr_b16 a[244:245], v24 offset:0\n\t"
    "v_add_f32 v40, v71, v40\n\t"
    "v_mfma_f32_16x16x32_bf16 v[84:87], a[212:215], v[240:243], v[84:87]\n\t"
    "ds_read_b64_tr_b16 a[246:247], v24 offset:2048\n\t"
    "v_cvt_pk_bf16_f32 v135, v70, v71\n\t"
    "v_mfma_f32_16x16x32_bf16 v[92:95], a[192:195], v[88:91], v[124:127]\n\t"
    "v_exp_f32 v76, v76\n\t"
    "v_mfma_f32_16x16x32_bf16 v[92:95], a[196:199], v[120:123], v[92:95]\n\t"
    "ds_read_b64_tr_b16 a[248:249], v25 offset:0\n\t"
    "v_add_f32 v41, v76, v41\n\t"
    "v_mfma_f32_16x16x32_bf16 v[92:95], a[200:203], v[152:155], v[92:95]\n\t"
    "buffer_load_dwordx4 v9, s[52:55], s60 offen offset:1024 lds\n\t"
    "v_mfma_f32_16x16x32_bf16 v[92:95], a[204:207], v[184:187], v[92:95]\n\t"
    "ds_read_b64_tr_b16 a[250:251], v25 offset:2048\n\t"
    "v_mfma_f32_16x16x32_bf16 v[92:95], a[208:211], v[216:219], v[92:95]\n\t"
    "v_exp_f32 v77, v77\n\t"
    "v_mfma_f32_16x16x32_bf16 v[92:95], a[212:215], v[248:251], v[92:95]\n\t"
    "ds_read_b128 a[192:195], v13 offset:36864\n\t"
    "v_add_f32 v41, v77, v41\n\t"
    "v_mfma_f32_16x16x32_bf16 a[48:51], a[216:219], v[196:199], a[48:51]\n\t"
    "ds_read_b64_tr_b16 a[252:253], v26 offset:0\n\t"
    "v_cvt_pk_bf16_f32 v142, v76, v77\n\t"
    "v_mfma_f32_16x16x32_bf16 a[48:51], a[220:223], v[228:231], a[48:51]\n\t"
    "ds_read_b64_tr_b16 a[254:255], v26 offset:2048\n\t"
    "v_mfma_f32_16x16x32_bf16 a[52:55], a[216:219], v[204:207], a[52:55]\n\t"
    "v_exp_f32 v78, v78\n\t"
    "v_mfma_f32_16x16x32_bf16 a[52:55], a[220:223], v[236:239], a[52:55]\n\t"
    "ds_read_b128 a[196:199], v14 offset:36864\n\t"
    "v_add_f32 v41, v78, v41\n\t"
    "v_mfma_f32_16x16x32_bf16 a[56:59], a[216:219], v[212:215], a[56:59]\n\t"
    "buffer_load_dwordx4 v10, s[52:55], s60 offen offset:2048 lds\n\t"
    "v_mfma_f32_16x16x32_bf16 a[56:59], a[220:223], v[244:247], a[56:59]\n\t"
    "s_add_u32 m0, s83, 3072\n\t"
    "ds_read_b128 a[200:203], v13 offset:36992\n\t"
    "v_mfma_f32_16x16x32_bf16 a[60:63], a[216:219], v[220:223], a[60:63]\n\t"
    "s_add_u32 s60, s60, 3072\n\t"
    "v_exp_f32 v79, v79\n\t"
    "v_mfma_f32_16x16x32_bf16 a[60:63], a[220:223], v[252:255], a[60:63]\n\t"
    "s_waitcnt lgkmcnt(7)\n\t"
    "ds_read_b128 a[204:207], v14 offset:36992\n\t"
    "v_add_f32 v41, v79, v41\n\t"
    "v_mfma_f32_16x16x32_bf16 a[64:67], a[224:227], v[196:199], a[64:67]\n\t"
    "ds_read_b128 a[208:211], v13 offset:37120\n\t"
    "v_cvt_pk_bf16_f32 v143, v78, v79\n\t"
    "v_mfma_f32_16x16x32_bf16 a[64:67], a[228:231], v[228:231], a[64:67]\n\t"
    "buffer_store_dwordx4 a[240:243], v4, s[40:43], s63 offen offset:0 sc1\n\t"
    "v_mfma_f32_16x16x32_bf16 a[68:71], a[224:227], v[204:207], a[68:71]\n\t"
    "s_bfe_u32 s63, s79, 0x80008\n\t"
    "v_exp_f32 v84, v84\n\t"
    "v_mfma_f32_16x16x32_bf16 a[68:71], a[228:231], v[236:239], a[68:71]\n\t"
    "s_lshl_b32 s63, s63, 10\n\t"
    "ds_read_b128 a[212:215], v14 offset:37120\n\t"
    "v_add_f32 v42, v84, v42\n\t"
    "v_mfma_f32_16x16x32_bf16 a[72:75], a[224:227], v[212:215], a[72:75]\n\t"
    "s_add_u32 s63, s63, s88\n\t"
    "v_exp_f32 v85, v85\n\t"
    "v_mfma_f32_16x16x32_bf16 a[72:75], a[228:231], v[244:247], a[72:75]\n\t"
    "ds_read_b64_tr_b16 a[216:217], v34 offset:128\n\t"
    "v_add_f32 v42, v85, v42\n\t"
    "v_mfma_f32_16x16x32_bf16 a[76:79], a[224:227], v[220:223], a[76:79]\n\t"
    "buffer_store_dwordx4 a[244:247], v4, s[40:43], s63 offen offset:0 sc1\n\t"
    "v_mfma_f32_16x16x32_bf16 a[76:79], a[228:231], v[252:255], a[76:79]\n\t"
    "s_bfe_u32 s63, s79, 0x80010\n\t"
    "v_cvt_pk_bf16_f32 v150, v84, v85\n\t"
    "v_mfma_f32_16x16x32_bf16 a[80:83], a[232:235], v[196:199], a[80:83]\n\t"
    "s_lshl_b32 s63, s63, 10\n\t"
    "ds_read_b64_tr_b16 a[218:219], v34 offset:6272\n\t"
    "v_mfma_f32_16x16x32_bf16 a[80:83], a[236:239], v[228:231], a[80:83]\n\t"
    "s_add_u32 s63, s63, s88\n\t"
    "v_exp_f32 v86, v86\n\t"
    "v_mfma_f32_16x16x32_bf16 a[84:87], a[232:235], v[204:207], a[84:87]\n\t"
    "ds_read_b64_tr_b16 a[220:221], v34 offset:12416\n\t"
    "v_add_f32 v42, v86, v42\n\t"
    "v_mfma_f32_16x16x32_bf16 a[84:87], a[236:239], v[236:239], a[84:87]\n\t"
    "s_waitcnt lgkmcnt(4)\n\t"
    "buffer_store_dwordx4 a[248:251], v4, s[40:43], s63 offen offset:0 sc1\n\t"
    "v_mfma_f32_16x16x32_bf16 a[88:91], a[232:235], v[212:215], a[88:91]\n\t"
    "s_bfe_u32 s63, s79, 0x80018\n\t"
    "v_exp_f32 v87, v87\n\t"
    "v_mfma_f32_16x16x32_bf16 a[88:91], a[236:239], v[244:247], a[88:91]\n\t"
    "s_lshl_b32 s63, s63, 10\n\t"
    "ds_read_b64_tr_b16 a[222:223], v34 offset:18560\n\t"
    "v_add_f32 v42, v87, v42\n\t"
    "v_mfma_f32_16x16x32_bf16 a[92:95], a[232:235], v[220:223], a[92:95]\n\t"
    "s_add_u32 s63, s63, s88\n\t"
    "ds_read_b64_tr_b16 a[224:225], v35 offset:128\n\t"
    "v_cvt_pk_bf16_f32 v151, v86, v87\n\t"
    "v_mfma_f32_16x16x32_bf16 a[92:95], a[236:239], v[252:255], a[92:95]\n\t"
    "v_exp_f32 v92, v92\n\t"
    "v_mfma_f32_16x16x32_bf16 v[68:71], a[192:195], v[64:67], v[100:103]\n\t"
    "buffer_store_dwordx4 a[252:255], v4, s[40:43], s63 offen offset:0 sc1\n\t"
    "v_mfma_f32_16x16x32_bf16 v[68:71], a[196:199], v[96:99], v[68:71]\n\t"
    "s_add_u32 s63, s91, 196608\n\t"
    "v_add_f32 v43, v92, v43\n\t"
    "v_mfma_f32_16x16x32_bf16 v[68:71], a[200:203], v[128:131], v[68:71]\n\t"
    "v_exp_f32 v93, v93\n\t"
    "v_mfma_f32_16x16x32_bf16 v[68:71], a[204:207], v[160:163], v[68:71]\n\t"
    "ds_read_b64_tr_b16 a[226:227], v35 offset:6272\n\t"
    "v_add_f32 v43, v93, v43\n\t"
    "v_mfma_f32_16x16x32_bf16 v[68:71], a[208:211], v[192:195], v[68:71]\n\t"
    "s_waitcnt lgkmcnt(4)\n\t"
    "v_exp_f32 v94, v94\n\t"
    "v_mfma_f32_16x16x32_bf16 v[68:71], a[212:215], v[224:227], v[68:71]\n\t"
    "buffer_load_dwordx4 v[44:47], v27, s[40:43], s63 offen offset:0 sc1\n\t"
    "v_mfma_f32_16x16x32_bf16 v[76:79], a[192:195], v[72:75], v[108:111]\n\t"
    "s_add_u32 s63, s91, 245760\n\t"
    "ds_read_b64_tr_b16 a[228:229], v35 offset:12416\n\t"
    "v_cvt_pk_bf16_f32 v158, v92, v93\n\t"
    "v_mfma_f32_16x16x32_bf16 v[76:79], a[196:199], v[104:107], v[76:79]\n\t"
    "ds_read_b64_tr_b16 a[230:231], v35 offset:18560\n\t"
    "v_add_f32 v43, v94, v43\n\t"
    "v_mfma_f32_16x16x32_bf16 v[76:79], a[200:203], v[136:139], v[76:79]\n\t"
    "v_exp_f32 v95, v95\n\t"
    "v_mfma_f32_16x16x32_bf16 v[76:79], a[204:207], v[168:171], v[76:79]\n\t"
    "ds_read_b64_tr_b16 a[232:233], v32 offset:256\n\t"
    "v_add_f32 v43, v95, v43\n\t"
    "v_mfma_f32_16x16x32_bf16 v[76:79], a[208:211], v[200:203], v[76:79]\n\t"
    "buffer_load_dwordx4 v[48:51], v27, s[40:43], s63 offen offset:0 sc1\n\t"
    "v_mfma_f32_16x16x32_bf16 v[76:79], a[212:215], v[232:235], v[76:79]\n\t"
    "s_add_u32 s63, s91, 294912\n\t"
    "ds_read_b64_tr_b16 a[234:235], v32 offset:6400\n\t"
    "v_cvt_pk_bf16_f32 v159, v94, v95\n\t"
    "v_mfma_f32_16x16x32_bf16 v[84:87], a[192:195], v[80:83], v[116:119]\n\t"
    "v_exp_f32 v68, v68\n\t"
    "v_mfma_f32_16x16x32_bf16 v[84:87], a[196:199], v[112:115], v[84:87]\n\t"
    "ds_read_b64_tr_b16 a[236:237], v32 offset:12544\n\t"
    "v_add_f32 v40, v68, v40\n\t"
    "v_mfma_f32_16x16x32_bf16 v[84:87], a[200:203], v[144:147], v[84:87]\n\t"
    "v_exp_f32 v69, v69\n\t"
    "v_mfma_f32_16x16x32_bf16 v[84:87], a[204:207], v[176:179], v[84:87]\n\t"
    "buffer_load_dwordx4 v[52:55], v27, s[40:43], s63 offen offset:0 sc1\n\t"
    "v_mfma_f32_16x16x32_bf16 v[84:87], a[208:211], v[208:211], v[84:87]\n\t"
    "s_add_u32 s63, s91, 344064\n\t"
    "ds_read_b64_tr_b16 a[238:239], v32 offset:18688\n\t"
    "v_add_f32 v40, v69, v40\n\t"
    "v_mfma_f32_16x16x32_bf16 v[84:87], a[212:215], v[240:243], v[84:87]\n\t"
    "v_exp_f32 v70, v70\n\t"
    "v_mfma_f32_16x16x32_bf16 v[92:95], a[192:195], v[88:91], v[124:127]\n\t"
    "ds_read_b128 a[192:195], v13 offset:43008\n\t"
    "v_cvt_pk_bf16_f32 v164, v68, v69\n\t"
    "v_mfma_f32_16x16x32_bf16 v[92:95], a[196:199], v[120:123], v[92:95]\n\t"
    "ds_read_b128 a[196:199], v14 offset:43008\n\t"
    "v_add_f32 v40, v70, v40\n\t"
    "v_mfma_f32_16x16x32_bf16 v[92:95], a[200:203], v[152:155], v[92:95]\n\t"
    "buffer_load_dwordx4 v8, s[52:55], s60 offen offset:0 lds\n\t"
    "v_mfma_f32_16x16x32_bf16 v[92:95], a[204:207], v[184:187], v[92:95]\n\t"
    "v_exp_f32 v71, v71\n\t"
    "v_mfma_f32_16x16x32_bf16 v[92:95], a[208:211], v[216:219], v[92:95]\n\t"
    "ds_read_b128 a[200:203], v13 offset:43136\n\t"
    "v_add_f32 v40, v71, v40\n\t"
    "v_mfma_f32_16x16x32_bf16 v[92:95], a[212:215], v[248:251], v[92:95]\n\t"
    "ds_read_b128 a[204:207], v14 offset:43136\n\t"
    "v_cvt_pk_bf16_f32 v165, v70, v71\n\t"
    "v_mfma_f32_16x16x32_bf16 a[96:99], a[216:219], v[196:199], a[96:99]\n\t"
    "s_waitcnt lgkmcnt(6)\n\t"
    "v_exp_f32 v76, v76\n\t"
    "v_mfma_f32_16x16x32_bf16 a[96:99], a[220:223], v[228:231], a[96:99]\n\t"
    "buffer_load_dwordx4 v[56:59], v27, s[40:43], s63 offen offset:0 sc1\n\t"
    "v_mfma_f32_16x16x32_bf16 a[100:103], a[216:219], v[204:207], a[100:103]\n\t"
    "s_add_u32 s63, s91, 393216\n\t"
    "ds_read_b128 a[208:211], v13 offset:43264\n\t"
    "v_add_f32 v41, v76, v41\n\t"
    "v_mfma_f32_16x16x32_bf16 a[100:103], a[220:223], v[236:239], a[100:103]\n\t"
    "v_exp_f32 v77, v77\n\t"
    "v_mfma_f32_16x16x32_bf16 a[104:107], a[216:219], v[212:215], a[104:107]\n\t"
    "ds_read_b128 a[212:215], v14 offset:43264\n\t"
    "v_add_f32 v41, v77, v41\n\t"
    "v_mfma_f32_16x16x32_bf16 a[104:107], a[220:223], v[244:247], a[104:107]\n\t"
    "v_exp_f32 v78, v78\n\t"
    "v_mfma_f32_16x16x32_bf16 a[108:111], a[216:219], v[220:223], a[108:111]\n\t"
    "buffer_load_dwordx4 v9, s[52:55], s60 offen offset:1024 lds\n\t"
    "v_mfma_f32_16x16x32_bf16 a[108:111], a[220:223], v[252:255], a[108:111]\n\t"
    "ds_read_b64_tr_b16 a[216:217], v33 offset:256\n\t"
    "v_cvt_pk_bf16_f32 v172, v76, v77\n\t"
    "v_mfma_f32_16x16x32_bf16 a[112:115], a[224:227], v[196:199], a[112:115]\n\t"
    "ds_read_b64_tr_b16 a[218:219], v33 offset:6400\n\t"
    "v_add_f32 v41, v78, v41\n\t"
    "v_mfma_f32_16x16x32_bf16 a[112:115], a[228:231], v[228:231], a[112:115]\n\t"
    "v_exp_f32 v79, v79\n\t"
    "v_mfma_f32_16x16x32_bf16 a[116:119], a[224:227], v[204:207], a[116:119]\n\t"
    "ds_read_b64_tr_b16 a[220:221], v33 offset:12544\n\t"
    "v_add_f32 v41, v79, v41\n\t"
    "v_mfma_f32_16x16x32_bf16 a[116:119], a[228:231], v[236:239], a[116:119]\n\t"
    "buffer_load_dwordx4 v10, s[52:55], s60 offen offset:2048 lds\n\t"
    "v_mfma_f32_16x16x32_bf16 a[120:123], a[224:227], v[212:215], a[120:123]\n\t"
    "s_mov_b64 s[56:57], s[84:85]\n\t"
    "ds_read_b64_tr_b16 a[222:223], v33 offset:18688\n\t"
    "v_cvt_pk_bf16_f32 v173, v78, v79\n\t"
    "v_mfma_f32_16x16x32_bf16 a[120:123], a[228:231], v[244:247], a[120:123]\n\t"
    "s_mov_b32 s58, 40960\n\t"
    "v_exp_f32 v84, v84\n\t"
    "v_mfma_f32_16x16x32_bf16 a[124:127], a[224:227], v[220:223], a[124:127]\n\t"
    "s_mov_b32 m0, s86\n\t"
    "ds_read_b64_tr_b16 a[224:225], v34 offset:256\n\t"
    "v_add_f32 v42, v84, v42\n\t"
    "v_mfma_f32_16x16x32_bf16 a[124:127], a[228:231], v[252:255], a[124:127]\n\t"
    "v_exp_f32 v85, v85\n\t"
    "v_mfma_f32_16x16x32_bf16 a[128:131], a[232:235], v[196:199], a[128:131]\n\t"
    "s_waitcnt lgkmcnt(5)\n\t"
    "ds_read_b64_tr_b16 a[226:227], v34 offset:6400\n\t"
    "v_add_f32 v42, v85, v42\n\t"
    "v_mfma_f32_16x16x32_bf16 a[128:131], a[236:239], v[228:231], a[128:131]\n\t"
    "s_mov_b32 s60, 0\n\t"
    "v_exp_f32 v86, v86\n\t"
    "v_mfma_f32_16x16x32_bf16 a[132:135], a[232:235], v[204:207], a[132:135]\n\t"
    "ds_read_b64_tr_b16 a[228:229], v34 offset:12544\n\t"
    "v_cvt_pk_bf16_f32 v180, v84, v85\n\t"
    "v_mfma_f32_16x16x32_bf16 a[132:135], a[236:239], v[236:239], a[132:135]\n\t"
    "ds_read_b64_tr_b16 a[230:231], v34 offset:18688\n\t"
    "v_add_f32 v42, v86, v42\n\t"
    "v_mfma_f32_16x16x32_bf16 a[136:139], a[232:235], v[212:215], a[136:139]\n\t"
    "v_exp_f32 v87, v87\n\t"
    "v_mfma_f32_16x16x32_bf16 a[136:139], a[236:239], v[244:247], a[136:139]\n\t"
    "v_add_f32 v42, v87, v42\n\t"
    "v_cvt_pk_bf16_f32 v181, v86, v87\n\t"
    "v_mfma_f32_16x16x32_bf16 a[140:143], a[232:235], v[220:223], a[140:143]\n\t"
    "v_exp_f32 v92, v92\n\t"
    "v_mfma_f32_16x16x32_bf16 a[140:143], a[236:239], v[252:255], a[140:143]\n\t"
    "ds_read_b64_tr_b16 a[232:233], v35 offset:256\n\t"
    "v_add_f32 v43, v92, v43\n\t"
    "v_mfma_f32_16x16x32_bf16 v[68:71], a[192:195], v[64:67], v[100:103]\n\t"
    "v_exp_f32 v93, v93\n\t"
    "v_mfma_f32_16x16x32_bf16 v[68:71], a[196:199], v[96:99], v[68:71]\n\t"
    "ds_read_b64_tr_b16 a[234:235], v35 offset:6400\n\t"
    "v_add_f32 v43, v93, v43\n\t"
    "v_mfma_f32_16x16x32_bf16 v[68:71], a[200:203], v[128:131], v[68:71]\n\t"
    "v_exp_f32 v94, v94\n\t"
    "v_mfma_f32_16x16x32_bf16 v[68:71], a[204:207], v[160:163], v[68:71]\n\t"
    "ds_read_b64_tr_b16 a[236:237], v35 offset:12544\n\t"
    "v_cvt_pk_bf16_f32 v188, v92, v93\n\t"
    "v_mfma_f32_16x16x32_bf16 v[68:71], a[208:211], v[192:195], v[68:71]\n\t"
    "buffer_load_dwordx4 v62, s[56:59], s60 offen offset:0 nt lds\n\t"
    "v_mfma_f32_16x16x32_bf16 v[68:71], a[212:215], v[224:227], v[68:71]\n\t"
    "s_mov_b32 s60, 9216\n\t"
    "ds_read_b64_tr_b16 a[238:239], v35 offset:18688\n\t"
    "v_add_f32 v43, v94, v43\n\t"
    "v_mfma_f32_16x16x32_bf16 v[76:79], a[192:195], v[72:75], v[108:111]\n\t"
    "v_exp_f32 v95, v95\n\t"
    "v_mfma_f32_16x16x32_bf16 v[76:79], a[196:199], v[104:107], v[76:79]\n\t"
    "v_add_f32 v43, v95, v43\n\t"
    "v_cvt_pk_bf16_f32 v189, v94, v95\n\t"
    "v_mfma_f32_16x16x32_bf16 v[76:79], a[200:203], v[136:139], v[76:79]\n\t"
    "v_exp_f32 v68, v68\n\t"
    "v_mfma_f32_16x16x32_bf16 v[76:79], a[204:207], v[168:171], v[76:79]\n\t"
    "buffer_load_dwordx4 v62, s[56:59], s60 offen offset:1024 nt lds\n\t"
    "v_mfma_f32_16x16x32_bf16 v[76:79], a[208:211], v[200:203], v[76:79]\n\t"
    "s_mov_b32 s60, 18432\n\t"
    "v_add_f32 v40, v68, v40\n\t"
    "v_mfma_f32_16x16x32_bf16 v[76:79], a[212:215], v[232:235], v[76:79]\n\t"
    "v_exp_f32 v69, v69\n\t"
    "v_mfma_f32_16x16x32_bf16 v[84:87], a[192:195], v[80:83], v[116:119]\n\t"
    "v_add_f32 v40, v69, v40\n\t"
    "v_cvt_pk_bf16_f32 v166, v68, v69\n\t"
    "v_mfma_f32_16x16x32_bf16 v[84:87], a[196:199], v[112:115], v[84:87]\n\t"
    "v_exp_f32 v70, v70\n\t"
    "v_mfma_f32_16x16x32_bf16 v[84:87], a[200:203], v[144:147], v[84:87]\n\t"
    "buffer_load_dwordx4 v62, s[56:59], s60 offen offset:2048 nt lds\n\t"
    "v_mfma_f32_16x16x32_bf16 v[84:87], a[204:207], v[176:179], v[84:87]\n\t"
    "s_mov_b32 s60, 27648\n\t"
    "v_exp_f32 v71, v71\n\t"
    "v_mfma_f32_16x16x32_bf16 v[84:87], a[208:211], v[208:211], v[84:87]\n\t"
    "v_add_f32 v40, v70, v40\n\t"
    "v_add_f32 v40, v71, v40\n\t"
    "v_mfma_f32_16x16x32_bf16 v[84:87], a[212:215], v[240:243], v[84:87]\n\t"
    "v_exp_f32 v76, v76\n\t"
    "v_mfma_f32_16x16x32_bf16 v[92:95], a[192:195], v[88:91], v[124:127]\n\t"
    "v_cvt_pk_bf16_f32 v167, v70, v71\n\t"
    "v_add_f32 v41, v76, v41\n\t"
    "v_mfma_f32_16x16x32_bf16 v[92:95], a[196:199], v[120:123], v[92:95]\n\t"
    "buffer_load_dwordx4 v62, s[56:59], s60 offen offset:3072 nt lds\n\t"
    "v_mfma_f32_16x16x32_bf16 v[92:95], a[200:203], v[152:155], v[92:95]\n\t"
    "s_waitcnt vmcnt(11) lgkmcnt(0)\n\t"
    "s_barrier\n\t"
    "ds_read_b128 a[192:195], v28 offset:24576\n\t"
    "v_mfma_f32_16x16x32_bf16 v[92:95], a[204:207], v[184:187], v[92:95]\n\t"
    "s_add_u32 m0, s83, 24576\n\t"
    "v_exp_f32 v77, v77\n\t"
    "v_mfma_f32_16x16x32_bf16 v[92:95], a[208:211], v[216:219], v[92:95]\n\t"
    "s_add_u32 s60, s87, 20480\n\t"
    "ds_read_b128 a[196:199], v29 offset:24576\n\t"
    "v_add_f32 v41, v77, v41\n\t"
    "v_mfma_f32_16x16x32_bf16 v[92:95], a[212:215], v[248:251], v[92:95]\n\t"
    "v_exp_f32 v78, v78\n\t"
    "v_mfma_f32_16x16x32_bf16 a[144:147], a[216:219], v[196:199], a[144:147]\n\t"
    "s_add_u32 s65, s87, 131072\n\t"
    "buffer_store_dword v60, v2, s[40:43], s65 offen sc1\n\t"
    "v_mfma_f32_16x16x32_bf16 a[144:147], a[220:223], v[228:231], a[144:147]\n\t"
    "s_bfe_u32 s60, s60, 0x2000c\n\t"
    "ds_read_b128 a[200:203], v28 offset:24704\n\t"
    "v_cvt_pk_bf16_f32 v174, v76, v77\n\t"
    "v_mfma_f32_16x16x32_bf16 a[148:151], a[216:219], v[204:207], a[148:151]\n\t"
    "s_add_u32 s60, s60, 4\n\t"
    "ds_read_b128 a[204:207], v29 offset:24704\n\t"
    "v_add_f32 v41, v78, v41\n\t"
    "v_mfma_f32_16x16x32_bf16 a[148:151], a[220:223], v[236:239], a[148:151]\n\t"
    "s_mulk_i32 s60, 24576\n\t"
    "v_exp_f32 v79, v79\n\t"
    "v_mfma_f32_16x16x32_bf16 a[152:155], a[216:219], v[212:215], a[152:155]\n\t"
    "ds_read_b128 a[208:211], v28 offset:24832\n\t"
    "v_add_f32 v41, v79, v41\n\t"
    "v_mfma_f32_16x16x32_bf16 a[152:155], a[220:223], v[244:247], a[152:155]\n\t"
    "ds_read_b128 a[212:215], v29 offset:24832\n\t"
    "v_cvt_pk_bf16_f32 v175, v78, v79\n\t"
    "v_mfma_f32_16x16x32_bf16 a[156:159], a[216:219], v[220:223], a[156:159]\n\t"
    "v_exp_f32 v84, v84\n\t"
    "v_mfma_f32_16x16x32_bf16 a[156:159], a[220:223], v[252:255], a[156:159]\n\t"
    "ds_read_b64_tr_b16 a[216:217], v36 offset:0\n\t"
    "v_add_f32 v42, v84, v42\n\t"
    "v_mfma_f32_16x16x32_bf16 a[160:163], a[224:227], v[196:199], a[160:163]\n\t"
    "v_exp_f32 v85, v85\n\t"
    "v_mfma_f32_16x16x32_bf16 a[160:163], a[228:231], v[228:231], a[160:163]\n\t"
    "ds_read_b64_tr_b16 a[218:219], v36 offset:6144\n\t"
    "v_add_f32 v42, v85, v42\n\t"
    "v_mfma_f32_16x16x32_bf16 a[164:167], a[224:227], v[204:207], a[164:167]\n\t"
    "v_exp_f32 v86, v86\n\t"
    "v_mfma_f32_16x16x32_bf16 a[164:167], a[228:231], v[236:239], a[164:167]\n\t"
    "ds_read_b64_tr_b16 a[220:221], v36 offset:12288\n\t"
    "v_cvt_pk_bf16_f32 v182, v84, v85\n\t"
    "v_mfma_f32_16x16x32_bf16 a[168:171], a[224:227], v[212:215], a[168:171]\n\t"
    "ds_read_b64_tr_b16 a[222:223], v36 offset:18432\n\t"
    "v_add_f32 v42, v86, v42\n\t"
    "v_mfma_f32_16x16x32_bf16 a[168:171], a[228:231], v[244:247], a[168:171]\n\t"
    "v_exp_f32 v87, v87\n\t"
    "v_mfma_f32_16x16x32_bf16 a[172:175], a[224:227], v[220:223], a[172:175]\n\t"
    "s_sub_u32 s85, s87, 49283072\n\t"
    "ds_read_b64_tr_b16 a[224:225], v37 offset:0\n\t"
    "v_add_f32 v42, v87, v42\n\t"
    "v_mfma_f32_16x16x32_bf16 a[172:175], a[228:231], v[252:255], a[172:175]\n\t"
    "ds_read_b64_tr_b16 a[226:227], v37 offset:6144\n\t"
    "v_cvt_pk_bf16_f32 v183, v86, v87\n\t"
    "v_mfma_f32_16x16x32_bf16 a[176:179], a[232:235], v[196:199], a[176:179]\n\t"
    "v_exp_f32 v92, v92\n\t"
    "v_mfma_f32_16x16x32_bf16 a[176:179], a[236:239], v[228:231], a[176:179]\n\t"
    "s_bfe_u32 s61, s86, 0x2000c\n\t"
    "ds_read_b64_tr_b16 a[228:229], v37 offset:12288\n\t"
    "v_add_f32 v43, v92, v43\n\t"
    "v_mfma_f32_16x16x32_bf16 a[180:183], a[232:235], v[204:207], a[180:183]\n\t"
    "s_mulk_i32 s85, 240\n\t"
    "v_exp_f32 v93, v93\n\t"
    "v_mfma_f32_16x16x32_bf16 a[180:183], a[236:239], v[236:239], a[180:183]\n\t"
    "s_mulk_i32 s61, 2560\n\t"
    "ds_read_b64_tr_b16 a[230:231], v37 offset:18432\n\t"
    "v_add_f32 v43, v93, v43\n\t"
    "v_mfma_f32_16x16x32_bf16 a[184:187], a[232:235], v[212:215], a[184:187]\n\t"
    "s_add_u32 s85, s85, s61\n\t"
    "v_exp_f32 v94, v94\n\t"
    "v_mfma_f32_16x16x32_bf16 a[184:187], a[236:239], v[244:247], a[184:187]\n\t"
    "s_add_u32 s85, s85, 50331648\n\t"
    "v_cvt_pk_bf16_f32 v190, v92, v93\n\t"
    "v_add_f32 v43, v94, v43\n\t"
    "v_mfma_f32_16x16x32_bf16 a[188:191], a[232:235], v[220:223], a[188:191]\n\t"
    "s_add_u32 s61, s85, 0\n\t"
    "v_exp_f32 v95, v95\n\t"
    "v_mfma_f32_16x16x32_bf16 a[188:191], a[236:239], v[252:255], a[188:191]\n\t"
    "s_waitcnt lgkmcnt(4)\n\t"
    "ds_read_b64_tr_b16 a[232:233], v38 offset:0\n\t"
    "v_add_f32 v43, v95, v43\n\t"
    "v_mfma_f32_16x16x32_bf16 v[68:71], a[192:195], v[64:67], v[100:103]\n\t"
    "ds_read_b64_tr_b16 a[234:235], v38 offset:6144\n\t"
    "v_cvt_pk_bf16_f32 v191, v94, v95\n\t"
    "v_mfma_f32_16x16x32_bf16 v[68:71], a[196:199], v[96:99], v[68:71]\n\t"
    "buffer_load_dwordx4 v8, s[48:51], s60 offen offset:0 lds\n\t"
    "v_mfma_f32_16x16x32_bf16 v[68:71], a[200:203], v[128:131], v[68:71]\n\t"
    "ds_read_b64_tr_b16 a[236:237], v38 offset:12288\n\t"
    "s_waitcnt vmcnt(10)\n\t"
    "v_add_f32 v20, v20, v44\n\t"
    "v_mfma_f32_16x16x32_bf16 v[68:71], a[204:207], v[160:163], v[68:71]\n\t"
    "ds_read_b64_tr_b16 a[238:239], v38 offset:18432\n\t"
    "v_add_f32 v21, v21, v45\n\t"
    "v_mfma_f32_16x16x32_bf16 v[68:71], a[208:211], v[192:195], v[68:71]\n\t"
    "v_add_f32 v22, v22, v46\n\t"
    "v_add_f32 v23, v23, v47\n\t"
    "v_mfma_f32_16x16x32_bf16 v[68:71], a[212:215], v[224:227], v[68:71]\n\t"
    "v_add_f32 v20, v20, v48\n\t"
    "v_add_f32 v21, v21, v49\n\t"
    "v_mfma_f32_16x16x32_bf16 v[76:79], a[192:195], v[72:75], v[108:111]\n\t"
    "buffer_load_dwordx4 v9, s[48:51], s60 offen offset:1024 lds\n\t"
    "v_mfma_f32_16x16x32_bf16 v[76:79], a[196:199], v[104:107], v[76:79]\n\t"
    "v_add_f32 v22, v22, v50\n\t"
    "v_add_f32 v23, v23, v51\n\t"
    "v_mfma_f32_16x16x32_bf16 v[76:79], a[200:203], v[136:139], v[76:79]\n\t"
    "v_exp_f32 v68, v68\n\t"
    "v_mfma_f32_16x16x32_bf16 v[76:79], a[204:207], v[168:171], v[76:79]\n\t"
    "v_add_f32 v40, v68, v40\n\t"
    "v_add_f32 v20, v20, v52\n\t"
    "v_mfma_f32_16x16x32_bf16 v[76:79], a[208:211], v[200:203], v[76:79]\n\t"
    "v_exp_f32 v69, v69\n\t"
    "v_mfma_f32_16x16x32_bf16 v[76:79], a[212:215], v[232:235], v[76:79]\n\t"
    "v_add_f32 v40, v69, v40\n\t"
    "v_cvt_pk_bf16_f32 v196, v68, v69\n\t"
    "v_mfma_f32_16x16x32_bf16 v[84:87], a[192:195], v[80:83], v[116:119]\n\t"
    "KFP8:\n\t"
    "s_cbranch_execz KFP12\n\t"
    "v_exp_f32 v70, v70\n\t"
    "v_mfma_f32_16x16x32_bf16 v[84:87], a[196:199], v[112:115], v[84:87]\n\t"
    "v_add_f32 v40, v70, v40\n\t"
    "v_add_f32 v21, v21, v53\n\t"
    "v_mfma_f32_16x16x32_bf16 v[84:87], a[200:203], v[144:147], v[84:87]\n\t"
    "v_exp_f32 v71, v71\n\t"
    "v_mfma_f32_16x16x32_bf16 v[84:87], a[204:207], v[176:179], v[84:87]\n\t"
    "v_add_f32 v40, v71, v40\n\t"
    "v_cvt_pk_bf16_f32 v197, v70, v71\n\t"
    "v_mfma_f32_16x16x32_bf16 v[84:87], a[208:211], v[208:211], v[84:87]\n\t"
    "v_exp_f32 v76, v76\n\t"
    "v_mfma_f32_16x16x32_bf16 v[84:87], a[212:215], v[240:243], v[84:87]\n\t"
    "buffer_load_dwordx4 v10, s[48:51], s60 offen offset:2048 lds\n\t"
    "v_mfma_f32_16x16x32_bf16 v[92:95], a[192:195], v[88:91], v[124:127]\n\t"
    "s_add_u32 m0, s83, 27648\n\t"
    "ds_read_b128 a[192:195], v28 offset:30720\n\t"
    "v_add_f32 v41, v76, v41\n\t"
    "v_mfma_f32_16x16x32_bf16 v[92:95], a[196:199], v[120:123], v[92:95]\n\t"
    "s_add_u32 s60, s60, 3072\n\t"
    "v_exp_f32 v77, v77\n\t"
    "v_mfma_f32_16x16x32_bf16 v[92:95], a[200:203], v[152:155], v[92:95]\n\t"
    "ds_read_b128 a[196:199], v29 offset:30720\n\t"
    "v_add_f32 v41, v77, v41\n\t"
    "v_mfma_f32_16x16x32_bf16 v[92:95], a[204:207], v[184:187], v[92:95]\n\t"
    "v_exp_f32 v78, v78\n\t"
    "v_mfma_f32_16x16x32_bf16 v[92:95], a[208:211], v[216:219], v[92:95]\n\t"
    "ds_read_b128 a[200:203], v28 offset:30848\n\t"
    "v_cvt_pk_bf16_f32 v204, v76, v77\n\t"
    "v_mfma_f32_16x16x32_bf16 v[92:95], a[212:215], v[248:251], v[92:95]\n\t"
    "ds_read_b128 a[204:207], v29 offset:30848\n\t"
    "v_add_f32 v41, v78, v41\n\t"
    "v_mfma_f32_16x16x32_bf16 a[0:3], a[216:219], v[132:135], a[0:3]\n\t"
    "v_exp_f32 v79, v79\n\t"
    "v_mfma_f32_16x16x32_bf16 a[0:3], a[220:223], v[164:167], a[0:3]\n\t"
    "ds_read_b128 a[208:211], v28 offset:30976\n\t"
    "v_add_f32 v41, v79, v41\n\t"
    "v_mfma_f32_16x16x32_bf16 a[4:7], a[216:219], v[140:143], a[4:7]\n\t"
    "ds_read_b128 a[212:215], v29 offset:30976\n\t"
    "v_cvt_pk_bf16_f32 v205, v78, v79\n\t"
    "v_mfma_f32_16x16x32_bf16 a[4:7], a[220:223], v[172:175], a[4:7]\n\t"
    "v_exp_f32 v84, v84\n\t"
    "v_mfma_f32_16x16x32_bf16 a[8:11], a[216:219], v[148:151], a[8:11]\n\t"
    "v_add_f32 v42, v84, v42\n\t"
    "v_add_f32 v22, v22, v54\n\t"
    "v_mfma_f32_16x16x32_bf16 a[8:11], a[220:223], v[180:183], a[8:11]\n\t"
    "v_exp_f32 v85, v85\n\t"
    "v_mfma_f32_16x16x32_bf16 a[12:15], a[216:219], v[156:159], a[12:15]\n\t"
    "ds_read_b64_tr_b16 a[216:217], v39 offset:0\n\t"
    "v_add_f32 v23, v23, v55\n\t"
    "v_mfma_f32_16x16x32_bf16 a[12:15], a[220:223], v[188:191], a[12:15]\n\t"
    "s_waitcnt lgkmcnt(6)\n\t"
    "ds_read_b64_tr_b16 a[218:219], v39 offset:6144\n\t"
    "v_add_f32 v42, v85, v42\n\t"
    "v_mfma_f32_16x16x32_bf16 a[16:19], a[224:227], v[132:135], a[16:19]\n\t"
    "ds_read_b64_tr_b16 a[220:221], v39 offset:12288\n\t"
    "s_waitcnt vmcnt(10)\n\t"
    "v_add_f32 v20, v20, v56\n\t"
    "v_mfma_f32_16x16x32_bf16 a[16:19], a[228:231], v[164:167], a[16:19]\n\t"
    "ds_read_b64_tr_b16 a[222:223], v39 offset:18432\n\t"
    "v_add_f32 v21, v21, v57\n\t"
    "v_mfma_f32_16x16x32_bf16 a[20:23], a[224:227], v[140:143], a[20:23]\n\t"
    "v_add_f32 v22, v22, v58\n\t"
    "v_add_f32 v23, v23, v59\n\t"
    "v_mfma_f32_16x16x32_bf16 a[20:23], a[228:231], v[172:175], a[20:23]\n\t"
    "v_exp_f32 v86, v86\n\t"
    "v_mfma_f32_16x16x32_bf16 a[24:27], a[224:227], v[148:151], a[24:27]\n\t"
    "v_cvt_pk_bf16_f32 v212, v84, v85\n\t"
    "v_add_f32 v42, v86, v42\n\t"
    "v_mfma_f32_16x16x32_bf16 a[24:27], a[228:231], v[180:183], a[24:27]\n\t"
    "buffer_load_dwordx4 v8, s[48:51], s60 offen offset:0 lds\n\t"
    "v_mfma_f32_16x16x32_bf16 a[28:31], a[224:227], v[156:159], a[28:31]\n\t"
    "v_exp_f32 v87, v87\n\t"
    "v_mfma_f32_16x16x32_bf16 a[28:31], a[228:231], v[188:191], a[28:31]\n\t"
    "ds_read_b64_tr_b16 a[224:225], v36 offset:128\n\t"
    "v_add_f32 v42, v87, v42\n\t"
    "v_mfma_f32_16x16x32_bf16 a[32:35], a[232:235], v[132:135], a[32:35]\n\t"
    "ds_read_b64_tr_b16 a[226:227], v36 offset:6272\n\t"
    "v_cvt_pk_bf16_f32 v213, v86, v87\n\t"
    "v_mfma_f32_16x16x32_bf16 a[32:35], a[236:239], v[164:167], a[32:35]\n\t"
    "v_exp_f32 v92, v92\n\t"
    "v_mfma_f32_16x16x32_bf16 a[36:39], a[232:235], v[140:143], a[36:39]\n\t"
    "buffer_load_dwordx4 v9, s[48:51], s60 offen offset:1024 lds\n\t"
    "v_mfma_f32_16x16x32_bf16 a[36:39], a[236:239], v[172:175], a[36:39]\n\t"
    "ds_read_b64_tr_b16 a[228:229], v36 offset:12416\n\t"
    "v_add_f32 v43, v92, v43\n\t"
    "v_mfma_f32_16x16x32_bf16 a[40:43], a[232:235], v[148:151], a[40:43]\n\t"
    "ds_read_b64_tr_b16 a[230:231], v36 offset:18560\n\t"
    "v_mov_b32 v56, -1\n\t"
    "v_mfma_f32_16x16x32_bf16 a[40:43], a[236:239], v[180:183], a[40:43]\n\t"
    "v_mov_b32 v57, -1\n\t"
    "v_mov_b32 v58, -1\n\t"
    "v_mfma_f32_16x16x32_bf16 a[44:47], a[232:235], v[156:159], a[44:47]\n\t"
    "ds_read_b64_tr_b16 a[232:233], v37 offset:128\n\t"
    "v_mov_b32 v59, -1\n\t"
    "v_mfma_f32_16x16x32_bf16 a[44:47], a[236:239], v[188:191], a[44:47]\n\t"
    "buffer_load_dwordx4 v10, s[48:51], s60 offen offset:2048 lds\n\t"
    "v_mfma_f32_16x16x32_bf16 v[68:71], a[192:195], v[64:67], v[100:103]\n\t"
    "s_waitcnt lgkmcnt(5)\n\t"
    "s_add_u32 m0, s83, 49152\n\t"
    "ds_read_b64_tr_b16 a[234:235], v37 offset:6272\n\t"
    "v_mfma_f32_16x16x32_bf16 v[68:71], a[196:199], v[96:99], v[68:71]\n\t"
    "s_add_u32 s60, s87, 16384\n\t"
    "ds_read_b64_tr_b16 a[236:237], v37 offset:12416\n\t"
    "v_mfma_f32_16x16x32_bf16 v[68:71], a[200:203], v[128:131], v[68:71]\n\t"
    "v_exp_f32 v93, v93\n\t"
    "v_mfma_f32_16x16x32_bf16 v[68:71], a[204:207], v[160:163], v[68:71]\n\t"
    "ds_read_b64_tr_b16 a[238:239], v37 offset:18560\n\t"
    "v_add_f32 v43, v93, v43\n\t"
    "v_mfma_f32_16x16x32_bf16 v[68:71], a[208:211], v[192:195], v[68:71]\n\t"
    "buffer_load_dwordx4 v[44:47], v27, s[40:43], s63 offen offset:0 sc1\n\t"
    "v_mfma_f32_16x16x32_bf16 v[68:71], a[212:215], v[224:227], v[68:71]\n\t"
    "s_add_u32 s63, s91, 442368\n\t"
    "v_exp_f32 v94, v94\n\t"
    "v_mfma_f32_16x16x32_bf16 v[76:79], a[192:195], v[72:75], v[108:111]\n\t"
    "s_bfe_u32 s60, s60, 0x2000c\n\t"
    "v_cvt_pk_bf16_f32 v220, v92, v93\n\t"
    "v_add_f32 v43, v94, v43\n\t"
    "v_mfma_f32_16x16x32_bf16 v[76:79], a[196:199], v[104:107], v[76:79]\n\t"
    "s_add_u32 s60, s60, 4\n\t"
    "v_exp_f32 v95, v95\n\t"
    "v_mfma_f32_16x16x32_bf16 v[76:79], a[200:203], v[136:139], v[76:79]\n\t"
    "s_mulk_i32 s60, 24576\n\t"
    "v_add_f32 v43, v95, v43\n\t"
    "v_cvt_pk_bf16_f32 v221, v94, v95\n\t"
    "v_mfma_f32_16x16x32_bf16 v[76:79], a[204:207], v[168:171], v[76:79]\n\t"
    "buffer_load_dwordx4 v[48:51], v27, s[40:43], s63 offen offset:0 sc1\n\t"
    "v_mfma_f32_16x16x32_bf16 v[76:79], a[208:211], v[200:203], v[76:79]\n\t"
    "s_bfe_u32 s63, s77, 0x1000b\n\t"
    "v_exp_f32 v68, v68\n\t"
    "v_mfma_f32_16x16x32_bf16 v[76:79], a[212:215], v[232:235], v[76:79]\n\t"
    "s_mul_i32 s63, s63, 11\n\t"
    "v_add_f32 v40, v68, v40\n\t"
    "v_lshlrev_b32 v12, 21, v4\n\t"
    "v_mfma_f32_16x16x32_bf16 v[84:87], a[192:195], v[80:83], v[116:119]\n\t"
    "s_lshr_b32 s63, s35, s63\n\t"
    "v_exp_f32 v69, v69\n\t"
    "v_mfma_f32_16x16x32_bf16 v[84:87], a[196:199], v[112:115], v[84:87]\n\t"
    "s_and_b32 s63, s63, 0x7ff\n\t"
    "v_add_f32 v40, v69, v40\n\t"
    "v_cvt_pk_bf16_f32 v198, v68, v69\n\t"
    "v_mfma_f32_16x16x32_bf16 v[84:87], a[200:203], v[144:147], v[84:87]\n\t"
    "s_lshl_b32 s63, s63, 1\n\t"
    "buffer_store_dwordx4 v[56:59], v4, s[40:43], s61 offen offset:0 sc1\n\t"
    "v_mfma_f32_16x16x32_bf16 v[84:87], a[204:207], v[176:179], v[84:87]\n\t"
    "s_lshl4_add_u32 s63, s63, 46202880\n\t"
    "v_exp_f32 v70, v70\n\t"
    "v_mfma_f32_16x16x32_bf16 v[84:87], a[208:211], v[208:211], v[84:87]\n\t"
    "v_add_f32 v40, v70, v40\n\t"
    "v_and_b32 v12, 0x40000000, v12\n\t"
    "v_mfma_f32_16x16x32_bf16 v[84:87], a[212:215], v[240:243], v[84:87]\n\t"
    "v_exp_f32 v71, v71\n\t"
    "v_mfma_f32_16x16x32_bf16 v[92:95], a[192:195], v[88:91], v[124:127]\n\t"
    "ds_read_b128 a[192:195], v28 offset:36864\n\t"
    "v_add_f32 v40, v71, v40\n\t"
    "v_mfma_f32_16x16x32_bf16 v[92:95], a[196:199], v[120:123], v[92:95]\n\t"
    "s_cmp_eq_u32 s87, 49283072\n\t"
    "s_cselect_b32 s65, s87, 0x70000000\n\t"
    "buffer_store_dword v60, v2, s[40:43], s65 offen sc1\n\t"
    "v_mfma_f32_16x16x32_bf16 v[92:95], a[200:203], v[152:155], v[92:95]\n\t"
    "ds_read_b128 a[196:199], v29 offset:36864\n\t"
    "v_cvt_pk_bf16_f32 v199, v70, v71\n\t"
    "v_mfma_f32_16x16x32_bf16 v[92:95], a[204:207], v[184:187], v[92:95]\n\t"
    "v_exp_f32 v76, v76\n\t"
    "v_mfma_f32_16x16x32_bf16 v[92:95], a[208:211], v[216:219], v[92:95]\n\t"
    "ds_read_b128 a[200:203], v28 offset:36992\n\t"
    "v_add_f32 v41, v76, v41\n\t"
    "v_mfma_f32_16x16x32_bf16 v[92:95], a[212:215], v[248:251], v[92:95]\n\t"
    "v_exp_f32 v77, v77\n\t"
    "v_mfma_f32_16x16x32_bf16 a[48:51], a[216:219], v[132:135], a[48:51]\n\t"
    "buffer_load_dwordx4 v8, s[52:55], s60 offen offset:0 lds\n\t"
    "v_mfma_f32_16x16x32_bf16 a[48:51], a[220:223], v[164:167], a[48:51]\n\t"
    "ds_read_b128 a[204:207], v29 offset:36992\n\t"
    "v_add_f32 v41, v77, v41\n\t"
    "v_mfma_f32_16x16x32_bf16 a[52:55], a[216:219], v[140:143], a[52:55]\n\t"
    "v_exp_f32 v78, v78\n\t"
    "v_mfma_f32_16x16x32_bf16 a[52:55], a[220:223], v[172:175], a[52:55]\n\t"
    "ds_read_b128 a[208:211], v28 offset:37120\n\t"
    "v_cvt_pk_bf16_f32 v206, v76, v77\n\t"
    "v_mfma_f32_16x16x32_bf16 a[56:59], a[216:219], v[148:151], a[56:59]\n\t"
    "ds_read_b128 a[212:215], v29 offset:37120\n\t"
    "v_add_f32 v41, v78, v41\n\t"
    "v_mfma_f32_16x16x32_bf16 a[56:59], a[220:223], v[180:183], a[56:59]\n\t"
    "buffer_load_dwordx4 v9, s[52:55], s60 offen offset:1024 lds\n\t"
    "v_mfma_f32_16x16x32_bf16 a[60:63], a[216:219], v[156:159], a[60:63]\n\t"
    "v_exp_f32 v79, v79\n\t"
    "v_mfma_f32_16x16x32_bf16 a[60:63], a[220:223], v[188:191], a[60:63]\n\t"
    "s_waitcnt lgkmcnt(5)\n\t"
    "ds_read_b64_tr_b16 a[216:217], v38 offset:128\n\t"
    "v_add_f32 v41, v79, v41\n\t"
    "v_mfma_f32_16x16x32_bf16 a[64:67], a[224:227], v[132:135], a[64:67]\n\t"
    "ds_read_b64_tr_b16 a[218:219], v38 offset:6272\n\t"
    "v_cvt_pk_bf16_f32 v207, v78, v79\n\t"
    "v_mfma_f32_16x16x32_bf16 a[64:67], a[228:231], v[164:167], a[64:67]\n\t"
    "v_exp_f32 v84, v84\n\t"
    "v_mfma_f32_16x16x32_bf16 a[68:71], a[224:227], v[140:143], a[68:71]\n\t"
    "buffer_load_dwordx4 v10, s[52:55], s60 offen offset:2048 lds\n\t"
    "v_mfma_f32_16x16x32_bf16 a[68:71], a[228:231], v[172:175], a[68:71]\n\t"
    "s_add_u32 m0, s83, 52224\n\t"
    "ds_read_b64_tr_b16 a[220:221], v38 offset:12416\n\t"
    "v_add_f32 v42, v84, v42\n\t"
    "v_mfma_f32_16x16x32_bf16 a[72:75], a[224:227], v[148:151], a[72:75]\n\t"
    "s_add_u32 s60, s60, 3072\n\t"
    "v_exp_f32 v85, v85\n\t"
    "v_mfma_f32_16x16x32_bf16 a[72:75], a[228:231], v[180:183], a[72:75]\n\t"
    "ds_read_b64_tr_b16 a[222:223], v38 offset:18560\n\t"
    "v_add_f32 v42, v85, v42\n\t"
    "v_mfma_f32_16x16x32_bf16 a[76:79], a[224:227], v[156:159], a[76:79]\n\t"
    "v_exp_f32 v86, v86\n\t"
    "v_mfma_f32_16x16x32_bf16 a[76:79], a[228:231], v[188:191], a[76:79]\n\t"
    "ds_read_b64_tr_b16 a[224:225], v39 offset:128\n\t"
    "v_cvt_pk_bf16_f32 v214, v84, v85\n\t"
    "v_mfma_f32_16x16x32_bf16 a[80:83], a[232:235], v[132:135], a[80:83]\n\t"
    "ds_read_b64_tr_b16 a[226:227], v39 offset:6272\n\t"
    "v_add_f32 v42, v86, v42\n\t"
    "v_mfma_f32_16x16x32_bf16 a[80:83], a[236:239], v[164:167], a[80:83]\n\t"
    "v_exp_f32 v87, v87\n\t"
    "v_mfma_f32_16x16x32_bf16 a[84:87], a[232:235], v[140:143], a[84:87]\n\t"
    "ds_read_b64_tr_b16 a[228:229], v39 offset:12416\n\t"
    "v_add_f32 v42, v87, v42\n\t"
    "v_mfma_f32_16x16x32_bf16 a[84:87], a[236:239], v[172:175], a[84:87]\n\t"
    "ds_read_b64_tr_b16 a[230:231], v39 offset:18560\n\t"
    "v_cvt_pk_bf16_f32 v215, v86, v87\n\t"
    "v_mfma_f32_16x16x32_bf16 a[88:91], a[232:235], v[148:151], a[88:91]\n\t"
    "v_exp_f32 v92, v92\n\t"
    "v_mfma_f32_16x16x32_bf16 a[88:91], a[236:239], v[180:183], a[88:91]\n\t"
    "v_add_f32 v43, v92, v43\n\t"
    "v_or_b32 v12, v12, v4\n\t"
    "v_mfma_f32_16x16x32_bf16 a[92:95], a[232:235], v[156:159], a[92:95]\n\t"
    "v_exp_f32 v93, v93\n\t"
    "v_mfma_f32_16x16x32_bf16 a[92:95], a[236:239], v[188:191], a[92:95]\n\t"
    "ds_read_b64_tr_b16 a[232:233], v36 offset:256\n\t"
    "v_add_f32 v43, v93, v43\n\t"
    "v_mfma_f32_16x16x32_bf16 v[68:71], a[192:195], v[64:67], v[100:103]\n\t"
    "s_waitcnt lgkmcnt(6)\n\t"
    "v_exp_f32 v94, v94\n\t"
    "v_mfma_f32_16x16x32_bf16 v[68:71], a[196:199], v[96:99], v[68:71]\n\t"
    "ds_read_b64_tr_b16 a[234:235], v36 offset:6400\n\t"
    "v_cvt_pk_bf16_f32 v222, v92, v93\n\t"
    "v_mfma_f32_16x16x32_bf16 v[68:71], a[200:203], v[128:131], v[68:71]\n\t"
    "buffer_load_dwordx4 v8, s[52:55], s60 offen offset:0 lds\n\t"
    "v_mfma_f32_16x16x32_bf16 v[68:71], a[204:207], v[160:163], v[68:71]\n\t"
    "ds_read_b64_tr_b16 a[236:237], v36 offset:12544\n\t"
    "v_add_f32 v43, v94, v43\n\t"
    "v_mfma_f32_16x16x32_bf16 v[68:71], a[208:211], v[192:195], v[68:71]\n\t"
    "v_exp_f32 v95, v95\n\t"
    "v_mfma_f32_16x16x32_bf16 v[68:71], a[212:215], v[224:227], v[68:71]\n\t"
    "ds_read_b64_tr_b16 a[238:239], v36 offset:18688\n\t"
    "v_add_f32 v43, v95, v43\n\t"
    "v_mfma_f32_16x16x32_bf16 v[76:79], a[192:195], v[72:75], v[108:111]\n\t"
    "v_cvt_pk_bf16_f32 v223, v94, v95\n\t"
    "v_mov_b32 v27, s80\n\t"
    "v_mfma_f32_16x16x32_bf16 v[76:79], a[196:199], v[104:107], v[76:79]\n\t"
    "buffer_load_dwordx4 v9, s[52:55], s60 offen offset:1024 lds\n\t"
    "v_mfma_f32_16x16x32_bf16 v[76:79], a[200:203], v[136:139], v[76:79]\n\t"
    "v_exp_f32 v68, v68\n\t"
    "v_mfma_f32_16x16x32_bf16 v[76:79], a[204:207], v[168:171], v[76:79]\n\t"
    "v_exp_f32 v69, v69\n\t"
    "v_mfma_f32_16x16x32_bf16 v[76:79], a[208:211], v[200:203], v[76:79]\n\t"
    "v_add_f32 v40, v68, v40\n\t"
    "v_add_f32 v40, v69, v40\n\t"
    "v_mfma_f32_16x16x32_bf16 v[76:79], a[212:215], v[232:235], v[76:79]\n\t"
    "v_exp_f32 v70, v70\n\t"
    "v_mfma_f32_16x16x32_bf16 v[84:87], a[192:195], v[80:83], v[116:119]\n\t"
    "v_cvt_pk_bf16_f32 v228, v68, v69\n\t"
    "v_add_f32 v40, v70, v40\n\t"
    "v_mfma_f32_16x16x32_bf16 v[84:87], a[196:199], v[112:115], v[84:87]\n\t"
    "buffer_load_dwordx4 v10, s[52:55], s60 offen offset:2048 lds\n\t"
    "v_mfma_f32_16x16x32_bf16 v[84:87], a[200:203], v[144:147], v[84:87]\n\t"
    "s_add_u32 m0, s83, 73728\n\t"
    "v_exp_f32 v71, v71\n\t"
    "v_mfma_f32_16x16x32_bf16 v[84:87], a[204:207], v[176:179], v[84:87]\n\t"
    "s_add_u32 s60, s87, 24576\n\t"
    "v_add_f32 v40, v71, v40\n\t"
    "v_cvt_pk_bf16_f32 v229, v70, v71\n\t"
    "v_mfma_f32_16x16x32_bf16 v[84:87], a[208:211], v[208:211], v[84:87]\n\t"
    "s_bfe_u32 s60, s60, 0x2000c\n\t"
    "v_exp_f32 v76, v76\n\t"
    "v_mfma_f32_16x16x32_bf16 v[84:87], a[212:215], v[240:243], v[84:87]\n\t"
    "s_add_u32 s60, s60, 4\n\t"
    "v_exp_f32 v77, v77\n\t"
    "v_mfma_f32_16x16x32_bf16 v[92:95], a[192:195], v[88:91], v[124:127]\n\t"
    "s_mulk_i32 s60, 24576\n\t"
    "buffer_store_dwordx4 v[56:59], v4, s[40:43], s61 offen offset:1024 sc1\n\t"
    "v_mfma_f32_16x16x32_bf16 v[92:95], a[196:199], v[120:123], v[92:95]\n\t"
    "ds_read_b128 a[192:195], v28 offset:43008\n\t"
    "v_add_f32 v41, v76, v41\n\t"
    "v_mfma_f32_16x16x32_bf16 v[92:95], a[200:203], v[152:155], v[92:95]\n\t"
    "ds_read_b128 a[196:199], v29 offset:43008\n\t"
    "v_add_f32 v41, v77, v41\n\t"
    "v_mfma_f32_16x16x32_bf16 v[92:95], a[204:207], v[184:187], v[92:95]\n\t"
    "v_exp_f32 v78, v78\n\t"
    "v_mfma_f32_16x16x32_bf16 v[92:95], a[208:211], v[216:219], v[92:95]\n\t"
    "ds_read_b128 a[200:203], v28 offset:43136\n\t"
    "v_cvt_pk_bf16_f32 v236, v76, v77\n\t"
    "v_mfma_f32_16x16x32_bf16 v[92:95], a[212:215], v[248:251], v[92:95]\n\t"
    "ds_read_b128 a[204:207], v29 offset:43136\n\t"
    "v_add_f32 v41, v78, v41\n\t"
    "v_mfma_f32_16x16x32_bf16 a[96:99], a[216:219], v[132:135], a[96:99]\n\t"
    "s_waitcnt lgkmcnt(4)\n\t"
    "v_exp_f32 v79, v79\n\t"
    "v_mfma_f32_16x16x32_bf16 a[96:99], a[220:223], v[164:167], a[96:99]\n\t"
    "ds_read_b128 a[208:211], v28 offset:43264\n\t"
    "v_add_f32 v41, v79, v41\n\t"
    "v_mfma_f32_16x16x32_bf16 a[100:103], a[216:219], v[140:143], a[100:103]\n\t"
    "ds_read_b128 a[212:215], v29 offset:43264\n\t"
    "v_cvt_pk_bf16_f32 v237, v78, v79\n\t"
    "v_mfma_f32_16x16x32_bf16 a[100:103], a[220:223], v[172:175], a[100:103]\n\t"
    "v_exp_f32 v84, v84\n\t"
    "v_mfma_f32_16x16x32_bf16 a[104:107], a[216:219], v[148:151], a[104:107]\n\t"
    "v_exp_f32 v85, v85\n\t"
    "v_mfma_f32_16x16x32_bf16 a[104:107], a[220:223], v[180:183], a[104:107]\n\t"
    "v_add_f32 v42, v84, v42\n\t"
    "v_add_f32 v42, v85, v42\n\t"
    "v_mfma_f32_16x16x32_bf16 a[108:111], a[216:219], v[156:159], a[108:111]\n\t"
    "v_exp_f32 v86, v86\n\t"
    "v_mfma_f32_16x16x32_bf16 a[108:111], a[220:223], v[188:191], a[108:111]\n\t"
    "ds_read_b64_tr_b16 a[216:217], v37 offset:256\n\t"
    "v_cvt_pk_bf16_f32 v244, v84, v85\n\t"
    "v_mfma_f32_16x16x32_bf16 a[112:115], a[224:227], v[132:135], a[112:115]\n\t"
    "ds_read_b64_tr_b16 a[218:219], v37 offset:6400\n\t"
    "v_add_f32 v42, v86, v42\n\t"
    "v_mfma_f32_16x16x32_bf16 a[112:115], a[228:231], v[164:167], a[112:115]\n\t"
    "v_exp_f32 v87, v87\n\t"
    "v_mfma_f32_16x16x32_bf16 a[116:119], a[224:227], v[140:143], a[116:119]\n\t"
    "ds_read_b64_tr_b16 a[220:221], v37 offset:12544\n\t"
    "v_add_f32 v42, v87, v42\n\t"
    "v_mfma_f32_16x16x32_bf16 a[116:119], a[228:231], v[172:175], a[116:119]\n\t"
    "ds_read_b64_tr_b16 a[222:223], v37 offset:18688\n\t"
    "v_cvt_pk_bf16_f32 v245, v86, v87\n\t"
    "v_mfma_f32_16x16x32_bf16 a[120:123], a[224:227], v[148:151], a[120:123]\n\t"
    "v_exp_f32 v92, v92\n\t"
    "v_mfma_f32_16x16x32_bf16 a[120:123], a[228:231], v[180:183], a[120:123]\n\t"
    "v_exp_f32 v93, v93\n\t"
    "v_mfma_f32_16x16x32_bf16 a[124:127], a[224:227], v[156:159], a[124:127]\n\t"
    "ds_read_b64_tr_b16 a[224:225], v38 offset:256\n\t"
    "v_add_f32 v43, v92, v43\n\t"
    "v_mfma_f32_16x16x32_bf16 a[124:127], a[228:231], v[188:191], a[124:127]\n\t"
    "ds_read_b64_tr_b16 a[226:227], v38 offset:6400\n\t"
    "v_add_f32 v43, v93, v43\n\t"
    "v_mfma_f32_16x16x32_bf16 a[128:131], a[232:235], v[132:135], a[128:131]\n\t"
    "v_exp_f32 v94, v94\n\t"
    "v_mfma_f32_16x16x32_bf16 a[128:131], a[236:239], v[164:167], a[128:131]\n\t"
    "ds_read_b64_tr_b16 a[228:229], v38 offset:12544\n\t"
    "v_cvt_pk_bf16_f32 v252, v92, v93\n\t"
    "v_mfma_f32_16x16x32_bf16 a[132:135], a[232:235], v[140:143], a[132:135]\n\t"
    "ds_read_b64_tr_b16 a[230:231], v38 offset:18688\n\t"
    "v_add_f32 v43, v94, v43\n\t"
    "v_mfma_f32_16x16x32_bf16 a[132:135], a[236:239], v[172:175], a[132:135]\n\t"
    "v_exp_f32 v95, v95\n\t"
    "v_mfma_f32_16x16x32_bf16 a[136:139], a[232:235], v[148:151], a[136:139]\n\t"
    "v_add_f32 v43, v95, v43\n\t"
    "v_cvt_pk_bf16_f32 v253, v94, v95\n\t"
    "v_mfma_f32_16x16x32_bf16 a[136:139], a[236:239], v[180:183], a[136:139]\n\t"
    "v_mfma_f32_16x16x32_bf16 a[140:143], a[232:235], v[156:159], a[140:143]\n\t"
    "ds_read_b64_tr_b16 a[232:233], v39 offset:256\n\t"
    "v_mfma_f32_16x16x32_bf16 a[140:143], a[236:239], v[188:191], a[140:143]\n\t"
    "s_waitcnt lgkmcnt(6)\n\t"
    "ds_read_b64_tr_b16 a[234:235], v39 offset:6400\n\t"
    "v_mfma_f32_16x16x32_bf16 v[68:71], a[192:195], v[64:67], v[100:103]\n\t"
    "ds_read_b64_tr_b16 a[236:237], v39 offset:12544\n\t"
    "s_waitcnt vmcnt(9)\n\t"
    "v_add_f32 v20, v20, v44\n\t"
    "v_mfma_f32_16x16x32_bf16 v[68:71], a[196:199], v[96:99], v[68:71]\n\t"
    "ds_read_b64_tr_b16 a[238:239], v39 offset:18688\n\t"
    "v_add_f32 v21, v21, v45\n\t"
    "v_mfma_f32_16x16x32_bf16 v[68:71], a[200:203], v[128:131], v[68:71]\n\t"
    "ds_read_b64_tr_b16 a[240:241], v63 offset:0\n\t"
    "v_add_f32 v22, v22, v46\n\t"
    "v_mfma_f32_16x16x32_bf16 v[68:71], a[204:207], v[160:163], v[68:71]\n\t"
    "ds_read_b64_tr_b16 a[242:243], v63 offset:2048\n\t"
    "v_add_f32 v23, v23, v47\n\t"
    "v_mfma_f32_16x16x32_bf16 v[68:71], a[208:211], v[192:195], v[68:71]\n\t"
    "ds_read_b64_tr_b16 a[244:245], v24 offset:0\n\t"
    "v_add_f32 v20, v20, v48\n\t"
    "v_mfma_f32_16x16x32_bf16 v[68:71], a[212:215], v[224:227], v[68:71]\n\t"
    "ds_read_b64_tr_b16 a[246:247], v24 offset:2048\n\t"
    "v_add_f32 v21, v21, v49\n\t"
    "v_mfma_f32_16x16x32_bf16 v[76:79], a[192:195], v[72:75], v[108:111]\n\t"
    "ds_read_b64_tr_b16 a[248:249], v25 offset:0\n\t"
    "v_add_f32 v22, v22, v50\n\t"
    "v_mfma_f32_16x16x32_bf16 v[76:79], a[196:199], v[104:107], v[76:79]\n\t"
    "ds_read_b64_tr_b16 a[250:251], v25 offset:2048\n\t"
    "v_add_f32 v23, v23, v51\n\t"
    "v_mfma_f32_16x16x32_bf16 v[76:79], a[200:203], v[136:139], v[76:79]\n\t"
    "v_exp_f32 v68, v68\n\t"
    "v_mfma_f32_16x16x32_bf16 v[76:79], a[204:207], v[168:171], v[76:79]\n\t"
    "ds_read_b64_tr_b16 a[252:253], v26 offset:0\n\t"
    "v_add_f32 v40, v68, v40\n\t"
    "v_mfma_f32_16x16x32_bf16 v[76:79], a[208:211], v[200:203], v[76:79]\n\t"
    "v_exp_f32 v69, v69\n\t"
    "v_mfma_f32_16x16x32_bf16 v[76:79], a[212:215], v[232:235], v[76:79]\n\t"
    "ds_read_b64_tr_b16 a[254:255], v26 offset:2048\n\t"
    "v_add_f32 v40, v69, v40\n\t"
    "v_mfma_f32_16x16x32_bf16 v[84:87], a[192:195], v[80:83], v[116:119]\n\t"
    "v_exp_f32 v70, v70\n\t"
    "v_mfma_f32_16x16x32_bf16 v[84:87], a[196:199], v[112:115], v[84:87]\n\t"
    "v_cvt_pk_bf16_f32 v230, v68, v69\n\t"
    "v_add_f32 v40, v70, v40\n\t"
    "v_mfma_f32_16x16x32_bf16 v[84:87], a[200:203], v[144:147], v[84:87]\n\t"
    "v_exp_f32 v71, v71\n\t"
    "v_mfma_f32_16x16x32_bf16 v[84:87], a[204:207], v[176:179], v[84:87]\n\t"
    "v_add_f32 v40, v71, v40\n\t"
    "v_cvt_pk_bf16_f32 v231, v70, v71\n\t"
    "v_mfma_f32_16x16x32_bf16 v[84:87], a[208:211], v[208:211], v[84:87]\n\t"
    "v_exp_f32 v76, v76\n\t"
    "v_mfma_f32_16x16x32_bf16 v[84:87], a[212:215], v[240:243], v[84:87]\n\t"
    "v_add_f32 v41, v76, v41\n\t"
    "v_permlane32_swap_b32 v20, v22\n\t"
    "v_mfma_f32_16x16x32_bf16 v[92:95], a[192:195], v[88:91], v[124:127]\n\t"
    "v_exp_f32 v77, v77\n\t"
    "v_mfma_f32_16x16x32_bf16 v[92:95], a[196:199], v[120:123], v[92:95]\n\t"
    "v_add_f32 v41, v77, v41\n\t"
    "v_cvt_pk_bf16_f32 v238, v76, v77\n\t"
    "v_mfma_f32_16x16x32_bf16 v[92:95], a[200:203], v[152:155], v[92:95]\n\t"
    "s_waitcnt lgkmcnt(0)\n\t"
    "s_barrier\n\t"
    "ds_read_b128 a[192:195], v30 offset:24576\n\t"
    "v_mfma_f32_16x16x32_bf16 v[92:95], a[204:207], v[184:187], v[92:95]\n\t"
    "v_exp_f32 v78, v78\n\t"
    "v_mfma_f32_16x16x32_bf16 v[92:95], a[208:211], v[216:219], v[92:95]\n\t"
    "ds_read_b128 a[196:199], v31 offset:24576\n\t"
    "v_add_f32 v41, v78, v41\n\t"
    "v_mfma_f32_16x16x32_bf16 v[92:95], a[212:215], v[248:251], v[92:95]\n\t"
    "ds_read_b128 a[200:203], v30 offset:24704\n\t"
    "v_permlane32_swap_b32 v21, v23\n\t"
    "v_mfma_f32_16x16x32_bf16 a[144:147], a[216:219], v[132:135], a[144:147]\n\t"
    "v_exp_f32 v79, v79\n\t"
    "v_mfma_f32_16x16x32_bf16 a[144:147], a[220:223], v[164:167], a[144:147]\n\t"
    "ds_read_b128 a[204:207], v31 offset:24704\n\t"
    "v_add_f32 v41, v79, v41\n\t"
    "v_mfma_f32_16x16x32_bf16 a[148:151], a[216:219], v[140:143], a[148:151]\n\t"
    "buffer_store_dwordx4 v[56:59], v12, s[40:43], s61 offen offset:2048 sc1\n\t"
    "v_mfma_f32_16x16x32_bf16 a[148:151], a[220:223], v[172:175], a[148:151]\n\t"
    "s_add_u32 s61, s85, 10240\n\t"
    "ds_read_b128 a[208:211], v30 offset:24832\n\t"
    "v_cvt_pk_bf16_f32 v239, v78, v79\n\t"
    "v_mfma_f32_16x16x32_bf16 a[152:155], a[216:219], v[148:151], a[152:155]\n\t"
    "v_exp_f32 v84, v84\n\t"
    "v_mfma_f32_16x16x32_bf16 a[152:155], a[220:223], v[180:183], a[152:155]\n\t"
    "ds_read_b128 a[212:215], v31 offset:24832\n\t"
    "v_add_f32 v42, v84, v42\n\t"
    "v_mfma_f32_16x16x32_bf16 a[156:159], a[216:219], v[156:159], a[156:159]\n\t"
    "v_exp_f32 v85, v85\n\t"
    "v_mfma_f32_16x16x32_bf16 a[156:159], a[220:223], v[188:191], a[156:159]\n\t"
    "ds_read_b64_tr_b16 a[216:217], v15 offset:0\n\t"
    "v_add_f32 v42, v85, v42\n\t"
    "v_mfma_f32_16x16x32_bf16 a[160:163], a[224:227], v[132:135], a[160:163]\n\t"
    "v_exp_f32 v86, v86\n\t"
    "v_mfma_f32_16x16x32_bf16 a[160:163], a[228:231], v[164:167], a[160:163]\n\t"
    "ds_read_b64_tr_b16 a[218:219], v15 offset:6144\n\t"
    "v_cvt_pk_bf16_f32 v246, v84, v85\n\t"
    "v_mfma_f32_16x16x32_bf16 a[164:167], a[224:227], v[140:143], a[164:167]\n\t"
    "ds_read_b64_tr_b16 a[220:221], v15 offset:12288\n\t"
    "v_add_f32 v42, v86, v42\n\t"
    "v_mfma_f32_16x16x32_bf16 a[164:167], a[228:231], v[172:175], a[164:167]\n\t"
    "v_exp_f32 v87, v87\n\t"
    "v_mfma_f32_16x16x32_bf16 a[168:171], a[224:227], v[148:151], a[168:171]\n\t"
    "ds_read_b64_tr_b16 a[222:223], v15 offset:18432\n\t"
    "v_add_f32 v42, v87, v42\n\t"
    "v_mfma_f32_16x16x32_bf16 a[168:171], a[228:231], v[180:183], a[168:171]\n\t"
    "v_cvt_pk_bf16_f32 v247, v86, v87\n\t"
    "v_add_f32 v22, v20, v22\n\t"
    "v_mfma_f32_16x16x32_bf16 a[172:175], a[224:227], v[156:159], a[172:175]\n\t"
    "v_exp_f32 v92, v92\n\t"
    "v_mfma_f32_16x16x32_bf16 a[172:175], a[228:231], v[188:191], a[172:175]\n\t"
    "ds_read_b64_tr_b16 a[224:225], v16 offset:0\n\t"
    "v_add_f32 v43, v92, v43\n\t"
    "v_mfma_f32_16x16x32_bf16 a[176:179], a[232:235], v[132:135], a[176:179]\n\t"
    "v_exp_f32 v93, v93\n\t"
    "v_mfma_f32_16x16x32_bf16 a[176:179], a[236:239], v[164:167], a[176:179]\n\t"
    "ds_read_b64_tr_b16 a[226:227], v16 offset:6144\n\t"
    "v_add_f32 v43, v93, v43\n\t"
    "v_mfma_f32_16x16x32_bf16 a[180:183], a[232:235], v[140:143], a[180:183]\n\t"
    "v_exp_f32 v94, v94\n\t"
    "v_mfma_f32_16x16x32_bf16 a[180:183], a[236:239], v[172:175], a[180:183]\n\t"
    "ds_read_b64_tr_b16 a[228:229], v16 offset:12288\n\t"
    "v_cvt_pk_bf16_f32 v254, v92, v93\n\t"
    "v_mfma_f32_16x16x32_bf16 a[184:187], a[232:235], v[148:151], a[184:187]\n\t"
    "ds_read_b64_tr_b16 a[230:231], v16 offset:18432\n\t"
    "v_add_f32 v43, v94, v43\n\t"
    "v_mfma_f32_16x16x32_bf16 a[184:187], a[236:239], v[180:183], a[184:187]\n\t"
    "v_exp_f32 v95, v95\n\t"
    "v_mfma_f32_16x16x32_bf16 a[188:191], a[232:235], v[156:159], a[188:191]\n\t"
    "ds_read_b64_tr_b16 a[232:233], v17 offset:0\n\t"
    "v_add_f32 v23, v21, v23\n\t"
    "v_mfma_f32_16x16x32_bf16 a[188:191], a[236:239], v[188:191], a[188:191]\n\t"
    "s_waitcnt lgkmcnt(6)\n\t"
    "ds_read_b64_tr_b16 a[234:235], v17 offset:6144\n\t"
    "v_add_f32 v43, v95, v43\n\t"
    "v_mfma_f32_16x16x32_bf16 v[68:71], a[192:195], v[64:67], v[100:103]\n\t"
    "ds_read_b64_tr_b16 a[236:237], v17 offset:12288\n\t"
    "v_permlane16_swap_b32 v22, v23\n\t"
    "v_mfma_f32_16x16x32_bf16 v[68:71], a[196:199], v[96:99], v[68:71]\n\t"
    "ds_read_b64_tr_b16 a[238:239], v17 offset:18432\n\t"
    "v_cvt_pk_bf16_f32 v255, v94, v95\n\t"
    "v_mfma_f32_16x16x32_bf16 v[68:71], a[200:203], v[128:131], v[68:71]\n\t"
    "v_mul_f32 v22, 0x4119ac22, v22\n\t"
    "v_mul_f32 v23, 0x4119ac22, v23\n\t"
    "v_mfma_f32_16x16x32_bf16 v[68:71], a[204:207], v[160:163], v[68:71]\n\t"
    "buffer_load_dwordx4 v8, s[48:51], s60 offen offset:0 lds\n\t"
    "v_mfma_f32_16x16x32_bf16 v[68:71], a[208:211], v[192:195], v[68:71]\n\t"
    "v_cvt_pk_bf16_f32 v23, v22, v23\n\t"
    "v_lshlrev_b32 v22, 16, v23\n\t"
    "v_mfma_f32_16x16x32_bf16 v[68:71], a[212:215], v[224:227], v[68:71]\n\t"
    "v_and_b32 v23, 0xffff0000, v23\n\t"
    "v_mul_f32 v21, 0xbfb8aa3b, v22\n\t"
    "v_mfma_f32_16x16x32_bf16 v[76:79], a[192:195], v[72:75], v[108:111]\n\t"
    "v_exp_f32 v21, v21\n\t"
    "v_mfma_f32_16x16x32_bf16 v[76:79], a[196:199], v[104:107], v[76:79]\n\t"
    "v_add_f32 v21, 1.0, v21\n\t"
    "v_lshrrev_b32 v20, 8, v4\n\t"
    "v_mfma_f32_16x16x32_bf16 v[76:79], a[200:203], v[136:139], v[76:79]\n\t"
    "v_exp_f32 v68, v68\n\t"
    "v_mfma_f32_16x16x32_bf16 v[76:79], a[204:207], v[168:171], v[76:79]\n\t"
    "v_add_f32 v40, v68, v40\n\t"
    "v_and_b32 v20, 2, v20\n\t"
    "v_mfma_f32_16x16x32_bf16 v[76:79], a[208:211], v[200:203], v[76:79]\n\t"
    "v_exp_f32 v69, v69\n\t"
    "v_mfma_f32_16x16x32_bf16 v[76:79], a[212:215], v[232:235], v[76:79]\n\t"
    "v_add_f32 v40, v69, v40\n\t"
    "v_cvt_pk_bf16_f32 v132, v68, v69\n\t"
    "v_mfma_f32_16x16x32_bf16 v[84:87], a[192:195], v[80:83], v[116:119]\n\t"
    "v_exp_f32 v70, v70\n\t"
    "v_mfma_f32_16x16x32_bf16 v[84:87], a[196:199], v[112:115], v[84:87]\n\t"
    "buffer_load_dwordx4 v9, s[48:51], s60 offen offset:1024 lds\n\t"
    "v_mfma_f32_16x16x32_bf16 v[84:87], a[200:203], v[144:147], v[84:87]\n\t"
    "v_add_f32 v40, v70, v40\n\t"
    "v_lshlrev_b32 v44, 2, v4\n\t"
    "v_mfma_f32_16x16x32_bf16 v[84:87], a[204:207], v[176:179], v[84:87]\n\t"
    "v_exp_f32 v71, v71\n\t"
    "v_mfma_f32_16x16x32_bf16 v[84:87], a[208:211], v[208:211], v[84:87]\n\t"
    "v_add_f32 v40, v71, v40\n\t"
    "v_cvt_pk_bf16_f32 v133, v70, v71\n\t"
    "v_mfma_f32_16x16x32_bf16 v[84:87], a[212:215], v[240:243], v[84:87]\n\t"
    "v_exp_f32 v76, v76\n\t"
    "v_mfma_f32_16x16x32_bf16 v[92:95], a[192:195], v[88:91], v[124:127]\n\t"
    "buffer_load_dwordx4 v10, s[48:51], s60 offen offset:2048 lds\n\t"
    "v_mfma_f32_16x16x32_bf16 v[92:95], a[196:199], v[120:123], v[92:95]\n\t"
    "s_add_u32 m0, s83, 76800\n\t"
    "ds_read_b128 a[192:195], v30 offset:30720\n\t"
    "v_add_f32 v41, v76, v41\n\t"
    "v_mfma_f32_16x16x32_bf16 v[92:95], a[200:203], v[152:155], v[92:95]\n\t"
    "s_add_u32 s60, s60, 3072\n\t"
    "v_exp_f32 v77, v77\n\t"
    "v_mfma_f32_16x16x32_bf16 v[92:95], a[204:207], v[184:187], v[92:95]\n\t"
    "ds_read_b128 a[196:199], v31 offset:30720\n\t"
    "v_add_f32 v41, v77, v41\n\t"
    "v_mfma_f32_16x16x32_bf16 v[92:95], a[208:211], v[216:219], v[92:95]\n\t"
    "v_rcp_f32 v21, v21\n\t"
    "v_mfma_f32_16x16x32_bf16 v[92:95], a[212:215], v[248:251], v[92:95]\n\t"
    "v_exp_f32 v78, v78\n\t"
    "v_mfma_f32_16x16x32_bf16 a[0:3], a[216:219], v[196:199], a[0:3]\n\t"
    "s_waitcnt lgkmcnt(2)\n\t"
    "ds_read_b128 a[200:203], v30 offset:30848\n\t"
    "v_cvt_pk_bf16_f32 v140, v76, v77\n\t"
    "v_mfma_f32_16x16x32_bf16 a[0:3], a[220:223], v[228:231], a[0:3]\n\t"
    "ds_read_b128 a[204:207], v31 offset:30848\n\t"
    "v_mul_f32 v22, v22, v21\n\t"
    "v_mfma_f32_16x16x32_bf16 a[4:7], a[216:219], v[204:207], a[4:7]\n\t"
    "ds_read_b128 a[208:211], v30 offset:30976\n\t"
    "v_add_f32 v41, v78, v41\n\t"
    "v_mfma_f32_16x16x32_bf16 a[4:7], a[220:223], v[236:239], a[4:7]\n\t"
    "v_exp_f32 v79, v79\n\t"
    "v_mfma_f32_16x16x32_bf16 a[8:11], a[216:219], v[212:215], a[8:11]\n\t"
    "ds_read_b128 a[212:215], v31 offset:30976\n\t"
    "v_cvt_pk_bf16_f32 v22, v22, v22\n\t"
    "v_mfma_f32_16x16x32_bf16 a[8:11], a[220:223], v[244:247], a[8:11]\n\t"
    "v_add_f32 v41, v79, v41\n\t"
    "v_lshlrev_b32 v22, 16, v22\n\t"
    "v_mfma_f32_16x16x32_bf16 a[12:15], a[216:219], v[220:223], a[12:15]\n\t"
    "ds_read_b64_tr_b16 a[216:217], v18 offset:0\n\t"
    "v_cvt_pk_bf16_f32 v141, v78, v79\n\t"
    "v_mfma_f32_16x16x32_bf16 a[12:15], a[220:223], v[252:255], a[12:15]\n\t"
    "v_exp_f32 v84, v84\n\t"
    "v_mfma_f32_16x16x32_bf16 a[16:19], a[224:227], v[196:199], a[16:19]\n\t"
    "ds_read_b64_tr_b16 a[218:219], v18 offset:6144\n\t"
    "v_mul_f32 v22, v22, v23\n\t"
    "v_mfma_f32_16x16x32_bf16 a[16:19], a[228:231], v[228:231], a[16:19]\n\t"
    "ds_read_b64_tr_b16 a[220:221], v18 offset:12288\n\t"
    "v_add_f32 v42, v84, v42\n\t"
    "v_mfma_f32_16x16x32_bf16 a[20:23], a[224:227], v[204:207], a[20:23]\n\t"
    "v_exp_f32 v85, v85\n\t"
    "v_mfma_f32_16x16x32_bf16 a[20:23], a[228:231], v[236:239], a[20:23]\n\t"
    "ds_read_b64_tr_b16 a[222:223], v18 offset:18432\n\t"
    "v_cvt_pk_bf16_f32 v22, v22, v22\n\t"
    "v_mfma_f32_16x16x32_bf16 a[24:27], a[224:227], v[212:215], a[24:27]\n\t"
    "v_add_f32 v42, v85, v42\n\t"
    "v_lshrrev_b32 v21, 2, v4\n\t"
    "v_mfma_f32_16x16x32_bf16 a[24:27], a[228:231], v[244:247], a[24:27]\n\t"
    "v_exp_f32 v86, v86\n\t"
    "v_mfma_f32_16x16x32_bf16 a[28:31], a[224:227], v[220:223], a[28:31]\n\t"
    "ds_read_b64_tr_b16 a[224:225], v15 offset:128\n\t"
    "v_cvt_pk_bf16_f32 v148, v84, v85\n\t"
    "v_mfma_f32_16x16x32_bf16 a[28:31], a[228:231], v[252:255], a[28:31]\n\t"
    "buffer_load_dwordx4 v8, s[48:51], s60 offen offset:0 lds\n\t"
    "v_mfma_f32_16x16x32_bf16 a[32:35], a[232:235], v[196:199], a[32:35]\n\t"
    "ds_read_b64_tr_b16 a[226:227], v15 offset:6272\n\t"
    "v_and_b32 v21, 0x7c, v21\n\t"
    "v_mfma_f32_16x16x32_bf16 a[32:35], a[236:239], v[228:231], a[32:35]\n\t"
    "ds_read_b64_tr_b16 a[228:229], v15 offset:12416\n\t"
    "v_add_f32 v42, v86, v42\n\t"
    "v_mfma_f32_16x16x32_bf16 a[36:39], a[232:235], v[204:207], a[36:39]\n\t"
    "v_exp_f32 v87, v87\n\t"
    "v_mfma_f32_16x16x32_bf16 a[36:39], a[236:239], v[236:239], a[36:39]\n\t"
    "ds_read_b64_tr_b16 a[230:231], v15 offset:18560\n\t"
    "v_add_f32 v42, v87, v42\n\t"
    "v_mfma_f32_16x16x32_bf16 a[40:43], a[232:235], v[212:215], a[40:43]\n\t"
    "buffer_load_dwordx4 v9, s[48:51], s60 offen offset:1024 lds\n\t"
    "v_mfma_f32_16x16x32_bf16 a[40:43], a[236:239], v[244:247], a[40:43]\n\t"
    "v_cvt_pk_bf16_f32 v149, v86, v87\n\t"
    "v_add3_u32 v21, v21, v20, s86\n\t"
    "v_mfma_f32_16x16x32_bf16 a[44:47], a[232:235], v[220:223], a[44:47]\n\t"
    "v_exp_f32 v92, v92\n\t"
    "v_mfma_f32_16x16x32_bf16 a[44:47], a[236:239], v[252:255], a[44:47]\n\t"
    "s_waitcnt lgkmcnt(4)\n\t"
    "ds_write_b16 v21, v22 offset:0\n\t"
    "v_add_f32 v43, v92, v43\n\t"
    "v_mfma_f32_16x16x32_bf16 v[68:71], a[192:195], v[64:67], v[100:103]\n\t"
    "KFP9:\n\t"
    "s_cbranch_execz KFP13\n\t"
    "v_exp_f32 v93, v93\n\t"
    "v_mfma_f32_16x16x32_bf16 v[68:71], a[196:199], v[96:99], v[68:71]\n\t"
    "buffer_load_dwordx4 v10, s[48:51], s60 offen offset:2048 lds\n\t"
    "v_mfma_f32_16x16x32_bf16 v[68:71], a[200:203], v[128:131], v[68:71]\n\t"
    "s_add_u32 m0, s83, 98304\n\t"
    "v_add_f32 v43, v93, v43\n\t"
    "v_mfma_f32_16x16x32_bf16 v[68:71], a[204:207], v[160:163], v[68:71]\n\t"
    "s_add_u32 s60, s87, 20480\n\t"
    "v_cvt_pk_bf16_f32 v156, v92, v93\n\t"
    "v_mfma_f32_16x16x32_bf16 v[68:71], a[208:211], v[192:195], v[68:71]\n\t"
    "v_exp_f32 v94, v94\n\t"
    "v_mfma_f32_16x16x32_bf16 v[68:71], a[212:215], v[224:227], v[68:71]\n\t"
    "s_bfe_u32 s60, s60, 0x2000c\n\t"
    "ds_read_b64_tr_b16 a[232:233], v16 offset:128\n\t"
    "v_and_b32 v44, 64, v44\n\t"
    "v_mfma_f32_16x16x32_bf16 v[76:79], a[192:195], v[72:75], v[108:111]\n\t"
    "s_add_u32 s60, s60, 4\n\t"
    "s_bfm_b64 exec, 60, 0\n\t"
    "buffer_load_dword v27, v61, s[40:43], s93 offen sc1\n\t"
    "s_mov_b64 exec, -1\n\t"
    "v_mfma_f32_16x16x32_bf16 v[76:79], a[196:199], v[104:107], v[76:79]\n\t"
    "s_mulk_i32 s60, 24576\n\t"
    "ds_read_b64_tr_b16 a[234:235], v16 offset:6272\n\t"
    "v_add_f32 v43, v94, v43\n\t"
    "v_mfma_f32_16x16x32_bf16 v[76:79], a[200:203], v[136:139], v[76:79]\n\t"
    "v_exp_f32 v95, v95\n\t"
    "v_mfma_f32_16x16x32_bf16 v[76:79], a[204:207], v[168:171], v[76:79]\n\t"
    "ds_read_b64_tr_b16 a[236:237], v16 offset:12416\n\t"
    "v_lshrrev_b32 v45, 5, v4\n\t"
    "v_mfma_f32_16x16x32_bf16 v[76:79], a[208:211], v[200:203], v[76:79]\n\t"
    "ds_read_b64_tr_b16 a[238:239], v16 offset:18560\n\t"
    "v_add_f32 v43, v95, v43\n\t"
    "v_mfma_f32_16x16x32_bf16 v[76:79], a[212:215], v[232:235], v[76:79]\n\t"
    "v_and_b32 v45, 24, v45\n\t"
    "v_cvt_pk_bf16_f32 v157, v94, v95\n\t"
    "v_mfma_f32_16x16x32_bf16 v[84:87], a[192:195], v[80:83], v[116:119]\n\t"
    "v_exp_f32 v68, v68\n\t"
    "v_mfma_f32_16x16x32_bf16 v[84:87], a[196:199], v[112:115], v[84:87]\n\t"
    "v_add3_u32 v44, v44, v45, s86\n\t"
    "v_add_f32 v40, v68, v40\n\t"
    "v_mfma_f32_16x16x32_bf16 v[84:87], a[200:203], v[144:147], v[84:87]\n\t"
    "v_exp_f32 v69, v69\n\t"
    "v_mfma_f32_16x16x32_bf16 v[84:87], a[204:207], v[176:179], v[84:87]\n\t"
    "s_waitcnt lgkmcnt(4)\n\t"
    "ds_read_b64 v[20:21], v44 offset:0\n\t"
    "v_add_f32 v40, v69, v40\n\t"
    "v_mfma_f32_16x16x32_bf16 v[84:87], a[208:211], v[208:211], v[84:87]\n\t"
    "ds_read_b64 v[22:23], v44 offset:32\n\t"
    "v_cvt_pk_bf16_f32 v134, v68, v69\n\t"
    "v_mfma_f32_16x16x32_bf16 v[84:87], a[212:215], v[240:243], v[84:87]\n\t"
    "v_exp_f32 v70, v70\n\t"
    "v_mfma_f32_16x16x32_bf16 v[92:95], a[192:195], v[88:91], v[124:127]\n\t"
    "ds_read_b128 a[192:195], v30 offset:36864\n\t"
    "v_add_f32 v40, v70, v40\n\t"
    "v_mfma_f32_16x16x32_bf16 v[92:95], a[196:199], v[120:123], v[92:95]\n\t"
    "v_exp_f32 v71, v71\n\t"
    "v_mfma_f32_16x16x32_bf16 v[92:95], a[200:203], v[152:155], v[92:95]\n\t"
    "ds_read_b128 a[196:199], v31 offset:36864\n\t"
    "v_add_f32 v40, v71, v40\n\t"
    "v_mfma_f32_16x16x32_bf16 v[92:95], a[204:207], v[184:187], v[92:95]\n\t"
    "ds_read_b128 a[200:203], v30 offset:36992\n\t"
    "v_cvt_pk_bf16_f32 v135, v70, v71\n\t"
    "v_mfma_f32_16x16x32_bf16 v[92:95], a[208:211], v[216:219], v[92:95]\n\t"
    "v_exp_f32 v76, v76\n\t"
    "v_mfma_f32_16x16x32_bf16 v[92:95], a[212:215], v[248:251], v[92:95]\n\t"
    "buffer_load_dwordx4 v8, s[52:55], s60 offen offset:0 lds\n\t"
    "v_mfma_f32_16x16x32_bf16 a[48:51], a[216:219], v[196:199], a[48:51]\n\t"
    "ds_read_b128 a[204:207], v31 offset:36992\n\t"
    "v_add_f32 v41, v76, v41\n\t"
    "v_mfma_f32_16x16x32_bf16 a[48:51], a[220:223], v[228:231], a[48:51]\n\t"
    "v_exp_f32 v77, v77\n\t"
    "v_mfma_f32_16x16x32_bf16 a[52:55], a[216:219], v[204:207], a[52:55]\n\t"
    "ds_read_b128 a[208:211], v30 offset:37120\n\t"
    "v_add_f32 v41, v77, v41\n\t"
    "v_mfma_f32_16x16x32_bf16 a[52:55], a[220:223], v[236:239], a[52:55]\n\t"
    "v_exp_f32 v78, v78\n\t"
    "v_mfma_f32_16x16x32_bf16 a[56:59], a[216:219], v[212:215], a[56:59]\n\t"
    "ds_read_b128 a[212:215], v31 offset:37120\n\t"
    "v_cvt_pk_bf16_f32 v142, v76, v77\n\t"
    "v_mfma_f32_16x16x32_bf16 a[56:59], a[220:223], v[244:247], a[56:59]\n\t"
    "buffer_load_dwordx4 v9, s[52:55], s60 offen offset:1024 lds\n\t"
    "v_mfma_f32_16x16x32_bf16 a[60:63], a[216:219], v[220:223], a[60:63]\n\t"
    "ds_read_b64_tr_b16 a[216:217], v17 offset:128\n\t"
    "v_add_f32 v41, v78, v41\n\t"
    "v_mfma_f32_16x16x32_bf16 a[60:63], a[220:223], v[252:255], a[60:63]\n\t"
    "v_exp_f32 v79, v79\n\t"
    "v_mfma_f32_16x16x32_bf16 a[64:67], a[224:227], v[196:199], a[64:67]\n\t"
    "ds_read_b64_tr_b16 a[218:219], v17 offset:6272\n\t"
    "v_add_f32 v41, v79, v41\n\t"
    "v_mfma_f32_16x16x32_bf16 a[64:67], a[228:231], v[228:231], a[64:67]\n\t"
    "ds_read_b64_tr_b16 a[220:221], v17 offset:12416\n\t"
    "v_cvt_pk_bf16_f32 v143, v78, v79\n\t"
    "v_mfma_f32_16x16x32_bf16 a[68:71], a[224:227], v[204:207], a[68:71]\n\t"
    "v_exp_f32 v84, v84\n\t"
    "v_mfma_f32_16x16x32_bf16 a[68:71], a[228:231], v[236:239], a[68:71]\n\t"
    "buffer_load_dwordx4 v10, s[52:55], s60 offen offset:2048 lds\n\t"
    "v_mfma_f32_16x16x32_bf16 a[72:75], a[224:227], v[212:215], a[72:75]\n\t"
    "s_add_u32 m0, s83, 101376\n\t"
    "ds_read_b64_tr_b16 a[222:223], v17 offset:18560\n\t"
    "v_add_f32 v42, v84, v42\n\t"
    "v_mfma_f32_16x16x32_bf16 a[72:75], a[228:231], v[244:247], a[72:75]\n\t"
    "s_add_u32 s60, s60, 3072\n\t"
    "v_exp_f32 v85, v85\n\t"
    "v_mfma_f32_16x16x32_bf16 a[76:79], a[224:227], v[220:223], a[76:79]\n\t"
    "ds_read_b64_tr_b16 a[224:225], v18 offset:128\n\t"
    "v_add_f32 v42, v85, v42\n\t"
    "v_mfma_f32_16x16x32_bf16 a[76:79], a[228:231], v[252:255], a[76:79]\n\t"
    "s_waitcnt lgkmcnt(6)\n\t"
    "v_exp_f32 v86, v86\n\t"
    "v_mfma_f32_16x16x32_bf16 a[80:83], a[232:235], v[196:199], a[80:83]\n\t"
    "ds_read_b64_tr_b16 a[226:227], v18 offset:6272\n\t"
    "v_cvt_pk_bf16_f32 v150, v84, v85\n\t"
    "v_mfma_f32_16x16x32_bf16 a[80:83], a[236:239], v[228:231], a[80:83]\n\t"
    "ds_read_b64_tr_b16 a[228:229], v18 offset:12416\n\t"
    "v_add_f32 v42, v86, v42\n\t"
    "v_mfma_f32_16x16x32_bf16 a[84:87], a[232:235], v[204:207], a[84:87]\n\t"
    "ds_read_b64_tr_b16 a[230:231], v18 offset:18560\n\t"
    "v_and_b32 v45, 0xf0, v4\n\t"
    "v_mfma_f32_16x16x32_bf16 a[84:87], a[236:239], v[236:239], a[84:87]\n\t"
    "v_lshrrev_b32 v47, 4, v4\n\t"
    "v_mul_u32_u24 v46, 0xa0, v45\n\t"
    "v_mfma_f32_16x16x32_bf16 a[88:91], a[232:235], v[212:215], a[88:91]\n\t"
    "v_exp_f32 v87, v87\n\t"
    "v_mfma_f32_16x16x32_bf16 a[88:91], a[236:239], v[244:247], a[88:91]\n\t"
    "v_and_b32 v47, 48, v47\n\t"
    "v_add3_u32 v46, v46, v47, s66\n\t"
    "v_mfma_f32_16x16x32_bf16 a[92:95], a[232:235], v[220:223], a[92:95]\n\t"
    "ds_read_b64_tr_b16 a[232:233], v15 offset:256\n\t"
    "v_add_f32 v42, v87, v42\n\t"
    "v_mfma_f32_16x16x32_bf16 a[92:95], a[236:239], v[252:255], a[92:95]\n\t"
    "ds_read_b64_tr_b16 a[234:235], v15 offset:6400\n\t"
    "v_mov_b32 v47, 0x7ffffff0\n\t"
    "v_cmp_gt_u32 vcc, 32, v45\n\t"
    "s_nop 1\n\t"
    "v_cndmask_b32 v62, v47, v46, vcc\n\t"
    "v_mfma_f32_16x16x32_bf16 v[44:47], a[240:243], v[20:23], 0\n\t"
    "ds_read_b64_tr_b16 a[236:237], v15 offset:12544\n\t"
    "v_cvt_pk_bf16_f32 v151, v86, v87\n\t"
    "v_mfma_f32_16x16x32_bf16 v[48:51], a[244:247], v[20:23], 0\n\t"
    "v_exp_f32 v92, v92\n\t"
    "v_mfma_f32_16x16x32_bf16 v[52:55], a[248:251], v[20:23], 0\n\t"
    "ds_read_b64_tr_b16 a[238:239], v15 offset:18688\n\t"
    "v_add_f32 v43, v92, v43\n\t"
    "v_mfma_f32_16x16x32_bf16 v[56:59], a[252:255], v[20:23], 0\n\t"
    "v_exp_f32 v93, v93\n\t"
    "v_mfma_f32_16x16x32_bf16 v[68:71], a[192:195], v[64:67], v[100:103]\n\t"
    "buffer_store_dwordx4 v[44:47], v62, s[40:43], s92 offen offset:0 sc1\n\t"
    "v_mfma_f32_16x16x32_bf16 v[68:71], a[196:199], v[96:99], v[68:71]\n\t"
    "v_add_f32 v43, v93, v43\n\t"
    "v_cvt_pk_bf16_f32 v158, v92, v93\n\t"
    "v_mfma_f32_16x16x32_bf16 v[68:71], a[200:203], v[128:131], v[68:71]\n\t"
    "v_exp_f32 v94, v94\n\t"
    "v_mfma_f32_16x16x32_bf16 v[68:71], a[204:207], v[160:163], v[68:71]\n\t"
    "buffer_load_dwordx4 v8, s[52:55], s60 offen offset:0 lds\n\t"
    "v_mfma_f32_16x16x32_bf16 v[68:71], a[208:211], v[192:195], v[68:71]\n\t"
    "s_waitcnt lgkmcnt(4)\n\t"
    "buffer_store_dwordx4 v[48:51], v62, s[40:43], s92 offen offset:64 sc1\n\t"
    "v_mfma_f32_16x16x32_bf16 v[68:71], a[212:215], v[224:227], v[68:71]\n\t"
    "v_add_f32 v43, v94, v43\n\t"
    "v_and_b32 v21, 0x3c0, v4\n\t"
    "v_mfma_f32_16x16x32_bf16 v[76:79], a[192:195], v[72:75], v[108:111]\n\t"
    "v_exp_f32 v95, v95\n\t"
    "v_mfma_f32_16x16x32_bf16 v[76:79], a[196:199], v[104:107], v[76:79]\n\t"
    "v_add_f32 v43, v95, v43\n\t"
    "v_cvt_pk_bf16_f32 v159, v94, v95\n\t"
    "v_mfma_f32_16x16x32_bf16 v[76:79], a[200:203], v[136:139], v[76:79]\n\t"
    "buffer_store_dwordx4 v[52:55], v62, s[40:43], s92 offen offset:128 sc1\n\t"
    "v_mfma_f32_16x16x32_bf16 v[76:79], a[204:207], v[168:171], v[76:79]\n\t"
    "v_exp_f32 v68, v68\n\t"
    "v_mfma_f32_16x16x32_bf16 v[76:79], a[208:211], v[200:203], v[76:79]\n\t"
    "buffer_load_dwordx4 v9, s[52:55], s60 offen offset:1024 lds\n\t"
    "v_mfma_f32_16x16x32_bf16 v[76:79], a[212:215], v[232:235], v[76:79]\n\t"
    "v_add_f32 v40, v68, v40\n\t"
    "v_mul_u32_u24 v20, 0x50, v21\n\t"
    "v_mfma_f32_16x16x32_bf16 v[84:87], a[192:195], v[80:83], v[116:119]\n\t"
    "buffer_store_dwordx4 v[56:59], v62, s[40:43], s92 offen offset:192 sc1\n\t"
    "v_mfma_f32_16x16x32_bf16 v[84:87], a[196:199], v[112:115], v[84:87]\n\t"
    "v_exp_f32 v69, v69\n\t"
    "v_mfma_f32_16x16x32_bf16 v[84:87], a[200:203], v[144:147], v[84:87]\n\t"
    "v_add_f32 v40, v69, v40\n\t"
    "v_cvt_pk_bf16_f32 v164, v68, v69\n\t"
    "v_mfma_f32_16x16x32_bf16 v[84:87], a[204:207], v[176:179], v[84:87]\n\t"
    "buffer_load_dwordx4 v10, s[52:55], s60 offen offset:2048 lds\n\t"
    "v_mfma_f32_16x16x32_bf16 v[84:87], a[208:211], v[208:211], v[84:87]\n\t"
    "s_add_u32 m0, s83, 122880\n\t"
    "v_exp_f32 v70, v70\n\t"
    "v_mfma_f32_16x16x32_bf16 v[84:87], a[212:215], v[240:243], v[84:87]\n\t"
    "s_add_u32 s60, s87, 28672\n\t"
    "v_add_f32 v40, v70, v40\n\t"
    "v_and_b32 v22, 48, v4\n\t"
    "v_mfma_f32_16x16x32_bf16 v[92:95], a[192:195], v[88:91], v[124:127]\n\t"
    "s_bfe_u32 s60, s60, 0x2000c\n\t"
    "v_exp_f32 v71, v71\n\t"
    "v_mfma_f32_16x16x32_bf16 v[92:95], a[196:199], v[120:123], v[92:95]\n\t"
    "s_add_u32 s60, s60, 4\n\t"
    "ds_read_b128 a[192:195], v30 offset:43008\n\t"
    "v_add_f32 v40, v71, v40\n\t"
    "v_mfma_f32_16x16x32_bf16 v[92:95], a[200:203], v[152:155], v[92:95]\n\t"
    "s_mulk_i32 s60, 24576\n\t"
    "ds_read_b128 a[196:199], v31 offset:43008\n\t"
    "v_cvt_pk_bf16_f32 v165, v70, v71\n\t"
    "v_mfma_f32_16x16x32_bf16 v[92:95], a[204:207], v[184:187], v[92:95]\n\t"
    "v_exp_f32 v76, v76\n\t"
    "v_mfma_f32_16x16x32_bf16 v[92:95], a[208:211], v[216:219], v[92:95]\n\t"
    "ds_read_b128 a[200:203], v30 offset:43136\n\t"
    "v_add_f32 v41, v76, v41\n\t"
    "v_mfma_f32_16x16x32_bf16 v[92:95], a[212:215], v[248:251], v[92:95]\n\t"
    "v_exp_f32 v77, v77\n\t"
    "v_mfma_f32_16x16x32_bf16 a[96:99], a[216:219], v[196:199], a[96:99]\n\t"
    "ds_read_b128 a[204:207], v31 offset:43136\n\t"
    "v_add_f32 v41, v77, v41\n\t"
    "v_mfma_f32_16x16x32_bf16 a[96:99], a[220:223], v[228:231], a[96:99]\n\t"
    "v_exp_f32 v78, v78\n\t"
    "v_mfma_f32_16x16x32_bf16 a[100:103], a[216:219], v[204:207], a[100:103]\n\t"
    "ds_read_b128 a[208:211], v30 offset:43264\n\t"
    "v_cvt_pk_bf16_f32 v172, v76, v77\n\t"
    "v_mfma_f32_16x16x32_bf16 a[100:103], a[220:223], v[236:239], a[100:103]\n\t"
    "ds_read_b128 a[212:215], v31 offset:43264\n\t"
    "v_add_f32 v41, v78, v41\n\t"
    "v_mfma_f32_16x16x32_bf16 a[104:107], a[216:219], v[212:215], a[104:107]\n\t"
    "v_exp_f32 v79, v79\n\t"
    "v_mfma_f32_16x16x32_bf16 a[104:107], a[220:223], v[244:247], a[104:107]\n\t"
    "v_add_f32 v41, v79, v41\n\t"
    "v_cvt_pk_bf16_f32 v173, v78, v79\n\t"
    "v_mfma_f32_16x16x32_bf16 a[108:111], a[216:219], v[220:223], a[108:111]\n\t"
    "v_exp_f32 v84, v84\n\t"
    "v_mfma_f32_16x16x32_bf16 a[108:111], a[220:223], v[252:255], a[108:111]\n\t"
    "ds_read_b64_tr_b16 a[216:217], v16 offset:256\n\t"
    "v_add_f32 v42, v84, v42\n\t"
    "v_mfma_f32_16x16x32_bf16 a[112:115], a[224:227], v[196:199], a[112:115]\n\t"
    "v_exp_f32 v85, v85\n\t"
    "v_mfma_f32_16x16x32_bf16 a[112:115], a[228:231], v[228:231], a[112:115]\n\t"
    "ds_read_b64_tr_b16 a[218:219], v16 offset:6400\n\t"
    "v_add_f32 v42, v85, v42\n\t"
    "v_mfma_f32_16x16x32_bf16 a[116:119], a[224:227], v[204:207], a[116:119]\n\t"
    "v_exp_f32 v86, v86\n\t"
    "v_mfma_f32_16x16x32_bf16 a[116:119], a[228:231], v[236:239], a[116:119]\n\t"
    "ds_read_b64_tr_b16 a[220:221], v16 offset:12544\n\t"
    "v_cvt_pk_bf16_f32 v180, v84, v85\n\t"
    "v_mfma_f32_16x16x32_bf16 a[120:123], a[224:227], v[212:215], a[120:123]\n\t"
    "ds_read_b64_tr_b16 a[222:223], v16 offset:18688\n\t"
    "v_add_f32 v42, v86, v42\n\t"
    "v_mfma_f32_16x16x32_bf16 a[120:123], a[228:231], v[244:247], a[120:123]\n\t"
    "v_exp_f32 v87, v87\n\t"
    "v_mfma_f32_16x16x32_bf16 a[124:127], a[224:227], v[220:223], a[124:127]\n\t"
    "ds_read_b64_tr_b16 a[224:225], v17 offset:256\n\t"
    "v_add_f32 v42, v87, v42\n\t"
    "v_mfma_f32_16x16x32_bf16 a[124:127], a[228:231], v[252:255], a[124:127]\n\t"
    "s_waitcnt lgkmcnt(5)\n\t"
    "ds_read_b64_tr_b16 a[226:227], v17 offset:6400\n\t"
    "v_cvt_pk_bf16_f32 v181, v86, v87\n\t"
    "v_mfma_f32_16x16x32_bf16 a[128:131], a[232:235], v[196:199], a[128:131]\n\t"
    "v_exp_f32 v92, v92\n\t"
    "v_mfma_f32_16x16x32_bf16 a[128:131], a[236:239], v[228:231], a[128:131]\n\t"
    "ds_read_b64_tr_b16 a[228:229], v17 offset:12544\n\t"
    "v_add_f32 v43, v92, v43\n\t"
    "v_mfma_f32_16x16x32_bf16 a[132:135], a[232:235], v[204:207], a[132:135]\n\t"
    "s_waitcnt vmcnt(10)\n\t"
    "v_cmp_ne_u32 vcc, s80, v27\n\t"
    "s_cbranch_vccnz KFASW\n\t"
    "KFARW:\n\t"
    "v_mfma_f32_16x16x32_bf16 a[132:135], a[236:239], v[236:239], a[132:135]\n\t"
    "v_exp_f32 v93, v93\n\t"
    "v_mfma_f32_16x16x32_bf16 a[136:139], a[232:235], v[212:215], a[136:139]\n\t"
    "ds_read_b64_tr_b16 a[230:231], v17 offset:18688\n\t"
    "v_add_f32 v43, v93, v43\n\t"
    "v_mfma_f32_16x16x32_bf16 a[136:139], a[236:239], v[244:247], a[136:139]\n\t"
    "v_exp_f32 v94, v94\n\t"
    "v_mfma_f32_16x16x32_bf16 a[140:143], a[232:235], v[220:223], a[140:143]\n\t"
    "ds_read_b64_tr_b16 a[232:233], v18 offset:256\n\t"
    "v_cvt_pk_bf16_f32 v188, v92, v93\n\t"
    "v_mfma_f32_16x16x32_bf16 a[140:143], a[236:239], v[252:255], a[140:143]\n\t"
    "ds_read_b64_tr_b16 a[234:235], v18 offset:6400\n\t"
    "v_add_f32 v43, v94, v43\n\t"
    "v_mfma_f32_16x16x32_bf16 v[68:71], a[192:195], v[64:67], v[100:103]\n\t"
    "v_exp_f32 v95, v95\n\t"
    "v_mfma_f32_16x16x32_bf16 v[68:71], a[196:199], v[96:99], v[68:71]\n\t"
    "ds_read_b64_tr_b16 a[236:237], v18 offset:12544\n\t"
    "v_add_f32 v43, v95, v43\n\t"
    "v_mfma_f32_16x16x32_bf16 v[68:71], a[200:203], v[128:131], v[68:71]\n\t"
    "ds_read_b64_tr_b16 a[238:239], v18 offset:18688\n\t"
    "v_cvt_pk_bf16_f32 v189, v94, v95\n\t"
    "v_mfma_f32_16x16x32_bf16 v[68:71], a[204:207], v[160:163], v[68:71]\n\t"
    "v_accvgpr_write_b32 a240, -1\n\t"
    "v_accvgpr_write_b32 a241, -1\n\t"
    "v_mfma_f32_16x16x32_bf16 v[68:71], a[208:211], v[192:195], v[68:71]\n\t"
    "v_accvgpr_write_b32 a242, -1\n\t"
    "v_accvgpr_write_b32 a243, -1\n\t"
    "v_mfma_f32_16x16x32_bf16 v[68:71], a[212:215], v[224:227], v[68:71]\n\t"
    "v_add3_u32 v20, v20, v22, s74\n\t"
    "v_lshrrev_b32 v22, 1, v22\n\t"
    "v_mfma_f32_16x16x32_bf16 v[76:79], a[192:195], v[72:75], v[108:111]\n\t"
    "v_lshrrev_b32 v21, 7, v4\n\t"
    "v_add3_u32 v22, v22, v21, s77\n\t"
    "v_mfma_f32_16x16x32_bf16 v[76:79], a[196:199], v[104:107], v[76:79]\n\t"
    "v_and_b32 v21, 0x1c0, v4\n\t"
    "v_mov_b32 v23, 0x7ffffff0\n\t"
    "v_mfma_f32_16x16x32_bf16 v[76:79], a[200:203], v[136:139], v[76:79]\n\t"
    "v_exp_f32 v68, v68\n\t"
    "v_mfma_f32_16x16x32_bf16 v[76:79], a[204:207], v[168:171], v[76:79]\n\t"
    "v_add_f32 v40, v68, v40\n\t"
    "v_mov_b32 v27, s80\n\t"
    "v_mfma_f32_16x16x32_bf16 v[76:79], a[208:211], v[200:203], v[76:79]\n\t"
    "v_exp_f32 v69, v69\n\t"
    "v_mfma_f32_16x16x32_bf16 v[76:79], a[212:215], v[232:235], v[76:79]\n\t"
    "v_add_f32 v40, v69, v40\n\t"
    "v_cvt_pk_bf16_f32 v166, v68, v69\n\t"
    "v_mfma_f32_16x16x32_bf16 v[84:87], a[192:195], v[80:83], v[116:119]\n\t"
    "v_exp_f32 v70, v70\n\t"
    "v_mfma_f32_16x16x32_bf16 v[84:87], a[196:199], v[112:115], v[84:87]\n\t"
    "v_add_f32 v40, v70, v40\n\t"
    "v_bfe_u32 v24, v4, 7, 1\n\t"
    "v_mfma_f32_16x16x32_bf16 v[84:87], a[200:203], v[144:147], v[84:87]\n\t"
    "v_exp_f32 v71, v71\n\t"
    "v_mfma_f32_16x16x32_bf16 v[84:87], a[204:207], v[176:179], v[84:87]\n\t"
    "v_add_f32 v40, v71, v40\n\t"
    "v_cvt_pk_bf16_f32 v167, v70, v71\n\t"
    "v_mfma_f32_16x16x32_bf16 v[84:87], a[208:211], v[208:211], v[84:87]\n\t"
    "v_exp_f32 v76, v76\n\t"
    "v_mfma_f32_16x16x32_bf16 v[84:87], a[212:215], v[240:243], v[84:87]\n\t"
    "v_add_f32 v41, v76, v41\n\t"
    "v_mul_i32_i24 v24, 0xffffd808, v24\n\t"
    "v_mfma_f32_16x16x32_bf16 v[92:95], a[192:195], v[88:91], v[124:127]\n\t"
    "v_exp_f32 v77, v77\n\t"
    "v_mfma_f32_16x16x32_bf16 v[92:95], a[196:199], v[120:123], v[92:95]\n\t"
    "v_add_f32 v41, v77, v41\n\t"
    "v_cvt_pk_bf16_f32 v174, v76, v77\n\t"
    "v_mfma_f32_16x16x32_bf16 v[92:95], a[200:203], v[152:155], v[92:95]\n\t"
    "s_waitcnt vmcnt(1) lgkmcnt(0)\n\t"
    "s_barrier\n\t"
    "ds_read_b128 a[192:195], v13 offset:24576\n\t"
    "v_mfma_f32_16x16x32_bf16 v[92:95], a[204:207], v[184:187], v[92:95]\n\t"
    "s_add_u32 s65, s87, 327680\n\t"
    "buffer_store_dword v60, v2, s[40:43], s65 offen sc1\n\t"
    "v_mfma_f32_16x16x32_bf16 v[92:95], a[208:211], v[216:219], v[92:95]\n\t"
    "v_exp_f32 v78, v78\n\t"
    "v_mfma_f32_16x16x32_bf16 v[92:95], a[212:215], v[248:251], v[92:95]\n\t"
    "ds_read_b128 a[196:199], v14 offset:24576\n\t"
    "v_add_f32 v41, v78, v41\n\t"
    "v_mfma_f32_16x16x32_bf16 a[144:147], a[216:219], v[196:199], a[144:147]\n\t"
    "ds_read_b128 a[200:203], v13 offset:24704\n\t"
    "v_and_b32 v25, 0xf0, v4\n\t"
    "v_mfma_f32_16x16x32_bf16 a[144:147], a[220:223], v[228:231], a[144:147]\n\t"
    "v_exp_f32 v79, v79\n\t"
    "v_mfma_f32_16x16x32_bf16 a[148:151], a[216:219], v[204:207], a[148:151]\n\t"
    "ds_read_b128 a[204:207], v14 offset:24704\n\t"
    "v_add_f32 v41, v79, v41\n\t"
    "v_mfma_f32_16x16x32_bf16 a[148:151], a[220:223], v[236:239], a[148:151]\n\t"
    "ds_read_b128 a[208:211], v13 offset:24832\n\t"
    "v_cvt_pk_bf16_f32 v175, v78, v79\n\t"
    "v_mfma_f32_16x16x32_bf16 a[152:155], a[216:219], v[212:215], a[152:155]\n\t"
    "v_exp_f32 v84, v84\n\t"
    "v_mfma_f32_16x16x32_bf16 a[152:155], a[220:223], v[244:247], a[152:155]\n\t"
    "ds_read_b128 a[212:215], v14 offset:24832\n\t"
    "v_add_f32 v42, v84, v42\n\t"
    "v_mfma_f32_16x16x32_bf16 a[156:159], a[216:219], v[220:223], a[156:159]\n\t"
    "buffer_store_dwordx4 a[240:243], v4, s[40:43], s61 offen offset:0 sc1\n\t"
    "v_mfma_f32_16x16x32_bf16 a[156:159], a[220:223], v[252:255], a[156:159]\n\t"
    "v_exp_f32 v85, v85\n\t"
    "v_mfma_f32_16x16x32_bf16 a[160:163], a[224:227], v[196:199], a[160:163]\n\t"
    "ds_read_b64_tr_b16 a[216:217], v32 offset:0\n\t"
    "v_add_f32 v42, v85, v42\n\t"
    "v_mfma_f32_16x16x32_bf16 a[160:163], a[228:231], v[228:231], a[160:163]\n\t"
    "v_exp_f32 v86, v86\n\t"
    "v_mfma_f32_16x16x32_bf16 a[164:167], a[224:227], v[204:207], a[164:167]\n\t"
    "ds_read_b64_tr_b16 a[218:219], v32 offset:6144\n\t"
    "v_cvt_pk_bf16_f32 v182, v84, v85\n\t"
    "v_mfma_f32_16x16x32_bf16 a[164:167], a[228:231], v[236:239], a[164:167]\n\t"
    "ds_read_b64_tr_b16 a[220:221], v32 offset:12288\n\t"
    "v_add_f32 v42, v86, v42\n\t"
    "v_mfma_f32_16x16x32_bf16 a[168:171], a[224:227], v[212:215], a[168:171]\n\t"
    "v_exp_f32 v87, v87\n\t"
    "v_mfma_f32_16x16x32_bf16 a[168:171], a[228:231], v[244:247], a[168:171]\n\t"
    "ds_read_b64_tr_b16 a[222:223], v32 offset:18432\n\t"
    "v_add_f32 v42, v87, v42\n\t"
    "v_mfma_f32_16x16x32_bf16 a[172:175], a[224:227], v[220:223], a[172:175]\n\t"
    "ds_read_b64_tr_b16 a[224:225], v33 offset:0\n\t"
    "v_cvt_pk_bf16_f32 v183, v86, v87\n\t"
    "v_mfma_f32_16x16x32_bf16 a[172:175], a[228:231], v[252:255], a[172:175]\n\t"
    "v_exp_f32 v92, v92\n\t"
    "v_mfma_f32_16x16x32_bf16 a[176:179], a[232:235], v[196:199], a[176:179]\n\t"
    "ds_read_b64_tr_b16 a[226:227], v33 offset:6144\n\t"
    "v_add_f32 v43, v92, v43\n\t"
    "v_mfma_f32_16x16x32_bf16 a[176:179], a[236:239], v[228:231], a[176:179]\n\t"
    "v_exp_f32 v93, v93\n\t"
    "v_mfma_f32_16x16x32_bf16 a[180:183], a[232:235], v[204:207], a[180:183]\n\t"
    "ds_read_b64_tr_b16 a[228:229], v33 offset:12288\n\t"
    "v_add_f32 v43, v93, v43\n\t"
    "v_mfma_f32_16x16x32_bf16 a[180:183], a[236:239], v[236:239], a[180:183]\n\t"
    "v_exp_f32 v94, v94\n\t"
    "v_mfma_f32_16x16x32_bf16 a[184:187], a[232:235], v[212:215], a[184:187]\n\t"
    "ds_read_b64_tr_b16 a[230:231], v33 offset:18432\n\t"
    "v_cvt_pk_bf16_f32 v190, v92, v93\n\t"
    "v_mfma_f32_16x16x32_bf16 a[184:187], a[236:239], v[244:247], a[184:187]\n\t"
    "v_add_f32 v43, v94, v43\n\t"
    "v_or_b32 v25, 0x8000, v25\n\t"
    "v_mfma_f32_16x16x32_bf16 a[188:191], a[232:235], v[220:223], a[188:191]\n\t"
    "v_exp_f32 v95, v95\n\t"
    "v_mfma_f32_16x16x32_bf16 a[188:191], a[236:239], v[252:255], a[188:191]\n\t"
    "s_waitcnt lgkmcnt(5)\n\t"
    "ds_read_b64_tr_b16 a[232:233], v34 offset:0\n\t"
    "v_add_f32 v43, v95, v43\n\t"
    "v_mfma_f32_16x16x32_bf16 v[68:71], a[192:195], v[64:67], v[100:103]\n\t"
    "ds_read_b64_tr_b16 a[234:235], v34 offset:6144\n\t"
    "v_cvt_pk_bf16_f32 v191, v94, v95\n\t"
    "v_mfma_f32_16x16x32_bf16 v[68:71], a[196:199], v[96:99], v[68:71]\n\t"
    "buffer_load_dwordx4 v8, s[48:51], s60 offen offset:0 lds\n\t"
    "v_mfma_f32_16x16x32_bf16 v[68:71], a[200:203], v[128:131], v[68:71]\n\t"
    "ds_read_b64_tr_b16 a[236:237], v34 offset:12288\n\t"
    "v_lshlrev_b32 v25, 1, v25\n\t"
    "v_mfma_f32_16x16x32_bf16 v[68:71], a[204:207], v[160:163], v[68:71]\n\t"
    "ds_read_b64_tr_b16 a[238:239], v34 offset:18432\n\t"
    "v_mfma_f32_16x16x32_bf16 v[68:71], a[208:211], v[192:195], v[68:71]\n\t"
    "v_and_b32 v44, 0xf0, v4\n\t"
    "v_mul_u32_u24 v11, 0x50, v44\n\t"
    "v_mfma_f32_16x16x32_bf16 v[68:71], a[212:215], v[224:227], v[68:71]\n\t"
    "s_sub_u32 s66, s87, 49283072\n\t"
    "v_lshrrev_b32 v44, 4, v4\n\t"
    "v_and_b32 v44, 48, v44\n\t"
    "v_mfma_f32_16x16x32_bf16 v[76:79], a[192:195], v[72:75], v[108:111]\n\t"
    "s_lshr_b32 s66, s66, 7\n\t"
    "buffer_load_dwordx4 v9, s[48:51], s60 offen offset:1024 lds\n\t"
    "v_mfma_f32_16x16x32_bf16 v[76:79], a[196:199], v[104:107], v[76:79]\n\t"
    "s_bfe_u32 s67, s86, 0x2000c\n\t"
    "v_add_u32 v11, v11, v44\n\t"
    "v_bfe_u32 v44, v4, 4, 3\n\t"
    "v_mfma_f32_16x16x32_bf16 v[76:79], a[200:203], v[136:139], v[76:79]\n\t"
    "s_and_b32 s71, s82, 1\n\t"
    "v_exp_f32 v68, v68\n\t"
    "v_mfma_f32_16x16x32_bf16 v[76:79], a[204:207], v[168:171], v[76:79]\n\t"
    "s_lshl_b32 s70, s67, 10\n\t"
    "v_add_f32 v40, v68, v40\n\t"
    "v_add_u32 v24, v24, v11\n\t"
    "v_mfma_f32_16x16x32_bf16 v[76:79], a[208:211], v[200:203], v[76:79]\n\t"
    "s_and_b32 s96, s66, 1\n\t"
    "v_exp_f32 v69, v69\n\t"
    "v_mfma_f32_16x16x32_bf16 v[76:79], a[212:215], v[232:235], v[76:79]\n\t"
    "s_bfe_u32 s91, s66, 0x10002\n\t"
    "buffer_store_dwordx4 a[240:243], v4, s[40:43], s61 offen offset:1024 sc1\n\t"
    "v_mfma_f32_16x16x32_bf16 v[84:87], a[192:195], v[80:83], v[116:119]\n\t"
    "s_mulk_i32 s91, 3\n\t"
    "v_add_f32 v40, v69, v40\n\t"
    "v_cvt_pk_bf16_f32 v196, v68, v69\n\t"
    "v_mfma_f32_16x16x32_bf16 v[84:87], a[196:199], v[112:115], v[84:87]\n\t"
    "s_add_u32 s91, s91, s71\n\t"
    "v_exp_f32 v70, v70\n\t"
    "v_mfma_f32_16x16x32_bf16 v[84:87], a[200:203], v[144:147], v[84:87]\n\t"
    "s_sub_u32 s79, s99, 46137344\n\t"
    "v_exp_f32 v71, v71\n\t"
    "v_mfma_f32_16x16x32_bf16 v[84:87], a[204:207], v[176:179], v[84:87]\n\t"
    "s_lshr_b32 s79, s79, 5\n\t"
    "v_add_f32 v40, v70, v40\n\t"
    "v_add_f32 v40, v71, v40\n\t"
    "v_mfma_f32_16x16x32_bf16 v[84:87], a[208:211], v[208:211], v[84:87]\n\t"
    "s_mul_i32 s76, s79, 1280\n\t"
    "v_exp_f32 v76, v76\n\t"
    "v_mfma_f32_16x16x32_bf16 v[84:87], a[212:215], v[240:243], v[84:87]\n\t"
    "s_add_u32 s84, s87, 524288\n\t"
    "v_cvt_pk_bf16_f32 v197, v70, v71\n\t"
    "v_add_f32 v41, v76, v41\n\t"
    "v_mfma_f32_16x16x32_bf16 v[92:95], a[192:195], v[88:91], v[124:127]\n\t"
    "s_xor_b32 s88, s66, 2\n\t"
    "buffer_load_dwordx4 v10, s[48:51], s60 offen offset:2048 lds\n\t"
    "v_mfma_f32_16x16x32_bf16 v[92:95], a[196:199], v[120:123], v[92:95]\n\t"
    "s_add_u32 m0, s83, 125952\n\t"
    "v_exp_f32 v77, v77\n\t"
    "v_mfma_f32_16x16x32_bf16 v[92:95], a[200:203], v[152:155], v[92:95]\n\t"
    "s_add_u32 s60, s60, 3072\n\t"
    "ds_read_b128 a[192:195], v13 offset:30720\n\t"
    "v_add_f32 v41, v77, v41\n\t"
    "v_mfma_f32_16x16x32_bf16 v[92:95], a[204:207], v[184:187], v[92:95]\n\t"
    "s_mulk_i32 s88, 30720\n\t"
    "v_exp_f32 v78, v78\n\t"
    "v_mfma_f32_16x16x32_bf16 v[92:95], a[208:211], v[216:219], v[92:95]\n\t"
    "s_xor_b32 s89, s66, 4\n\t"
    "ds_read_b128 a[196:199], v14 offset:30720\n\t"
    "v_cvt_pk_bf16_f32 v204, v76, v77\n\t"
    "v_mfma_f32_16x16x32_bf16 v[92:95], a[212:215], v[248:251], v[92:95]\n\t"
    "ds_read_b128 a[200:203], v13 offset:30848\n\t"
    "v_add_f32 v41, v78, v41\n\t"
    "v_mfma_f32_16x16x32_bf16 a[0:3], a[216:219], v[132:135], a[0:3]\n\t"
    "s_waitcnt lgkmcnt(3)\n\t"
    "v_exp_f32 v79, v79\n\t"
    "v_mfma_f32_16x16x32_bf16 a[0:3], a[220:223], v[164:167], a[0:3]\n\t"
    "s_mulk_i32 s89, 30720\n\t"
    "ds_read_b128 a[204:207], v14 offset:30848\n\t"
    "v_add_f32 v41, v79, v41\n\t"
    "v_mfma_f32_16x16x32_bf16 a[4:7], a[216:219], v[140:143], a[4:7]\n\t"
    "s_xor_b32 s90, s66, 6\n\t"
    "ds_read_b128 a[208:211], v13 offset:30976\n\t"
    "v_cvt_pk_bf16_f32 v205, v78, v79\n\t"
    "v_mfma_f32_16x16x32_bf16 a[4:7], a[220:223], v[172:175], a[4:7]\n\t"
    "s_mulk_i32 s90, 30720\n\t"
    "v_exp_f32 v84, v84\n\t"
    "v_mfma_f32_16x16x32_bf16 a[8:11], a[216:219], v[148:151], a[8:11]\n\t"
    "s_xor_b32 s92, s82, 0\n\t"
    "ds_read_b128 a[212:215], v14 offset:30976\n\t"
    "v_add_f32 v42, v84, v42\n\t"
    "v_mfma_f32_16x16x32_bf16 a[8:11], a[220:223], v[180:183], a[8:11]\n\t"
    "s_and_b32 s92, s92, 3\n\t"
    "v_exp_f32 v85, v85\n\t"
    "v_mfma_f32_16x16x32_bf16 a[12:15], a[216:219], v[156:159], a[12:15]\n\t"
    "s_mul_i32 s92, s92, 196608\n\t"
    "ds_read_b64_tr_b16 a[216:217], v35 offset:0\n\t"
    "v_add_f32 v42, v85, v42\n\t"
    "v_mfma_f32_16x16x32_bf16 a[12:15], a[220:223], v[188:191], a[12:15]\n\t"
    "v_exp_f32 v86, v86\n\t"
    "v_mfma_f32_16x16x32_bf16 a[16:19], a[224:227], v[132:135], a[16:19]\n\t"
    "buffer_store_dwordx4 a[240:243], v12, s[40:43], s61 offen offset:2048 sc1\n\t"
    "v_mfma_f32_16x16x32_bf16 a[16:19], a[228:231], v[164:167], a[16:19]\n\t"
    "s_add_u32 s61, s85, 20480\n\t"
    "ds_read_b64_tr_b16 a[218:219], v35 offset:6144\n\t"
    "v_cvt_pk_bf16_f32 v212, v84, v85\n\t"
    "v_mfma_f32_16x16x32_bf16 a[20:23], a[224:227], v[140:143], a[20:23]\n\t"
    "s_add_u32 s92, s92, s72\n\t"
    "ds_read_b64_tr_b16 a[220:221], v35 offset:12288\n\t"
    "v_add_f32 v42, v86, v42\n\t"
    "v_mfma_f32_16x16x32_bf16 a[20:23], a[228:231], v[172:175], a[20:23]\n\t"
    "s_xor_b32 s93, s82, 1\n\t"
    "v_exp_f32 v87, v87\n\t"
    "v_mfma_f32_16x16x32_bf16 a[24:27], a[224:227], v[148:151], a[24:27]\n\t"
    "s_and_b32 s93, s93, 3\n\t"
    "ds_read_b64_tr_b16 a[222:223], v35 offset:18432\n\t"
    "v_add_f32 v42, v87, v42\n\t"
    "v_mfma_f32_16x16x32_bf16 a[24:27], a[228:231], v[180:183], a[24:27]\n\t"
    "s_mul_i32 s93, s93, 196608\n\t"
    "buffer_load_dwordx4 v8, s[48:51], s60 offen offset:0 lds\n\t"
    "v_mfma_f32_16x16x32_bf16 a[28:31], a[224:227], v[156:159], a[28:31]\n\t"
    "s_add_u32 s93, s93, s72\n\t"
    "ds_read_b64_tr_b16 a[224:225], v32 offset:128\n\t"
    "v_cvt_pk_bf16_f32 v213, v86, v87\n\t"
    "v_mfma_f32_16x16x32_bf16 a[28:31], a[228:231], v[188:191], a[28:31]\n\t"
    "v_exp_f32 v92, v92\n\t"
    "v_mfma_f32_16x16x32_bf16 a[32:35], a[232:235], v[132:135], a[32:35]\n\t"
    "ds_read_b64_tr_b16 a[226:227], v32 offset:6272\n\t"
    "v_add_f32 v43, v92, v43\n\t"
    "v_mfma_f32_16x16x32_bf16 a[32:35], a[236:239], v[164:167], a[32:35]\n\t"
    "s_xor_b32 s94, s82, 2\n\t"
    "v_exp_f32 v93, v93\n\t"
    "v_mfma_f32_16x16x32_bf16 a[36:39], a[232:235], v[140:143], a[36:39]\n\t"
    "s_and_b32 s94, s94, 3\n\t"
    "buffer_load_dwordx4 v9, s[48:51], s60 offen offset:1024 lds\n\t"
    "v_mfma_f32_16x16x32_bf16 a[36:39], a[236:239], v[172:175], a[36:39]\n\t"
    "s_mul_i32 s94, s94, 196608\n\t"
    "ds_read_b64_tr_b16 a[228:229], v32 offset:12416\n\t"
    "v_add_f32 v43, v93, v43\n\t"
    "v_mfma_f32_16x16x32_bf16 a[40:43], a[232:235], v[148:151], a[40:43]\n\t"
    "s_add_u32 s94, s94, s72\n\t"
    "v_exp_f32 v94, v94\n\t"
    "v_mfma_f32_16x16x32_bf16 a[40:43], a[236:239], v[180:183], a[40:43]\n\t"
    "s_xor_b32 s95, s82, 3\n\t"
    "ds_read_b64_tr_b16 a[230:231], v32 offset:18560\n\t"
    "v_cvt_pk_bf16_f32 v220, v92, v93\n\t"
    "v_mfma_f32_16x16x32_bf16 a[44:47], a[232:235], v[156:159], a[44:47]\n\t"
    "s_and_b32 s95, s95, 3\n\t"
    "ds_read_b64_tr_b16 a[232:233], v33 offset:128\n\t"
    "v_add_f32 v43, v94, v43\n\t"
    "v_mfma_f32_16x16x32_bf16 a[44:47], a[236:239], v[188:191], a[44:47]\n\t"
    "s_waitcnt lgkmcnt(5)\n\t"
    "buffer_load_dwordx4 v10, s[48:51], s60 offen offset:2048 lds\n\t"
    "v_mfma_f32_16x16x32_bf16 v[68:71], a[192:195], v[64:67], v[100:103]\n\t"
    "s_add_u32 m0, s83, 0\n\t"
    "ds_read_b64_tr_b16 a[234:235], v33 offset:6272\n\t"
    "v_mfma_f32_16x16x32_bf16 v[68:71], a[196:199], v[96:99], v[68:71]\n\t"
    "s_add_u32 s60, s87, 24576\n\t"
    "ds_read_b64_tr_b16 a[236:237], v33 offset:12416\n\t"
    "v_mfma_f32_16x16x32_bf16 v[68:71], a[200:203], v[128:131], v[68:71]\n\t"
    "v_exp_f32 v95, v95\n\t"
    "v_mfma_f32_16x16x32_bf16 v[68:71], a[204:207], v[160:163], v[68:71]\n\t"
    "ds_read_b64_tr_b16 a[238:239], v33 offset:18560\n\t"
    "v_add_f32 v43, v95, v43\n\t"
    "v_mfma_f32_16x16x32_bf16 v[68:71], a[208:211], v[192:195], v[68:71]\n\t"
    "s_bfe_u32 s60, s60, 0x2000c\n\t"
    "v_cvt_pk_bf16_f32 v221, v94, v95\n\t"
    "v_mfma_f32_16x16x32_bf16 v[68:71], a[212:215], v[224:227], v[68:71]\n\t"
    "s_add_u32 s60, s60, 4\n\t"
    "v_mfma_f32_16x16x32_bf16 v[76:79], a[192:195], v[72:75], v[108:111]\n\t"
    "s_mulk_i32 s60, 24576\n\t"
    "buffer_load_dwordx4 v8, s[52:55], s60 offen offset:0 lds\n\t"
    "v_mfma_f32_16x16x32_bf16 v[76:79], a[196:199], v[104:107], v[76:79]\n\t"
    "s_mul_i32 s95, s95, 196608\n\t"
    "v_mfma_f32_16x16x32_bf16 v[76:79], a[200:203], v[136:139], v[76:79]\n\t"
    "s_add_u32 s95, s95, s72\n\t"
    "v_exp_f32 v68, v68\n\t"
    "v_mfma_f32_16x16x32_bf16 v[76:79], a[204:207], v[168:171], v[76:79]\n\t"
    "s_mov_b64 s[44:45], s[38:39]\n\t"
    "v_exp_f32 v69, v69\n\t"
    "v_mfma_f32_16x16x32_bf16 v[76:79], a[208:211], v[200:203], v[76:79]\n\t"
    "s_mov_b32 s46, 2621440\n\t"
    "v_add_f32 v40, v68, v40\n\t"
    "v_add_f32 v40, v69, v40\n\t"
    "v_mfma_f32_16x16x32_bf16 v[76:79], a[212:215], v[232:235], v[76:79]\n\t"
    "buffer_store_dwordx4 a[240:243], v4, s[40:43], s61 offen offset:0 sc1\n\t"
    "v_mfma_f32_16x16x32_bf16 v[84:87], a[192:195], v[80:83], v[116:119]\n\t"
    "v_exp_f32 v70, v70\n\t"
    "v_mfma_f32_16x16x32_bf16 v[84:87], a[196:199], v[112:115], v[84:87]\n\t"
    "v_cvt_pk_bf16_f32 v198, v68, v69\n\t"
    "v_add_f32 v40, v70, v40\n\t"
    "v_mfma_f32_16x16x32_bf16 v[84:87], a[200:203], v[144:147], v[84:87]\n\t"
    "v_exp_f32 v71, v71\n\t"
    "v_mfma_f32_16x16x32_bf16 v[84:87], a[204:207], v[176:179], v[84:87]\n\t"
    "v_add_f32 v40, v71, v40\n\t"
    "v_cvt_pk_bf16_f32 v199, v70, v71\n\t"
    "v_mfma_f32_16x16x32_bf16 v[84:87], a[208:211], v[208:211], v[84:87]\n\t"
    "v_exp_f32 v76, v76\n\t"
    "v_mfma_f32_16x16x32_bf16 v[84:87], a[212:215], v[240:243], v[84:87]\n\t"
    "v_exp_f32 v77, v77\n\t"
    "v_mfma_f32_16x16x32_bf16 v[92:95], a[192:195], v[88:91], v[124:127]\n\t"
    "ds_read_b128 a[192:195], v13 offset:36864\n\t"
    "v_add_f32 v41, v76, v41\n\t"
    "v_mfma_f32_16x16x32_bf16 v[92:95], a[196:199], v[120:123], v[92:95]\n\t"
    "ds_read_b128 a[196:199], v14 offset:36864\n\t"
    "v_add_f32 v41, v77, v41\n\t"
    "v_mfma_f32_16x16x32_bf16 v[92:95], a[200:203], v[152:155], v[92:95]\n\t"
    "v_exp_f32 v78, v78\n\t"
    "v_mfma_f32_16x16x32_bf16 v[92:95], a[204:207], v[184:187], v[92:95]\n\t"
    "KFP10:\n\t"
    "s_cbranch_execz KFP14\n\t"
    "ds_read_b128 a[200:203], v13 offset:36992\n\t"
    "v_cvt_pk_bf16_f32 v206, v76, v77\n\t"
    "v_mfma_f32_16x16x32_bf16 v[92:95], a[208:211], v[216:219], v[92:95]\n\t"
    "buffer_load_dwordx4 v9, s[52:55], s60 offen offset:1024 lds\n\t"
    "v_mfma_f32_16x16x32_bf16 v[92:95], a[212:215], v[248:251], v[92:95]\n\t"
    "ds_read_b128 a[204:207], v14 offset:36992\n\t"
    "v_add_f32 v41, v78, v41\n\t"
    "v_mfma_f32_16x16x32_bf16 a[48:51], a[216:219], v[132:135], a[48:51]\n\t"
    "v_exp_f32 v79, v79\n\t"
    "v_mfma_f32_16x16x32_bf16 a[48:51], a[220:223], v[164:167], a[48:51]\n\t"
    "ds_read_b128 a[208:211], v13 offset:37120\n\t"
    "v_add_f32 v41, v79, v41\n\t"
    "v_mfma_f32_16x16x32_bf16 a[52:55], a[216:219], v[140:143], a[52:55]\n\t"
    "ds_read_b128 a[212:215], v14 offset:37120\n\t"
    "v_cvt_pk_bf16_f32 v207, v78, v79\n\t"
    "v_mfma_f32_16x16x32_bf16 a[52:55], a[220:223], v[172:175], a[52:55]\n\t"
    "v_exp_f32 v84, v84\n\t"
    "v_mfma_f32_16x16x32_bf16 a[56:59], a[216:219], v[148:151], a[56:59]\n\t"
    "buffer_load_dwordx4 v10, s[52:55], s60 offen offset:2048 lds\n\t"
    "v_mfma_f32_16x16x32_bf16 a[56:59], a[220:223], v[180:183], a[56:59]\n\t"
    "s_add_u32 m0, s83, 3072\n\t"
    "v_exp_f32 v85, v85\n\t"
    "v_mfma_f32_16x16x32_bf16 a[60:63], a[216:219], v[156:159], a[60:63]\n\t"
    "s_add_u32 s60, s60, 3072\n\t"
    "ds_read_b64_tr_b16 a[216:217], v34 offset:128\n\t"
    "v_add_f32 v42, v84, v42\n\t"
    "v_mfma_f32_16x16x32_bf16 a[60:63], a[220:223], v[188:191], a[60:63]\n\t"
    "s_waitcnt lgkmcnt(5)\n\t"
    "ds_read_b64_tr_b16 a[218:219], v34 offset:6272\n\t"
    "v_add_f32 v42, v85, v42\n\t"
    "v_mfma_f32_16x16x32_bf16 a[64:67], a[224:227], v[132:135], a[64:67]\n\t"
    "v_exp_f32 v86, v86\n\t"
    "v_mfma_f32_16x16x32_bf16 a[64:67], a[228:231], v[164:167], a[64:67]\n\t"
    "ds_read_b64_tr_b16 a[220:221], v34 offset:12416\n\t"
    "v_cvt_pk_bf16_f32 v214, v84, v85\n\t"
    "v_mfma_f32_16x16x32_bf16 a[68:71], a[224:227], v[140:143], a[68:71]\n\t"
    "buffer_store_dwordx4 a[240:243], v4, s[40:43], s61 offen offset:1024 sc1\n\t"
    "v_mfma_f32_16x16x32_bf16 a[68:71], a[228:231], v[172:175], a[68:71]\n\t"
    "ds_read_b64_tr_b16 a[222:223], v34 offset:18560\n\t"
    "v_add_f32 v42, v86, v42\n\t"
    "v_mfma_f32_16x16x32_bf16 a[72:75], a[224:227], v[148:151], a[72:75]\n\t"
    "v_exp_f32 v87, v87\n\t"
    "v_mfma_f32_16x16x32_bf16 a[72:75], a[228:231], v[180:183], a[72:75]\n\t"
    "v_add_f32 v42, v87, v42\n\t"
    "v_cvt_pk_bf16_f32 v215, v86, v87\n\t"
    "v_mfma_f32_16x16x32_bf16 a[76:79], a[224:227], v[156:159], a[76:79]\n\t"
    "v_exp_f32 v92, v92\n\t"
    "v_mfma_f32_16x16x32_bf16 a[76:79], a[228:231], v[188:191], a[76:79]\n\t"
    "ds_read_b64_tr_b16 a[224:225], v35 offset:128\n\t"
    "v_add_f32 v43, v92, v43\n\t"
    "v_mfma_f32_16x16x32_bf16 a[80:83], a[232:235], v[132:135], a[80:83]\n\t"
    "v_exp_f32 v93, v93\n\t"
    "v_mfma_f32_16x16x32_bf16 a[80:83], a[236:239], v[164:167], a[80:83]\n\t"
    "ds_read_b64_tr_b16 a[226:227], v35 offset:6272\n\t"
    "v_add_f32 v43, v93, v43\n\t"
    "v_mfma_f32_16x16x32_bf16 a[84:87], a[232:235], v[140:143], a[84:87]\n\t"
    "v_exp_f32 v94, v94\n\t"
    "v_mfma_f32_16x16x32_bf16 a[84:87], a[236:239], v[172:175], a[84:87]\n\t"
    "ds_read_b64_tr_b16 a[228:229], v35 offset:12416\n\t"
    "v_cvt_pk_bf16_f32 v222, v92, v93\n\t"
    "v_mfma_f32_16x16x32_bf16 a[88:91], a[232:235], v[148:151], a[88:91]\n\t"
    "buffer_load_dwordx4 v8, s[52:55], s60 offen offset:0 lds\n\t"
    "v_mfma_f32_16x16x32_bf16 a[88:91], a[236:239], v[180:183], a[88:91]\n\t"
    "ds_read_b64_tr_b16 a[230:231], v35 offset:18560\n\t"
    "v_add_f32 v43, v94, v43\n\t"
    "v_mfma_f32_16x16x32_bf16 a[92:95], a[232:235], v[156:159], a[92:95]\n\t"
    "v_exp_f32 v95, v95\n\t"
    "v_mfma_f32_16x16x32_bf16 a[92:95], a[236:239], v[188:191], a[92:95]\n\t"
    "ds_read_b64_tr_b16 a[232:233], v32 offset:256\n\t"
    "v_add_f32 v43, v95, v43\n\t"
    "v_mfma_f32_16x16x32_bf16 v[68:71], a[192:195], v[64:67], v[100:103]\n\t"
    "ds_read_b64_tr_b16 a[234:235], v32 offset:6400\n\t"
    "v_cvt_pk_bf16_f32 v223, v94, v95\n\t"
    "v_mfma_f32_16x16x32_bf16 v[68:71], a[196:199], v[96:99], v[68:71]\n\t"
    "s_waitcnt lgkmcnt(6)\n\t"
    "buffer_load_dwordx4 v9, s[52:55], s60 offen offset:1024 lds\n\t"
    "v_mfma_f32_16x16x32_bf16 v[68:71], a[200:203], v[128:131], v[68:71]\n\t"
    "ds_read_b64_tr_b16 a[236:237], v32 offset:12544\n\t"
    "v_mfma_f32_16x16x32_bf16 v[68:71], a[204:207], v[160:163], v[68:71]\n\t"
    "ds_read_b64_tr_b16 a[238:239], v32 offset:18688\n\t"
    "v_mfma_f32_16x16x32_bf16 v[68:71], a[208:211], v[192:195], v[68:71]\n\t"
    "v_mfma_f32_16x16x32_bf16 v[68:71], a[212:215], v[224:227], v[68:71]\n\t"
    "v_mfma_f32_16x16x32_bf16 v[76:79], a[192:195], v[72:75], v[108:111]\n\t"
    "buffer_load_dwordx4 v10, s[52:55], s60 offen offset:2048 lds\n\t"
    "v_mfma_f32_16x16x32_bf16 v[76:79], a[196:199], v[104:107], v[76:79]\n\t"
    "s_add_u32 m0, s83, 49152\n\t"
    "v_mfma_f32_16x16x32_bf16 v[76:79], a[200:203], v[136:139], v[76:79]\n\t"
    "s_add_u32 s60, s87, 28672\n\t"
    "s_nop 1\n\t"
    "v_exp_f32 v68, v68\n\t"
    "v_mfma_f32_16x16x32_bf16 v[76:79], a[204:207], v[168:171], v[76:79]\n\t"
    "s_bfe_u32 s60, s60, 0x2000c\n\t"
    "v_exp_f32 v69, v69\n\t"
    "v_mfma_f32_16x16x32_bf16 v[76:79], a[208:211], v[200:203], v[76:79]\n\t"
    "s_add_u32 s60, s60, 4\n\t"
    "v_add_f32 v40, v68, v40\n\t"
    "v_add_f32 v40, v69, v40\n\t"
    "v_mfma_f32_16x16x32_bf16 v[76:79], a[212:215], v[232:235], v[76:79]\n\t"
    "s_mulk_i32 s60, 24576\n\t"
    "buffer_store_dwordx4 a[240:243], v12, s[40:43], s61 offen offset:2048 sc1\n\t"
    "v_mfma_f32_16x16x32_bf16 v[84:87], a[192:195], v[80:83], v[116:119]\n\t"
    "s_bfe_u32 s61, s66, 0x20003\n\t"
    "v_exp_f32 v70, v70\n\t"
    "v_mfma_f32_16x16x32_bf16 v[84:87], a[196:199], v[112:115], v[84:87]\n\t"
    "s_lshl2_add_u32 s96, s96, s61\n\t"
    "v_add3_u32 v45, v44, s96, 1\n\t"
    "v_and_b32 v46, 3, v45\n\t"
    "v_mfma_f32_16x16x32_bf16 v[84:87], a[200:203], v[144:147], v[84:87]\n\t"
    "s_mul_i32 s64, s61, 5\n\t"
    "v_bfe_u32 v45, v45, 2, 1\n\t"
    "v_lshl_or_b32 v45, v46, 3, v45\n\t"
    "v_mfma_f32_16x16x32_bf16 v[84:87], a[204:207], v[176:179], v[84:87]\n\t"
    "s_add_u32 s91, s91, s64\n\t"
    "v_and_b32 v46, 3, v44\n\t"
    "v_lshlrev_b32 v46, 1, v46\n\t"
    "v_mfma_f32_16x16x32_bf16 v[84:87], a[208:211], v[208:211], v[84:87]\n\t"
    "s_lshl_b32 s91, s91, 6\n\t"
    "v_xor_b32 v46, s66, v46\n\t"
    "v_add_u32 v46, 0x100, v46\n\t"
    "v_mfma_f32_16x16x32_bf16 v[84:87], a[212:215], v[240:243], v[84:87]\n\t"
    "s_add_u32 s76, s76, s91\n\t"
    "v_cvt_pk_bf16_f32 v228, v68, v69\n\t"
    "v_add_f32 v40, v70, v40\n\t"
    "v_mfma_f32_16x16x32_bf16 v[92:95], a[192:195], v[88:91], v[124:127]\n\t"
    "s_mulk_i32 s61, 10240\n\t"
    "v_exp_f32 v71, v71\n\t"
    "v_mfma_f32_16x16x32_bf16 v[92:95], a[196:199], v[120:123], v[92:95]\n\t"
    "s_bfe_u32 s64, s66, 0x20001\n\t"
    "ds_read_b128 a[192:195], v13 offset:43008\n\t"
    "v_add_f32 v40, v71, v40\n\t"
    "v_mfma_f32_16x16x32_bf16 v[92:95], a[200:203], v[152:155], v[92:95]\n\t"
    "s_mul_i32 s64, s64, 1966080\n\t"
    "ds_read_b128 a[196:199], v14 offset:43008\n\t"
    "v_cvt_pk_bf16_f32 v229, v70, v71\n\t"
    "v_mfma_f32_16x16x32_bf16 v[92:95], a[204:207], v[184:187], v[92:95]\n\t"
    "s_add_u32 s61, s61, s64\n\t"
    "v_exp_f32 v76, v76\n\t"
    "v_mfma_f32_16x16x32_bf16 v[92:95], a[208:211], v[216:219], v[92:95]\n\t"
    "s_add_u32 s61, s61, 33554432\n\t"
    "ds_read_b128 a[200:203], v13 offset:43136\n\t"
    "v_add_f32 v41, v76, v41\n\t"
    "v_mfma_f32_16x16x32_bf16 v[92:95], a[212:215], v[248:251], v[92:95]\n\t"
    "s_xor_b32 s98, s96, 7\n\t"
    "ds_read_b128 a[204:207], v14 offset:43136\n\t"
    "v_mfma_f32_16x16x32_bf16 a[96:99], a[216:219], v[132:135], a[96:99]\n\t"
    "s_mul_i32 s98, s98, 245760\n\t"
    "ds_read_b128 a[208:211], v13 offset:43264\n\t"
    "v_mfma_f32_16x16x32_bf16 a[96:99], a[220:223], v[164:167], a[96:99]\n\t"
    "s_add_u32 s98, s98, s61\n\t"
    "v_exp_f32 v77, v77\n\t"
    "v_mfma_f32_16x16x32_bf16 a[100:103], a[216:219], v[140:143], a[100:103]\n\t"
    "s_lshl1_add_u32 s98, s70, s98\n\t"
    "ds_read_b128 a[212:215], v14 offset:43264\n\t"
    "v_add_f32 v41, v77, v41\n\t"
    "v_mfma_f32_16x16x32_bf16 a[100:103], a[220:223], v[172:175], a[100:103]\n\t"
    "s_and_b32 s64, s86, 4096\n\t"
    "v_exp_f32 v78, v78\n\t"
    "v_mfma_f32_16x16x32_bf16 a[104:107], a[216:219], v[148:151], a[104:107]\n\t"
    "s_add_u32 s61, s61, s64\n\t"
    "v_cvt_pk_bf16_f32 v236, v76, v77\n\t"
    "v_add_f32 v41, v78, v41\n\t"
    "v_mfma_f32_16x16x32_bf16 a[104:107], a[220:223], v[180:183], a[104:107]\n\t"
    "s_lshr_b32 s64, s67, 1\n\t"
    "v_exp_f32 v79, v79\n\t"
    "v_mfma_f32_16x16x32_bf16 a[108:111], a[216:219], v[156:159], a[108:111]\n\t"
    "s_mul_i32 s68, s64, 5\n\t"
    "ds_read_b64_tr_b16 a[216:217], v33 offset:256\n\t"
    "v_add_f32 v41, v79, v41\n\t"
    "v_mfma_f32_16x16x32_bf16 a[108:111], a[220:223], v[188:191], a[108:111]\n\t"
    "s_waitcnt lgkmcnt(5)\n\t"
    "s_xor_b32 s68, s68, 1\n\t"
    "v_cvt_pk_bf16_f32 v237, v78, v79\n\t"
    "v_mfma_f32_16x16x32_bf16 a[112:115], a[224:227], v[132:135], a[112:115]\n\t"
    "s_xor_b32 s68, s68, s96\n\t"
    "ds_read_b64_tr_b16 a[218:219], v33 offset:6400\n\t"
    "v_mfma_f32_16x16x32_bf16 a[112:115], a[228:231], v[164:167], a[112:115]\n\t"
    "s_mul_i32 s68, s68, 245760\n\t"
    "v_exp_f32 v84, v84\n\t"
    "v_mfma_f32_16x16x32_bf16 a[116:119], a[224:227], v[140:143], a[116:119]\n\t"
    "s_add_u32 s68, s68, s61\n\t"
    "ds_read_b64_tr_b16 a[220:221], v33 offset:12544\n\t"
    "v_add_f32 v42, v84, v42\n\t"
    "v_mfma_f32_16x16x32_bf16 a[116:119], a[228:231], v[172:175], a[116:119]\n\t"
    "s_mul_i32 s69, s64, 1\n\t"
    "v_exp_f32 v85, v85\n\t"
    "v_mfma_f32_16x16x32_bf16 a[120:123], a[224:227], v[148:151], a[120:123]\n\t"
    "s_xor_b32 s69, s69, 2\n\t"
    "ds_read_b64_tr_b16 a[222:223], v33 offset:18688\n\t"
    "v_add_f32 v42, v85, v42\n\t"
    "v_mfma_f32_16x16x32_bf16 a[120:123], a[228:231], v[180:183], a[120:123]\n\t"
    "s_xor_b32 s69, s69, s96\n\t"
    "v_exp_f32 v86, v86\n\t"
    "v_mfma_f32_16x16x32_bf16 a[124:127], a[224:227], v[156:159], a[124:127]\n\t"
    "s_mul_i32 s69, s69, 245760\n\t"
    "ds_read_b64_tr_b16 a[224:225], v34 offset:256\n\t"
    "v_cvt_pk_bf16_f32 v244, v84, v85\n\t"
    "v_mfma_f32_16x16x32_bf16 a[124:127], a[228:231], v[188:191], a[124:127]\n\t"
    "s_add_u32 s69, s69, s61\n\t"
    "v_add_f32 v42, v86, v42\n\t"
    "v_mfma_f32_16x16x32_bf16 a[128:131], a[232:235], v[132:135], a[128:131]\n\t"
    "s_mul_i32 s97, s64, 3\n\t"
    "ds_read_b64_tr_b16 a[226:227], v34 offset:6400\n\t"
    "v_mfma_f32_16x16x32_bf16 a[128:131], a[236:239], v[164:167], a[128:131]\n\t"
    "s_xor_b32 s97, s97, 5\n\t"
    "v_exp_f32 v87, v87\n\t"
    "v_mfma_f32_16x16x32_bf16 a[132:135], a[232:235], v[140:143], a[132:135]\n\t"
    "s_xor_b32 s97, s97, s96\n\t"
    "ds_read_b64_tr_b16 a[228:229], v34 offset:12544\n\t"
    "v_add_f32 v42, v87, v42\n\t"
    "v_mfma_f32_16x16x32_bf16 a[132:135], a[236:239], v[172:175], a[132:135]\n\t"
    "s_mul_i32 s97, s97, 245760\n\t"
    "ds_read_b64_tr_b16 a[230:231], v34 offset:18688\n\t"
    "v_cvt_pk_bf16_f32 v245, v86, v87\n\t"
    "v_mfma_f32_16x16x32_bf16 a[136:139], a[232:235], v[148:151], a[136:139]\n\t"
    "s_add_u32 s97, s97, s61\n\t"
    "v_exp_f32 v92, v92\n\t"
    "v_mfma_f32_16x16x32_bf16 a[136:139], a[236:239], v[180:183], a[136:139]\n\t"
    "s_mulk_i32 s67, 2560\n\t"
    "v_exp_f32 v93, v93\n\t"
    "v_mfma_f32_16x16x32_bf16 a[140:143], a[232:235], v[156:159], a[140:143]\n\t"
    "s_add_u32 s67, s67, 50331648\n\t"
    "ds_read_b64_tr_b16 a[232:233], v35 offset:256\n\t"
    "v_add_f32 v43, v92, v43\n\t"
    "v_mfma_f32_16x16x32_bf16 a[140:143], a[236:239], v[188:191], a[140:143]\n\t"
    "s_add_u32 s88, s88, s67\n\t"
    "v_add_f32 v43, v93, v43\n\t"
    "v_mfma_f32_16x16x32_bf16 v[68:71], a[192:195], v[64:67], v[100:103]\n\t"
    "s_add_u32 s89, s89, s67\n\t"
    "v_cvt_pk_bf16_f32 v252, v92, v93\n\t"
    "v_mfma_f32_16x16x32_bf16 v[68:71], a[196:199], v[96:99], v[68:71]\n\t"
    "s_waitcnt lgkmcnt(4)\n\t"
    "s_add_u32 s89, s89, 10240\n\t"
    "ds_read_b64_tr_b16 a[234:235], v35 offset:6400\n\t"
    "v_mfma_f32_16x16x32_bf16 v[68:71], a[200:203], v[128:131], v[68:71]\n\t"
    "s_add_u32 s90, s90, s67\n\t"
    "ds_read_b64_tr_b16 a[236:237], v35 offset:12544\n\t"
    "v_mfma_f32_16x16x32_bf16 v[68:71], a[204:207], v[160:163], v[68:71]\n\t"
    "s_add_u32 s90, s90, 20480\n\t"
    "ds_read_b64_tr_b16 a[238:239], v35 offset:18688\n\t"
    "v_mfma_f32_16x16x32_bf16 v[68:71], a[208:211], v[192:195], v[68:71]\n\t"
    "s_mul_i32 s61, s71, 196608\n\t"
    "v_add_u32 v5, s61, v4\n\t"
    "v_mfma_f32_16x16x32_bf16 v[68:71], a[212:215], v[224:227], v[68:71]\n\t"
    "s_sub_u32 s61, 196608, s61\n\t"
    "v_add_u32 v6, s61, v4\n\t"
    "v_mfma_f32_16x16x32_bf16 v[76:79], a[192:195], v[72:75], v[108:111]\n\t"
    "s_andn2_b32 s61, s66, 25\n\t"
    "v_or_b32 v45, s61, v45\n\t"
    "v_add_u32 v45, 0x500, v45\n\t"
    "v_mfma_f32_16x16x32_bf16 v[76:79], a[196:199], v[104:107], v[76:79]\n\t"
    "s_movk_i32 s64, 0x80\n\t"
    "v_cmp_gt_u32 vcc, s64, v4\n\t"
    "s_nop 1\n\t"
    "v_cndmask_b32 v45, v46, v45, vcc\n\t"
    "v_mfma_f32_16x16x32_bf16 v[76:79], a[200:203], v[136:139], v[76:79]\n\t"
    "s_bfe_u32 s61, s87, 0x10009\n\t"
    "v_and_b32 v46, 1, v44\n\t"
    "v_mul_u32_u24 v46, 40, v46\n\t"
    "v_mfma_f32_16x16x32_bf16 v[76:79], a[204:207], v[168:171], v[76:79]\n\t"
    "s_mul_i32 s61, s61, -3\n\t"
    "v_and_b32 v44, 2, v44\n\t"
    "v_mfma_f32_16x16x32_bf16 v[76:79], a[208:211], v[200:203], v[76:79]\n\t"
    "s_mul_i32 s64, s71, -1\n\t"
    "v_exp_f32 v94, v94\n\t"
    "v_mfma_f32_16x16x32_bf16 v[76:79], a[212:215], v[232:235], v[76:79]\n\t"
    "s_add_u32 s64, s64, s61\n\t"
    "v_add_f32 v43, v94, v43\n\t"
    "v_mfma_f32_16x16x32_bf16 v[84:87], a[192:195], v[80:83], v[116:119]\n\t"
    "s_add_u32 s64, s64, 2\n\t"
    "v_mad_i32_i24 v46, v44, s64, v46\n\t"
    "v_mfma_f32_16x16x32_bf16 v[84:87], a[196:199], v[112:115], v[84:87]\n\t"
    "s_lshr_b32 s61, s91, 5\n\t"
    "v_add_u32 v46, s61, v46\n\t"
    "v_lshrrev_b32 v46, 2, v46\n\t"
    "v_mfma_f32_16x16x32_bf16 v[84:87], a[200:203], v[144:147], v[84:87]\n\t"
    "s_movk_i32 s64, 0xc0\n\t"
    "v_add_u32 v46, 0x300, v46\n\t"
    "v_cmp_gt_u32 vcc, s64, v4\n\t"
    "v_mfma_f32_16x16x32_bf16 v[84:87], a[204:207], v[176:179], v[84:87]\n\t"
    "s_sub_u32 s64, s86, 145408\n\t"
    "v_cndmask_b32 v45, v46, v45, vcc\n\t"
    "v_lshlrev_b32 v7, 7, v45\n\t"
    "v_mfma_f32_16x16x32_bf16 v[84:87], a[208:211], v[208:211], v[84:87]\n\t"
    "s_lshl_b32 s64, s64, 1\n\t"
    "v_exp_f32 v95, v95\n\t"
    "v_mfma_f32_16x16x32_bf16 v[84:87], a[212:215], v[240:243], v[84:87]\n\t"
    "s_mul_i32 s61, s71, 20416\n\t"
    "v_add_f32 v43, v95, v43\n\t"
    "v_cvt_pk_bf16_f32 v253, v94, v95\n\t"
    "v_mfma_f32_16x16x32_bf16 v[92:95], a[192:195], v[88:91], v[124:127]\n\t"
    "v_exp_f32 v68, v68\n\t"
    "v_mfma_f32_16x16x32_bf16 v[92:95], a[196:199], v[120:123], v[92:95]\n\t"
    "v_add_f32 v40, v68, v40\n\t"
    "v_cmp_eq_u32 vcc, 0, v21\n\t"
    "v_mfma_f32_16x16x32_bf16 v[92:95], a[200:203], v[152:155], v[92:95]\n\t"
    "s_waitcnt vmcnt(19) lgkmcnt(0)\n\t"
    "s_barrier\n\t"
    "ds_read_b128 a[192:195], v28 offset:24576\n\t"
    "v_mfma_f32_16x16x32_bf16 v[92:95], a[204:207], v[184:187], v[92:95]\n\t"
    "v_exp_f32 v69, v69\n\t"
    "v_mfma_f32_16x16x32_bf16 v[92:95], a[208:211], v[216:219], v[92:95]\n\t"
    "ds_read_b128 a[196:199], v29 offset:24576\n\t"
    "v_add_f32 v40, v69, v40\n\t"
    "v_mfma_f32_16x16x32_bf16 v[92:95], a[212:215], v[248:251], v[92:95]\n\t"
    "v_exp_f32 v70, v70\n\t"
    "v_mfma_f32_16x16x32_bf16 a[144:147], a[216:219], v[132:135], a[144:147]\n\t"
    "ds_read_b128 a[200:203], v28 offset:24704\n\t"
    "v_cvt_pk_bf16_f32 v230, v68, v69\n\t"
    "v_mfma_f32_16x16x32_bf16 a[144:147], a[220:223], v[164:167], a[144:147]\n\t"
    "ds_read_b128 a[204:207], v29 offset:24704\n\t"
    "v_add_f32 v40, v70, v40\n\t"
    "v_mfma_f32_16x16x32_bf16 a[148:151], a[216:219], v[140:143], a[148:151]\n\t"
    "v_exp_f32 v71, v71\n\t"
    "v_mfma_f32_16x16x32_bf16 a[148:151], a[220:223], v[172:175], a[148:151]\n\t"
    "ds_read_b128 a[208:211], v28 offset:24832\n\t"
    "v_add_f32 v40, v71, v40\n\t"
    "v_mfma_f32_16x16x32_bf16 a[152:155], a[216:219], v[148:151], a[152:155]\n\t"
    "ds_read_b128 a[212:215], v29 offset:24832\n\t"
    "v_cvt_pk_bf16_f32 v231, v70, v71\n\t"
    "v_mfma_f32_16x16x32_bf16 a[152:155], a[220:223], v[180:183], a[152:155]\n\t"
    "v_exp_f32 v76, v76\n\t"
    "v_mfma_f32_16x16x32_bf16 a[156:159], a[216:219], v[156:159], a[156:159]\n\t"
    "ds_read_b64_tr_b16 a[216:217], v36 offset:0\n\t"
    "v_add_f32 v41, v76, v41\n\t"
    "v_mfma_f32_16x16x32_bf16 a[156:159], a[220:223], v[188:191], a[156:159]\n\t"
    "v_exp_f32 v77, v77\n\t"
    "v_mfma_f32_16x16x32_bf16 a[160:163], a[224:227], v[132:135], a[160:163]\n\t"
    "ds_read_b64_tr_b16 a[218:219], v36 offset:6144\n\t"
    "v_add_f32 v41, v77, v41\n\t"
    "v_mfma_f32_16x16x32_bf16 a[160:163], a[228:231], v[164:167], a[160:163]\n\t"
    "v_exp_f32 v78, v78\n\t"
    "v_mfma_f32_16x16x32_bf16 a[164:167], a[224:227], v[140:143], a[164:167]\n\t"
    "ds_read_b64_tr_b16 a[220:221], v36 offset:12288\n\t"
    "v_cvt_pk_bf16_f32 v238, v76, v77\n\t"
    "v_mfma_f32_16x16x32_bf16 a[164:167], a[228:231], v[172:175], a[164:167]\n\t"
    "ds_read_b64_tr_b16 a[222:223], v36 offset:18432\n\t"
    "v_cndmask_b32 v22, v23, v22, vcc\n\t"
    "v_mfma_f32_16x16x32_bf16 a[168:171], a[224:227], v[148:151], a[168:171]\n\t"
    "buffer_load_dword v21, off, s[40:43], s63 sc1\n\t"
    "v_mfma_f32_16x16x32_bf16 a[168:171], a[228:231], v[180:183], a[168:171]\n\t"
    "s_mov_b32 s63, 0\n\t"
    "v_add_f32 v41, v78, v41\n\t"
    "v_mov_b32 v23, 0x3f803f80\n\t"
    "v_mfma_f32_16x16x32_bf16 a[172:175], a[224:227], v[156:159], a[172:175]\n\t"
    "v_exp_f32 v79, v79\n\t"
    "v_mfma_f32_16x16x32_bf16 a[172:175], a[228:231], v[188:191], a[172:175]\n\t"
    "ds_read_b64_tr_b16 a[224:225], v37 offset:0\n\t"
    "v_add_f32 v41, v79, v41\n\t"
    "v_mfma_f32_16x16x32_bf16 a[176:179], a[232:235], v[132:135], a[176:179]\n\t"
    "ds_read_b64_tr_b16 a[226:227], v37 offset:6144\n\t"
    "v_cvt_pk_bf16_f32 v239, v78, v79\n\t"
    "v_mfma_f32_16x16x32_bf16 a[176:179], a[236:239], v[164:167], a[176:179]\n\t"
    "v_exp_f32 v84, v84\n\t"
    "v_mfma_f32_16x16x32_bf16 a[180:183], a[232:235], v[140:143], a[180:183]\n\t"
    "ds_read_b64_tr_b16 a[228:229], v37 offset:12288\n\t"
    "v_add_f32 v42, v84, v42\n\t"
    "v_mfma_f32_16x16x32_bf16 a[180:183], a[236:239], v[172:175], a[180:183]\n\t"
    "v_exp_f32 v85, v85\n\t"
    "v_mfma_f32_16x16x32_bf16 a[184:187], a[232:235], v[148:151], a[184:187]\n\t"
    "s_getpc_b64 s[56:57]\n\t"
    "AC6_%=:\n\t"
    "s_mov_b32 s58, KF_END-AC6_%=\n\t"
    "ds_read_b64_tr_b16 a[230:231], v37 offset:18432\n\t"
    "v_add_f32 v42, v85, v42\n\t"
    "v_mfma_f32_16x16x32_bf16 a[184:187], a[236:239], v[180:183], a[184:187]\n\t"
    "s_bfm_b64 exec, 12, 0\n\t"
    "buffer_load_dword v27, v61, s[40:43], s75 offen sc1\n\t"
    "s_mov_b64 exec, -1\n\t"
    "v_mfma_f32_16x16x32_bf16 a[188:191], a[232:235], v[156:159], a[188:191]\n\t"
    "v_exp_f32 v86, v86\n\t"
    "v_mfma_f32_16x16x32_bf16 a[188:191], a[236:239], v[188:191], a[188:191]\n\t"
    "s_waitcnt lgkmcnt(4)\n\t"
    "ds_read_b64_tr_b16 a[232:233], v38 offset:0\n\t"
    "v_cvt_pk_bf16_f32 v246, v84, v85\n\t"
    "v_mfma_f32_16x16x32_bf16 v[68:71], a[192:195], v[64:67], v[100:103]\n\t"
    "ds_read_b64_tr_b16 a[234:235], v38 offset:6144\n\t"
    "v_add_f32 v42, v86, v42\n\t"
    "v_mfma_f32_16x16x32_bf16 v[68:71], a[196:199], v[96:99], v[68:71]\n\t"
    "v_exp_f32 v87, v87\n\t"
    "v_mfma_f32_16x16x32_bf16 v[68:71], a[200:203], v[128:131], v[68:71]\n\t"
    "ds_read_b64_tr_b16 a[236:237], v38 offset:12288\n\t"
    "v_add_f32 v42, v87, v42\n\t"
    "v_mfma_f32_16x16x32_bf16 v[68:71], a[204:207], v[160:163], v[68:71]\n\t"
    "ds_read_b64_tr_b16 a[238:239], v38 offset:18432\n\t"
    "v_cvt_pk_bf16_f32 v247, v86, v87\n\t"
    "v_mfma_f32_16x16x32_bf16 v[68:71], a[208:211], v[192:195], v[68:71]\n\t"
    "v_exp_f32 v92, v92\n\t"
    "v_mfma_f32_16x16x32_bf16 v[68:71], a[212:215], v[224:227], v[68:71]\n\t"
    "v_add_f32 v43, v92, v43\n\t"
    "v_mfma_f32_16x16x32_bf16 v[76:79], a[192:195], v[72:75], v[108:111]\n\t"
    "v_exp_f32 v93, v93\n\t"
    "v_mfma_f32_16x16x32_bf16 v[76:79], a[196:199], v[104:107], v[76:79]\n\t"
    "v_add_f32 v43, v93, v43\n\t"
    "v_cvt_pk_bf16_f32 v254, v92, v93\n\t"
    "v_mfma_f32_16x16x32_bf16 v[76:79], a[200:203], v[136:139], v[76:79]\n\t"
    "v_exp_f32 v94, v94\n\t"
    "v_mfma_f32_16x16x32_bf16 v[76:79], a[204:207], v[168:171], v[76:79]\n\t"
    "buffer_load_dword a255, v61, s[56:59], s64 offen\n\t"
    "v_mfma_f32_16x16x32_bf16 v[76:79], a[208:211], v[200:203], v[76:79]\n\t"
    "s_bfe_u32 s64, s87, 0x10009\n\t"
    "v_add_f32 v43, v94, v43\n\t"
    "v_mfma_f32_16x16x32_bf16 v[76:79], a[212:215], v[232:235], v[76:79]\n\t"
    "s_mul_i32 s64, s64, 10048\n\t"
    "v_exp_f32 v95, v95\n\t"
    "v_mfma_f32_16x16x32_bf16 v[84:87], a[192:195], v[80:83], v[116:119]\n\t"
    "s_add_u32 s61, s61, s64\n\t"
    "v_add_f32 v43, v95, v43\n\t"
    "v_cvt_pk_bf16_f32 v255, v94, v95\n\t"
    "v_mfma_f32_16x16x32_bf16 v[84:87], a[196:199], v[112:115], v[84:87]\n\t"
    "s_mov_b64 s[56:57], s[36:37]\n\t"
    "v_exp_f32 v68, v68\n\t"
    "v_mfma_f32_16x16x32_bf16 v[84:87], a[200:203], v[144:147], v[84:87]\n\t"
    "s_mov_b32 s58, 40960\n\t"
    "v_add_f32 v40, v68, v40\n\t"
    "v_mfma_f32_16x16x32_bf16 v[84:87], a[204:207], v[176:179], v[84:87]\n\t"
    "s_add_u32 s61, s61, 128\n\t"
    "v_exp_f32 v69, v69\n\t"
    "v_mfma_f32_16x16x32_bf16 v[84:87], a[208:211], v[208:211], v[84:87]\n\t"
    "s_and_b32 s64, s82, 1\n\t"
    "v_add_f32 v40, v69, v40\n\t"
    "v_cvt_pk_bf16_f32 v132, v68, v69\n\t"
    "v_mfma_f32_16x16x32_bf16 v[84:87], a[212:215], v[240:243], v[84:87]\n\t"
    "v_exp_f32 v70, v70\n\t"
    "v_mfma_f32_16x16x32_bf16 v[92:95], a[192:195], v[88:91], v[124:127]\n\t"
    "ds_read_b128 a[192:195], v28 offset:30720\n\t"
    "v_add_f32 v40, v70, v40\n\t"
    "v_mfma_f32_16x16x32_bf16 v[92:95], a[196:199], v[120:123], v[92:95]\n\t"
    "v_exp_f32 v71, v71\n\t"
    "v_mfma_f32_16x16x32_bf16 v[92:95], a[200:203], v[152:155], v[92:95]\n\t"
    "ds_read_b128 a[196:199], v29 offset:30720\n\t"
    "v_add_f32 v40, v71, v40\n\t"
    "v_mfma_f32_16x16x32_bf16 v[92:95], a[204:207], v[184:187], v[92:95]\n\t"
    "ds_read_b128 a[200:203], v28 offset:30848\n\t"
    "v_cvt_pk_bf16_f32 v133, v70, v71\n\t"
    "v_mfma_f32_16x16x32_bf16 v[92:95], a[208:211], v[216:219], v[92:95]\n\t"
    "v_exp_f32 v76, v76\n\t"
    "v_mfma_f32_16x16x32_bf16 v[92:95], a[212:215], v[248:251], v[92:95]\n\t"
    "ds_read_b128 a[204:207], v29 offset:30848\n\t"
    "v_add_f32 v41, v76, v41\n\t"
    "v_mfma_f32_16x16x32_bf16 a[0:3], a[216:219], v[196:199], a[0:3]\n\t"
    "v_exp_f32 v77, v77\n\t"
    "v_mfma_f32_16x16x32_bf16 a[0:3], a[220:223], v[228:231], a[0:3]\n\t"
    "ds_read_b128 a[208:211], v28 offset:30976\n\t"
    "v_add_f32 v41, v77, v41\n\t"
    "v_mfma_f32_16x16x32_bf16 a[4:7], a[216:219], v[204:207], a[4:7]\n\t"
    "v_exp_f32 v78, v78\n\t"
    "v_mfma_f32_16x16x32_bf16 a[4:7], a[220:223], v[236:239], a[4:7]\n\t"
    "ds_read_b128 a[212:215], v29 offset:30976\n\t"
    "v_cvt_pk_bf16_f32 v140, v76, v77\n\t"
    "v_mfma_f32_16x16x32_bf16 a[8:11], a[216:219], v[212:215], a[8:11]\n\t"
    "v_add_f32 v41, v78, v41\n\t"
    "v_mfma_f32_16x16x32_bf16 a[8:11], a[220:223], v[244:247], a[8:11]\n\t"
    "v_exp_f32 v79, v79\n\t"
    "v_mfma_f32_16x16x32_bf16 a[12:15], a[216:219], v[220:223], a[12:15]\n\t"
    "ds_read_b64_tr_b16 a[216:217], v39 offset:0\n\t"
    "v_add_f32 v41, v79, v41\n\t"
    "v_mfma_f32_16x16x32_bf16 a[12:15], a[220:223], v[252:255], a[12:15]\n\t"
    "s_waitcnt lgkmcnt(6)\n\t"
    "ds_read_b64_tr_b16 a[218:219], v39 offset:6144\n\t"
    "v_cvt_pk_bf16_f32 v141, v78, v79\n\t"
    "v_mfma_f32_16x16x32_bf16 a[16:19], a[224:227], v[196:199], a[16:19]\n\t"
    "v_exp_f32 v84, v84\n\t"
    "v_mfma_f32_16x16x32_bf16 a[16:19], a[228:231], v[228:231], a[16:19]\n\t"
    "ds_read_b64_tr_b16 a[220:221], v39 offset:12288\n\t"
    "v_add_f32 v42, v84, v42\n\t"
    "v_mfma_f32_16x16x32_bf16 a[20:23], a[224:227], v[204:207], a[20:23]\n\t"
    "v_exp_f32 v85, v85\n\t"
    "v_mfma_f32_16x16x32_bf16 a[20:23], a[228:231], v[236:239], a[20:23]\n\t"
    "ds_read_b64_tr_b16 a[222:223], v39 offset:18432\n\t"
    "v_add_f32 v42, v85, v42\n\t"
    "v_mfma_f32_16x16x32_bf16 a[24:27], a[224:227], v[212:215], a[24:27]\n\t"
    "v_exp_f32 v86, v86\n\t"
    "v_mfma_f32_16x16x32_bf16 a[24:27], a[228:231], v[244:247], a[24:27]\n\t"
    "v_cvt_pk_bf16_f32 v148, v84, v85\n\t"
    "v_add_f32 v42, v86, v42\n\t"
    "v_mfma_f32_16x16x32_bf16 a[28:31], a[224:227], v[220:223], a[28:31]\n\t"
    "v_exp_f32 v87, v87\n\t"
    "v_mfma_f32_16x16x32_bf16 a[28:31], a[228:231], v[252:255], a[28:31]\n\t"
    "ds_read_b64_tr_b16 a[224:225], v36 offset:128\n\t"
    "v_add_f32 v42, v87, v42\n\t"
    "v_mfma_f32_16x16x32_bf16 a[32:35], a[232:235], v[196:199], a[32:35]\n\t"
    "ds_read_b64_tr_b16 a[226:227], v36 offset:6272\n\t"
    "v_cvt_pk_bf16_f32 v149, v86, v87\n\t"
    "v_mfma_f32_16x16x32_bf16 a[32:35], a[236:239], v[228:231], a[32:35]\n\t"
    "v_exp_f32 v92, v92\n\t"
    "v_mfma_f32_16x16x32_bf16 a[36:39], a[232:235], v[204:207], a[36:39]\n\t"
    "ds_read_b64_tr_b16 a[228:229], v36 offset:12416\n\t"
    "v_add_f32 v43, v92, v43\n\t"
    "v_mfma_f32_16x16x32_bf16 a[36:39], a[236:239], v[236:239], a[36:39]\n\t"
    "v_exp_f32 v93, v93\n\t"
    "v_mfma_f32_16x16x32_bf16 a[40:43], a[232:235], v[212:215], a[40:43]\n\t"
    "ds_read_b64_tr_b16 a[230:231], v36 offset:18560\n\t"
    "v_add_f32 v43, v93, v43\n\t"
    "v_mfma_f32_16x16x32_bf16 a[40:43], a[236:239], v[244:247], a[40:43]\n\t"
    "v_exp_f32 v94, v94\n\t"
    "v_mfma_f32_16x16x32_bf16 a[44:47], a[232:235], v[220:223], a[44:47]\n\t"
    "ds_read_b64_tr_b16 a[232:233], v37 offset:128\n\t"
    "v_cvt_pk_bf16_f32 v156, v92, v93\n\t"
    "v_mfma_f32_16x16x32_bf16 a[44:47], a[236:239], v[252:255], a[44:47]\n\t"
    "ds_read_b64_tr_b16 a[234:235], v37 offset:6272\n\t"
    "v_add_f32 v43, v94, v43\n\t"
    "v_mfma_f32_16x16x32_bf16 v[68:71], a[192:195], v[64:67], v[100:103]\n\t"
    "s_waitcnt lgkmcnt(6)\n\t"
    "v_exp_f32 v95, v95\n\t"
    "v_mfma_f32_16x16x32_bf16 v[68:71], a[196:199], v[96:99], v[68:71]\n\t"
    "ds_read_b64_tr_b16 a[236:237], v37 offset:12416\n\t"
    "v_add_f32 v43, v95, v43\n\t"
    "v_mfma_f32_16x16x32_bf16 v[68:71], a[200:203], v[128:131], v[68:71]\n\t"
    "ds_read_b64_tr_b16 a[238:239], v37 offset:18560\n\t"
    "v_cvt_pk_bf16_f32 v157, v94, v95\n\t"
    "v_mfma_f32_16x16x32_bf16 v[68:71], a[204:207], v[160:163], v[68:71]\n\t"
    "buffer_load_dwordx4 v8, s[52:55], s60 offen offset:0 lds\n\t"
    "v_mfma_f32_16x16x32_bf16 v[68:71], a[208:211], v[192:195], v[68:71]\n\t"
    "v_mfma_f32_16x16x32_bf16 v[68:71], a[212:215], v[224:227], v[68:71]\n\t"
    "v_mfma_f32_16x16x32_bf16 v[76:79], a[192:195], v[72:75], v[108:111]\n\t"
    "v_mfma_f32_16x16x32_bf16 v[76:79], a[196:199], v[104:107], v[76:79]\n\t"
    "v_mfma_f32_16x16x32_bf16 v[76:79], a[200:203], v[136:139], v[76:79]\n\t"
    "s_waitcnt vmcnt(2)\n\t"
    "v_cmp_ne_u32 vcc, s80, v27\n\t"
    "s_cbranch_vccnz KFASY\n\t"
    "KFARY:\n\t"
    "v_mfma_f32_16x16x32_bf16 v[76:79], a[204:207], v[168:171], v[76:79]\n\t"
    "s_nop 0\n\t"
    "v_exp_f32 v68, v68\n\t"
    "v_mfma_f32_16x16x32_bf16 v[76:79], a[208:211], v[200:203], v[76:79]\n\t"
    "buffer_load_dwordx4 v[44:47], v20, s[40:43], s63 offen offset:0 sc1\n\t"
    "v_mfma_f32_16x16x32_bf16 v[76:79], a[212:215], v[232:235], v[76:79]\n\t"
    "s_mov_b32 s63, 81920\n\t"
    "v_add_f32 v40, v68, v40\n\t"
    "v_mfma_f32_16x16x32_bf16 v[84:87], a[192:195], v[80:83], v[116:119]\n\t"
    "v_exp_f32 v69, v69\n\t"
    "v_mfma_f32_16x16x32_bf16 v[84:87], a[196:199], v[112:115], v[84:87]\n\t"
    "v_add_f32 v40, v69, v40\n\t"
    "v_cvt_pk_bf16_f32 v134, v68, v69\n\t"
    "v_mfma_f32_16x16x32_bf16 v[84:87], a[200:203], v[144:147], v[84:87]\n\t"
    "v_exp_f32 v70, v70\n\t"
    "v_mfma_f32_16x16x32_bf16 v[84:87], a[204:207], v[176:179], v[84:87]\n\t"
    "buffer_load_dwordx4 v[48:51], v20, s[40:43], s63 offen offset:0 sc1\n\t"
    "v_mfma_f32_16x16x32_bf16 v[84:87], a[208:211], v[208:211], v[84:87]\n\t"
    "s_mov_b32 s63, 163840\n\t"
    "v_add_f32 v40, v70, v40\n\t"
    "v_mfma_f32_16x16x32_bf16 v[84:87], a[212:215], v[240:243], v[84:87]\n\t"
    "v_exp_f32 v71, v71\n\t"
    "v_mfma_f32_16x16x32_bf16 v[92:95], a[192:195], v[88:91], v[124:127]\n\t"
    "ds_read_b128 a[192:195], v28 offset:36864\n\t"
    "v_add_f32 v40, v71, v40\n\t"
    "v_mfma_f32_16x16x32_bf16 v[92:95], a[196:199], v[120:123], v[92:95]\n\t"
    "ds_read_b128 a[196:199], v29 offset:36864\n\t"
    "v_cvt_pk_bf16_f32 v135, v70, v71\n\t"
    "v_mfma_f32_16x16x32_bf16 v[92:95], a[200:203], v[152:155], v[92:95]\n\t"
    "buffer_load_dwordx4 v[52:55], v20, s[40:43], s63 offen offset:0 sc1\n\t"
    "v_mfma_f32_16x16x32_bf16 v[92:95], a[204:207], v[184:187], v[92:95]\n\t"
    "s_mov_b32 s63, 0x5f800000\n\t"
    "v_exp_f32 v76, v76\n\t"
    "v_mfma_f32_16x16x32_bf16 v[92:95], a[208:211], v[216:219], v[92:95]\n\t"
    "ds_read_b128 a[200:203], v28 offset:36992\n\t"
    "v_add_f32 v41, v76, v41\n\t"
    "v_mfma_f32_16x16x32_bf16 v[92:95], a[212:215], v[248:251], v[92:95]\n\t"
    "v_exp_f32 v77, v77\n\t"
    "v_mfma_f32_16x16x32_bf16 a[48:51], a[216:219], v[196:199], a[48:51]\n\t"
    "ds_read_b128 a[204:207], v29 offset:36992\n\t"
    "v_add_f32 v41, v77, v41\n\t"
    "v_mfma_f32_16x16x32_bf16 a[48:51], a[220:223], v[228:231], a[48:51]\n\t"
    "v_exp_f32 v78, v78\n\t"
    "v_mfma_f32_16x16x32_bf16 a[52:55], a[216:219], v[204:207], a[52:55]\n\t"
    "ds_read_b128 a[208:211], v28 offset:37120\n\t"
    "v_cvt_pk_bf16_f32 v142, v76, v77\n\t"
    "v_mfma_f32_16x16x32_bf16 a[52:55], a[220:223], v[236:239], a[52:55]\n\t"
    "buffer_load_dwordx4 v9, s[52:55], s60 offen offset:1024 lds\n\t"
    "v_mfma_f32_16x16x32_bf16 a[56:59], a[216:219], v[212:215], a[56:59]\n\t"
    "ds_read_b128 a[212:215], v29 offset:37120\n\t"
    "v_add_f32 v41, v78, v41\n\t"
    "v_mfma_f32_16x16x32_bf16 a[56:59], a[220:223], v[244:247], a[56:59]\n\t"
    "v_exp_f32 v79, v79\n\t"
    "v_mfma_f32_16x16x32_bf16 a[60:63], a[216:219], v[220:223], a[60:63]\n\t"
    "ds_read_b64_tr_b16 a[216:217], v38 offset:128\n\t"
    "v_add_f32 v41, v79, v41\n\t"
    "v_mfma_f32_16x16x32_bf16 a[60:63], a[220:223], v[252:255], a[60:63]\n\t"
    "s_waitcnt lgkmcnt(5)\n\t"
    "ds_read_b64_tr_b16 a[218:219], v38 offset:6272\n\t"
    "v_cvt_pk_bf16_f32 v143, v78, v79\n\t"
    "v_mfma_f32_16x16x32_bf16 a[64:67], a[224:227], v[196:199], a[64:67]\n\t"
    "buffer_load_dwordx4 v10, s[52:55], s60 offen offset:2048 lds\n\t"
    "v_mfma_f32_16x16x32_bf16 a[64:67], a[228:231], v[228:231], a[64:67]\n\t"
    "s_add_u32 m0, s83, 52224\n\t"
    "v_exp_f32 v84, v84\n\t"
    "v_mfma_f32_16x16x32_bf16 a[68:71], a[224:227], v[204:207], a[68:71]\n\t"
    "s_add_u32 s60, s60, 3072\n\t"
    "ds_read_b64_tr_b16 a[220:221], v38 offset:12416\n\t"
    "v_add_f32 v42, v84, v42\n\t"
    "v_mfma_f32_16x16x32_bf16 a[68:71], a[228:231], v[236:239], a[68:71]\n\t"
    "v_exp_f32 v85, v85\n\t"
    "v_mfma_f32_16x16x32_bf16 a[72:75], a[224:227], v[212:215], a[72:75]\n\t"
    "ds_read_b64_tr_b16 a[222:223], v38 offset:18560\n\t"
    "v_add_f32 v42, v85, v42\n\t"
    "v_mfma_f32_16x16x32_bf16 a[72:75], a[228:231], v[244:247], a[72:75]\n\t"
    "buffer_load_dwordx4 v8, s[52:55], s60 offen offset:0 lds\n\t"
    "v_mfma_f32_16x16x32_bf16 a[76:79], a[224:227], v[220:223], a[76:79]\n\t"
    "v_exp_f32 v86, v86\n\t"
    "v_mfma_f32_16x16x32_bf16 a[76:79], a[228:231], v[252:255], a[76:79]\n\t"
    "ds_read_b64_tr_b16 a[224:225], v39 offset:128\n\t"
    "v_cvt_pk_bf16_f32 v150, v84, v85\n\t"
    "v_mfma_f32_16x16x32_bf16 a[80:83], a[232:235], v[196:199], a[80:83]\n\t"
    "ds_read_b64_tr_b16 a[226:227], v39 offset:6272\n\t"
    "v_add_f32 v42, v86, v42\n\t"
    "v_mfma_f32_16x16x32_bf16 a[80:83], a[236:239], v[228:231], a[80:83]\n\t"
    "v_exp_f32 v87, v87\n\t"
    "v_mfma_f32_16x16x32_bf16 a[84:87], a[232:235], v[204:207], a[84:87]\n\t"
    "buffer_load_dwordx4 v9, s[52:55], s60 offen offset:1024 lds\n\t"
    "v_mfma_f32_16x16x32_bf16 a[84:87], a[236:239], v[236:239], a[84:87]\n\t"
    "ds_read_b64_tr_b16 a[228:229], v39 offset:12416\n\t"
    "v_add_f32 v42, v87, v42\n\t"
    "v_mfma_f32_16x16x32_bf16 a[88:91], a[232:235], v[212:215], a[88:91]\n\t"
    "ds_read_b64_tr_b16 a[230:231], v39 offset:18560\n\t"
    "v_cvt_pk_bf16_f32 v151, v86, v87\n\t"
    "v_mfma_f32_16x16x32_bf16 a[88:91], a[236:239], v[244:247], a[88:91]\n\t"
    "v_exp_f32 v92, v92\n\t"
    "v_mfma_f32_16x16x32_bf16 a[92:95], a[232:235], v[220:223], a[92:95]\n\t"
    "ds_read_b64_tr_b16 a[232:233], v36 offset:256\n\t"
    "v_add_f32 v43, v92, v43\n\t"
    "v_mfma_f32_16x16x32_bf16 a[92:95], a[236:239], v[252:255], a[92:95]\n\t"
    "buffer_load_dwordx4 v10, s[52:55], s60 offen offset:2048 lds\n\t"
    "v_mfma_f32_16x16x32_bf16 v[68:71], a[192:195], v[64:67], v[100:103]\n\t"
    "s_add_u32 m0, s78, 24576\n\t"
    "ds_read_b64_tr_b16 a[234:235], v36 offset:6400\n\t"
    "v_mfma_f32_16x16x32_bf16 v[68:71], a[196:199], v[96:99], v[68:71]\n\t"
    "KFP11:\n\t"
    "s_cbranch_execz KFR1\n\t"
    "s_waitcnt lgkmcnt(6)\n\t"
    "s_add_u32 s60, s73, 0\n\t"
    "ds_read_b64_tr_b16 a[236:237], v36 offset:12544\n\t"
    "v_mfma_f32_16x16x32_bf16 v[68:71], a[200:203], v[128:131], v[68:71]\n\t"
    "v_exp_f32 v93, v93\n\t"
    "v_mfma_f32_16x16x32_bf16 v[68:71], a[204:207], v[160:163], v[68:71]\n\t"
    "ds_read_b64_tr_b16 a[238:239], v36 offset:18688\n\t"
    "v_add_f32 v43, v93, v43\n\t"
    "v_mfma_f32_16x16x32_bf16 v[68:71], a[208:211], v[192:195], v[68:71]\n\t"
    "buffer_load_dwordx4 v4, s[40:43], s60 offen offset:0 sc1 lds\n\t"
    "v_mfma_f32_16x16x32_bf16 v[68:71], a[212:215], v[224:227], v[68:71]\n\t"
    "v_exp_f32 v94, v94\n\t"
    "v_mfma_f32_16x16x32_bf16 v[76:79], a[192:195], v[72:75], v[108:111]\n\t"
    "v_cvt_pk_bf16_f32 v158, v92, v93\n\t"
    "v_add_f32 v43, v94, v43\n\t"
    "v_mfma_f32_16x16x32_bf16 v[76:79], a[196:199], v[104:107], v[76:79]\n\t"
    "v_exp_f32 v95, v95\n\t"
    "v_mfma_f32_16x16x32_bf16 v[76:79], a[200:203], v[136:139], v[76:79]\n\t"
    "v_add_f32 v43, v95, v43\n\t"
    "v_cvt_pk_bf16_f32 v159, v94, v95\n\t"
    "v_mfma_f32_16x16x32_bf16 v[76:79], a[204:207], v[168:171], v[76:79]\n\t"
    "buffer_load_dwordx4 v4, s[40:43], s60 offen offset:1024 sc1 lds\n\t"
    "v_mfma_f32_16x16x32_bf16 v[76:79], a[208:211], v[200:203], v[76:79]\n\t"
    "v_exp_f32 v68, v68\n\t"
    "v_mfma_f32_16x16x32_bf16 v[76:79], a[212:215], v[232:235], v[76:79]\n\t"
    "v_add_f32 v40, v68, v40\n\t"
    "v_mul_f32 v21, 0xbfb8aa3b, v21\n\t"
    "v_mfma_f32_16x16x32_bf16 v[84:87], a[192:195], v[80:83], v[116:119]\n\t"
    "v_exp_f32 v69, v69\n\t"
    "v_mfma_f32_16x16x32_bf16 v[84:87], a[196:199], v[112:115], v[84:87]\n\t"
    "v_add_f32 v40, v69, v40\n\t"
    "v_cvt_pk_bf16_f32 v164, v68, v69\n\t"
    "v_mfma_f32_16x16x32_bf16 v[84:87], a[200:203], v[144:147], v[84:87]\n\t"
    "buffer_load_dwordx4 v4, s[40:43], s60 offen offset:2048 sc1 lds\n\t"
    "v_mfma_f32_16x16x32_bf16 v[84:87], a[204:207], v[176:179], v[84:87]\n\t"
    "v_exp_f32 v70, v70\n\t"
    "v_mfma_f32_16x16x32_bf16 v[84:87], a[208:211], v[208:211], v[84:87]\n\t"
    "v_add_f32 v40, v70, v40\n\t"
    "v_mov_b32 v20, 0x3f803f80\n\t"
    "v_mfma_f32_16x16x32_bf16 v[84:87], a[212:215], v[240:243], v[84:87]\n\t"
    "v_exp_f32 v71, v71\n\t"
    "v_mfma_f32_16x16x32_bf16 v[92:95], a[192:195], v[88:91], v[124:127]\n\t"
    "ds_read_b128 a[192:195], v28 offset:43008\n\t"
    "v_add_f32 v40, v71, v40\n\t"
    "v_mfma_f32_16x16x32_bf16 v[92:95], a[196:199], v[120:123], v[92:95]\n\t"
    "buffer_load_dwordx4 v4, s[40:43], s60 offen offset:3072 sc1 lds\n\t"
    "v_mfma_f32_16x16x32_bf16 v[92:95], a[200:203], v[152:155], v[92:95]\n\t"
    "s_add_u32 m0, m0, 1024\n\t"
    "ds_read_b128 a[196:199], v29 offset:43008\n\t"
    "v_cvt_pk_bf16_f32 v165, v70, v71\n\t"
    "v_mfma_f32_16x16x32_bf16 v[92:95], a[204:207], v[184:187], v[92:95]\n\t"
    "s_add_u32 s60, s60, 1024\n\t"
    "v_exp_f32 v76, v76\n\t"
    "v_mfma_f32_16x16x32_bf16 v[92:95], a[208:211], v[216:219], v[92:95]\n\t"
    "ds_read_b128 a[200:203], v28 offset:43136\n\t"
    "v_add_f32 v41, v76, v41\n\t"
    "v_mfma_f32_16x16x32_bf16 v[92:95], a[212:215], v[248:251], v[92:95]\n\t"
    "v_exp_f32 v77, v77\n\t"
    "v_mfma_f32_16x16x32_bf16 a[96:99], a[216:219], v[196:199], a[96:99]\n\t"
    "buffer_load_dwordx2 a[248:249], v24, s[56:59], s61 offen offset:0\n\t"
    "v_mfma_f32_16x16x32_bf16 a[96:99], a[220:223], v[228:231], a[96:99]\n\t"
    "s_xor_b32 s61, s64, 0\n\t"
    "ds_read_b128 a[204:207], v29 offset:43136\n\t"
    "v_add_f32 v41, v77, v41\n\t"
    "v_mfma_f32_16x16x32_bf16 a[100:103], a[216:219], v[204:207], a[100:103]\n\t"
    "s_mulk_i32 s61, 20480\n\t"
    "v_exp_f32 v78, v78\n\t"
    "v_mfma_f32_16x16x32_bf16 a[100:103], a[220:223], v[236:239], a[100:103]\n\t"
    "ds_read_b128 a[208:211], v28 offset:43264\n\t"
    "v_cvt_pk_bf16_f32 v172, v76, v77\n\t"
    "v_mfma_f32_16x16x32_bf16 a[104:107], a[216:219], v[212:215], a[104:107]\n\t"
    "ds_read_b128 a[212:215], v29 offset:43264\n\t"
    "v_add_f32 v41, v78, v41\n\t"
    "v_mfma_f32_16x16x32_bf16 a[104:107], a[220:223], v[244:247], a[104:107]\n\t"
    "buffer_load_dwordx4 v4, s[40:43], s60 offen offset:3072 sc1 lds\n\t"
    "v_mfma_f32_16x16x32_bf16 a[108:111], a[216:219], v[220:223], a[108:111]\n\t"
    "s_add_u32 m0, s78, 73728\n\t"
    "v_exp_f32 v79, v79\n\t"
    "v_mfma_f32_16x16x32_bf16 a[108:111], a[220:223], v[252:255], a[108:111]\n\t"
    "s_waitcnt lgkmcnt(4)\n\t"
    "ds_read_b64_tr_b16 a[216:217], v37 offset:256\n\t"
    "v_add_f32 v41, v79, v41\n\t"
    "v_mfma_f32_16x16x32_bf16 a[112:115], a[224:227], v[196:199], a[112:115]\n\t"
    "ds_read_b64_tr_b16 a[218:219], v37 offset:6400\n\t"
    "v_cvt_pk_bf16_f32 v173, v78, v79\n\t"
    "v_mfma_f32_16x16x32_bf16 a[112:115], a[228:231], v[228:231], a[112:115]\n\t"
    "s_add_u32 s60, s73, 81920\n\t"
    "v_exp_f32 v84, v84\n\t"
    "v_mfma_f32_16x16x32_bf16 a[116:119], a[224:227], v[204:207], a[116:119]\n\t"
    "buffer_load_dwordx4 a[240:243], v11, s[56:59], s61 offen offset:0\n\t"
    "v_mfma_f32_16x16x32_bf16 a[116:119], a[228:231], v[236:239], a[116:119]\n\t"
    "s_xor_b32 s61, s64, 1\n\t"
    "ds_read_b64_tr_b16 a[220:221], v37 offset:12544\n\t"
    "v_add_f32 v42, v84, v42\n\t"
    "v_mfma_f32_16x16x32_bf16 a[120:123], a[224:227], v[212:215], a[120:123]\n\t"
    "s_mulk_i32 s61, 20480\n\t"
    "v_exp_f32 v85, v85\n\t"
    "v_mfma_f32_16x16x32_bf16 a[120:123], a[228:231], v[244:247], a[120:123]\n\t"
    "ds_read_b64_tr_b16 a[222:223], v37 offset:18688\n\t"
    "v_add_f32 v42, v85, v42\n\t"
    "v_mfma_f32_16x16x32_bf16 a[124:127], a[224:227], v[220:223], a[124:127]\n\t"
    "v_exp_f32 v86, v86\n\t"
    "v_mfma_f32_16x16x32_bf16 a[124:127], a[228:231], v[252:255], a[124:127]\n\t"
    "buffer_load_dwordx4 a[244:247], v11, s[56:59], s61 offen offset:0\n\t"
    "v_mfma_f32_16x16x32_bf16 a[128:131], a[232:235], v[196:199], a[128:131]\n\t"
    "s_xor_b32 s61, s64, 0\n\t"
    "v_cvt_pk_bf16_f32 v180, v84, v85\n\t"
    "v_mfma_f32_16x16x32_bf16 a[128:131], a[236:239], v[228:231], a[128:131]\n\t"
    "s_lshl_b32 s61, s61, 9\n\t"
    "ds_read_b64_tr_b16 a[224:225], v38 offset:256\n\t"
    "v_add_f32 v42, v86, v42\n\t"
    "v_mfma_f32_16x16x32_bf16 a[132:135], a[232:235], v[204:207], a[132:135]\n\t"
    "s_add_u32 s61, s61, s99\n\t"
    "v_exp_f32 v87, v87\n\t"
    "v_mfma_f32_16x16x32_bf16 a[132:135], a[236:239], v[236:239], a[132:135]\n\t"
    "ds_read_b64_tr_b16 a[226:227], v38 offset:6400\n\t"
    "v_add_f32 v42, v87, v42\n\t"
    "v_mfma_f32_16x16x32_bf16 a[136:139], a[232:235], v[212:215], a[136:139]\n\t"
    "buffer_load_dword a250, v25, s[40:43], s61 offen offset:4 sc1\n\t"
    "v_mfma_f32_16x16x32_bf16 a[136:139], a[236:239], v[244:247], a[136:139]\n\t"
    "s_xor_b32 s61, s64, 1\n\t"
    "ds_read_b64_tr_b16 a[228:229], v38 offset:12544\n\t"
    "v_cvt_pk_bf16_f32 v181, v86, v87\n\t"
    "v_mfma_f32_16x16x32_bf16 a[140:143], a[232:235], v[220:223], a[140:143]\n\t"
    "s_lshl_b32 s61, s61, 9\n\t"
    "v_exp_f32 v92, v92\n\t"
    "v_mfma_f32_16x16x32_bf16 a[140:143], a[236:239], v[252:255], a[140:143]\n\t"
    "s_add_u32 s61, s61, s99\n\t"
    "v_add_f32 v43, v92, v43\n\t"
    "v_mfma_f32_16x16x32_bf16 v[68:71], a[192:195], v[64:67], v[100:103]\n\t"
    "v_exp_f32 v93, v93\n\t"
    "v_mfma_f32_16x16x32_bf16 v[68:71], a[196:199], v[96:99], v[68:71]\n\t"
    "s_waitcnt lgkmcnt(3)\n\t"
    "buffer_load_dword a251, v25, s[40:43], s61 offen offset:4 sc1\n\t"
    "v_mfma_f32_16x16x32_bf16 v[68:71], a[200:203], v[128:131], v[68:71]\n\t"
    "ds_read_b64_tr_b16 a[230:231], v38 offset:18688\n\t"
    "v_add_f32 v43, v93, v43\n\t"
    "v_mfma_f32_16x16x32_bf16 v[68:71], a[204:207], v[160:163], v[68:71]\n\t"
    "v_exp_f32 v21, v21\n\t"
    "v_mfma_f32_16x16x32_bf16 v[68:71], a[208:211], v[192:195], v[68:71]\n\t"
    "v_exp_f32 v94, v94\n\t"
    "v_mfma_f32_16x16x32_bf16 v[68:71], a[212:215], v[224:227], v[68:71]\n\t"
    "ds_read_b64_tr_b16 a[232:233], v39 offset:256\n\t"
    "v_cvt_pk_bf16_f32 v188, v92, v93\n\t"
    "v_mfma_f32_16x16x32_bf16 v[76:79], a[192:195], v[72:75], v[108:111]\n\t"
    "ds_read_b64_tr_b16 a[234:235], v39 offset:6400\n\t"
    "v_add_f32 v21, 1.0, v21\n\t"
    "v_mfma_f32_16x16x32_bf16 v[76:79], a[196:199], v[104:107], v[76:79]\n\t"
    "v_rcp_f32 v21, v21\n\t"
    "v_mfma_f32_16x16x32_bf16 v[76:79], a[200:203], v[136:139], v[76:79]\n\t"
    "ds_read_b64_tr_b16 a[236:237], v39 offset:12544\n\t"
    "v_add_f32 v43, v94, v43\n\t"
    "v_mfma_f32_16x16x32_bf16 v[76:79], a[204:207], v[168:171], v[76:79]\n\t"
    "ds_read_b64_tr_b16 a[238:239], v39 offset:18688\n\t"
    "v_cvt_pk_bf16_f32 v21, v21, v21\n\t"
    "v_mfma_f32_16x16x32_bf16 v[76:79], a[208:211], v[200:203], v[76:79]\n\t"
    "v_exp_f32 v95, v95\n\t"
    "v_mfma_f32_16x16x32_bf16 v[76:79], a[212:215], v[232:235], v[76:79]\n\t"
    "v_lshlrev_b32 v21, 16, v21\n\t"
    "v_add_f32 v43, v95, v43\n\t"
    "v_mfma_f32_16x16x32_bf16 v[84:87], a[192:195], v[80:83], v[116:119]\n\t"
    "v_cvt_pk_bf16_f32 v189, v94, v95\n\t"
    "v_mfma_f32_16x16x32_bf16 v[84:87], a[196:199], v[112:115], v[84:87]\n\t"
    "v_exp_f32 v68, v68\n\t"
    "v_mfma_f32_16x16x32_bf16 v[84:87], a[200:203], v[144:147], v[84:87]\n\t"
    "v_add_f32 v40, v68, v40\n\t"
    "v_mfma_f32_16x16x32_bf16 v[84:87], a[204:207], v[176:179], v[84:87]\n\t"
    "v_exp_f32 v69, v69\n\t"
    "v_mfma_f32_16x16x32_bf16 v[84:87], a[208:211], v[208:211], v[84:87]\n\t"
    "v_add_f32 v40, v69, v40\n\t"
    "v_cvt_pk_bf16_f32 v166, v68, v69\n\t"
    "v_mfma_f32_16x16x32_bf16 v[84:87], a[212:215], v[240:243], v[84:87]\n\t"
    "v_exp_f32 v70, v70\n\t"
    "v_mfma_f32_16x16x32_bf16 v[92:95], a[192:195], v[88:91], v[124:127]\n\t"
    "v_add_f32 v40, v70, v40\n\t"
    "v_mfma_f32_16x16x32_bf16 v[92:95], a[196:199], v[120:123], v[92:95]\n\t"
    "v_exp_f32 v71, v71\n\t"
    "v_mfma_f32_16x16x32_bf16 v[92:95], a[200:203], v[152:155], v[92:95]\n\t"
    "s_waitcnt lgkmcnt(0)\n\t"
    "s_barrier\n\t"
    "s_waitcnt vmcnt(15)\n\t"
    "v_add_f32 v44, v44, v48\n\t"
    "v_mfma_f32_16x16x32_bf16 v[92:95], a[204:207], v[184:187], v[92:95]\n\t"
    "ds_read_b128 a[192:195], v30 offset:24576\n\t"
    "v_add_f32 v45, v45, v49\n\t"
    "v_mfma_f32_16x16x32_bf16 v[92:95], a[208:211], v[216:219], v[92:95]\n\t"
    "ds_read_b128 a[196:199], v31 offset:24576\n\t"
    "v_add_f32 v46, v46, v50\n\t"
    "v_mfma_f32_16x16x32_bf16 v[92:95], a[212:215], v[248:251], v[92:95]\n\t"
    "ds_read_b128 a[200:203], v30 offset:24704\n\t"
    "v_add_f32 v47, v47, v51\n\t"
    "v_mfma_f32_16x16x32_bf16 a[144:147], a[216:219], v[196:199], a[144:147]\n\t"
    "ds_read_b128 a[204:207], v31 offset:24704\n\t"
    "v_add_f32 v44, v44, v52\n\t"
    "v_mfma_f32_16x16x32_bf16 a[144:147], a[220:223], v[228:231], a[144:147]\n\t"
    "ds_read_b128 a[208:211], v30 offset:24832\n\t"
    "v_add_f32 v45, v45, v53\n\t"
    "v_mfma_f32_16x16x32_bf16 a[148:151], a[216:219], v[204:207], a[148:151]\n\t"
    "ds_read_b128 a[212:215], v31 offset:24832\n\t"
    "v_add_f32 v46, v46, v54\n\t"
    "v_mfma_f32_16x16x32_bf16 a[148:151], a[220:223], v[236:239], a[148:151]\n\t"
    "v_add_f32 v47, v47, v55\n\t"
    "v_permlane16_swap_b32 v44, v45\n\t"
    "v_mfma_f32_16x16x32_bf16 a[152:155], a[216:219], v[212:215], a[152:155]\n\t"
    "v_permlane16_swap_b32 v46, v47\n\t"
    "v_add_f32 v44, v44, v45\n\t"
    "v_mfma_f32_16x16x32_bf16 a[152:155], a[220:223], v[244:247], a[152:155]\n\t"
    "v_add_f32 v46, v46, v47\n\t"
    "s_nop 1\n\t"
    "v_permlane32_swap_b32 v44, v46\n\t"
    "v_mfma_f32_16x16x32_bf16 a[156:159], a[216:219], v[220:223], a[156:159]\n\t"
    "ds_read_b128 a[216:219], v30 offset:30720\n\t"
    "v_add_f32 v44, v44, v46\n\t"
    "v_mfma_f32_16x16x32_bf16 a[156:159], a[220:223], v[252:255], a[156:159]\n\t"
    "ds_read_b128 a[220:223], v31 offset:30720\n\t"
    "v_add_f32_dpp v44, v44, v44 row_ror:4 row_mask:0xf bank_mask:0xf\n\t"
    "v_mfma_f32_16x16x32_bf16 a[160:163], a[224:227], v[196:199], a[160:163]\n\t"
    "s_nop 0\n\t"
    "v_add_f32_dpp v44, v44, v44 row_ror:8 row_mask:0xf bank_mask:0xf\n\t"
    "v_cvt_pk_bf16_f32 v44, v44, v44\n\t"
    "v_mfma_f32_16x16x32_bf16 a[160:163], a[228:231], v[228:231], a[160:163]\n\t"
    "v_lshlrev_b32 v44, 16, v44\n\t"
    "v_mul_f32 v44, v44, v21\n\t"
    "v_mfma_f32_16x16x32_bf16 a[164:167], a[224:227], v[204:207], a[164:167]\n\t"
    "v_mov_b32 v45, v44\n\t"
    "v_add_f32 v40, v71, v40\n\t"
    "v_mfma_f32_16x16x32_bf16 a[164:167], a[228:231], v[236:239], a[164:167]\n\t"
    "v_permlane16_swap_b32 v44, v45\n\t"
    "v_cvt_pk_bf16_f32 v167, v70, v71\n\t"
    "v_mfma_f32_16x16x32_bf16 a[168:171], a[224:227], v[212:215], a[168:171]\n\t"
    "v_exp_f32 v76, v76\n\t"
    "v_mfma_f32_16x16x32_bf16 a[168:171], a[228:231], v[244:247], a[168:171]\n\t"
    "v_cvt_pk_bf16_f32 v52, v44, v45\n\t"
    "v_add_f32 v41, v76, v41\n\t"
    "v_mfma_f32_16x16x32_bf16 a[172:175], a[224:227], v[220:223], a[172:175]\n\t"
    "buffer_store_dword v52, v22, s[40:43], 0 offen offset:0 sc1\n\t"
    "v_mfma_f32_16x16x32_bf16 a[172:175], a[228:231], v[252:255], a[172:175]\n\t"
    "v_exp_f32 v77, v77\n\t"
    "v_mfma_f32_16x16x32_bf16 a[176:179], a[232:235], v[196:199], a[176:179]\n\t"
    "ds_read_b128 a[224:227], v30 offset:30848\n\t"
    "v_add_f32 v41, v77, v41\n\t"
    "v_mfma_f32_16x16x32_bf16 a[176:179], a[236:239], v[228:231], a[176:179]\n\t"
    "v_exp_f32 v78, v78\n\t"
    "v_mfma_f32_16x16x32_bf16 a[180:183], a[232:235], v[204:207], a[180:183]\n\t"
    "ds_read_b128 a[228:231], v31 offset:30848\n\t"
    "v_cvt_pk_bf16_f32 v174, v76, v77\n\t"
    "v_mfma_f32_16x16x32_bf16 a[180:183], a[236:239], v[236:239], a[180:183]\n\t"
    "v_add_f32 v41, v78, v41\n\t"
    "v_mov_b32 v21, 0x3f803f80\n\t"
    "v_mfma_f32_16x16x32_bf16 a[184:187], a[232:235], v[212:215], a[184:187]\n\t"
    "v_exp_f32 v79, v79\n\t"
    "v_mfma_f32_16x16x32_bf16 a[184:187], a[236:239], v[244:247], a[184:187]\n\t"
    "v_add_f32 v41, v79, v41\n\t"
    "v_cvt_pk_bf16_f32 v175, v78, v79\n\t"
    "v_mfma_f32_16x16x32_bf16 a[188:191], a[232:235], v[220:223], a[188:191]\n\t"
    "v_exp_f32 v84, v84\n\t"
    "v_mfma_f32_16x16x32_bf16 a[188:191], a[236:239], v[252:255], a[188:191]\n\t"
    "s_waitcnt lgkmcnt(2)\n\t"
    "buffer_load_dwordx4 v4, s[40:43], s60 offen offset:0 sc1 lds\n\t"
    "v_mfma_f32_16x16x32_bf16 v[68:71], a[192:195], v[64:67], v[100:103]\n\t"
    "ds_read_b128 a[232:235], v30 offset:30976\n\t"
    "v_add_f32 v42, v84, v42\n\t"
    "v_mfma_f32_16x16x32_bf16 v[68:71], a[196:199], v[96:99], v[68:71]\n\t"
    "v_exp_f32 v85, v85\n\t"
    "v_mfma_f32_16x16x32_bf16 v[68:71], a[200:203], v[128:131], v[68:71]\n\t"
    "ds_read_b128 a[236:239], v31 offset:30976\n\t"
    "v_add_f32 v42, v85, v42\n\t"
    "v_mfma_f32_16x16x32_bf16 v[68:71], a[204:207], v[160:163], v[68:71]\n\t"
    "v_exp_f32 v86, v86\n\t"
    "v_mfma_f32_16x16x32_bf16 v[68:71], a[208:211], v[192:195], v[68:71]\n\t"
    "buffer_load_dwordx4 v4, s[40:43], s60 offen offset:1024 sc1 lds\n\t"
    "v_mfma_f32_16x16x32_bf16 v[68:71], a[212:215], v[224:227], v[68:71]\n\t"
    "v_cvt_pk_bf16_f32 v182, v84, v85\n\t"
    "v_add_f32 v42, v86, v42\n\t"
    "v_mfma_f32_16x16x32_bf16 v[76:79], a[192:195], v[72:75], v[108:111]\n\t"
    "v_exp_f32 v87, v87\n\t"
    "v_mfma_f32_16x16x32_bf16 v[76:79], a[196:199], v[104:107], v[76:79]\n\t"
    "v_add_f32 v42, v87, v42\n\t"
    "v_cvt_pk_bf16_f32 v183, v86, v87\n\t"
    "v_mfma_f32_16x16x32_bf16 v[76:79], a[200:203], v[136:139], v[76:79]\n\t"
    "v_exp_f32 v92, v92\n\t"
    "v_mfma_f32_16x16x32_bf16 v[76:79], a[204:207], v[168:171], v[76:79]\n\t"
    "buffer_load_dwordx4 v4, s[40:43], s60 offen offset:2048 sc1 lds\n\t"
    "v_mfma_f32_16x16x32_bf16 v[76:79], a[208:211], v[200:203], v[76:79]\n\t"
    "v_add_f32 v43, v92, v43\n\t"
    "v_mov_b32 v22, 0x3f803f80\n\t"
    "v_mfma_f32_16x16x32_bf16 v[76:79], a[212:215], v[232:235], v[76:79]\n\t"
    "v_exp_f32 v93, v93\n\t"
    "v_mfma_f32_16x16x32_bf16 v[84:87], a[192:195], v[80:83], v[116:119]\n\t"
    "v_add_f32 v43, v93, v43\n\t"
    "v_cvt_pk_bf16_f32 v190, v92, v93\n\t"
    "v_mfma_f32_16x16x32_bf16 v[84:87], a[196:199], v[112:115], v[84:87]\n\t"
    "v_exp_f32 v94, v94\n\t"
    "v_mfma_f32_16x16x32_bf16 v[84:87], a[200:203], v[144:147], v[84:87]\n\t"
    "buffer_load_dwordx4 v4, s[40:43], s60 offen offset:3072 sc1 lds\n\t"
    "v_mfma_f32_16x16x32_bf16 v[84:87], a[204:207], v[176:179], v[84:87]\n\t"
    "s_add_u32 m0, m0, 1024\n\t"
    "v_add_f32 v43, v94, v43\n\t"
    "v_permlane16_swap_b32 v40, v41\n\t"
    "v_mfma_f32_16x16x32_bf16 v[84:87], a[208:211], v[208:211], v[84:87]\n\t"
    "s_add_u32 s60, s60, 1024\n\t"
    "v_exp_f32 v95, v95\n\t"
    "v_mfma_f32_16x16x32_bf16 v[84:87], a[212:215], v[240:243], v[84:87]\n\t"
    "v_add_f32 v43, v95, v43\n\t"
    "v_cvt_pk_bf16_f32 v191, v94, v95\n\t"
    "v_mfma_f32_16x16x32_bf16 v[92:95], a[192:195], v[88:91], v[124:127]\n\t"
    "ds_read_b128 a[192:195], v30 offset:36864\n\t"
    "v_permlane16_swap_b32 v42, v43\n\t"
    "v_mfma_f32_16x16x32_bf16 v[92:95], a[196:199], v[120:123], v[92:95]\n\t"
    "buffer_load_dwordx4 v4, s[40:43], s60 offen offset:3072 sc1 lds\n\t"
    "v_mfma_f32_16x16x32_bf16 v[92:95], a[200:203], v[152:155], v[92:95]\n\t"
    "s_add_u32 m0, s78, 94208\n\t"
    "ds_read_b128 a[196:199], v31 offset:36864\n\t"
    "v_add_f32 v40, v40, v41\n\t"
    "v_mfma_f32_16x16x32_bf16 v[92:95], a[204:207], v[184:187], v[92:95]\n\t"
    "s_add_u32 s60, s73, 163840\n\t"
    "ds_read_b128 a[200:203], v30 offset:36992\n\t"
    "v_add_f32 v42, v42, v43\n\t"
    "v_mfma_f32_16x16x32_bf16 v[92:95], a[208:211], v[216:219], v[92:95]\n\t"
    "ds_read_b128 a[204:207], v31 offset:36992\n\t"
    "v_permlane32_swap_b32 v40, v42\n\t"
    "v_mfma_f32_16x16x32_bf16 v[92:95], a[212:215], v[248:251], v[92:95]\n\t"
    "ds_read_b128 a[208:211], v30 offset:37120\n\t"
    "v_add_f32 v40, v40, v42\n\t"
    "v_mfma_f32_16x16x32_bf16 v[44:47], a[216:219], v[64:67], v[100:103]\n\t"
    "buffer_load_dwordx4 v4, s[40:43], s60 offen offset:0 sc1 lds\n\t"
    "v_mfma_f32_16x16x32_bf16 v[44:47], a[220:223], v[96:99], v[44:47]\n\t"
    "s_waitcnt lgkmcnt(5)\n\t"
    "v_exp_f32 v68, v68\n\t"
    "v_mfma_f32_16x16x32_bf16 v[44:47], a[224:227], v[128:131], v[44:47]\n\t"
    "v_exp_f32 v69, v69\n\t"
    "v_mfma_f32_16x16x32_bf16 v[44:47], a[228:231], v[160:163], v[44:47]\n\t"
    "ds_read_b128 a[212:215], v31 offset:37120\n\t"
    "v_cvt_pk_bf16_f32 v196, v68, v69\n\t"
    "v_mfma_f32_16x16x32_bf16 v[44:47], a[232:235], v[192:195], v[44:47]\n\t"
    "v_exp_f32 v70, v70\n\t"
    "v_mfma_f32_16x16x32_bf16 v[44:47], a[236:239], v[224:227], v[44:47]\n\t"
    "buffer_load_dwordx4 v4, s[40:43], s60 offen offset:1024 sc1 lds\n\t"
    "v_mfma_f32_16x16x32_bf16 v[48:51], a[216:219], v[72:75], v[108:111]\n\t"
    "v_exp_f32 v71, v71\n\t"
    "v_mfma_f32_16x16x32_bf16 v[48:51], a[220:223], v[104:107], v[48:51]\n\t"
    "v_exp_f32 v76, v76\n\t"
    "v_mfma_f32_16x16x32_bf16 v[48:51], a[224:227], v[136:139], v[48:51]\n\t"
    "v_exp_f32 v77, v77\n\t"
    "v_mfma_f32_16x16x32_bf16 v[48:51], a[228:231], v[168:171], v[48:51]\n\t"
    "v_cvt_pk_bf16_f32 v197, v70, v71\n\t"
    "v_cvt_pk_bf16_f32 v204, v76, v77\n\t"
    "v_mfma_f32_16x16x32_bf16 v[48:51], a[232:235], v[200:203], v[48:51]\n\t"
    "buffer_load_dwordx4 v4, s[40:43], s60 offen offset:2048 sc1 lds\n\t"
    "v_mfma_f32_16x16x32_bf16 v[48:51], a[236:239], v[232:235], v[48:51]\n\t"
    "v_exp_f32 v78, v78\n\t"
    "v_mfma_f32_16x16x32_bf16 v[52:55], a[216:219], v[80:83], v[116:119]\n\t"
    "v_exp_f32 v79, v79\n\t"
    "v_mfma_f32_16x16x32_bf16 v[52:55], a[220:223], v[112:115], v[52:55]\n\t"
    "v_exp_f32 v84, v84\n\t"
    "v_mfma_f32_16x16x32_bf16 v[52:55], a[224:227], v[144:147], v[52:55]\n\t"
    "v_exp_f32 v85, v85\n\t"
    "v_mfma_f32_16x16x32_bf16 v[52:55], a[228:231], v[176:179], v[52:55]\n\t"
    "buffer_load_dwordx4 v4, s[40:43], s60 offen offset:3072 sc1 lds\n\t"
    "v_mfma_f32_16x16x32_bf16 v[52:55], a[232:235], v[208:211], v[52:55]\n\t"
    "s_add_u32 m0, m0, 1024\n\t"
    "v_cvt_pk_bf16_f32 v205, v78, v79\n\t"
    "v_cvt_pk_bf16_f32 v212, v84, v85\n\t"
    "v_mfma_f32_16x16x32_bf16 v[52:55], a[236:239], v[240:243], v[52:55]\n\t"
    "s_add_u32 s60, s60, 1024\n\t"
    "v_exp_f32 v86, v86\n\t"
    "v_mfma_f32_16x16x32_bf16 v[56:59], a[216:219], v[88:91], v[124:127]\n\t"
    "v_exp_f32 v87, v87\n\t"
    "v_mfma_f32_16x16x32_bf16 v[56:59], a[220:223], v[120:123], v[56:59]\n\t"
    "ds_read_b128 a[216:219], v30 offset:43008\n\t"
    "v_cvt_pk_bf16_f32 v213, v86, v87\n\t"
    "v_mfma_f32_16x16x32_bf16 v[56:59], a[224:227], v[152:155], v[56:59]\n\t"
    "buffer_load_dwordx4 v4, s[40:43], s60 offen offset:3072 sc1 lds\n\t"
    "v_mfma_f32_16x16x32_bf16 v[56:59], a[228:231], v[184:187], v[56:59]\n\t"
    "s_add_u32 m0, s86, 0\n\t"
    "v_exp_f32 v92, v92\n\t"
    "v_mfma_f32_16x16x32_bf16 v[56:59], a[232:235], v[216:219], v[56:59]\n\t"
    "s_add_u32 s60, s68, 0\n\t"
    "ds_read_b128 a[220:223], v31 offset:43008\n\t"
    "v_lshrrev_b32 v10, 6, v4\n\t"
    "v_mfma_f32_16x16x32_bf16 v[56:59], a[236:239], v[248:251], v[56:59]\n\t"
    "s_waitcnt lgkmcnt(2)\n\t"
    "ds_read_b128 a[224:227], v30 offset:43136\n\t"
    "v_and_b32 v10, 8, v10\n\t"
    "v_mfma_f32_16x16x32_bf16 v[68:71], a[192:195], v[64:67], v[100:103]\n\t"
    "ds_read_b128 a[228:231], v31 offset:43136\n\t"
    "v_add_u32 v8, 65536, v4\n\t"
    "v_mfma_f32_16x16x32_bf16 v[68:71], a[196:199], v[96:99], v[68:71]\n\t"
    "buffer_load_dwordx4 v4, s[40:43], s60 offen offset:0 sc1 lds\n\t"
    "v_mfma_f32_16x16x32_bf16 v[68:71], a[200:203], v[128:131], v[68:71]\n\t"
    "ds_read_b128 a[232:235], v30 offset:43264\n\t"
    "v_add_u32 v9, 131072, v4\n\t"
    "v_mfma_f32_16x16x32_bf16 v[68:71], a[204:207], v[160:163], v[68:71]\n\t"
    "v_exp_f32 v93, v93\n\t"
    "v_mfma_f32_16x16x32_bf16 v[68:71], a[208:211], v[192:195], v[68:71]\n\t"
    "v_exp_f32 v94, v94\n\t"
    "v_mfma_f32_16x16x32_bf16 v[68:71], a[212:215], v[224:227], v[68:71]\n\t"
    "s_movk_i32 s55, 0x1f0\n\t"
    "v_exp_f32 v95, v95\n\t"
    "v_mfma_f32_16x16x32_bf16 v[76:79], a[192:195], v[72:75], v[108:111]\n\t"
    "s_bfe_u32 s61, s87, 0x20008\n\t"
    "buffer_load_dwordx4 v4, s[40:43], s60 offen offset:1024 sc1 lds\n\t"
    "v_mfma_f32_16x16x32_bf16 v[76:79], a[196:199], v[104:107], v[76:79]\n\t"
    "s_mul_i32 s61, s61, 49152\n\t"
    "ds_read_b128 a[236:239], v31 offset:43264\n\t"
    "v_cvt_pk_bf16_f32 v220, v92, v93\n\t"
    "v_mfma_f32_16x16x32_bf16 v[76:79], a[200:203], v[136:139], v[76:79]\n\t"
    "s_xor_b32 s48, s96, 1\n\t"
    "v_cvt_pk_bf16_f32 v221, v94, v95\n\t"
    "v_and_or_b32 v10, v4, s55, v10\n\t"
    "v_mfma_f32_16x16x32_bf16 v[76:79], a[204:207], v[168:171], v[76:79]\n\t"
    "s_mul_i32 s55, s79, 12288\n\t"
    "v_exp_f32 v44, v44\n\t"
    "v_mfma_f32_16x16x32_bf16 v[76:79], a[208:211], v[200:203], v[76:79]\n\t"
    "s_add_u32 s55, s55, s61\n\t"
    "v_exp_f32 v45, v45\n\t"
    "v_mfma_f32_16x16x32_bf16 v[76:79], a[212:215], v[232:235], v[76:79]\n\t"
    "s_add_u32 s55, s55, 4194304\n\t"
    "buffer_load_dwordx4 v4, s[40:43], s60 offen offset:2048 sc1 lds\n\t"
    "v_mfma_f32_16x16x32_bf16 v[84:87], a[192:195], v[80:83], v[116:119]\n\t"
    "s_xor_b32 s49, s96, 4\n\t"
    "v_exp_f32 v46, v46\n\t"
    "v_mfma_f32_16x16x32_bf16 v[84:87], a[196:199], v[112:115], v[84:87]\n\t"
    "s_xor_b32 s50, s96, 2\n\t"
    "v_exp_f32 v47, v47\n\t"
    "v_mfma_f32_16x16x32_bf16 v[84:87], a[200:203], v[144:147], v[84:87]\n\t"
    "s_xor_b32 s51, s96, 3\n\t"
    "v_cvt_pk_bf16_f32 v198, v44, v45\n\t"
    "v_cvt_pk_bf16_f32 v199, v46, v47\n\t"
    "v_mfma_f32_16x16x32_bf16 v[84:87], a[204:207], v[176:179], v[84:87]\n\t"
    "s_xor_b32 s52, s96, 5\n\t"
    "v_exp_f32 v48, v48\n\t"
    "v_mfma_f32_16x16x32_bf16 v[84:87], a[208:211], v[208:211], v[84:87]\n\t"
    "s_xor_b32 s53, s96, 6\n\t"
    "buffer_load_dwordx4 v4, s[40:43], s60 offen offset:3072 sc1 lds\n\t"
    "v_mfma_f32_16x16x32_bf16 v[84:87], a[212:215], v[240:243], v[84:87]\n\t"
    "s_add_u32 m0, s86, 4294934528\n\t"
    "v_exp_f32 v49, v49\n\t"
    "v_mfma_f32_16x16x32_bf16 v[92:95], a[192:195], v[88:91], v[124:127]\n\t"
    "s_add_u32 s60, s69, 0\n\t"
    "v_exp_f32 v50, v50\n\t"
    "v_mfma_f32_16x16x32_bf16 v[92:95], a[196:199], v[120:123], v[92:95]\n\t"
    "s_xor_b32 s54, s96, 7\n\t"
    "v_exp_f32 v51, v51\n\t"
    "v_mfma_f32_16x16x32_bf16 v[92:95], a[200:203], v[152:155], v[92:95]\n\t"
    "s_mulk_i32 s48, 6144\n\t"
    "ds_read_b64_tr_b16 a[192:193], v15 offset:0\n\t"
    "v_cvt_pk_bf16_f32 v206, v48, v49\n\t"
    "v_mfma_f32_16x16x32_bf16 v[92:95], a[204:207], v[184:187], v[92:95]\n\t"
    "s_mulk_i32 s49, 6144\n\t"
    "ds_read_b64_tr_b16 a[194:195], v15 offset:6144\n\t"
    "v_cvt_pk_bf16_f32 v207, v50, v51\n\t"
    "v_mfma_f32_16x16x32_bf16 v[92:95], a[208:211], v[216:219], v[92:95]\n\t"
    "s_mulk_i32 s50, 6144\n\t"
    "v_exp_f32 v52, v52\n\t"
    "v_mfma_f32_16x16x32_bf16 v[92:95], a[212:215], v[248:251], v[92:95]\n\t"
    "s_waitcnt lgkmcnt(2)\n\t"
    "v_exp_f32 v53, v53\n\t"
    "v_mfma_f32_16x16x32_bf16 v[44:47], a[216:219], v[64:67], v[100:103]\n\t"
    "ds_read_b64_tr_b16 a[196:197], v15 offset:12288\n\t"
    "v_cvt_pk_bf16_f32 v214, v52, v53\n\t"
    "v_mfma_f32_16x16x32_bf16 v[44:47], a[220:223], v[96:99], v[44:47]\n\t"
    "v_exp_f32 v54, v54\n\t"
    "v_mfma_f32_16x16x32_bf16 v[44:47], a[224:227], v[128:131], v[44:47]\n\t"
    "v_exp_f32 v55, v55\n\t"
    "v_mfma_f32_16x16x32_bf16 v[44:47], a[228:231], v[160:163], v[44:47]\n\t"
    "ds_read_b64_tr_b16 a[198:199], v15 offset:18432\n\t"
    "v_cvt_pk_bf16_f32 v215, v54, v55\n\t"
    "v_mfma_f32_16x16x32_bf16 v[44:47], a[232:235], v[192:195], v[44:47]\n\t"
    "v_exp_f32 v56, v56\n\t"
    "v_mfma_f32_16x16x32_bf16 v[44:47], a[236:239], v[224:227], v[44:47]\n\t"
    "s_mulk_i32 s51, 6144\n\t"
    "v_exp_f32 v57, v57\n\t"
    "v_mfma_f32_16x16x32_bf16 v[48:51], a[216:219], v[72:75], v[108:111]\n\t"
    "s_mulk_i32 s52, 6144\n\t"
    "ds_read_b64_tr_b16 a[200:201], v16 offset:0\n\t"
    "v_cvt_pk_bf16_f32 v222, v56, v57\n\t"
    "v_mfma_f32_16x16x32_bf16 v[48:51], a[220:223], v[104:107], v[48:51]\n\t"
    "s_mulk_i32 s53, 6144\n\t"
    "ds_read_b64_tr_b16 a[202:203], v16 offset:6144\n\t"
    "v_mfma_f32_16x16x32_bf16 v[48:51], a[224:227], v[136:139], v[48:51]\n\t"
    "s_mulk_i32 s54, 6144\n\t"
    "ds_read_b64_tr_b16 a[204:205], v16 offset:12288\n\t"
    "v_mfma_f32_16x16x32_bf16 v[48:51], a[228:231], v[168:171], v[48:51]\n\t"
    "s_add_u32 s48, s48, s55\n\t"
    "ds_read_b64_tr_b16 a[206:207], v16 offset:18432\n\t"
    "v_mfma_f32_16x16x32_bf16 v[48:51], a[232:235], v[200:203], v[48:51]\n\t"
    "s_add_u32 s49, s49, s55\n\t"
    "v_exp_f32 v58, v58\n\t"
    "v_mfma_f32_16x16x32_bf16 v[48:51], a[236:239], v[232:235], v[48:51]\n\t"
    "s_add_u32 s50, s50, s55\n\t"
    "v_exp_f32 v59, v59\n\t"
    "v_mfma_f32_16x16x32_bf16 v[52:55], a[216:219], v[80:83], v[116:119]\n\t"
    "s_add_u32 s51, s51, s55\n\t"
    "ds_read_b64_tr_b16 a[208:209], v17 offset:0\n\t"
    "v_cvt_pk_bf16_f32 v223, v58, v59\n\t"
    "v_mfma_f32_16x16x32_bf16 v[52:55], a[220:223], v[112:115], v[52:55]\n\t"
    "s_add_u32 s52, s52, s55\n\t"
    "ds_read_b64_tr_b16 a[210:211], v17 offset:6144\n\t"
    "v_mfma_f32_16x16x32_bf16 v[52:55], a[224:227], v[144:147], v[52:55]\n\t"
    "s_add_u32 s53, s53, s55\n\t"
    "ds_read_b64_tr_b16 a[212:213], v17 offset:12288\n\t"
    "v_mfma_f32_16x16x32_bf16 v[52:55], a[228:231], v[176:179], v[52:55]\n\t"
    "s_add_u32 s54, s54, s55\n\t"
    "ds_read_b64_tr_b16 a[214:215], v17 offset:18432\n\t"
    "v_mfma_f32_16x16x32_bf16 v[52:55], a[232:235], v[208:211], v[52:55]\n\t"
    "s_bfe_u32 s55, s87, 0x10009\n\t"
    "v_exp_f32 v68, v68\n\t"
    "v_mfma_f32_16x16x32_bf16 v[52:55], a[236:239], v[240:243], v[52:55]\n\t"
    "s_mul_i32 s55, s55, -192\n\t"
    "v_exp_f32 v69, v69\n\t"
    "v_mfma_f32_16x16x32_bf16 v[56:59], a[216:219], v[88:91], v[124:127]\n\t"
    "v_exp_f32 v70, v70\n\t"
    "v_mfma_f32_16x16x32_bf16 v[56:59], a[220:223], v[120:123], v[56:59]\n\t"
    "v_exp_f32 v71, v71\n\t"
    "v_mfma_f32_16x16x32_bf16 v[56:59], a[224:227], v[152:155], v[56:59]\n\t"
    "s_waitcnt vmcnt(4) lgkmcnt(0)\n\t"
    "s_barrier\n\t"
    "ds_read_b64_tr_b16 a[216:217], v32 offset:0\n\t"
    "v_mfma_f32_16x16x32_bf16 v[56:59], a[228:231], v[184:187], v[56:59]\n\t"
    "buffer_load_dwordx4 v4, s[40:43], s60 offen offset:0 sc1 lds\n\t"
    "v_mfma_f32_16x16x32_bf16 v[56:59], a[232:235], v[216:219], v[56:59]\n\t"
    "ds_read_b64_tr_b16 a[218:219], v32 offset:6144\n\t"
    "v_cvt_pk_bf16_f32 v228, v68, v69\n\t"
    "v_mfma_f32_16x16x32_bf16 v[56:59], a[236:239], v[248:251], v[56:59]\n\t"
    "ds_read_b64_tr_b16 a[220:221], v32 offset:12288\n\t"
    "v_cvt_pk_bf16_f32 v229, v70, v71\n\t"
    "v_mfma_f32_16x16x32_bf16 v[100:103], v[20:23], v[196:199], 0\n\t"
    "v_exp_f32 v44, v44\n\t"
    "v_mfma_f32_16x16x32_bf16 v[108:111], v[20:23], v[204:207], 0\n\t"
    "v_exp_f32 v45, v45\n\t"
    "v_mfma_f32_16x16x32_bf16 v[116:119], v[20:23], v[212:215], 0\n\t"
    "buffer_load_dwordx4 v4, s[40:43], s60 offen offset:1024 sc1 lds\n\t"
    "v_mfma_f32_16x16x32_bf16 v[124:127], v[20:23], v[220:223], 0\n\t"
    "ds_read_b64_tr_b16 a[222:223], v32 offset:18432\n\t"
    "v_cvt_pk_bf16_f32 v230, v44, v45\n\t"
    "v_mfma_f32_16x16x32_bf16 a[0:3], a[192:195], v[132:135], a[0:3]\n\t"
    "v_exp_f32 v46, v46\n\t"
    "v_mfma_f32_16x16x32_bf16 a[0:3], a[196:199], v[164:167], a[0:3]\n\t"
    "v_exp_f32 v47, v47\n\t"
    "v_mfma_f32_16x16x32_bf16 a[4:7], a[192:195], v[140:143], a[4:7]\n\t"
    "v_exp_f32 v76, v76\n\t"
    "v_mfma_f32_16x16x32_bf16 a[4:7], a[196:199], v[172:175], a[4:7]\n\t"
    "buffer_load_dwordx4 v4, s[40:43], s60 offen offset:2048 sc1 lds\n\t"
    "v_mfma_f32_16x16x32_bf16 a[8:11], a[192:195], v[148:151], a[8:11]\n\t"
    "v_exp_f32 v77, v77\n\t"
    "v_mfma_f32_16x16x32_bf16 a[8:11], a[196:199], v[180:183], a[8:11]\n\t"
    "v_exp_f32 v78, v78\n\t"
    "v_mfma_f32_16x16x32_bf16 a[12:15], a[192:195], v[156:159], a[12:15]\n\t"
    "v_exp_f32 v79, v79\n\t"
    "v_mfma_f32_16x16x32_bf16 a[12:15], a[196:199], v[188:191], a[12:15]\n\t"
    "v_exp_f32 v48, v48\n\t"
    "v_mfma_f32_16x16x32_bf16 a[16:19], a[200:203], v[132:135], a[16:19]\n\t"
    "buffer_load_dwordx4 v4, s[40:43], s60 offen offset:3072 sc1 lds\n\t"
    "v_mfma_f32_16x16x32_bf16 a[16:19], a[204:207], v[164:167], a[16:19]\n\t"
    "s_add_u32 m0, s86, 4294950912\n\t"
    "v_exp_f32 v49, v49\n\t"
    "v_mfma_f32_16x16x32_bf16 a[20:23], a[200:203], v[140:143], a[20:23]\n\t"
    "s_add_u32 s60, s97, 0\n\t"
    "v_exp_f32 v50, v50\n\t"
    "v_mfma_f32_16x16x32_bf16 a[20:23], a[204:207], v[172:175], a[20:23]\n\t"
    "v_exp_f32 v51, v51\n\t"
    "v_mfma_f32_16x16x32_bf16 a[24:27], a[200:203], v[148:151], a[24:27]\n\t"
    "v_exp_f32 v84, v84\n\t"
    "v_mfma_f32_16x16x32_bf16 a[24:27], a[204:207], v[180:183], a[24:27]\n\t"
    "v_exp_f32 v85, v85\n\t"
    "v_mfma_f32_16x16x32_bf16 a[28:31], a[200:203], v[156:159], a[28:31]\n\t"
    "v_exp_f32 v86, v86\n\t"
    "v_mfma_f32_16x16x32_bf16 a[28:31], a[204:207], v[188:191], a[28:31]\n\t"
    "v_exp_f32 v87, v87\n\t"
    "v_mfma_f32_16x16x32_bf16 a[32:35], a[208:211], v[132:135], a[32:35]\n\t"
    "ds_read_b64_tr_b16 a[224:225], v33 offset:0\n\t"
    "v_cvt_pk_bf16_f32 v231, v46, v47\n\t"
    "v_mfma_f32_16x16x32_bf16 a[32:35], a[212:215], v[164:167], a[32:35]\n\t"
    "v_exp_f32 v52, v52\n\t"
    "v_mfma_f32_16x16x32_bf16 a[36:39], a[208:211], v[140:143], a[36:39]\n\t"
    "v_exp_f32 v53, v53\n\t"
    "v_exp_f32 v54, v54\n\t"
    "v_mfma_f32_16x16x32_bf16 a[36:39], a[212:215], v[172:175], a[36:39]\n\t"
    "v_exp_f32 v55, v55\n\t"
    "buffer_load_dwordx4 v4, s[40:43], s60 offen offset:0 sc1 lds\n\t"
    "v_mfma_f32_16x16x32_bf16 a[40:43], a[208:211], v[148:151], a[40:43]\n\t"
    "ds_read_b64_tr_b16 a[226:227], v33 offset:6144\n\t"
    "v_cvt_pk_bf16_f32 v236, v76, v77\n\t"
    "v_cvt_pk_bf16_f32 v237, v78, v79\n\t"
    "v_mfma_f32_16x16x32_bf16 a[40:43], a[212:215], v[180:183], a[40:43]\n\t"
    "v_exp_f32 v92, v92\n\t"
    "v_exp_f32 v93, v93\n\t"
    "v_mfma_f32_16x16x32_bf16 a[44:47], a[208:211], v[156:159], a[44:47]\n\t"
    "v_exp_f32 v94, v94\n\t"
    "v_exp_f32 v95, v95\n\t"
    "v_mfma_f32_16x16x32_bf16 a[44:47], a[212:215], v[188:191], a[44:47]\n\t"
    "ds_read_b64_tr_b16 a[228:229], v33 offset:12288\n\t"
    "v_cvt_pk_bf16_f32 v238, v48, v49\n\t"
    "v_cvt_pk_bf16_f32 v239, v50, v51\n\t"
    "v_mfma_f32_16x16x32_bf16 v[100:103], v[20:23], v[228:231], v[100:103]\n\t"
    "v_exp_f32 v56, v56\n\t"
    "v_exp_f32 v57, v57\n\t"
    "v_mfma_f32_16x16x32_bf16 v[108:111], v[20:23], v[236:239], v[108:111]\n\t"
    "v_exp_f32 v58, v58\n\t"
    "v_exp_f32 v59, v59\n\t"
    "v_cvt_pk_bf16_f32 v244, v84, v85\n\t"
    "v_cvt_pk_bf16_f32 v245, v86, v87\n\t"
    "v_cvt_pk_bf16_f32 v246, v52, v53\n\t"
    "v_cvt_pk_bf16_f32 v247, v54, v55\n\t"
    "s_nop 1\n\t"
    "v_mfma_f32_16x16x32_bf16 v[116:119], v[20:23], v[244:247], v[116:119]\n\t"
    "buffer_load_dwordx4 v4, s[40:43], s60 offen offset:1024 sc1 lds\n\t"
    "v_cvt_pk_bf16_f32 v252, v92, v93\n\t"
    "v_cvt_pk_bf16_f32 v253, v94, v95\n\t"
    "v_cvt_pk_bf16_f32 v254, v56, v57\n\t"
    "v_cvt_pk_bf16_f32 v255, v58, v59\n\t"
    "s_nop 1\n\t"
    "v_mfma_f32_16x16x32_bf16 v[124:127], v[20:23], v[252:255], v[124:127]\n\t"
    "s_waitcnt lgkmcnt(2)\n\t"
    "ds_read_b64_tr_b16 a[230:231], v33 offset:18432\n\t"
    "v_add_f32_dpp v40, v100, v40 quad_perm:[0,1,2,3] row_mask:0x1 bank_mask:0xf\n\t"
    "v_mfma_f32_16x16x32_bf16 a[0:3], a[216:219], v[196:199], a[0:3]\n\t"
    "ds_read_b64_tr_b16 a[232:233], v34 offset:0\n\t"
    "v_add_f32_dpp v40, v108, v40 quad_perm:[0,1,2,3] row_mask:0x2 bank_mask:0xf\n\t"
    "v_mfma_f32_16x16x32_bf16 a[0:3], a[220:223], v[228:231], a[0:3]\n\t"
    "KFP12:\n\t"
    "s_cbranch_execz KFR1\n\t"
    "ds_read_b64_tr_b16 a[234:235], v34 offset:6144\n\t"
    "v_add_f32_dpp v40, v116, v40 quad_perm:[0,1,2,3] row_mask:0x4 bank_mask:0xf\n\t"
    "v_mfma_f32_16x16x32_bf16 a[4:7], a[216:219], v[204:207], a[4:7]\n\t"
    "ds_read_b64_tr_b16 a[236:237], v34 offset:12288\n\t"
    "v_add_f32_dpp v40, v124, v40 quad_perm:[0,1,2,3] row_mask:0x8 bank_mask:0xf\n\t"
    "v_rcp_f32 v44, v40\n\t"
    "v_mfma_f32_16x16x32_bf16 a[4:7], a[220:223], v[236:239], a[4:7]\n\t"
    "ds_read_b64_tr_b16 a[238:239], v34 offset:18432\n\t"
    "v_mov_b32 v46, v44\n\t"
    "s_nop 1\n\t"
    "v_permlane32_swap_b32 v44, v46\n\t"
    "v_mfma_f32_16x16x32_bf16 a[8:11], a[216:219], v[212:215], a[8:11]\n\t"
    "ds_read_b64_tr_b16 a[192:193], v18 offset:0\n\t"
    "v_mov_b32 v45, v44\n\t"
    "v_mov_b32 v47, v46\n\t"
    "v_mfma_f32_16x16x32_bf16 a[8:11], a[220:223], v[244:247], a[8:11]\n\t"
    "ds_read_b64_tr_b16 a[194:195], v18 offset:6144\n\t"
    "v_permlane16_swap_b32 v44, v45\n\t"
    "v_permlane16_swap_b32 v46, v47\n\t"
    "v_mfma_f32_16x16x32_bf16 a[12:15], a[216:219], v[220:223], a[12:15]\n\t"
    "ds_read_b64_tr_b16 a[196:197], v18 offset:12288\n\t"
    "v_cmp_ngt_f32 vcc, s63, v40\n\t"
    "buffer_load_dwordx4 v4, s[40:43], s60 offen offset:2048 sc1 lds\n\t"
    "v_mfma_f32_16x16x32_bf16 a[12:15], a[220:223], v[252:255], a[12:15]\n\t"
    "s_cmp_lg_u64 vcc, 0\n\t"
    "s_cselect_b32 s63, 0x80000000, 0\n\t"
    "s_or_b32 s35, s35, s63\n\t"
      : CARRY, KEPT("=&"), "=&{v[24:55]}"(k0), REDO("=&") : "{s[8:15]}"(ka), "{s[16:23]}"(kb), "{s[24:27]}"(kd), "{s28}"(T) : CLOB_V, "s34", "vcc", "scc", "memory");

    if (__builtin_expect((int)res < 0, 0)) {
      unsigned m0s;
      asm volatile("s_mov_b32 %0, m0\n\ts_waitcnt vmcnt(0) lgkmcnt(0)\n\ts_nop 15" : "=s"(m0s), "+v"(tid), "+{a[240:255]}"(xa) :: "memory");
      asm volatile("" : "+s"(b));
      const unsigned h = (b & 7) * 4 + ((b >> 3) & 3), rb = b >> 5, xq = (b & 1) * 2 + ((b >> 1) & 1);
      float* scr = (float*)(WSOFF ? ws_ - ((size_t)WSOFF << 20) : ws_ + WS_SLOW);
      attn_slow((const u16*)(ws_ + WS_XS), wq_, kc_, vc_, scr, h, rb * 256 + tid);
      drain();
      const float* pr = scr + ((size_t)(rb * 256 + (tid & 192) + (tid & 15)) * H_ + h) * (DH + WIN) + ((tid >> 4) & 3) * 4;
      constexpr size_t RS = 16 * H_ * (DH + WIN);
      asm volatile(
    "global_load_dwordx4 a[0:3], %[p0], off offset:0 sc1\n\t"
    "global_load_dwordx4 a[4:7], %[p1], off offset:0 sc1\n\t"
    "global_load_dwordx4 a[8:11], %[p2], off offset:0 sc1\n\t"
    "global_load_dwordx4 a[12:15], %[p3], off offset:0 sc1\n\t"
    "global_load_dwordx4 a[16:19], %[p0], off offset:64 sc1\n\t"
    "global_load_dwordx4 a[20:23], %[p1], off offset:64 sc1\n\t"
    "global_load_dwordx4 a[24:27], %[p2], off offset:64 sc1\n\t"
    "global_load_dwordx4 a[28:31], %[p3], off offset:64 sc1\n\t"
    "global_load_dwordx4 a[32:35], %[p0], off offset:128 sc1\n\t"
    "global_load_dwordx4 a[36:39], %[p1], off offset:128 sc1\n\t"
    "global_load_dwordx4 a[40:43], %[p2], off offset:128 sc1\n\t"
    "global_load_dwordx4 a[44:47], %[p3], off offset:128 sc1\n\t"
    "global_load_dwordx4 a[48:51], %[p0], off offset:192 sc1\n\t"
    "global_load_dwordx4 a[52:55], %[p1], off offset:192 sc1\n\t"
    "global_load_dwordx4 a[56:59], %[p2], off offset:192 sc1\n\t"
    "global_load_dwordx4 a[60:63], %[p3], off offset:192 sc1\n\t"
    "global_load_dwordx4 a[64:67], %[p0], off offset:256 sc1\n\t"
    "global_load_dwordx4 a[68:71], %[p1], off offset:256 sc1\n\t"
    "global_load_dwordx4 a[72:75], %[p2], off offset:256 sc1\n\t"
    "global_load_dwordx4 a[76:79], %[p3], off offset:256 sc1\n\t"
    "global_load_dwordx4 a[80:83], %[p0], off offset:320 sc1\n\t"
    "global_load_dwordx4 a[84:87], %[p1], off offset:320 sc1\n\t"
    "global_load_dwordx4 a[88:91], %[p2], off offset:320 sc1\n\t"
    "global_load_dwordx4 a[92:95], %[p3], off offset:320 sc1\n\t"
    "global_load_dwordx4 a[96:99], %[p0], off offset:384 sc1\n\t"
    "global_load_dwordx4 a[100:103], %[p1], off offset:384 sc1\n\t"
    "global_load_dwordx4 a[104:107], %[p2], off offset:384 sc1\n\t"
    "global_load_dwordx4 a[108:111], %[p3], off offset:384 sc1\n\t"
    "global_load_dwordx4 a[112:115], %[p0], off offset:448 sc1\n\t"
    "global_load_dwordx4 a[116:119], %[p1], off offset:448 sc1\n\t"
    "global_load_dwordx4 a[120:123], %[p2], off offset:448 sc1\n\t"
    "global_load_dwordx4 a[124:127], %[p3], off offset:448 sc1\n\t"
    "global_load_dwordx4 a[128:131], %[p0], off offset:512 sc1\n\t"
    "global_load_dwordx4 a[132:135], %[p1], off offset:512 sc1\n\t"
    "global_load_dwordx4 a[136:139], %[p2], off offset:512 sc1\n\t"
    "global_load_dwordx4 a[140:143], %[p3], off offset:512 sc1\n\t"
    "global_load_dwordx4 a[144:147], %[p0], off offset:576 sc1\n\t"
    "global_load_dwordx4 a[148:151], %[p1], off offset:576 sc1\n\t"
    "global_load_dwordx4 a[152:155], %[p2], off offset:576 sc1\n\t"
    "global_load_dwordx4 a[156:159], %[p3], off offset:576 sc1\n\t"
    "global_load_dwordx4 a[160:163], %[p0], off offset:640 sc1\n\t"
    "global_load_dwordx4 a[164:167], %[p1], off offset:640 sc1\n\t"
    "global_load_dwordx4 a[168:171], %[p2], off offset:640 sc1\n\t"
    "global_load_dwordx4 a[172:175], %[p3], off offset:640 sc1\n\t"
    "global_load_dwordx4 a[176:179], %[p0], off offset:704 sc1\n\t"
    "global_load_dwordx4 a[180:183], %[p1], off offset:704 sc1\n\t"
    "global_load_dwordx4 a[184:187], %[p2], off offset:704 sc1\n\t"
    "global_load_dwordx4 a[188:191], %[p3], off offset:704 sc1\n\t"
    "v_accvgpr_write_b32 a192, 0\n\t"
    "v_accvgpr_write_b32 a193, 0\n\t"
    "v_accvgpr_write_b32 a194, 0\n\t"
    "v_accvgpr_write_b32 a195, 0\n\t"
    "v_accvgpr_write_b32 a196, 0\n\t"
    "v_accvgpr_write_b32 a197, 0\n\t"
    "v_accvgpr_write_b32 a198, 0\n\t"
    "v_accvgpr_write_b32 a199, 0\n\t"
    "v_accvgpr_write_b32 a200, 0\n\t"
    "v_accvgpr_write_b32 a201, 0\n\t"
    "v_accvgpr_write_b32 a202, 0\n\t"
    "v_accvgpr_write_b32 a203, 0\n\t"
    "v_accvgpr_write_b32 a204, 0\n\t"
    "v_accvgpr_write_b32 a205, 0\n\t"
    "v_accvgpr_write_b32 a206, 0\n\t"
    "v_accvgpr_write_b32 a207, 0\n\t"
    "v_accvgpr_write_b32 a208, 0\n\t"
    "v_accvgpr_write_b32 a209, 0\n\t"
    "v_accvgpr_write_b32 a210, 0\n\t"
    "v_accvgpr_write_b32 a211, 0\n\t"
    "v_accvgpr_write_b32 a212, 0\n\t"
    "v_accvgpr_write_b32 a213, 0\n\t"
    "v_accvgpr_write_b32 a214, 0\n\t"
    "v_accvgpr_write_b32 a215, 0\n\t"
    "v_accvgpr_write_b32 a216, 0\n\t"
    "v_accvgpr_write_b32 a217, 0\n\t"
    "v_accvgpr_write_b32 a218, 0\n\t"
    "v_accvgpr_write_b32 a219, 0\n\t"
    "v_accvgpr_write_b32 a220, 0\n\t"
    "v_accvgpr_write_b32 a221, 0\n\t"
    "v_accvgpr_write_b32 a222, 0\n\t"
    "v_accvgpr_write_b32 a223, 0\n\t"
    "v_accvgpr_write_b32 a224, 0\n\t"
    "v_accvgpr_write_b32 a225, 0\n\t"
    "v_accvgpr_write_b32 a226, 0\n\t"
    "v_accvgpr_write_b32 a227, 0\n\t"
    "v_accvgpr_write_b32 a228, 0\n\t"
    "v_accvgpr_write_b32 a229, 0\n\t"
    "v_accvgpr_write_b32 a230, 0\n\t"
    "v_accvgpr_write_b32 a231, 0\n\t"
    "v_accvgpr_write_b32 a232, 0\n\t"
    "v_accvgpr_write_b32 a233, 0\n\t"
    "v_accvgpr_write_b32 a234, 0\n\t"
    "v_accvgpr_write_b32 a235, 0\n\t"
    "v_accvgpr_write_b32 a236, 0\n\t"
    "v_accvgpr_write_b32 a237, 0\n\t"
    "v_accvgpr_write_b32 a238, 0\n\t"
    "v_accvgpr_write_b32 a239, 0\n\t"
    "v_mov_b32 v132, 0\n\t"
    "v_mov_b32 v133, 0\n\t"
    "v_mov_b32 v134, 0\n\t"
    "v_mov_b32 v135, 0\n\t"
    "v_mov_b32 v164, 0\n\t"
    "v_mov_b32 v165, 0\n\t"
    "v_mov_b32 v166, 0\n\t"
    "v_mov_b32 v167, 0\n\t"
    "v_mov_b32 v140, 0\n\t"
    "v_mov_b32 v141, 0\n\t"
    "v_mov_b32 v142, 0\n\t"
    "v_mov_b32 v143, 0\n\t"
    "v_mov_b32 v172, 0\n\t"
    "v_mov_b32 v173, 0\n\t"
    "v_mov_b32 v174, 0\n\t"
    "v_mov_b32 v175, 0\n\t"
    "v_mov_b32 v148, 0\n\t"
    "v_mov_b32 v149, 0\n\t"
    "v_mov_b32 v150, 0\n\t"
    "v_mov_b32 v151, 0\n\t"
    "v_mov_b32 v180, 0\n\t"
    "v_mov_b32 v181, 0\n\t"
    "v_mov_b32 v182, 0\n\t"
    "v_mov_b32 v183, 0\n\t"
    "v_mov_b32 v156, 0\n\t"
    "v_mov_b32 v157, 0\n\t"
    "v_mov_b32 v158, 0\n\t"
    "v_mov_b32 v159, 0\n\t"
    "v_mov_b32 v188, 0\n\t"
    "v_mov_b32 v189, 0\n\t"
    "v_mov_b32 v190, 0\n\t"
    "v_mov_b32 v191, 0\n\t"
    "v_mov_b32 v196, 0\n\t"
    "v_mov_b32 v197, 0\n\t"
    "v_mov_b32 v198, 0\n\t"
    "v_mov_b32 v199, 0\n\t"
    "v_mov_b32 v228, 0\n\t"
    "v_mov_b32 v229, 0\n\t"
    "v_mov_b32 v230, 0\n\t"
    "v_mov_b32 v231, 0\n\t"
    "v_mov_b32 v204, 0\n\t"
    "v_mov_b32 v205, 0\n\t"
    "v_mov_b32 v206, 0\n\t"
    "v_mov_b32 v207, 0\n\t"
    "v_mov_b32 v236, 0\n\t"
    "v_mov_b32 v237, 0\n\t"
    "v_mov_b32 v238, 0\n\t"
    "v_mov_b32 v239, 0\n\t"
    "v_mov_b32 v212, 0\n\t"
    "v_mov_b32 v213, 0\n\t"
    "v_mov_b32 v214, 0\n\t"
    "v_mov_b32 v215, 0\n\t"
    "v_mov_b32 v244, 0\n\t"
    "v_mov_b32 v245, 0\n\t"
    "v_mov_b32 v246, 0\n\t"
    "v_mov_b32 v247, 0\n\t"
    "v_mov_b32 v220, 0\n\t"
    "v_mov_b32 v221, 0\n\t"
    "v_mov_b32 v222, 0\n\t"
    "v_mov_b32 v223, 0\n\t"
    "v_mov_b32 v252, 0\n\t"
    "v_mov_b32 v253, 0\n\t"
    "v_mov_b32 v254, 0\n\t"
    "v_mov_b32 v255, 0\n\t"
    "v_mov_b32 v44, 1.0\n\t"
    "v_mov_b32 v45, 1.0\n\t"
    "v_mov_b32 v46, 1.0\n\t"
    "v_mov_b32 v47, 1.0\n\t"
    "s_mov_b32 m0, %[m0]\n\t"
    "s_waitcnt vmcnt(0)\n\t"
    "s_nop 4\n\t"
        : "+{v[24:55]}"(k0), "+{a[240:255]}"(xa), REDO("=&")
        : [p0] "v"(pr + xq * RS), [p1] "v"(pr + (xq ^ 1) * RS), [p2] "v"(pr + (xq ^ 2) * RS), [p3] "v"(pr + (xq ^ 3) * RS), [m0] "s"(m0s) : "memory");
    }
    asm volatile(
    "KFB0:\n\t"
    "v_accvgpr_read_b32 v84, a0\n\t"
    "s_waitcnt lgkmcnt(6)\n\t"
    "v_mfma_f32_16x16x32_bf16 a[16:19], a[224:227], v[196:199], a[16:19]\n\t"
    "ds_read_b64_tr_b16 a[198:199], v18 offset:18432\n\t"
    "v_accvgpr_read_b32 v85, a1\n\t"
    "v_accvgpr_read_b32 v86, a2\n\t"
    "v_mfma_f32_16x16x32_bf16 a[16:19], a[228:231], v[228:231], a[16:19]\n\t"
    "ds_read_b64_tr_b16 a[200:201], v35 offset:0\n\t"
    "v_accvgpr_read_b32 v87, a3\n\t"
    "v_mul_f32 v84, v44, v84\n\t"
    "v_mfma_f32_16x16x32_bf16 a[20:23], a[224:227], v[204:207], a[20:23]\n\t"
    "buffer_load_dwordx4 v4, s[40:43], s60 offen offset:3072 sc1 lds\n\t"
    "v_mul_f32 v85, v44, v85\n\t"
    "v_mfma_f32_16x16x32_bf16 a[20:23], a[228:231], v[236:239], a[20:23]\n\t"
    "s_lshl1_add_u32 m0, s70, 0\n\t"
    "ds_read_b64_tr_b16 a[202:203], v35 offset:6144\n\t"
    "v_mul_f32 v86, v44, v86\n\t"
    "v_mul_f32 v87, v44, v87\n\t"
    "v_mfma_f32_16x16x32_bf16 a[24:27], a[224:227], v[212:215], a[24:27]\n\t"
    "s_add_u32 s60, s98, 0\n\t"
    "ds_read_b64_tr_b16 a[204:205], v35 offset:12288\n\t"
    "v_accvgpr_read_b32 v88, a16\n\t"
    "v_accvgpr_read_b32 v89, a17\n\t"
    "v_mfma_f32_16x16x32_bf16 a[24:27], a[228:231], v[244:247], a[24:27]\n\t"
    "ds_read_b64_tr_b16 a[206:207], v35 offset:18432\n\t"
    "v_accvgpr_read_b32 v90, a18\n\t"
    "v_accvgpr_read_b32 v91, a19\n\t"
    "v_mfma_f32_16x16x32_bf16 a[28:31], a[224:227], v[220:223], a[28:31]\n\t"
    "ds_read_b64_tr_b16 a[208:209], v15 offset:128\n\t"
    "v_mul_f32 v88, v44, v88\n\t"
    "v_mul_f32 v89, v44, v89\n\t"
    "v_mfma_f32_16x16x32_bf16 a[28:31], a[228:231], v[252:255], a[28:31]\n\t"
    "s_waitcnt lgkmcnt(6)\n\t"
    "ds_read_b64_tr_b16 a[210:211], v15 offset:6272\n\t"
    "v_mul_f32 v90, v44, v90\n\t"
    "v_mul_f32 v91, v44, v91\n\t"
    "v_mfma_f32_16x16x32_bf16 a[32:35], a[232:235], v[196:199], a[32:35]\n\t"
    "ds_read_b64_tr_b16 a[212:213], v15 offset:12416\n\t"
    "v_cvt_pk_bf16_f32 v192, v84, v85\n\t"
    "v_cvt_pk_bf16_f32 v193, v86, v87\n\t"
    "v_mfma_f32_16x16x32_bf16 a[32:35], a[236:239], v[228:231], a[32:35]\n\t"
    "ds_read_b64_tr_b16 a[214:215], v15 offset:18560\n\t"
    "v_cvt_pk_bf16_f32 v194, v88, v89\n\t"
    "v_cvt_pk_bf16_f32 v195, v90, v91\n\t"
    "v_mfma_f32_16x16x32_bf16 a[36:39], a[232:235], v[204:207], a[36:39]\n\t"
    "ds_read_b64_tr_b16 a[216:217], v32 offset:128\n\t"
    "v_accvgpr_read_b32 v92, a4\n\t"
    "v_accvgpr_read_b32 v93, a5\n\t"
    "v_mfma_f32_16x16x32_bf16 a[36:39], a[236:239], v[236:239], a[36:39]\n\t"
    "ds_read_b64_tr_b16 a[218:219], v32 offset:6272\n\t"
    "v_accvgpr_read_b32 v94, a6\n\t"
    "v_accvgpr_read_b32 v95, a7\n\t"
    "v_mfma_f32_16x16x32_bf16 a[40:43], a[232:235], v[212:215], a[40:43]\n\t"
    "ds_read_b64_tr_b16 a[220:221], v32 offset:12416\n\t"
    "v_accvgpr_read_b32 v96, a20\n\t"
    "v_accvgpr_read_b32 v97, a21\n\t"
    "v_mfma_f32_16x16x32_bf16 a[40:43], a[236:239], v[244:247], a[40:43]\n\t"
    "ds_read_b64_tr_b16 a[222:223], v32 offset:18560\n\t"
    "v_accvgpr_read_b32 v98, a22\n\t"
    "v_accvgpr_read_b32 v99, a23\n\t"
    "v_mfma_f32_16x16x32_bf16 a[44:47], a[232:235], v[220:223], a[44:47]\n\t"
    "buffer_store_dwordx4 v[192:195], v4, s[40:43], s92 offen offset:0 sc1\n\t"
    "v_mul_f32 v92, v45, v92\n\t"
    "v_mfma_f32_16x16x32_bf16 a[44:47], a[236:239], v[252:255], a[44:47]\n\t"
    "ds_read_b64_tr_b16 a[224:225], v16 offset:128\n\t"
    "v_mul_f32 v93, v45, v93\n\t"
    "v_mul_f32 v94, v45, v94\n\t"
    "v_mfma_f32_16x16x32_bf16 a[48:51], a[192:195], v[132:135], a[48:51]\n\t"
    "s_waitcnt lgkmcnt(6)\n\t"
    "ds_read_b64_tr_b16 a[226:227], v16 offset:6272\n\t"
    "v_mul_f32 v95, v45, v95\n\t"
    "v_mul_f32 v96, v45, v96\n\t"
    "v_mfma_f32_16x16x32_bf16 a[48:51], a[196:199], v[164:167], a[48:51]\n\t"
    "ds_read_b64_tr_b16 a[228:229], v16 offset:12416\n\t"
    "v_mul_f32 v97, v45, v97\n\t"
    "v_mul_f32 v98, v45, v98\n\t"
    "v_mfma_f32_16x16x32_bf16 a[52:55], a[192:195], v[140:143], a[52:55]\n\t"
    "ds_read_b64_tr_b16 a[230:231], v16 offset:18560\n\t"
    "v_mul_f32 v99, v45, v99\n\t"
    "v_cvt_pk_bf16_f32 v200, v92, v93\n\t"
    "v_mfma_f32_16x16x32_bf16 a[52:55], a[196:199], v[172:175], a[52:55]\n\t"
    "ds_read_b64_tr_b16 a[232:233], v33 offset:128\n\t"
    "v_cvt_pk_bf16_f32 v201, v94, v95\n\t"
    "v_cvt_pk_bf16_f32 v202, v96, v97\n\t"
    "v_mfma_f32_16x16x32_bf16 a[56:59], a[192:195], v[148:151], a[56:59]\n\t"
    "ds_read_b64_tr_b16 a[234:235], v33 offset:6272\n\t"
    "v_cvt_pk_bf16_f32 v203, v98, v99\n\t"
    "buffer_store_dwordx4 v[200:203], v4, s[40:43], s93 offen offset:0 sc1\n\t"
    "v_mfma_f32_16x16x32_bf16 a[56:59], a[196:199], v[180:183], a[56:59]\n\t"
    "ds_read_b64_tr_b16 a[236:237], v33 offset:12416\n\t"
    "v_accvgpr_read_b32 v100, a8\n\t"
    "v_accvgpr_read_b32 v101, a9\n\t"
    "v_mfma_f32_16x16x32_bf16 a[60:63], a[192:195], v[156:159], a[60:63]\n\t"
    "ds_read_b64_tr_b16 a[238:239], v33 offset:18560\n\t"
    "v_accvgpr_read_b32 v102, a10\n\t"
    "v_accvgpr_read_b32 v103, a11\n\t"
    "v_mfma_f32_16x16x32_bf16 a[60:63], a[196:199], v[188:191], a[60:63]\n\t"
    "ds_read_b64_tr_b16 a[192:193], v17 offset:128\n\t"
    "v_accvgpr_read_b32 v104, a24\n\t"
    "v_accvgpr_read_b32 v105, a25\n\t"
    "v_mfma_f32_16x16x32_bf16 a[48:51], a[200:203], v[196:199], a[48:51]\n\t"
    "ds_read_b64_tr_b16 a[194:195], v17 offset:6272\n\t"
    "v_accvgpr_read_b32 v106, a26\n\t"
    "v_accvgpr_read_b32 v107, a27\n\t"
    "v_mfma_f32_16x16x32_bf16 a[48:51], a[204:207], v[228:231], a[48:51]\n\t"
    "ds_read_b64_tr_b16 a[196:197], v17 offset:12416\n\t"
    "v_mul_f32 v100, v46, v100\n\t"
    "v_mul_f32 v101, v46, v101\n\t"
    "v_mfma_f32_16x16x32_bf16 a[52:55], a[200:203], v[204:207], a[52:55]\n\t"
    "ds_read_b64_tr_b16 a[198:199], v17 offset:18560\n\t"
    "v_mul_f32 v102, v46, v102\n\t"
    "v_mul_f32 v103, v46, v103\n\t"
    "v_mfma_f32_16x16x32_bf16 a[52:55], a[204:207], v[236:239], a[52:55]\n\t"
    "v_mul_f32 v104, v46, v104\n\t"
    "v_mul_f32 v105, v46, v105\n\t"
    "v_mul_f32 v106, v46, v106\n\t"
    "v_mfma_f32_16x16x32_bf16 a[56:59], a[200:203], v[212:215], a[56:59]\n\t"
    "v_mul_f32 v107, v46, v107\n\t"
    "v_cvt_pk_bf16_f32 v116, v100, v101\n\t"
    "v_cvt_pk_bf16_f32 v117, v102, v103\n\t"
    "v_mfma_f32_16x16x32_bf16 a[56:59], a[204:207], v[244:247], a[56:59]\n\t"
    "v_cvt_pk_bf16_f32 v118, v104, v105\n\t"
    "v_cvt_pk_bf16_f32 v119, v106, v107\n\t"
    "buffer_store_dwordx4 v[116:119], v4, s[40:43], s94 offen offset:0 sc1\n\t"
    "v_mfma_f32_16x16x32_bf16 a[60:63], a[200:203], v[220:223], a[60:63]\n\t"
    "ds_read_b64_tr_b16 a[200:201], v34 offset:128\n\t"
    "v_accvgpr_read_b32 v108, a12\n\t"
    "v_accvgpr_read_b32 v109, a13\n\t"
    "v_mfma_f32_16x16x32_bf16 a[60:63], a[204:207], v[252:255], a[60:63]\n\t"
    "ds_read_b64_tr_b16 a[202:203], v34 offset:6272\n\t"
    "v_accvgpr_read_b32 v110, a14\n\t"
    "v_accvgpr_read_b32 v111, a15\n\t"
    "v_mfma_f32_16x16x32_bf16 a[64:67], a[208:211], v[132:135], a[64:67]\n\t"
    "s_waitcnt lgkmcnt(4)\n\t"
    "ds_read_b64_tr_b16 a[204:205], v34 offset:12416\n\t"
    "v_accvgpr_read_b32 v112, a28\n\t"
    "v_accvgpr_read_b32 v113, a29\n\t"
    "v_mfma_f32_16x16x32_bf16 a[64:67], a[212:215], v[164:167], a[64:67]\n\t"
    "ds_read_b64_tr_b16 a[206:207], v34 offset:18560\n\t"
    "v_accvgpr_read_b32 v114, a30\n\t"
    "v_accvgpr_read_b32 v115, a31\n\t"
    "v_mfma_f32_16x16x32_bf16 a[68:71], a[208:211], v[140:143], a[68:71]\n\t"
    "v_mul_f32 v108, v47, v108\n\t"
    "v_mul_f32 v109, v47, v109\n\t"
    "v_mul_f32 v110, v47, v110\n\t"
    "v_mfma_f32_16x16x32_bf16 a[68:71], a[212:215], v[172:175], a[68:71]\n\t"
    "v_mul_f32 v111, v47, v111\n\t"
    "v_mul_f32 v112, v47, v112\n\t"
    "v_mul_f32 v113, v47, v113\n\t"
    "v_mfma_f32_16x16x32_bf16 a[72:75], a[208:211], v[148:151], a[72:75]\n\t"
    "v_mul_f32 v114, v47, v114\n\t"
    "v_mul_f32 v115, v47, v115\n\t"
    "v_cvt_pk_bf16_f32 v120, v108, v109\n\t"
    "v_mfma_f32_16x16x32_bf16 a[72:75], a[212:215], v[180:183], a[72:75]\n\t"
    "v_cvt_pk_bf16_f32 v121, v110, v111\n\t"
    "v_cvt_pk_bf16_f32 v122, v112, v113\n\t"
    "v_cvt_pk_bf16_f32 v123, v114, v115\n\t"
    "v_mfma_f32_16x16x32_bf16 a[76:79], a[208:211], v[156:159], a[76:79]\n\t"
    "buffer_store_dwordx4 v[120:123], v4, s[40:43], s95 offen offset:0 sc1\n\t"
    "v_accvgpr_read_b32 v84, a32\n\t"
    "v_mfma_f32_16x16x32_bf16 a[76:79], a[212:215], v[188:191], a[76:79]\n\t"
    "ds_read_b64_tr_b16 a[208:209], v18 offset:128\n\t"
    "v_accvgpr_read_b32 v85, a33\n\t"
    "v_accvgpr_read_b32 v86, a34\n\t"
    "v_mfma_f32_16x16x32_bf16 a[64:67], a[216:219], v[196:199], a[64:67]\n\t"
    "ds_read_b64_tr_b16 a[210:211], v18 offset:6272\n\t"
    "v_accvgpr_read_b32 v87, a35\n\t"
    "v_accvgpr_read_b32 v88, a48\n\t"
    "v_mfma_f32_16x16x32_bf16 a[64:67], a[220:223], v[228:231], a[64:67]\n\t"
    "ds_read_b64_tr_b16 a[212:213], v18 offset:12416\n\t"
    "v_accvgpr_read_b32 v89, a49\n\t"
    "v_accvgpr_read_b32 v90, a50\n\t"
    "v_mfma_f32_16x16x32_bf16 a[68:71], a[216:219], v[204:207], a[68:71]\n\t"
    "ds_read_b64_tr_b16 a[214:215], v18 offset:18560\n\t"
    "v_accvgpr_read_b32 v91, a51\n\t"
    "v_mul_f32 v84, v44, v84\n\t"
    "v_mfma_f32_16x16x32_bf16 a[68:71], a[220:223], v[236:239], a[68:71]\n\t"
    "v_mul_f32 v85, v44, v85\n\t"
    "v_mul_f32 v86, v44, v86\n\t"
    "v_mul_f32 v87, v44, v87\n\t"
    "v_mfma_f32_16x16x32_bf16 a[72:75], a[216:219], v[212:215], a[72:75]\n\t"
    "v_mul_f32 v88, v44, v88\n\t"
    "v_mul_f32 v89, v44, v89\n\t"
    "v_mul_f32 v90, v44, v90\n\t"
    "v_mfma_f32_16x16x32_bf16 a[72:75], a[220:223], v[244:247], a[72:75]\n\t"
    "v_mul_f32 v91, v44, v91\n\t"
    "v_cvt_pk_bf16_f32 v208, v84, v85\n\t"
    "v_cvt_pk_bf16_f32 v209, v86, v87\n\t"
    "v_mfma_f32_16x16x32_bf16 a[76:79], a[216:219], v[220:223], a[76:79]\n\t"
    "ds_read_b64_tr_b16 a[216:217], v35 offset:128\n\t"
    "v_cvt_pk_bf16_f32 v210, v88, v89\n\t"
    "v_cvt_pk_bf16_f32 v211, v90, v91\n\t"
    "v_mfma_f32_16x16x32_bf16 a[76:79], a[220:223], v[252:255], a[76:79]\n\t"
    "ds_read_b64_tr_b16 a[218:219], v35 offset:6272\n\t"
    "v_accvgpr_read_b32 v92, a36\n\t"
    "v_accvgpr_read_b32 v93, a37\n\t"
    "v_mfma_f32_16x16x32_bf16 a[80:83], a[224:227], v[132:135], a[80:83]\n\t"
    "ds_read_b64_tr_b16 a[220:221], v35 offset:12416\n\t"
    "v_accvgpr_read_b32 v94, a38\n\t"
    "v_accvgpr_read_b32 v95, a39\n\t"
    "v_mfma_f32_16x16x32_bf16 a[80:83], a[228:231], v[164:167], a[80:83]\n\t"
    "ds_read_b64_tr_b16 a[222:223], v35 offset:18560\n\t"
    "v_accvgpr_read_b32 v96, a52\n\t"
    "v_accvgpr_read_b32 v97, a53\n\t"
    "v_mfma_f32_16x16x32_bf16 a[84:87], a[224:227], v[140:143], a[84:87]\n\t"
    "v_accvgpr_read_b32 v98, a54\n\t"
    "v_accvgpr_read_b32 v99, a55\n\t"
    "v_mul_f32 v92, v45, v92\n\t"
    "v_mfma_f32_16x16x32_bf16 a[84:87], a[228:231], v[172:175], a[84:87]\n\t"
    "v_mul_f32 v93, v45, v93\n\t"
    "v_mul_f32 v94, v45, v94\n\t"
    "v_mul_f32 v95, v45, v95\n\t"
    "v_mfma_f32_16x16x32_bf16 a[88:91], a[224:227], v[148:151], a[88:91]\n\t"
    "v_mul_f32 v96, v45, v96\n\t"
    "v_mul_f32 v97, v45, v97\n\t"
    "v_mul_f32 v98, v45, v98\n\t"
    "v_mfma_f32_16x16x32_bf16 a[88:91], a[228:231], v[180:183], a[88:91]\n\t"
    "v_mul_f32 v99, v45, v99\n\t"
    "v_cvt_pk_bf16_f32 v216, v92, v93\n\t"
    "v_cvt_pk_bf16_f32 v217, v94, v95\n\t"
    "v_mfma_f32_16x16x32_bf16 a[92:95], a[224:227], v[156:159], a[92:95]\n\t"
    "ds_read_b64_tr_b16 a[224:225], v15 offset:256\n\t"
    "v_cvt_pk_bf16_f32 v218, v96, v97\n\t"
    "v_cvt_pk_bf16_f32 v219, v98, v99\n\t"
    "v_mfma_f32_16x16x32_bf16 a[92:95], a[228:231], v[188:191], a[92:95]\n\t"
    "buffer_store_dwordx4 v[208:211], v4, s[40:43], s92 offen offset:1024 sc1\n\t"
    "v_accvgpr_read_b32 v100, a40\n\t"
    "v_mfma_f32_16x16x32_bf16 a[80:83], a[232:235], v[196:199], a[80:83]\n\t"
    "ds_read_b64_tr_b16 a[226:227], v15 offset:6400\n\t"
    "v_accvgpr_read_b32 v101, a41\n\t"
    "v_accvgpr_read_b32 v102, a42\n\t"
    "v_mfma_f32_16x16x32_bf16 a[80:83], a[236:239], v[228:231], a[80:83]\n\t"
    "ds_read_b64_tr_b16 a[228:229], v15 offset:12544\n\t"
    "v_accvgpr_read_b32 v103, a43\n\t"
    "v_accvgpr_read_b32 v104, a56\n\t"
    "v_mfma_f32_16x16x32_bf16 a[84:87], a[232:235], v[204:207], a[84:87]\n\t"
    "ds_read_b64_tr_b16 a[230:231], v15 offset:18688\n\t"
    "v_accvgpr_read_b32 v105, a57\n\t"
    "v_accvgpr_read_b32 v106, a58\n\t"
    "v_mfma_f32_16x16x32_bf16 a[84:87], a[236:239], v[236:239], a[84:87]\n\t"
    "v_accvgpr_read_b32 v107, a59\n\t"
    "v_mul_f32 v100, v46, v100\n\t"
    "v_mul_f32 v101, v46, v101\n\t"
    "v_mfma_f32_16x16x32_bf16 a[88:91], a[232:235], v[212:215], a[88:91]\n\t"
    "v_mul_f32 v102, v46, v102\n\t"
    "v_mul_f32 v103, v46, v103\n\t"
    "v_mul_f32 v104, v46, v104\n\t"
    "v_mfma_f32_16x16x32_bf16 a[88:91], a[236:239], v[244:247], a[88:91]\n\t"
    "v_mul_f32 v105, v46, v105\n\t"
    "v_mul_f32 v106, v46, v106\n\t"
    "v_mul_f32 v107, v46, v107\n\t"
    "v_mfma_f32_16x16x32_bf16 a[92:95], a[232:235], v[220:223], a[92:95]\n\t"
    "ds_read_b64_tr_b16 a[232:233], v32 offset:256\n\t"
    "v_accvgpr_read_b32 v108, a44\n\t"
    "v_accvgpr_read_b32 v109, a45\n\t"
    "v_mfma_f32_16x16x32_bf16 a[92:95], a[236:239], v[252:255], a[92:95]\n\t"
    "ds_read_b64_tr_b16 a[234:235], v32 offset:6400\n\t"
    "v_accvgpr_read_b32 v110, a46\n\t"
    "v_accvgpr_read_b32 v111, a47\n\t"
    "v_mfma_f32_16x16x32_bf16 a[96:99], a[192:195], v[132:135], a[96:99]\n\t"
    "s_waitcnt lgkmcnt(4)\n\t"
    "ds_read_b64_tr_b16 a[236:237], v32 offset:12544\n\t"
    "v_accvgpr_read_b32 v112, a60\n\t"
    "v_accvgpr_read_b32 v113, a61\n\t"
    "v_mfma_f32_16x16x32_bf16 a[96:99], a[196:199], v[164:167], a[96:99]\n\t"
    "buffer_store_dwordx4 v[216:219], v4, s[40:43], s93 offen offset:1024 sc1\n\t"
    "v_accvgpr_read_b32 v114, a62\n\t"
    "v_mfma_f32_16x16x32_bf16 a[100:103], a[192:195], v[140:143], a[100:103]\n\t"
    "ds_read_b64_tr_b16 a[238:239], v32 offset:18688\n\t"
    "v_accvgpr_read_b32 v115, a63\n\t"
    "v_cvt_pk_bf16_f32 v64, v100, v101\n\t"
    "v_mfma_f32_16x16x32_bf16 a[100:103], a[196:199], v[172:175], a[100:103]\n\t"
    "v_cvt_pk_bf16_f32 v65, v102, v103\n\t"
    "v_cvt_pk_bf16_f32 v66, v104, v105\n\t"
    "v_cvt_pk_bf16_f32 v67, v106, v107\n\t"
    "v_mfma_f32_16x16x32_bf16 a[104:107], a[192:195], v[148:151], a[104:107]\n\t"
    "v_mul_f32 v108, v47, v108\n\t"
    "v_mul_f32 v109, v47, v109\n\t"
    "v_mul_f32 v110, v47, v110\n\t"
    "v_mfma_f32_16x16x32_bf16 a[104:107], a[196:199], v[180:183], a[104:107]\n\t"
    "v_mul_f32 v111, v47, v111\n\t"
    "v_mul_f32 v112, v47, v112\n\t"
    "v_mul_f32 v113, v47, v113\n\t"
    "v_mfma_f32_16x16x32_bf16 a[108:111], a[192:195], v[156:159], a[108:111]\n\t"
    "ds_read_b64_tr_b16 a[192:193], v16 offset:256\n\t"
    "v_mul_f32 v114, v47, v114\n\t"
    "v_mul_f32 v115, v47, v115\n\t"
    "v_mfma_f32_16x16x32_bf16 a[108:111], a[196:199], v[188:191], a[108:111]\n\t"
    "ds_read_b64_tr_b16 a[194:195], v16 offset:6400\n\t"
    "v_cvt_pk_bf16_f32 v72, v108, v109\n\t"
    "v_cvt_pk_bf16_f32 v73, v110, v111\n\t"
    "v_mfma_f32_16x16x32_bf16 a[96:99], a[200:203], v[196:199], a[96:99]\n\t"
    "ds_read_b64_tr_b16 a[196:197], v16 offset:12544\n\t"
    "v_cvt_pk_bf16_f32 v74, v112, v113\n\t"
    "v_cvt_pk_bf16_f32 v75, v114, v115\n\t"
    "v_mfma_f32_16x16x32_bf16 a[96:99], a[204:207], v[228:231], a[96:99]\n\t"
    "ds_read_b64_tr_b16 a[198:199], v16 offset:18688\n\t"
    "v_accvgpr_read_b32 v84, a64\n\t"
    "v_accvgpr_read_b32 v85, a65\n\t"
    "v_mfma_f32_16x16x32_bf16 a[100:103], a[200:203], v[204:207], a[100:103]\n\t"
    "v_accvgpr_read_b32 v86, a66\n\t"
    "v_accvgpr_read_b32 v87, a67\n\t"
    "v_accvgpr_read_b32 v88, a80\n\t"
    "v_mfma_f32_16x16x32_bf16 a[100:103], a[204:207], v[236:239], a[100:103]\n\t"
    "buffer_store_dwordx4 v[64:67], v4, s[40:43], s94 offen offset:1024 sc1\n\t"
    "v_accvgpr_read_b32 v89, a81\n\t"
    "v_mfma_f32_16x16x32_bf16 a[104:107], a[200:203], v[212:215], a[104:107]\n\t"
    "v_accvgpr_read_b32 v90, a82\n\t"
    "v_accvgpr_read_b32 v91, a83\n\t"
    "v_mul_f32 v84, v44, v84\n\t"
    "v_mfma_f32_16x16x32_bf16 a[104:107], a[204:207], v[244:247], a[104:107]\n\t"
    "v_mul_f32 v85, v44, v85\n\t"
    "v_mul_f32 v86, v44, v86\n\t"
    "v_mul_f32 v87, v44, v87\n\t"
    "v_mfma_f32_16x16x32_bf16 a[108:111], a[200:203], v[220:223], a[108:111]\n\t"
    "ds_read_b64_tr_b16 a[200:201], v33 offset:256\n\t"
    "v_mul_f32 v88, v44, v88\n\t"
    "v_mul_f32 v89, v44, v89\n\t"
    "v_mfma_f32_16x16x32_bf16 a[108:111], a[204:207], v[252:255], a[108:111]\n\t"
    "ds_read_b64_tr_b16 a[202:203], v33 offset:6400\n\t"
    "v_mul_f32 v90, v44, v90\n\t"
    "v_mul_f32 v91, v44, v91\n\t"
    "v_mfma_f32_16x16x32_bf16 a[112:115], a[208:211], v[132:135], a[112:115]\n\t"
    "ds_read_b64_tr_b16 a[204:205], v33 offset:12544\n\t"
    "v_cvt_pk_bf16_f32 v224, v84, v85\n\t"
    "v_cvt_pk_bf16_f32 v225, v86, v87\n\t"
    "v_mfma_f32_16x16x32_bf16 a[112:115], a[212:215], v[164:167], a[112:115]\n\t"
    "ds_read_b64_tr_b16 a[206:207], v33 offset:18688\n\t"
    "v_cvt_pk_bf16_f32 v226, v88, v89\n\t"
    "v_cvt_pk_bf16_f32 v227, v90, v91\n\t"
    "v_mfma_f32_16x16x32_bf16 a[116:119], a[208:211], v[140:143], a[116:119]\n\t"
    "v_accvgpr_read_b32 v92, a68\n\t"
    "v_accvgpr_read_b32 v93, a69\n\t"
    "v_accvgpr_read_b32 v94, a70\n\t"
    "v_mfma_f32_16x16x32_bf16 a[116:119], a[212:215], v[172:175], a[116:119]\n\t"
    "v_accvgpr_read_b32 v95, a71\n\t"
    "v_accvgpr_read_b32 v96, a84\n\t"
    "v_accvgpr_read_b32 v97, a85\n\t"
    "v_mfma_f32_16x16x32_bf16 a[120:123], a[208:211], v[148:151], a[120:123]\n\t"
    "v_accvgpr_read_b32 v98, a86\n\t"
    "v_accvgpr_read_b32 v99, a87\n\t"
    "v_mul_f32 v92, v45, v92\n\t"
    "v_mfma_f32_16x16x32_bf16 a[120:123], a[212:215], v[180:183], a[120:123]\n\t"
    "buffer_store_dwordx4 v[72:75], v4, s[40:43], s95 offen offset:1024 sc1\n\t"
    "v_mul_f32 v93, v45, v93\n\t"
    "v_mfma_f32_16x16x32_bf16 a[124:127], a[208:211], v[156:159], a[124:127]\n\t"
    "ds_read_b64_tr_b16 a[208:209], v17 offset:256\n\t"
    "v_mul_f32 v94, v45, v94\n\t"
    "v_mul_f32 v95, v45, v95\n\t"
    "v_mfma_f32_16x16x32_bf16 a[124:127], a[212:215], v[188:191], a[124:127]\n\t"
    "ds_read_b64_tr_b16 a[210:211], v17 offset:6400\n\t"
    "v_mul_f32 v96, v45, v96\n\t"
    "v_mul_f32 v97, v45, v97\n\t"
    "v_mfma_f32_16x16x32_bf16 a[112:115], a[216:219], v[196:199], a[112:115]\n\t"
    "ds_read_b64_tr_b16 a[212:213], v17 offset:12544\n\t"
    "v_mul_f32 v98, v45, v98\n\t"
    "v_mul_f32 v99, v45, v99\n\t"
    "v_mfma_f32_16x16x32_bf16 a[112:115], a[220:223], v[228:231], a[112:115]\n\t"
    "ds_read_b64_tr_b16 a[214:215], v17 offset:18688\n\t"
    "v_cvt_pk_bf16_f32 v232, v92, v93\n\t"
    "v_cvt_pk_bf16_f32 v233, v94, v95\n\t"
    "v_mfma_f32_16x16x32_bf16 a[116:119], a[216:219], v[204:207], a[116:119]\n\t"
    "v_cvt_pk_bf16_f32 v234, v96, v97\n\t"
    "v_cvt_pk_bf16_f32 v235, v98, v99\n\t"
    "v_accvgpr_read_b32 v100, a72\n\t"
    "v_mfma_f32_16x16x32_bf16 a[116:119], a[220:223], v[236:239], a[116:119]\n\t"
    "v_accvgpr_read_b32 v101, a73\n\t"
    "v_accvgpr_read_b32 v102, a74\n\t"
    "v_accvgpr_read_b32 v103, a75\n\t"
    "v_mfma_f32_16x16x32_bf16 a[120:123], a[216:219], v[212:215], a[120:123]\n\t"
    "v_accvgpr_read_b32 v104, a88\n\t"
    "v_accvgpr_read_b32 v105, a89\n\t"
    "v_accvgpr_read_b32 v106, a90\n\t"
    "v_mfma_f32_16x16x32_bf16 a[120:123], a[220:223], v[244:247], a[120:123]\n\t"
    "v_accvgpr_read_b32 v107, a91\n\t"
    "v_mul_f32 v100, v46, v100\n\t"
    "v_mul_f32 v101, v46, v101\n\t"
    "v_mfma_f32_16x16x32_bf16 a[124:127], a[216:219], v[220:223], a[124:127]\n\t"
    "v_mul_f32 v102, v46, v102\n\t"
    "v_mul_f32 v103, v46, v103\n\t"
    "v_mul_f32 v104, v46, v104\n\t"
    "v_mfma_f32_16x16x32_bf16 a[124:127], a[220:223], v[252:255], a[124:127]\n\t"
    "buffer_store_dwordx4 v[224:227], v4, s[40:43], s92 offen offset:2048 sc1\n\t"
    "v_mul_f32 v105, v46, v105\n\t"
    "v_mfma_f32_16x16x32_bf16 a[128:131], a[224:227], v[132:135], a[128:131]\n\t"
    "s_waitcnt lgkmcnt(1)\n\t"
    "v_mul_f32 v106, v46, v106\n\t"
    "v_mul_f32 v107, v46, v107\n\t"
    "v_cvt_pk_bf16_f32 v48, v100, v101\n\t"
    "v_mfma_f32_16x16x32_bf16 a[128:131], a[228:231], v[164:167], a[128:131]\n\t"
    "v_cvt_pk_bf16_f32 v49, v102, v103\n\t"
    "v_cvt_pk_bf16_f32 v50, v104, v105\n\t"
    "v_cvt_pk_bf16_f32 v51, v106, v107\n\t"
    "v_mfma_f32_16x16x32_bf16 a[132:135], a[224:227], v[140:143], a[132:135]\n\t"
    "v_accvgpr_read_b32 v108, a76\n\t"
    "v_accvgpr_read_b32 v109, a77\n\t"
    "v_accvgpr_read_b32 v110, a78\n\t"
    "v_mfma_f32_16x16x32_bf16 a[132:135], a[228:231], v[172:175], a[132:135]\n\t"
    "v_accvgpr_read_b32 v111, a79\n\t"
    "v_accvgpr_read_b32 v112, a92\n\t"
    "v_accvgpr_read_b32 v113, a93\n\t"
    "v_mfma_f32_16x16x32_bf16 a[136:139], a[224:227], v[148:151], a[136:139]\n\t"
    "v_accvgpr_read_b32 v114, a94\n\t"
    "v_accvgpr_read_b32 v115, a95\n\t"
    "v_mul_f32 v108, v47, v108\n\t"
    "v_mfma_f32_16x16x32_bf16 a[136:139], a[228:231], v[180:183], a[136:139]\n\t"
    "v_mul_f32 v109, v47, v109\n\t"
    "v_mul_f32 v110, v47, v110\n\t"
    "v_mul_f32 v111, v47, v111\n\t"
    "v_mfma_f32_16x16x32_bf16 a[140:143], a[224:227], v[156:159], a[140:143]\n\t"
    "v_mul_f32 v112, v47, v112\n\t"
    "v_mul_f32 v113, v47, v113\n\t"
    "v_mul_f32 v114, v47, v114\n\t"
    "v_mfma_f32_16x16x32_bf16 a[140:143], a[228:231], v[188:191], a[140:143]\n\t"
    "v_mul_f32 v115, v47, v115\n\t"
    "v_cvt_pk_bf16_f32 v52, v108, v109\n\t"
    "v_cvt_pk_bf16_f32 v53, v110, v111\n\t"
    "v_mfma_f32_16x16x32_bf16 a[128:131], a[232:235], v[196:199], a[128:131]\n\t"
    "v_cvt_pk_bf16_f32 v54, v112, v113\n\t"
    "v_cvt_pk_bf16_f32 v55, v114, v115\n\t"
    "v_accvgpr_read_b32 v84, a96\n\t"
    "v_mfma_f32_16x16x32_bf16 a[128:131], a[236:239], v[228:231], a[128:131]\n\t"
    "buffer_store_dwordx4 v[232:235], v4, s[40:43], s93 offen offset:2048 sc1\n\t"
    "v_accvgpr_read_b32 v85, a97\n\t"
    "v_mfma_f32_16x16x32_bf16 a[132:135], a[232:235], v[204:207], a[132:135]\n\t"
    "v_accvgpr_read_b32 v86, a98\n\t"
    "v_accvgpr_read_b32 v87, a99\n\t"
    "v_accvgpr_read_b32 v88, a112\n\t"
    "v_mfma_f32_16x16x32_bf16 a[132:135], a[236:239], v[236:239], a[132:135]\n\t"
    "v_accvgpr_read_b32 v89, a113\n\t"
    "v_accvgpr_read_b32 v90, a114\n\t"
    "v_accvgpr_read_b32 v91, a115\n\t"
    "v_mfma_f32_16x16x32_bf16 a[136:139], a[232:235], v[212:215], a[136:139]\n\t"
    "v_mul_f32 v84, v44, v84\n\t"
    "v_mul_f32 v85, v44, v85\n\t"
    "v_mul_f32 v86, v44, v86\n\t"
    "v_mfma_f32_16x16x32_bf16 a[136:139], a[236:239], v[244:247], a[136:139]\n\t"
    "v_mul_f32 v87, v44, v87\n\t"
    "v_mul_f32 v88, v44, v88\n\t"
    "v_mul_f32 v89, v44, v89\n\t"
    "v_mfma_f32_16x16x32_bf16 a[140:143], a[232:235], v[220:223], a[140:143]\n\t"
    "v_mul_f32 v90, v44, v90\n\t"
    "v_mul_f32 v91, v44, v91\n\t"
    "v_cvt_pk_bf16_f32 v240, v84, v85\n\t"
    "v_mfma_f32_16x16x32_bf16 a[140:143], a[236:239], v[252:255], a[140:143]\n\t"
    "v_cvt_pk_bf16_f32 v241, v86, v87\n\t"
    "v_cvt_pk_bf16_f32 v242, v88, v89\n\t"
    "v_cvt_pk_bf16_f32 v243, v90, v91\n\t"
    "v_mfma_f32_16x16x32_bf16 a[144:147], a[192:195], v[132:135], a[144:147]\n\t"
    "v_accvgpr_read_b32 v92, a100\n\t"
    "v_accvgpr_read_b32 v93, a101\n\t"
    "v_accvgpr_read_b32 v94, a102\n\t"
    "v_mfma_f32_16x16x32_bf16 a[144:147], a[196:199], v[164:167], a[144:147]\n\t"
    "v_accvgpr_read_b32 v95, a103\n\t"
    "v_accvgpr_read_b32 v96, a116\n\t"
    "v_accvgpr_read_b32 v97, a117\n\t"
    "v_mfma_f32_16x16x32_bf16 a[148:151], a[192:195], v[140:143], a[148:151]\n\t"
    "v_accvgpr_read_b32 v98, a118\n\t"
    "v_accvgpr_read_b32 v99, a119\n\t"
    "v_mul_f32 v92, v45, v92\n\t"
    "v_mfma_f32_16x16x32_bf16 a[148:151], a[196:199], v[172:175], a[148:151]\n\t"
    "buffer_store_dwordx4 v[48:51], v4, s[40:43], s94 offen offset:2048 sc1\n\t"
    "v_mul_f32 v93, v45, v93\n\t"
    "v_mfma_f32_16x16x32_bf16 a[152:155], a[192:195], v[148:151], a[152:155]\n\t"
    "v_mul_f32 v94, v45, v94\n\t"
    "v_mul_f32 v95, v45, v95\n\t"
    "v_mul_f32 v96, v45, v96\n\t"
    "v_mfma_f32_16x16x32_bf16 a[152:155], a[196:199], v[180:183], a[152:155]\n\t"
    "v_mul_f32 v97, v45, v97\n\t"
    "v_mul_f32 v98, v45, v98\n\t"
    "v_mul_f32 v99, v45, v99\n\t"
    "v_mfma_f32_16x16x32_bf16 a[156:159], a[192:195], v[156:159], a[156:159]\n\t"
    "v_cvt_pk_bf16_f32 v248, v92, v93\n\t"
    "v_cvt_pk_bf16_f32 v249, v94, v95\n\t"
    "v_cvt_pk_bf16_f32 v250, v96, v97\n\t"
    "v_mfma_f32_16x16x32_bf16 a[156:159], a[196:199], v[188:191], a[156:159]\n\t"
    "v_cvt_pk_bf16_f32 v251, v98, v99\n\t"
    "v_accvgpr_read_b32 v100, a104\n\t"
    "v_accvgpr_read_b32 v101, a105\n\t"
    "v_mfma_f32_16x16x32_bf16 a[144:147], a[200:203], v[196:199], a[144:147]\n\t"
    "v_accvgpr_read_b32 v102, a106\n\t"
    "v_accvgpr_read_b32 v103, a107\n\t"
    "v_accvgpr_read_b32 v104, a120\n\t"
    "v_mfma_f32_16x16x32_bf16 a[144:147], a[204:207], v[228:231], a[144:147]\n\t"
    "v_accvgpr_read_b32 v105, a121\n\t"
    "v_accvgpr_read_b32 v106, a122\n\t"
    "v_accvgpr_read_b32 v107, a123\n\t"
    "v_mfma_f32_16x16x32_bf16 a[148:151], a[200:203], v[204:207], a[148:151]\n\t"
    "v_mul_f32 v100, v46, v100\n\t"
    "v_mul_f32 v101, v46, v101\n\t"
    "v_mul_f32 v102, v46, v102\n\t"
    "v_mfma_f32_16x16x32_bf16 a[148:151], a[204:207], v[236:239], a[148:151]\n\t"
    "v_mul_f32 v103, v46, v103\n\t"
    "v_mul_f32 v104, v46, v104\n\t"
    "v_mul_f32 v105, v46, v105\n\t"
    "v_mfma_f32_16x16x32_bf16 a[152:155], a[200:203], v[212:215], a[152:155]\n\t"
    "v_mul_f32 v106, v46, v106\n\t"
    "v_mul_f32 v107, v46, v107\n\t"
    "v_cvt_pk_bf16_f32 v20, v100, v101\n\t"
    "v_mfma_f32_16x16x32_bf16 a[152:155], a[204:207], v[244:247], a[152:155]\n\t"
    "buffer_store_dwordx4 v[52:55], v4, s[40:43], s95 offen offset:2048 sc1\n\t"
    "v_cvt_pk_bf16_f32 v21, v102, v103\n\t"
    "v_mfma_f32_16x16x32_bf16 a[156:159], a[200:203], v[220:223], a[156:159]\n\t"
    "v_cvt_pk_bf16_f32 v22, v104, v105\n\t"
    "v_cvt_pk_bf16_f32 v23, v106, v107\n\t"
    "v_accvgpr_read_b32 v108, a108\n\t"
    "v_mfma_f32_16x16x32_bf16 a[156:159], a[204:207], v[252:255], a[156:159]\n\t"
    "v_accvgpr_read_b32 v109, a109\n\t"
    "v_accvgpr_read_b32 v110, a110\n\t"
    "v_accvgpr_read_b32 v111, a111\n\t"
    "v_mfma_f32_16x16x32_bf16 a[160:163], a[208:211], v[132:135], a[160:163]\n\t"
    "s_waitcnt lgkmcnt(0)\n\t"
    "v_accvgpr_read_b32 v112, a124\n\t"
    "v_accvgpr_read_b32 v113, a125\n\t"
    "v_accvgpr_read_b32 v114, a126\n\t"
    "v_mfma_f32_16x16x32_bf16 a[160:163], a[212:215], v[164:167], a[160:163]\n\t"
    "s_waitcnt vmcnt(4)\n\t"
    "s_barrier\n\t"
    "ds_read_b64_tr_b16 a[216:217], v34 offset:256\n\t"
    "buffer_store_dword v60, v2, s[40:43], s84 offen sc1\n\t"
    "v_mfma_f32_16x16x32_bf16 a[164:167], a[208:211], v[140:143], a[164:167]\n\t"
    "s_add_u32 s84, s84, 65536\n\t"
    "ds_read_b64_tr_b16 a[218:219], v34 offset:6400\n\t"
    "v_accvgpr_read_b32 v115, a127\n\t"
    "v_mul_f32 v108, v47, v108\n\t"
    "v_mfma_f32_16x16x32_bf16 a[164:167], a[212:215], v[172:175], a[164:167]\n\t"
    "ds_read_b64_tr_b16 a[220:221], v34 offset:12544\n\t"
    "v_mul_f32 v109, v47, v109\n\t"
    "buffer_load_dwordx4 v4, s[40:43], s60 offen offset:0 sc1 lds\n\t"
    "v_mfma_f32_16x16x32_bf16 a[168:171], a[208:211], v[148:151], a[168:171]\n\t"
    "ds_read_b64_tr_b16 a[222:223], v34 offset:18688\n\t"
    "v_mul_f32 v110, v47, v110\n\t"
    "v_mul_f32 v111, v47, v111\n\t"
    "v_mfma_f32_16x16x32_bf16 a[168:171], a[212:215], v[180:183], a[168:171]\n\t"
    "ds_read_b64_tr_b16 a[224:225], v18 offset:256\n\t"
    "v_mul_f32 v112, v47, v112\n\t"
    "v_mul_f32 v113, v47, v113\n\t"
    "v_mfma_f32_16x16x32_bf16 a[172:175], a[208:211], v[156:159], a[172:175]\n\t"
    "ds_read_b64_tr_b16 a[226:227], v18 offset:6400\n\t"
    "v_mul_f32 v114, v47, v114\n\t"
    "v_mul_f32 v115, v47, v115\n\t"
    "v_mfma_f32_16x16x32_bf16 a[172:175], a[212:215], v[188:191], a[172:175]\n\t"
    "s_waitcnt lgkmcnt(4)\n\t"
    "buffer_store_dwordx4 v[240:243], v4, s[40:43], s92 offen offset:3072 sc1\n\t"
    "v_cvt_pk_bf16_f32 v24, v108, v109\n\t"
    "v_mfma_f32_16x16x32_bf16 a[160:163], a[216:219], v[196:199], a[160:163]\n\t"
    "s_waitcnt lgkmcnt(2)\n\t"
    "ds_read_b64_tr_b16 a[228:229], v18 offset:12544\n\t"
    "v_cvt_pk_bf16_f32 v25, v110, v111\n\t"
    "v_cvt_pk_bf16_f32 v26, v112, v113\n\t"
    "v_mfma_f32_16x16x32_bf16 a[160:163], a[220:223], v[228:231], a[160:163]\n\t"
    "s_add_u32 s92, s92, 4096\n\t"
    "buffer_load_dwordx4 v4, s[40:43], s60 offen offset:1024 sc1 lds\n\t"
    "v_cvt_pk_bf16_f32 v27, v114, v115\n\t"
    "v_mfma_f32_16x16x32_bf16 a[164:167], a[216:219], v[204:207], a[164:167]\n\t"
    "s_add_u32 m0, s86, 4294828032\n\t"
    "ds_read_b64_tr_b16 a[230:231], v18 offset:18688\n\t"
    "v_accvgpr_read_b32 v78, a150\n\t"
    "v_accvgpr_read_b32 v79, a151\n\t"
    "v_mfma_f32_16x16x32_bf16 a[164:167], a[220:223], v[236:239], a[164:167]\n\t"
    "KFP13:\n\t"
    "s_cbranch_execz KFR1\n\t"
    "s_add_u32 s60, s68, 40960\n\t"
    "ds_read_b64_tr_b16 a[232:233], v35 offset:256\n\t"
    "v_mul_f32 v78, v45, v78\n\t"
    "v_mul_f32 v79, v45, v79\n\t"
    "v_mfma_f32_16x16x32_bf16 a[168:171], a[216:219], v[212:215], a[168:171]\n\t"
    "ds_read_b64_tr_b16 a[234:235], v35 offset:6400\n\t"
    "v_accvgpr_read_b32 v70, a146\n\t"
    "v_accvgpr_read_b32 v71, a147\n\t"
    "v_mfma_f32_16x16x32_bf16 a[168:171], a[220:223], v[244:247], a[168:171]\n\t"
    "buffer_load_dwordx4 v4, s[40:43], s60 offen offset:0 sc1 lds\n\t"
    "v_accvgpr_read_b32 v72, a132\n\t"
    "v_mfma_f32_16x16x32_bf16 a[172:175], a[216:219], v[220:223], a[172:175]\n\t"
    "ds_read_b64_tr_b16 a[236:237], v35 offset:12544\n\t"
    "v_accvgpr_read_b32 v73, a133\n\t"
    "v_cvt_pk_bf16_f32 v187, v78, v79\n\t"
    "v_mfma_f32_16x16x32_bf16 a[172:175], a[220:223], v[252:255], a[172:175]\n\t"
    "s_waitcnt lgkmcnt(5)\n\t"
    "ds_read_b64_tr_b16 a[238:239], v35 offset:18688\n\t"
    "v_mul_f32 v70, v44, v70\n\t"
    "v_mul_f32 v71, v44, v71\n\t"
    "v_mfma_f32_16x16x32_bf16 a[176:179], a[224:227], v[132:135], a[176:179]\n\t"
    "s_waitcnt lgkmcnt(4)\n\t"
    "v_accvgpr_read_b32 v74, a134\n\t"
    "v_accvgpr_read_b32 v75, a135\n\t"
    "v_accvgpr_read_b32 v76, a148\n\t"
    "v_mfma_f32_16x16x32_bf16 a[176:179], a[228:231], v[164:167], a[176:179]\n\t"
    "buffer_store_dwordx4 v[248:251], v4, s[40:43], s93 offen offset:3072 sc1\n\t"
    "v_accvgpr_read_b32 v77, a149\n\t"
    "v_mfma_f32_16x16x32_bf16 a[180:183], a[224:227], v[140:143], a[180:183]\n\t"
    "s_add_u32 s93, s93, 4096\n\t"
    "ds_read_b128 v[142:145], v4 offset:24576\n\t"
    "v_mul_f32 v72, v45, v72\n\t"
    "v_mul_f32 v73, v45, v73\n\t"
    "v_mfma_f32_16x16x32_bf16 a[180:183], a[228:231], v[172:175], a[180:183]\n\t"
    "buffer_load_dwordx4 v4, s[40:43], s60 offen offset:1024 sc1 lds\n\t"
    "v_accvgpr_read_b32 v64, a128\n\t"
    "v_mfma_f32_16x16x32_bf16 a[184:187], a[224:227], v[148:151], a[184:187]\n\t"
    "ds_read_b128 v[146:149], v4 offset:25600\n\t"
    "v_accvgpr_read_b32 v65, a129\n\t"
    "v_cvt_pk_bf16_f32 v179, v70, v71\n\t"
    "v_mfma_f32_16x16x32_bf16 a[184:187], a[228:231], v[180:183], a[184:187]\n\t"
    "ds_read_b128 v[150:153], v4 offset:26624\n\t"
    "v_mul_f32 v74, v45, v74\n\t"
    "v_mul_f32 v75, v45, v75\n\t"
    "v_mfma_f32_16x16x32_bf16 a[188:191], a[224:227], v[156:159], a[188:191]\n\t"
    "ds_read_b128 v[154:157], v4 offset:27648\n\t"
    "v_mul_f32 v76, v45, v76\n\t"
    "v_mul_f32 v77, v45, v77\n\t"
    "v_mfma_f32_16x16x32_bf16 a[188:191], a[228:231], v[188:191], a[188:191]\n\t"
    "s_waitcnt lgkmcnt(4)\n\t"
    "buffer_load_dwordx4 v4, s[40:43], s60 offen offset:2048 sc1 lds\n\t"
    "v_cvt_pk_bf16_f32 v184, v72, v73\n\t"
    "v_mfma_f32_16x16x32_bf16 a[176:179], a[232:235], v[196:199], a[176:179]\n\t"
    "ds_read_b128 v[158:161], v4 offset:28672\n\t"
    "v_accvgpr_read_b32 v66, a130\n\t"
    "v_accvgpr_read_b32 v67, a131\n\t"
    "v_mfma_f32_16x16x32_bf16 a[176:179], a[236:239], v[228:231], a[176:179]\n\t"
    "ds_read_b128 v[162:165], v4 offset:29696\n\t"
    "v_accvgpr_read_b32 v68, a144\n\t"
    "v_accvgpr_read_b32 v69, a145\n\t"
    "v_mfma_f32_16x16x32_bf16 a[180:183], a[232:235], v[204:207], a[180:183]\n\t"
    "v_mul_f32 v64, v44, v64\n\t"
    "v_mul_f32 v65, v44, v65\n\t"
    "v_cvt_pk_bf16_f32 v185, v74, v75\n\t"
    "v_mfma_f32_16x16x32_bf16 a[180:183], a[236:239], v[236:239], a[180:183]\n\t"
    "buffer_store_dwordx4 v[20:23], v4, s[40:43], s94 offen offset:3072 sc1\n\t"
    "v_cvt_pk_bf16_f32 v186, v76, v77\n\t"
    "v_mfma_f32_16x16x32_bf16 a[184:187], a[232:235], v[212:215], a[184:187]\n\t"
    "s_add_u32 s94, s94, 4096\n\t"
    "v_mul_f32 v66, v44, v66\n\t"
    "v_mul_f32 v67, v44, v67\n\t"
    "v_mul_f32 v68, v44, v68\n\t"
    "v_mfma_f32_16x16x32_bf16 a[184:187], a[236:239], v[244:247], a[184:187]\n\t"
    "buffer_load_dwordx4 v4, s[40:43], s60 offen offset:3072 sc1 lds\n\t"
    "v_mul_f32 v69, v44, v69\n\t"
    "v_mfma_f32_16x16x32_bf16 a[188:191], a[232:235], v[220:223], a[188:191]\n\t"
    "s_add_u32 m0, s86, 4294864896\n\t"
    "v_cvt_pk_bf16_f32 v176, v64, v65\n\t"
    "v_cvt_pk_bf16_f32 v177, v66, v67\n\t"
    "v_cvt_pk_bf16_f32 v178, v68, v69\n\t"
    "v_mfma_f32_16x16x32_bf16 a[188:191], a[236:239], v[252:255], a[188:191]\n\t"
    "s_waitcnt lgkmcnt(2)\n\t"
    "s_add_u32 s60, s69, 40960\n\t"
    "v_accvgpr_read_b32 v196, a160\n\t"
    "v_accvgpr_read_b32 v197, a161\n\t"
    "v_mfma_f32_16x16x32_bf16 v[82:85], v[142:145], v[192:195], 0\n\t"
    "v_accvgpr_read_b32 v198, a162\n\t"
    "v_accvgpr_read_b32 v199, a163\n\t"
    "v_accvgpr_read_b32 v204, a176\n\t"
    "v_mfma_f32_16x16x32_bf16 v[86:89], v[142:145], v[200:203], 0\n\t"
    "buffer_load_dwordx4 v4, s[40:43], s60 offen offset:0 sc1 lds\n\t"
    "v_accvgpr_read_b32 v205, a177\n\t"
    "v_mfma_f32_16x16x32_bf16 v[90:93], v[146:149], v[192:195], 0\n\t"
    "ds_read_b128 v[142:145], v4 offset:30720\n\t"
    "v_accvgpr_read_b32 v206, a178\n\t"
    "v_accvgpr_read_b32 v207, a179\n\t"
    "v_mfma_f32_16x16x32_bf16 v[94:97], v[146:149], v[200:203], 0\n\t"
    "ds_read_b128 v[146:149], v4 offset:31744\n\t"
    "v_mul_f32 v196, v44, v196\n\t"
    "v_mul_f32 v197, v44, v197\n\t"
    "v_mfma_f32_16x16x32_bf16 v[98:101], v[150:153], v[192:195], 0\n\t"
    "v_mul_f32 v198, v44, v198\n\t"
    "v_mul_f32 v199, v44, v199\n\t"
    "v_mul_f32 v204, v44, v204\n\t"
    "v_mfma_f32_16x16x32_bf16 v[102:105], v[150:153], v[200:203], 0\n\t"
    "buffer_store_dwordx4 v[24:27], v4, s[40:43], s95 offen offset:3072 sc1\n\t"
    "v_mul_f32 v205, v44, v205\n\t"
    "v_mfma_f32_16x16x32_bf16 v[106:109], v[154:157], v[192:195], 0\n\t"
    "s_add_u32 s95, s95, 4096\n\t"
    "ds_read_b128 v[150:153], v4 offset:32768\n\t"
    "v_mul_f32 v206, v44, v206\n\t"
    "v_mul_f32 v207, v44, v207\n\t"
    "v_mfma_f32_16x16x32_bf16 v[110:113], v[154:157], v[200:203], 0\n\t"
    "s_waitcnt lgkmcnt(3)\n\t"
    "buffer_load_dwordx4 v4, s[40:43], s60 offen offset:1024 sc1 lds\n\t"
    "v_accvgpr_read_b32 v212, a164\n\t"
    "v_mfma_f32_16x16x32_bf16 v[114:117], v[158:161], v[192:195], 0\n\t"
    "ds_read_b128 v[154:157], v4 offset:33792\n\t"
    "v_accvgpr_read_b32 v213, a165\n\t"
    "v_accvgpr_read_b32 v214, a166\n\t"
    "v_mfma_f32_16x16x32_bf16 v[118:121], v[158:161], v[200:203], 0\n\t"
    "ds_read_b128 v[158:161], v4 offset:34816\n\t"
    "v_accvgpr_read_b32 v215, a167\n\t"
    "v_accvgpr_read_b32 v220, a180\n\t"
    "v_mfma_f32_16x16x32_bf16 v[122:125], v[162:165], v[192:195], 0\n\t"
    "v_accvgpr_read_b32 v221, a181\n\t"
    "v_accvgpr_read_b32 v222, a182\n\t"
    "v_accvgpr_read_b32 v223, a183\n\t"
    "v_mfma_f32_16x16x32_bf16 v[126:129], v[162:165], v[200:203], 0\n\t"
    "s_waitcnt lgkmcnt(3)\n\t"
    "buffer_load_dwordx4 v4, s[40:43], s60 offen offset:2048 sc1 lds\n\t"
    "v_cvt_pk_bf16_f32 v180, v196, v197\n\t"
    "v_mfma_f32_16x16x32_bf16 v[130:133], v[142:145], v[192:195], 0\n\t"
    "ds_read_b128 v[162:165], v4 offset:35840\n\t"
    "v_cvt_pk_bf16_f32 v181, v198, v199\n\t"
    "v_cvt_pk_bf16_f32 v182, v204, v205\n\t"
    "v_mfma_f32_16x16x32_bf16 v[134:137], v[142:145], v[200:203], 0\n\t"
    "ds_read_b128 v[142:145], v4 offset:36864\n\t"
    "v_cvt_pk_bf16_f32 v183, v206, v207\n\t"
    "v_mul_f32 v212, v45, v212\n\t"
    "v_mfma_f32_16x16x32_bf16 v[78:81], v[146:149], v[192:195], 0\n\t"
    "s_mov_b32 s64, 49643520\n\t"
    "v_mul_f32 v213, v45, v213\n\t"
    "v_mul_f32 v214, v45, v214\n\t"
    "v_mul_f32 v215, v45, v215\n\t"
    "v_mfma_f32_16x16x32_bf16 v[138:141], v[146:149], v[200:203], 0\n\t"
    "s_waitcnt lgkmcnt(4)\n\t"
    "buffer_load_dwordx4 v4, s[40:43], s60 offen offset:3072 sc1 lds\n\t"
    "v_mul_f32 v220, v45, v220\n\t"
    "v_mfma_f32_16x16x32_bf16 v[70:73], v[150:153], v[192:195], 0\n\t"
    "s_add_u32 m0, s86, 4294844416\n\t"
    "ds_read_b128 v[146:149], v4 offset:37888\n\t"
    "v_mul_f32 v221, v45, v221\n\t"
    "v_mul_f32 v222, v45, v222\n\t"
    "v_mfma_f32_16x16x32_bf16 v[74:77], v[150:153], v[200:203], 0\n\t"
    "s_waitcnt lgkmcnt(3)\n\t"
    "ds_read_b128 v[150:153], v4 offset:38912\n\t"
    "v_mul_f32 v223, v45, v223\n\t"
    "v_cvt_pk_bf16_f32 v188, v212, v213\n\t"
    "v_mfma_f32_16x16x32_bf16 v[62:65], v[154:157], v[192:195], 0\n\t"
    "s_add_u32 s60, s97, 40960\n\t"
    "v_cvt_pk_bf16_f32 v189, v214, v215\n\t"
    "v_cvt_pk_bf16_f32 v190, v220, v221\n\t"
    "v_cvt_pk_bf16_f32 v191, v222, v223\n\t"
    "v_mfma_f32_16x16x32_bf16 v[66:69], v[154:157], v[200:203], 0\n\t"
    "s_mov_b64 exec, 0x7f\n\t"
    "buffer_load_dword v59, v7, s[40:43], s64 offen sc1\n\t"
    "s_mov_b64 exec, -1\n\t"
    "v_accvgpr_read_b32 v228, a136\n\t"
    "v_mfma_f32_16x16x32_bf16 v[82:85], v[158:161], v[208:211], v[82:85]\n\t"
    "ds_read_b128 v[154:157], v4 offset:39936\n\t"
    "v_accvgpr_read_b32 v229, a137\n\t"
    "v_accvgpr_read_b32 v230, a138\n\t"
    "v_mfma_f32_16x16x32_bf16 v[86:89], v[158:161], v[216:219], v[86:89]\n\t"
    "s_waitcnt lgkmcnt(3)\n\t"
    "ds_read_b128 v[158:161], v4 offset:40960\n\t"
    "v_accvgpr_read_b32 v231, a139\n\t"
    "v_accvgpr_read_b32 v236, a152\n\t"
    "v_mfma_f32_16x16x32_bf16 v[90:93], v[162:165], v[208:211], v[90:93]\n\t"
    "v_accvgpr_read_b32 v237, a153\n\t"
    "v_accvgpr_read_b32 v238, a154\n\t"
    "v_accvgpr_read_b32 v239, a155\n\t"
    "v_mfma_f32_16x16x32_bf16 v[94:97], v[162:165], v[216:219], v[94:97]\n\t"
    "ds_read_b128 v[162:165], v4 offset:41984\n\t"
    "v_mul_f32 v228, v46, v228\n\t"
    "v_mul_f32 v229, v46, v229\n\t"
    "v_mfma_f32_16x16x32_bf16 v[98:101], v[142:145], v[208:211], v[98:101]\n\t"
    "v_mul_f32 v230, v46, v230\n\t"
    "v_mul_f32 v231, v46, v231\n\t"
    "v_mul_f32 v236, v46, v236\n\t"
    "v_mfma_f32_16x16x32_bf16 v[102:105], v[142:145], v[216:219], v[102:105]\n\t"
    "s_waitcnt lgkmcnt(3)\n\t"
    "ds_read_b128 v[142:145], v4 offset:43008\n\t"
    "v_mul_f32 v237, v46, v237\n\t"
    "v_mul_f32 v238, v46, v238\n\t"
    "v_mfma_f32_16x16x32_bf16 v[106:109], v[146:149], v[208:211], v[106:109]\n\t"
    "v_mul_f32 v239, v46, v239\n\t"
    "v_cvt_pk_bf16_f32 v28, v228, v229\n\t"
    "v_cvt_pk_bf16_f32 v29, v230, v231\n\t"
    "v_mfma_f32_16x16x32_bf16 v[110:113], v[146:149], v[216:219], v[110:113]\n\t"
    "ds_read_b128 v[146:149], v4 offset:44032\n\t"
    "v_cvt_pk_bf16_f32 v30, v236, v237\n\t"
    "v_cvt_pk_bf16_f32 v31, v238, v239\n\t"
    "v_mfma_f32_16x16x32_bf16 v[114:117], v[150:153], v[208:211], v[114:117]\n\t"
    "v_accvgpr_read_b32 v196, a140\n\t"
    "v_accvgpr_read_b32 v197, a141\n\t"
    "v_accvgpr_read_b32 v198, a142\n\t"
    "v_mfma_f32_16x16x32_bf16 v[118:121], v[150:153], v[216:219], v[118:121]\n\t"
    "s_waitcnt lgkmcnt(3)\n\t"
    "ds_read_b128 v[150:153], v8 offset:8192\n\t"
    "v_accvgpr_read_b32 v199, a143\n\t"
    "v_accvgpr_read_b32 v204, a156\n\t"
    "v_mfma_f32_16x16x32_bf16 v[122:125], v[154:157], v[208:211], v[122:125]\n\t"
    "v_accvgpr_read_b32 v205, a157\n\t"
    "v_accvgpr_read_b32 v206, a158\n\t"
    "v_accvgpr_read_b32 v207, a159\n\t"
    "v_mfma_f32_16x16x32_bf16 v[126:129], v[154:157], v[216:219], v[126:129]\n\t"
    "ds_read_b128 v[154:157], v8 offset:9216\n\t"
    "v_accvgpr_read_b32 v212, a168\n\t"
    "v_accvgpr_read_b32 v213, a169\n\t"
    "v_mfma_f32_16x16x32_bf16 v[130:133], v[158:161], v[208:211], v[130:133]\n\t"
    "v_accvgpr_read_b32 v214, a170\n\t"
    "v_accvgpr_read_b32 v215, a171\n\t"
    "v_accvgpr_read_b32 v220, a184\n\t"
    "v_mfma_f32_16x16x32_bf16 v[134:137], v[158:161], v[216:219], v[134:137]\n\t"
    "s_waitcnt lgkmcnt(3)\n\t"
    "ds_read_b128 v[158:161], v8 offset:10240\n\t"
    "v_accvgpr_read_b32 v221, a185\n\t"
    "v_accvgpr_read_b32 v222, a186\n\t"
    "v_mfma_f32_16x16x32_bf16 v[78:81], v[162:165], v[208:211], v[78:81]\n\t"
    "v_accvgpr_read_b32 v223, a187\n\t"
    "v_accvgpr_read_b32 v228, a172\n\t"
    "v_accvgpr_read_b32 v229, a173\n\t"
    "v_mfma_f32_16x16x32_bf16 v[138:141], v[162:165], v[216:219], v[138:141]\n\t"
    "ds_read_b128 v[162:165], v8 offset:11264\n\t"
    "v_accvgpr_read_b32 v230, a174\n\t"
    "v_accvgpr_read_b32 v231, a175\n\t"
    "v_mfma_f32_16x16x32_bf16 v[70:73], v[142:145], v[208:211], v[70:73]\n\t"
    "v_accvgpr_read_b32 v236, a188\n\t"
    "v_accvgpr_read_b32 v237, a189\n\t"
    "v_accvgpr_read_b32 v238, a190\n\t"
    "v_mfma_f32_16x16x32_bf16 v[74:77], v[142:145], v[216:219], v[74:77]\n\t"
    "s_waitcnt lgkmcnt(3)\n\t"
    "ds_read_b128 v[142:145], v8 offset:12288\n\t"
    "v_accvgpr_read_b32 v239, a191\n\t"
    "v_mul_f32 v196, v47, v196\n\t"
    "v_mfma_f32_16x16x32_bf16 v[62:65], v[146:149], v[208:211], v[62:65]\n\t"
    "v_mul_f32 v197, v47, v197\n\t"
    "v_mul_f32 v198, v47, v198\n\t"
    "v_mul_f32 v199, v47, v199\n\t"
    "v_mfma_f32_16x16x32_bf16 v[66:69], v[146:149], v[216:219], v[66:69]\n\t"
    "s_barrier\n\t"
    "buffer_load_dwordx4 v4, s[40:43], s60 offen offset:0 sc1 lds\n\t"
    "v_mfma_f32_16x16x32_bf16 v[82:85], v[150:153], v[224:227], v[82:85]\n\t"
    "ds_read_b128 v[146:149], v8 offset:13312\n\t"
    "v_mul_f32 v204, v47, v204\n\t"
    "v_mfma_f32_16x16x32_bf16 v[86:89], v[150:153], v[232:235], v[86:89]\n\t"
    "s_waitcnt lgkmcnt(3)\n\t"
    "ds_read_b128 v[150:153], v8 offset:14336\n\t"
    "v_mul_f32 v205, v47, v205\n\t"
    "v_mfma_f32_16x16x32_bf16 v[90:93], v[154:157], v[224:227], v[90:93]\n\t"
    "v_mul_f32 v206, v47, v206\n\t"
    "v_mul_f32 v207, v47, v207\n\t"
    "v_mfma_f32_16x16x32_bf16 v[94:97], v[154:157], v[232:235], v[94:97]\n\t"
    "buffer_load_dwordx4 v4, s[40:43], s60 offen offset:1024 sc1 lds\n\t"
    "v_mfma_f32_16x16x32_bf16 v[98:101], v[158:161], v[224:227], v[98:101]\n\t"
    "ds_read_b128 v[154:157], v8 offset:15360\n\t"
    "v_mul_f32 v212, v46, v212\n\t"
    "v_mfma_f32_16x16x32_bf16 v[102:105], v[158:161], v[232:235], v[102:105]\n\t"
    "s_waitcnt lgkmcnt(4)\n\t"
    "ds_read_b128 v[158:161], v8 offset:16384\n\t"
    "v_mul_f32 v213, v46, v213\n\t"
    "v_mfma_f32_16x16x32_bf16 v[106:109], v[162:165], v[224:227], v[106:109]\n\t"
    "v_mul_f32 v214, v46, v214\n\t"
    "v_mul_f32 v215, v46, v215\n\t"
    "v_mfma_f32_16x16x32_bf16 v[110:113], v[162:165], v[232:235], v[110:113]\n\t"
    "s_waitcnt lgkmcnt(4)\n\t"
    "buffer_load_dwordx4 v4, s[40:43], s60 offen offset:2048 sc1 lds\n\t"
    "v_mfma_f32_16x16x32_bf16 v[114:117], v[142:145], v[224:227], v[114:117]\n\t"
    "ds_read_b128 v[162:165], v8 offset:17408\n\t"
    "v_mul_f32 v220, v46, v220\n\t"
    "v_mfma_f32_16x16x32_bf16 v[118:121], v[142:145], v[232:235], v[118:121]\n\t"
    "s_waitcnt lgkmcnt(4)\n\t"
    "ds_read_b128 v[142:145], v8 offset:18432\n\t"
    "v_mul_f32 v221, v46, v221\n\t"
    "v_mfma_f32_16x16x32_bf16 v[122:125], v[146:149], v[224:227], v[122:125]\n\t"
    "v_mul_f32 v222, v46, v222\n\t"
    "v_mul_f32 v223, v46, v223\n\t"
    "v_mfma_f32_16x16x32_bf16 v[126:129], v[146:149], v[232:235], v[126:129]\n\t"
    "s_waitcnt lgkmcnt(4)\n\t"
    "buffer_load_dwordx4 v4, s[40:43], s60 offen offset:3072 sc1 lds\n\t"
    "v_mfma_f32_16x16x32_bf16 v[130:133], v[150:153], v[224:227], v[130:133]\n\t"
    "s_lshl1_add_u32 m0, s70, 61440\n\t"
    "ds_read_b128 v[146:149], v8 offset:19456\n\t"
    "v_mul_f32 v228, v47, v228\n\t"
    "v_mfma_f32_16x16x32_bf16 v[134:137], v[150:153], v[232:235], v[134:137]\n\t"
    "s_waitcnt lgkmcnt(4)\n\t"
    "s_add_u32 s60, s98, 40960\n\t"
    "ds_read_b128 v[150:153], v8 offset:20480\n\t"
    "v_mfma_f32_16x16x32_bf16 v[78:81], v[154:157], v[224:227], v[78:81]\n\t"
    "v_mul_f32 v229, v47, v229\n\t"
    "v_mul_f32 v230, v47, v230\n\t"
    "v_mfma_f32_16x16x32_bf16 v[138:141], v[154:157], v[232:235], v[138:141]\n\t"
    "s_waitcnt lgkmcnt(4)\n\t"
    "buffer_load_dwordx4 v4, s[40:43], s60 offen offset:0 sc1 lds\n\t"
    "v_mfma_f32_16x16x32_bf16 v[70:73], v[158:161], v[224:227], v[70:73]\n\t"
    "ds_read_b128 v[154:157], v8 offset:21504\n\t"
    "v_mul_f32 v231, v47, v231\n\t"
    "v_mfma_f32_16x16x32_bf16 v[74:77], v[158:161], v[232:235], v[74:77]\n\t"
    "s_waitcnt lgkmcnt(4)\n\t"
    "ds_read_b128 v[158:161], v8 offset:22528\n\t"
    "v_mul_f32 v236, v47, v236\n\t"
    "v_mfma_f32_16x16x32_bf16 v[62:65], v[162:165], v[224:227], v[62:65]\n\t"
    "v_mul_f32 v237, v47, v237\n\t"
    "v_mul_f32 v238, v47, v238\n\t"
    "v_mfma_f32_16x16x32_bf16 v[66:69], v[162:165], v[232:235], v[66:69]\n\t"
    "s_waitcnt lgkmcnt(4)\n\t"
    "s_waitcnt vmcnt(9)\n\t"
    "s_barrier\n\t"
    "v_mfma_f32_16x16x32_bf16 v[82:85], v[142:145], v[240:243], v[82:85]\n\t"
    "buffer_store_dword v60, v2, s[40:43], s84 offen sc1\n\t"
    "v_mfma_f32_16x16x32_bf16 v[86:89], v[142:145], v[248:251], v[86:89]\n\t"
    "s_waitcnt lgkmcnt(3)\n\t"
    "ds_read_b128 v[162:165], v8 offset:23552\n\t"
    "v_mul_f32 v239, v47, v239\n\t"
    "v_mfma_f32_16x16x32_bf16 v[90:93], v[146:149], v[240:243], v[90:93]\n\t"
    "s_add_u32 s84, s84, 65536\n\t"
    "buffer_load_dwordx4 v4, s[40:43], s60 offen offset:1024 sc1 lds\n\t"
    "v_mfma_f32_16x16x32_bf16 v[94:97], v[146:149], v[248:251], v[94:97]\n\t"
    "s_waitcnt lgkmcnt(3)\n\t"
    "ds_read_b128 v[142:145], v8 offset:24576\n\t"
    "v_cvt_pk_bf16_f32 v36, v196, v197\n\t"
    "v_mfma_f32_16x16x32_bf16 v[98:101], v[150:153], v[240:243], v[98:101]\n\t"
    "s_add_u32 m0, s86, 4294889472\n\t"
    "ds_read_b128 v[146:149], v8 offset:25600\n\t"
    "v_cvt_pk_bf16_f32 v37, v198, v199\n\t"
    "v_mfma_f32_16x16x32_bf16 v[102:105], v[150:153], v[248:251], v[102:105]\n\t"
    "s_waitcnt lgkmcnt(4)\n\t"
    "ds_read_b128 v[150:153], v8 offset:26624\n\t"
    "v_cvt_pk_bf16_f32 v38, v204, v205\n\t"
    "v_mfma_f32_16x16x32_bf16 v[106:109], v[154:157], v[240:243], v[106:109]\n\t"
    "s_add_u32 s60, s68, 81920\n\t"
    "v_cvt_pk_bf16_f32 v39, v206, v207\n\t"
    "v_cvt_pk_bf16_f32 v244, v212, v213\n\t"
    "v_mfma_f32_16x16x32_bf16 v[110:113], v[154:157], v[248:251], v[110:113]\n\t"
    "s_waitcnt lgkmcnt(4)\n\t"
    "ds_read_b128 v[154:157], v8 offset:27648\n\t"
    "v_cvt_pk_bf16_f32 v245, v214, v215\n\t"
    "v_mfma_f32_16x16x32_bf16 v[114:117], v[158:161], v[240:243], v[114:117]\n\t"
    "v_cvt_pk_bf16_f32 v246, v220, v221\n\t"
    "v_cvt_pk_bf16_f32 v247, v222, v223\n\t"
    "v_mfma_f32_16x16x32_bf16 v[118:121], v[158:161], v[248:251], v[118:121]\n\t"
    "s_waitcnt lgkmcnt(4)\n\t"
    "s_waitcnt vmcnt(7)\n\t"
    "v_cmp_ne_u32 vcc, s80, v59\n\t"
    "s_and_b32 s65, vcc_lo, 0x7f\n\t"
    "s_cbranch_scc1 LS0_%=\n\t"
    "LR0_%=:\n\t"
    "v_mfma_f32_16x16x32_bf16 v[122:125], v[162:165], v[240:243], v[122:125]\n\t"
    "buffer_load_dwordx4 a[192:195], v5, s[40:43], s48 offen offset:0 sc1\n\t"
    "v_mfma_f32_16x16x32_bf16 v[126:129], v[162:165], v[248:251], v[126:129]\n\t"
    "s_waitcnt lgkmcnt(3)\n\t"
    "ds_read_b128 v[158:161], v8 offset:28672\n\t"
    "v_cvt_pk_bf16_f32 v252, v228, v229\n\t"
    "v_mfma_f32_16x16x32_bf16 v[130:133], v[142:145], v[240:243], v[130:133]\n\t"
    "ds_read_b128 v[162:165], v8 offset:29696\n\t"
    "v_cvt_pk_bf16_f32 v253, v230, v231\n\t"
    "v_mfma_f32_16x16x32_bf16 v[134:137], v[142:145], v[248:251], v[134:137]\n\t"
    "s_waitcnt lgkmcnt(3)\n\t"
    "ds_read_b128 v[142:145], v8 offset:30720\n\t"
    "v_cvt_pk_bf16_f32 v254, v236, v237\n\t"
    "v_mfma_f32_16x16x32_bf16 v[78:81], v[146:149], v[240:243], v[78:81]\n\t"
    "buffer_load_dwordx4 a[196:199], v6, s[40:43], s48 offen offset:0 sc1\n\t"
    "v_mfma_f32_16x16x32_bf16 v[138:141], v[146:149], v[248:251], v[138:141]\n\t"
    "ds_read_b128 v[146:149], v8 offset:31744\n\t"
    "v_cvt_pk_bf16_f32 v255, v238, v239\n\t"
    "v_mfma_f32_16x16x32_bf16 v[70:73], v[150:153], v[240:243], v[70:73]\n\t"
    "v_mfma_f32_16x16x32_bf16 v[74:77], v[150:153], v[248:251], v[74:77]\n\t"
    "s_waitcnt lgkmcnt(4)\n\t"
    "ds_read_b128 v[150:153], v8 offset:32768\n\t"
    "v_mfma_f32_16x16x32_bf16 v[62:65], v[154:157], v[240:243], v[62:65]\n\t"
    "buffer_load_dwordx4 a[200:203], v5, s[40:43], s49 offen offset:0 sc1\n\t"
    "v_mfma_f32_16x16x32_bf16 v[66:69], v[154:157], v[248:251], v[66:69]\n\t"
    "s_waitcnt lgkmcnt(4)\n\t"
    "s_barrier\n\t"
    "v_mfma_f32_16x16x32_bf16 v[82:85], v[158:161], v[176:179], v[82:85]\n\t"
    "ds_read_b128 v[154:157], v8 offset:33792\n\t"
    "v_mfma_f32_16x16x32_bf16 v[86:89], v[158:161], v[184:187], v[86:89]\n\t"
    "s_waitcnt lgkmcnt(4)\n\t"
    "ds_read_b128 v[158:161], v8 offset:34816\n\t"
    "v_mfma_f32_16x16x32_bf16 v[90:93], v[162:165], v[176:179], v[90:93]\n\t"
    "buffer_load_dwordx4 a[204:207], v6, s[40:43], s49 offen offset:0 sc1\n\t"
    "v_mfma_f32_16x16x32_bf16 v[94:97], v[162:165], v[184:187], v[94:97]\n\t"
    "s_waitcnt lgkmcnt(4)\n\t"
    "ds_read_b128 v[162:165], v8 offset:35840\n\t"
    "v_mfma_f32_16x16x32_bf16 v[98:101], v[142:145], v[176:179], v[98:101]\n\t"
    "v_mfma_f32_16x16x32_bf16 v[102:105], v[142:145], v[184:187], v[102:105]\n\t"
    "s_waitcnt lgkmcnt(4)\n\t"
    "ds_read_b128 v[142:145], v8 offset:36864\n\t"
    "v_mfma_f32_16x16x32_bf16 v[106:109], v[146:149], v[176:179], v[106:109]\n\t"
    "buffer_load_dwordx4 a[208:211], v5, s[40:43], s50 offen offset:0 sc1\n\t"
    "v_mfma_f32_16x16x32_bf16 v[110:113], v[146:149], v[184:187], v[110:113]\n\t"
    "s_waitcnt lgkmcnt(4)\n\t"
    "ds_read_b128 v[146:149], v8 offset:37888\n\t"
    "v_mfma_f32_16x16x32_bf16 v[114:117], v[150:153], v[176:179], v[114:117]\n\t"
    "v_mfma_f32_16x16x32_bf16 v[118:121], v[150:153], v[184:187], v[118:121]\n\t"
    "s_waitcnt lgkmcnt(4)\n\t"
    "ds_read_b128 v[150:153], v8 offset:38912\n\t"
    "v_mfma_f32_16x16x32_bf16 v[122:125], v[154:157], v[176:179], v[122:125]\n\t"
    "buffer_load_dwordx4 a[212:215], v6, s[40:43], s50 offen offset:0 sc1\n\t"
    "v_mfma_f32_16x16x32_bf16 v[126:129], v[154:157], v[184:187], v[126:129]\n\t"
    "s_waitcnt lgkmcnt(4)\n\t"
    "ds_read_b128 v[154:157], v8 offset:39936\n\t"
    "v_mfma_f32_16x16x32_bf16 v[130:133], v[158:161], v[176:179], v[130:133]\n\t"
    "v_mfma_f32_16x16x32_bf16 v[134:137], v[158:161], v[184:187], v[134:137]\n\t"
    "s_waitcnt lgkmcnt(4)\n\t"
    "ds_read_b128 v[158:161], v8 offset:40960\n\t"
    "v_mfma_f32_16x16x32_bf16 v[78:81], v[162:165], v[176:179], v[78:81]\n\t"
    "buffer_load_dwordx4 a[216:219], v5, s[40:43], s51 offen offset:0 sc1\n\t"
    "v_mfma_f32_16x16x32_bf16 v[138:141], v[162:165], v[184:187], v[138:141]\n\t"
    "s_waitcnt lgkmcnt(4)\n\t"
    "ds_read_b128 v[162:165], v8 offset:41984\n\t"
    "v_mfma_f32_16x16x32_bf16 v[70:73], v[142:145], v[176:179], v[70:73]\n\t"
    "v_mfma_f32_16x16x32_bf16 v[74:77], v[142:145], v[184:187], v[74:77]\n\t"
    "s_waitcnt lgkmcnt(4)\n\t"
    "ds_read_b128 v[142:145], v8 offset:43008\n\t"
    "v_mfma_f32_16x16x32_bf16 v[62:65], v[146:149], v[176:179], v[62:65]\n\t"
    "buffer_load_dwordx4 a[220:223], v6, s[40:43], s51 offen offset:0 sc1\n\t"
    "v_mfma_f32_16x16x32_bf16 v[66:69], v[146:149], v[184:187], v[66:69]\n\t"
    "s_waitcnt lgkmcnt(4)\n\t"
    "ds_read_b128 v[146:149], v8 offset:44032\n\t"
    "v_mfma_f32_16x16x32_bf16 v[82:85], v[150:153], v[180:183], v[82:85]\n\t"
    "s_lshr_b32 s73, s70, 11\n\t"
    "v_mfma_f32_16x16x32_bf16 v[86:89], v[150:153], v[188:191], v[86:89]\n\t"
    "s_waitcnt lgkmcnt(4)\n\t"
    "ds_read_b128 v[150:153], v8 offset:45056\n\t"
    "v_mfma_f32_16x16x32_bf16 v[90:93], v[154:157], v[180:183], v[90:93]\n\t"
    "s_mulk_i32 s73, 18432\n\t"
    "buffer_load_dwordx4 v4, s[40:43], s60 offen offset:0 sc1 lds\n\t"
    "v_mfma_f32_16x16x32_bf16 v[94:97], v[154:157], v[188:191], v[94:97]\n\t"
    "s_waitcnt lgkmcnt(4)\n\t"
    "ds_read_b128 v[154:157], v8 offset:46080\n\t"
    "v_mfma_f32_16x16x32_bf16 v[98:101], v[158:161], v[180:183], v[98:101]\n\t"
    "s_and_b32 s72, s70, 1024\n\t"
    "v_mfma_f32_16x16x32_bf16 v[102:105], v[158:161], v[188:191], v[102:105]\n\t"
    "s_waitcnt lgkmcnt(4)\n\t"
    "ds_read_b128 v[158:161], v8 offset:47104\n\t"
    "v_mfma_f32_16x16x32_bf16 v[106:109], v[162:165], v[180:183], v[106:109]\n\t"
    "s_lshl_b32 s73, s73, 1\n\t"
    "buffer_load_dwordx4 v4, s[40:43], s60 offen offset:1024 sc1 lds\n\t"
    "v_mfma_f32_16x16x32_bf16 v[110:113], v[162:165], v[188:191], v[110:113]\n\t"
    "s_waitcnt lgkmcnt(4)\n\t"
    "ds_read_b128 v[162:165], v8 offset:48128\n\t"
    "v_mfma_f32_16x16x32_bf16 v[114:117], v[142:145], v[180:183], v[114:117]\n\t"
    "s_sub_u32 s73, s73, s72\n\t"
    "v_mfma_f32_16x16x32_bf16 v[118:121], v[142:145], v[188:191], v[118:121]\n\t"
    "s_waitcnt lgkmcnt(4)\n\t"
    "ds_read_b128 v[142:145], v9 offset:16384\n\t"
    "v_mfma_f32_16x16x32_bf16 v[122:125], v[146:149], v[180:183], v[122:125]\n\t"
    "s_mulk_i32 s72, -3\n\t"
    "buffer_store_dwordx4 v[176:179], v4, s[40:43], s92 offen offset:0 sc1\n\t"
    "v_mfma_f32_16x16x32_bf16 v[126:129], v[146:149], v[188:191], v[126:129]\n\t"
    "s_waitcnt lgkmcnt(4)\n\t"
    "ds_read_b128 v[146:149], v9 offset:24576\n\t"
    "v_mfma_f32_16x16x32_bf16 v[130:133], v[150:153], v[180:183], v[130:133]\n\t"
    "s_add_u32 s73, s73, 8192\n\t"
    "buffer_load_dwordx4 v4, s[40:43], s60 offen offset:2048 sc1 lds\n\t"
    "v_mfma_f32_16x16x32_bf16 v[134:137], v[150:153], v[188:191], v[134:137]\n\t"
    "s_waitcnt lgkmcnt(4)\n\t"
    "ds_read_b128 v[150:153], v9 offset:17408\n\t"
    "v_mfma_f32_16x16x32_bf16 v[78:81], v[154:157], v[180:183], v[78:81]\n\t"
    "s_add_u32 s72, s72, 8192\n\t"
    "v_mfma_f32_16x16x32_bf16 v[138:141], v[154:157], v[188:191], v[138:141]\n\t"
    "s_waitcnt lgkmcnt(4)\n\t"
    "ds_read_b128 v[154:157], v9 offset:25600\n\t"
    "v_mfma_f32_16x16x32_bf16 v[70:73], v[158:161], v[180:183], v[70:73]\n\t"
    "buffer_load_dwordx4 v4, s[40:43], s60 offen offset:3072 sc1 lds\n\t"
    "v_mfma_f32_16x16x32_bf16 v[74:77], v[158:161], v[188:191], v[74:77]\n\t"
    "s_waitcnt lgkmcnt(4)\n\t"
    "ds_read_b128 v[158:161], v9 offset:18432\n\t"
    "v_mfma_f32_16x16x32_bf16 v[62:65], v[162:165], v[180:183], v[62:65]\n\t"
    "s_add_u32 m0, s86, 4294905856\n\t"
    "v_mfma_f32_16x16x32_bf16 v[66:69], v[162:165], v[188:191], v[66:69]\n\t"
    "s_waitcnt vmcnt(12) lgkmcnt(4)\n\t"
    "s_barrier\n\t"
    "v_mfma_f32_16x16x32_bf16 v[82:85], v[142:145], a[192:195], v[82:85]\n\t"
    "s_waitcnt vmcnt(10) lgkmcnt(3)\n\t"
    "buffer_load_dwordx4 a[224:227], v5, s[40:43], s52 offen offset:0 sc1\n\t"
    "v_mfma_f32_16x16x32_bf16 v[82:85], v[146:149], a[200:203], v[82:85]\n\t"
    "s_waitcnt vmcnt(10)\n\t"
    "ds_read_b128 v[162:165], v9 offset:26624\n\t"
    "v_mfma_f32_16x16x32_bf16 v[86:89], v[146:149], a[204:207], v[86:89]\n\t"
    "ds_read_b128 v[146:149], v9 offset:27648\n\t"
    "v_mfma_f32_16x16x32_bf16 v[86:89], v[142:145], a[196:199], v[86:89]\n\t"
    "s_waitcnt lgkmcnt(4)\n\t"
    "ds_read_b128 v[142:145], v9 offset:19456\n\t"
    "v_mfma_f32_16x16x32_bf16 v[90:93], v[150:153], a[192:195], v[90:93]\n\t"
    "s_waitcnt lgkmcnt(4)\n\t"
    "buffer_load_dwordx4 a[228:231], v6, s[40:43], s52 offen offset:0 sc1\n\t"
    "v_mfma_f32_16x16x32_bf16 v[90:93], v[154:157], a[200:203], v[90:93]\n\t"
    "s_add_u32 s60, s69, 81920\n\t"
    "v_mfma_f32_16x16x32_bf16 v[94:97], v[154:157], a[204:207], v[94:97]\n\t"
    "ds_read_b128 v[154:157], v9 offset:28672\n\t"
    "v_mfma_f32_16x16x32_bf16 v[94:97], v[150:153], a[196:199], v[94:97]\n\t"
    "s_waitcnt lgkmcnt(4)\n\t"
    "ds_read_b128 v[150:153], v9 offset:20480\n\t"
    "v_mfma_f32_16x16x32_bf16 v[98:101], v[158:161], a[192:195], v[98:101]\n\t"
    "s_waitcnt lgkmcnt(4)\n\t"
    "buffer_load_dwordx4 a[232:235], v5, s[40:43], s53 offen offset:0 sc1\n\t"
    "v_mfma_f32_16x16x32_bf16 v[98:101], v[162:165], a[200:203], v[98:101]\n\t"
    "v_mfma_f32_16x16x32_bf16 v[102:105], v[162:165], a[204:207], v[102:105]\n\t"
    "ds_read_b128 v[162:165], v9 offset:29696\n\t"
    "v_mfma_f32_16x16x32_bf16 v[102:105], v[158:161], a[196:199], v[102:105]\n\t"
    "s_waitcnt lgkmcnt(3)\n\t"
    "ds_read_b128 v[158:161], v9 offset:21504\n\t"
    "v_mfma_f32_16x16x32_bf16 v[106:109], v[142:145], a[192:195], v[106:109]\n\t"
    "buffer_load_dwordx4 a[236:239], v6, s[40:43], s53 offen offset:0 sc1\n\t"
    "v_mfma_f32_16x16x32_bf16 v[106:109], v[146:149], a[200:203], v[106:109]\n\t"
    "v_mfma_f32_16x16x32_bf16 v[110:113], v[146:149], a[204:207], v[110:113]\n\t"
    "buffer_store_dwordx4 v[184:187], v4, s[40:43], s93 offen offset:0 sc1\n\t"
    "v_mfma_f32_16x16x32_bf16 v[110:113], v[142:145], a[196:199], v[110:113]\n\t"
    "s_waitcnt lgkmcnt(2)\n\t"
    "ds_read_b128 v[142:145], v9 offset:22528\n\t"
    "v_lshrrev_b32 v50, 5, v4\n\t"
    "v_mfma_f32_16x16x32_bf16 v[114:117], v[150:153], a[192:195], v[114:117]\n\t"
    "buffer_load_dwordx4 a[0:3], v5, s[40:43], s54 offen offset:0 sc1\n\t"
    "v_mfma_f32_16x16x32_bf16 v[114:117], v[154:157], a[200:203], v[114:117]\n\t"
    "s_xor_b32 s62, s71, 0\n\t"
    "ds_read_b128 v[146:149], v9 offset:30720\n\t"
    "v_and_b32 v50, 24, v50\n\t"
    "v_mfma_f32_16x16x32_bf16 v[118:121], v[154:157], a[204:207], v[118:121]\n\t"
    "s_mov_b32 s64, 49709056\n\t"
    "ds_read_b128 v[154:157], v9 offset:31744\n\t"
    "v_add_u32 v51, 0x2c20000, v50\n\t"
    "v_mfma_f32_16x16x32_bf16 v[118:121], v[150:153], a[196:199], v[118:121]\n\t"
    "s_waitcnt lgkmcnt(3)\n\t"
    "ds_read_b128 v[150:153], v9 offset:23552\n\t"
    "v_add_u32 v52, 0x2c20800, v50\n\t"
    "v_mfma_f32_16x16x32_bf16 v[122:125], v[158:161], a[192:195], v[122:125]\n\t"
    "buffer_load_dwordx4 a[4:7], v6, s[40:43], s54 offen offset:0 sc1\n\t"
    "v_mfma_f32_16x16x32_bf16 v[122:125], v[162:165], a[200:203], v[122:125]\n\t"
    "s_lshl4_add_u32 s62, s62, s79\n\t"
    "v_bfe_u32 v50, v4, 4, 4\n\t"
    "v_add_u32 v53, s62, v50\n\t"
    "v_mfma_f32_16x16x32_bf16 v[126:129], v[162:165], a[204:207], v[126:129]\n\t"
    "s_and_b32 s56, s35, 0x7ff\n\t"
    "ds_read_b128 v[162:165], v8 offset:57344\n\t"
    "v_mov_b32 v13, 0x7ffffff0\n\t"
    "v_mfma_f32_16x16x32_bf16 v[126:129], v[158:161], a[196:199], v[126:129]\n\t"
    "s_waitcnt lgkmcnt(4)\n\t"
    "s_mov_b64 exec, 0xf07f\n\t"
    "buffer_load_dword v59, v7, s[40:43], s64 offen sc1\n\t"
    "s_mov_b64 exec, -1\n\t"
    "v_mfma_f32_16x16x32_bf16 v[130:133], v[142:145], a[192:195], v[130:133]\n\t"
    "s_waitcnt lgkmcnt(3)\n\t"
    "ds_read_b128 v[158:161], v8 offset:49152\n\t"
    "v_cmp_eq_u32 vcc, s56, v53\n\t"
    "v_mfma_f32_16x16x32_bf16 v[130:133], v[146:149], a[200:203], v[130:133]\n\t"
    "s_bfe_u32 s57, s35, 0xb000b\n\t"
    "buffer_load_dwordx4 a[8:11], v5, s[40:43], s48 offen offset:1024 sc1\n\t"
    "v_mfma_f32_16x16x32_bf16 v[134:137], v[146:149], a[204:207], v[134:137]\n\t"
    "s_xor_b32 s62, s71, 1\n\t"
    "ds_read_b128 v[146:149], v8 offset:58368\n\t"
    "v_cndmask_b32 v13, v13, v51, vcc\n\t"
    "v_mfma_f32_16x16x32_bf16 v[134:137], v[142:145], a[196:199], v[134:137]\n\t"
    "s_waitcnt lgkmcnt(3)\n\t"
    "ds_read_b128 v[142:145], v8 offset:50176\n\t"
    "v_cmp_eq_u32 vcc, s57, v53\n\t"
    "v_mfma_f32_16x16x32_bf16 v[78:81], v[150:153], a[192:195], v[78:81]\n\t"
    "s_nop 0\n\t"
    "v_cndmask_b32 v13, v13, v52, vcc\n\t"
    "v_mov_b32 v14, 0x7ffffff0\n\t"
    "v_mfma_f32_16x16x32_bf16 v[78:81], v[154:157], a[200:203], v[78:81]\n\t"
    "s_lshl4_add_u32 s62, s62, s79\n\t"
    "buffer_load_dwordx4 a[12:15], v6, s[40:43], s48 offen offset:1024 sc1\n\t"
    "v_mfma_f32_16x16x32_bf16 v[138:141], v[154:157], a[204:207], v[138:141]\n\t"
    "s_bfe_u32 s74, s87, 0x10009\n\t"
    "ds_read_b128 v[154:157], v8 offset:59392\n\t"
    "v_add_u32 v53, s62, v50\n\t"
    "v_mfma_f32_16x16x32_bf16 v[138:141], v[150:153], a[196:199], v[138:141]\n\t"
    "s_waitcnt vmcnt(18) lgkmcnt(3)\n\t"
    "ds_read_b128 v[150:153], v8 offset:51200\n\t"
    "v_cmp_eq_u32 vcc, s56, v53\n\t"
    "v_mfma_f32_16x16x32_bf16 v[82:85], v[158:161], a[208:211], v[82:85]\n\t"
    "s_waitcnt vmcnt(16)\n\t"
    "v_accvgpr_read_b32 v20, a240\n\t"
    "v_accvgpr_read_b32 v21, a241\n\t"
    "v_mfma_f32_16x16x32_bf16 v[82:85], v[162:165], a[216:219], v[82:85]\n\t"
    "s_waitcnt vmcnt(15)\n\t"
    "buffer_load_dwordx4 a[16:19], v5, s[40:43], s49 offen offset:1024 sc1\n\t"
    "v_mfma_f32_16x16x32_bf16 v[86:89], v[162:165], a[220:223], v[86:89]\n\t"
    "KFP14:\n\t"
    "s_cbranch_execz KFR1\n\t"
    "ds_read_b128 v[162:165], v8 offset:60416\n\t"
    "v_accvgpr_read_b32 v41, a250\n\t"
    "v_mfma_f32_16x16x32_bf16 v[86:89], v[158:161], a[212:215], v[86:89]\n\t"
    "s_waitcnt lgkmcnt(3)\n\t"
    "ds_read_b128 v[158:161], v8 offset:52224\n\t"
    "v_cndmask_b32 v14, v14, v51, vcc\n\t"
    "v_mfma_f32_16x16x32_bf16 v[90:93], v[142:145], a[208:211], v[90:93]\n\t"
    "v_lshlrev_b32 v40, 16, v41\n\t"
    "v_lshlrev_b32 v50, 16, v20\n\t"
    "v_mfma_f32_16x16x32_bf16 v[90:93], v[146:149], a[216:219], v[90:93]\n\t"
    "s_lshl_b32 s74, s74, 3\n\t"
    "buffer_load_dwordx4 a[20:23], v6, s[40:43], s49 offen offset:1024 sc1\n\t"
    "v_mfma_f32_16x16x32_bf16 v[94:97], v[146:149], a[220:223], v[94:97]\n\t"
    "s_lshl_b32 s74, 0xff00ff, s74\n\t"
    "buffer_store_dwordx4 v[28:31], v4, s[40:43], s94 offen offset:0 sc1\n\t"
    "v_mfma_f32_16x16x32_bf16 v[94:97], v[142:145], a[212:215], v[94:97]\n\t"
    "s_waitcnt lgkmcnt(2)\n\t"
    "ds_read_b128 v[142:145], v8 offset:53248\n\t"
    "v_and_b32 v51, 0xffff0000, v20\n\t"
    "v_mfma_f32_16x16x32_bf16 v[98:101], v[150:153], a[208:211], v[98:101]\n\t"
    "ds_read_b128 v[146:149], v8 offset:61440\n\t"
    "v_lshlrev_b32 v54, 16, v21\n\t"
    "v_mfma_f32_16x16x32_bf16 v[98:101], v[154:157], a[216:219], v[98:101]\n\t"
    "s_mul_i32 s62, s71, -64\n\t"
    "buffer_load_dwordx4 a[24:27], v5, s[40:43], s50 offen offset:1024 sc1\n\t"
    "v_mfma_f32_16x16x32_bf16 v[102:105], v[154:157], a[220:223], v[102:105]\n\t"
    "s_add_u32 s62, s62, s55\n\t"
    "ds_read_b128 v[154:157], v8 offset:62464\n\t"
    "v_and_b32 v55, 0xffff0000, v21\n\t"
    "v_mfma_f32_16x16x32_bf16 v[102:105], v[150:153], a[212:215], v[102:105]\n\t"
    "s_waitcnt lgkmcnt(3)\n\t"
    "ds_read_b128 v[150:153], v8 offset:54272\n\t"
    "v_sub_f32 v50, v50, v40\n\t"
    "v_mfma_f32_16x16x32_bf16 v[106:109], v[158:161], a[208:211], v[106:109]\n\t"
    "v_sub_f32 v51, v51, v40\n\t"
    "v_sub_f32 v54, v54, v40\n\t"
    "v_mfma_f32_16x16x32_bf16 v[106:109], v[162:165], a[216:219], v[106:109]\n\t"
    "s_add_u32 s62, s62, s91\n\t"
    "buffer_load_dwordx4 a[28:31], v6, s[40:43], s50 offen offset:1024 sc1\n\t"
    "v_mfma_f32_16x16x32_bf16 v[110:113], v[162:165], a[220:223], v[110:113]\n\t"
    "s_movk_i32 s56, 0x3f80\n\t"
    "ds_read_b128 v[162:165], v8 offset:63488\n\t"
    "v_sub_f32 v55, v55, v40\n\t"
    "v_mfma_f32_16x16x32_bf16 v[110:113], v[158:161], a[212:215], v[110:113]\n\t"
    "s_waitcnt lgkmcnt(4)\n\t"
    "ds_read_b128 v[158:161], v8 offset:55296\n\t"
    "v_cvt_pk_bf16_f32 v51, v50, v51\n\t"
    "v_mfma_f32_16x16x32_bf16 v[114:117], v[142:145], a[208:211], v[114:117]\n\t"
    "s_waitcnt lgkmcnt(4)\n\t"
    "v_cvt_pk_bf16_f32 v55, v54, v55\n\t"
    "v_and_b32 v41, 0xffff0000, v41\n\t"
    "v_mfma_f32_16x16x32_bf16 v[114:117], v[146:149], a[216:219], v[114:117]\n\t"
    "buffer_load_dwordx4 a[32:35], v5, s[40:43], s51 offen offset:1024 sc1\n\t"
    "v_mfma_f32_16x16x32_bf16 v[118:121], v[146:149], a[220:223], v[118:121]\n\t"
    "ds_read_b128 v[146:149], v8 offset:64512\n\t"
    "v_cmp_eq_u32 vcc, s57, v53\n\t"
    "v_mfma_f32_16x16x32_bf16 v[118:121], v[142:145], a[212:215], v[118:121]\n\t"
    "s_waitcnt lgkmcnt(3)\n\t"
    "ds_read_b128 v[142:145], v8 offset:56320\n\t"
    "v_lshlrev_b32 v50, 16, v51\n\t"
    "v_mfma_f32_16x16x32_bf16 v[122:125], v[150:153], a[208:211], v[122:125]\n\t"
    "v_lshlrev_b32 v54, 16, v55\n\t"
    "v_cndmask_b32 v14, v14, v52, vcc\n\t"
    "v_mfma_f32_16x16x32_bf16 v[122:125], v[154:157], a[216:219], v[122:125]\n\t"
    "s_mov_b32 vcc_lo, s74\n\t"
    "buffer_load_dwordx4 a[36:39], v6, s[40:43], s51 offen offset:1024 sc1\n\t"
    "v_mfma_f32_16x16x32_bf16 v[126:129], v[154:157], a[220:223], v[126:129]\n\t"
    "s_mov_b32 vcc_hi, s74\n\t"
    "ds_read_b128 v[154:157], v9 offset:8192\n\t"
    "v_and_b32 v51, 0xffff0000, v51\n\t"
    "v_mfma_f32_16x16x32_bf16 v[126:129], v[150:153], a[212:215], v[126:129]\n\t"
    "s_waitcnt lgkmcnt(3)\n\t"
    "ds_read_b128 v[150:153], v9 offset:0\n\t"
    "v_and_b32 v55, 0xffff0000, v55\n\t"
    "v_mfma_f32_16x16x32_bf16 v[130:133], v[158:161], a[208:211], v[130:133]\n\t"
    "v_rcp_f32 v48, v41\n\t"
    "v_mfma_f32_16x16x32_bf16 v[130:133], v[162:165], a[216:219], v[130:133]\n\t"
    "s_mov_b32 s57, 0x3f800000\n\t"
    "buffer_load_dwordx4 a[40:43], v5, s[40:43], s52 offen offset:1024 sc1\n\t"
    "v_mfma_f32_16x16x32_bf16 v[134:137], v[162:165], a[220:223], v[134:137]\n\t"
    "ds_read_b128 v[162:165], v9 offset:9216\n\t"
    "v_mul_f32 v52, v50, v48\n\t"
    "v_mfma_f32_16x16x32_bf16 v[134:137], v[158:161], a[212:215], v[134:137]\n\t"
    "s_waitcnt lgkmcnt(3)\n\t"
    "ds_read_b128 v[158:161], v9 offset:1024\n\t"
    "v_mul_f32 v53, v51, v48\n\t"
    "v_mfma_f32_16x16x32_bf16 v[78:81], v[142:145], a[208:211], v[78:81]\n\t"
    "v_mul_f32 v56, v54, v48\n\t"
    "v_mul_f32 v57, v55, v48\n\t"
    "v_mfma_f32_16x16x32_bf16 v[78:81], v[146:149], a[216:219], v[78:81]\n\t"
    "s_bfe_u32 s65, s86, 0x2000c\n\t"
    "s_mov_b64 exec, 0\n\t"
    "s_cmp_eq_u32 s65, 0\n\t"
    "s_cbranch_scc1 KFP15\n\t"
    "s_cmp_eq_u32 s65, 1\n\t"
    "s_cbranch_scc1 KFP16\n\t"
    "s_cmp_eq_u32 s65, 2\n\t"
    "s_cbranch_scc1 KFP17\n\t"
    "s_cmp_eq_u32 s65, 3\n\t"
    "s_cbranch_scc1 KFP18\n\t"
    "KFR2:\n\t"
    "s_mov_b64 exec, -1\n\t"
    "v_mfma_f32_16x16x32_bf16 v[138:141], v[146:149], a[220:223], v[138:141]\n\t"
    "buffer_store_dwordx4 v[36:39], v4, s[40:43], s95 offen offset:0 sc1\n\t"
    "v_mfma_f32_16x16x32_bf16 v[138:141], v[142:145], a[212:215], v[138:141]\n\t"
    "s_waitcnt vmcnt(18) lgkmcnt(2)\n\t"
    "s_barrier\n\t"
    "v_mfma_f32_16x16x32_bf16 v[82:85], v[150:153], a[224:227], v[82:85]\n\t"
    "s_waitcnt vmcnt(16)\n\t"
    "ds_read_b128 v[142:145], v9 offset:2048\n\t"
    "v_fma_f32 v50, -v52, v41, v50\n\t"
    "v_mfma_f32_16x16x32_bf16 v[82:85], v[154:157], a[232:235], v[82:85]\n\t"
    "s_waitcnt vmcnt(15)\n\t"
    "ds_read_b128 v[146:149], v9 offset:10240\n\t"
    "v_fma_f32 v51, -v53, v41, v51\n\t"
    "v_mfma_f32_16x16x32_bf16 v[86:89], v[154:157], a[236:239], v[86:89]\n\t"
    "buffer_load_dwordx4 a[44:47], v6, s[40:43], s52 offen offset:1024 sc1\n\t"
    "v_mfma_f32_16x16x32_bf16 v[86:89], v[150:153], a[228:231], v[86:89]\n\t"
    "s_waitcnt lgkmcnt(2)\n\t"
    "ds_read_b128 v[150:153], v9 offset:3072\n\t"
    "v_fma_f32 v54, -v56, v41, v54\n\t"
    "v_mfma_f32_16x16x32_bf16 v[90:93], v[158:161], a[224:227], v[90:93]\n\t"
    "ds_read_b128 v[154:157], v9 offset:11264\n\t"
    "v_fma_f32 v55, -v57, v41, v55\n\t"
    "v_mfma_f32_16x16x32_bf16 v[90:93], v[162:165], a[232:235], v[90:93]\n\t"
    "v_fma_f32 v50, v50, v48, v52\n\t"
    "v_fma_f32 v51, v51, v48, v53\n\t"
    "v_mfma_f32_16x16x32_bf16 v[94:97], v[162:165], a[236:239], v[94:97]\n\t"
    "buffer_load_dwordx4 a[48:51], v5, s[40:43], s53 offen offset:1024 sc1\n\t"
    "v_mfma_f32_16x16x32_bf16 v[94:97], v[158:161], a[228:231], v[94:97]\n\t"
    "s_waitcnt lgkmcnt(3)\n\t"
    "ds_read_b128 v[158:161], v9 offset:4096\n\t"
    "v_fma_f32 v54, v54, v48, v56\n\t"
    "v_mfma_f32_16x16x32_bf16 v[98:101], v[142:145], a[224:227], v[98:101]\n\t"
    "s_waitcnt lgkmcnt(3)\n\t"
    "ds_read_b128 v[162:165], v9 offset:12288\n\t"
    "v_fma_f32 v55, v55, v48, v57\n\t"
    "v_mfma_f32_16x16x32_bf16 v[98:101], v[146:149], a[232:235], v[98:101]\n\t"
    "v_accvgpr_read_b32 v22, a244\n\t"
    "v_accvgpr_read_b32 v23, a245\n\t"
    "v_mfma_f32_16x16x32_bf16 v[102:105], v[146:149], a[236:239], v[102:105]\n\t"
    "buffer_load_dwordx4 a[52:55], v6, s[40:43], s53 offen offset:1024 sc1\n\t"
    "v_mfma_f32_16x16x32_bf16 v[102:105], v[142:145], a[228:231], v[102:105]\n\t"
    "s_waitcnt lgkmcnt(3)\n\t"
    "ds_read_b128 v[142:145], v9 offset:5120\n\t"
    "v_accvgpr_read_b32 v43, a251\n\t"
    "v_mfma_f32_16x16x32_bf16 v[106:109], v[150:153], a[224:227], v[106:109]\n\t"
    "s_waitcnt lgkmcnt(3)\n\t"
    "ds_read_b128 v[146:149], v9 offset:13312\n\t"
    "v_cvt_pk_bf16_f32 v30, v50, v51\n\t"
    "v_mfma_f32_16x16x32_bf16 v[106:109], v[154:157], a[232:235], v[106:109]\n\t"
    "v_cvt_pk_bf16_f32 v31, v54, v55\n\t"
    "v_lshlrev_b32 v42, 16, v43\n\t"
    "v_mfma_f32_16x16x32_bf16 v[110:113], v[154:157], a[236:239], v[110:113]\n\t"
    "buffer_load_dwordx4 a[56:59], v5, s[40:43], s54 offen offset:1024 sc1\n\t"
    "v_mfma_f32_16x16x32_bf16 v[110:113], v[150:153], a[228:231], v[110:113]\n\t"
    "s_waitcnt lgkmcnt(3)\n\t"
    "ds_read_b128 v[150:153], v9 offset:6144\n\t"
    "v_lshlrev_b32 v50, 16, v22\n\t"
    "v_mfma_f32_16x16x32_bf16 v[114:117], v[158:161], a[224:227], v[114:117]\n\t"
    "s_waitcnt lgkmcnt(3)\n\t"
    "ds_read_b128 v[154:157], v9 offset:14336\n\t"
    "v_and_b32 v51, 0xffff0000, v22\n\t"
    "v_mfma_f32_16x16x32_bf16 v[114:117], v[162:165], a[232:235], v[114:117]\n\t"
    "v_lshlrev_b32 v54, 16, v23\n\t"
    "v_and_b32 v55, 0xffff0000, v23\n\t"
    "v_mfma_f32_16x16x32_bf16 v[118:121], v[162:165], a[236:239], v[118:121]\n\t"
    "buffer_load_dwordx4 a[60:63], v6, s[40:43], s54 offen offset:1024 sc1\n\t"
    "v_mfma_f32_16x16x32_bf16 v[118:121], v[158:161], a[228:231], v[118:121]\n\t"
    "s_waitcnt lgkmcnt(3)\n\t"
    "ds_read_b128 v[158:161], v9 offset:7168\n\t"
    "v_sub_f32 v50, v50, v42\n\t"
    "v_mfma_f32_16x16x32_bf16 v[122:125], v[142:145], a[224:227], v[122:125]\n\t"
    "s_waitcnt lgkmcnt(3)\n\t"
    "ds_read_b128 v[162:165], v9 offset:15360\n\t"
    "v_sub_f32 v51, v51, v42\n\t"
    "v_mfma_f32_16x16x32_bf16 v[122:125], v[146:149], a[232:235], v[122:125]\n\t"
    "v_sub_f32 v54, v54, v42\n\t"
    "v_sub_f32 v55, v55, v42\n\t"
    "v_mfma_f32_16x16x32_bf16 v[126:129], v[146:149], a[236:239], v[126:129]\n\t"
    "buffer_load_dwordx4 v4, s[40:43], s60 offen offset:0 sc1 lds\n\t"
    "v_mfma_f32_16x16x32_bf16 v[126:129], v[142:145], a[228:231], v[126:129]\n\t"
    "s_waitcnt lgkmcnt(3)\n\t"
    "ds_read_b128 v[142:145], v4 offset:0\n\t"
    "v_cvt_pk_bf16_f32 v51, v50, v51\n\t"
    "v_mfma_f32_16x16x32_bf16 v[130:133], v[150:153], a[224:227], v[130:133]\n\t"
    "s_waitcnt lgkmcnt(3)\n\t"
    "ds_read_b128 v[146:149], v4 offset:1024\n\t"
    "v_cvt_pk_bf16_f32 v55, v54, v55\n\t"
    "v_mfma_f32_16x16x32_bf16 v[130:133], v[154:157], a[232:235], v[130:133]\n\t"
    "v_and_b32 v43, 0xffff0000, v43\n\t"
    "v_lshlrev_b32 v50, 16, v51\n\t"
    "v_mfma_f32_16x16x32_bf16 v[134:137], v[154:157], a[236:239], v[134:137]\n\t"
    "buffer_store_dwordx4 v[180:183], v4, s[40:43], s92 offen offset:1024 sc1\n\t"
    "v_mfma_f32_16x16x32_bf16 v[134:137], v[150:153], a[228:231], v[134:137]\n\t"
    "s_waitcnt lgkmcnt(3)\n\t"
    "ds_read_b128 v[150:153], v4 offset:2048\n\t"
    "v_lshlrev_b32 v54, 16, v55\n\t"
    "v_mfma_f32_16x16x32_bf16 v[78:81], v[158:161], a[224:227], v[78:81]\n\t"
    "s_waitcnt lgkmcnt(3)\n\t"
    "ds_read_b128 v[154:157], v4 offset:3072\n\t"
    "v_and_b32 v51, 0xffff0000, v51\n\t"
    "v_mfma_f32_16x16x32_bf16 v[78:81], v[162:165], a[232:235], v[78:81]\n\t"
    "v_rcp_f32 v49, v43\n\t"
    "v_mfma_f32_16x16x32_bf16 v[138:141], v[162:165], a[236:239], v[138:141]\n\t"
    "buffer_load_dwordx4 v4, s[40:43], s60 offen offset:1024 sc1 lds\n\t"
    "v_mfma_f32_16x16x32_bf16 v[138:141], v[158:161], a[228:231], v[138:141]\n\t"
    "s_waitcnt vmcnt(21) lgkmcnt(3)\n\t"
    "ds_read_b128 v[158:161], v4 offset:4096\n\t"
    "v_and_b32 v55, 0xffff0000, v55\n\t"
    "v_mfma_f32_16x16x32_bf16 v[82:85], v[142:145], a[0:3], v[82:85]\n\t"
    "s_waitcnt vmcnt(20)\n\t"
    "ds_read_b128 v[162:165], v4 offset:5120\n\t"
    "v_mul_f32 v52, v50, v49\n\t"
    "v_mfma_f32_16x16x32_bf16 v[86:89], v[142:145], a[4:7], v[86:89]\n\t"
    "s_waitcnt lgkmcnt(4)\n\t"
    "ds_read_b128 v[142:145], v4 offset:6144\n\t"
    "v_mul_f32 v53, v51, v49\n\t"
    "v_mfma_f32_16x16x32_bf16 v[90:93], v[146:149], a[0:3], v[90:93]\n\t"
    "buffer_load_dwordx4 v4, s[40:43], s60 offen offset:2048 sc1 lds\n\t"
    "v_mfma_f32_16x16x32_bf16 v[94:97], v[146:149], a[4:7], v[94:97]\n\t"
    "s_waitcnt lgkmcnt(4)\n\t"
    "ds_read_b128 v[146:149], v4 offset:7168\n\t"
    "v_mul_f32 v56, v54, v49\n\t"
    "v_mfma_f32_16x16x32_bf16 v[98:101], v[150:153], a[0:3], v[98:101]\n\t"
    "v_mul_f32 v57, v55, v49\n\t"
    "v_fma_f32 v50, -v52, v43, v50\n\t"
    "v_mfma_f32_16x16x32_bf16 v[102:105], v[150:153], a[4:7], v[102:105]\n\t"
    "s_waitcnt lgkmcnt(4)\n\t"
    "ds_read_b128 v[150:153], v4 offset:8192\n\t"
    "v_fma_f32 v51, -v53, v43, v51\n\t"
    "v_mfma_f32_16x16x32_bf16 v[106:109], v[154:157], a[0:3], v[106:109]\n\t"
    "buffer_load_dwordx4 v4, s[40:43], s60 offen offset:3072 sc1 lds\n\t"
    "v_mfma_f32_16x16x32_bf16 v[110:113], v[154:157], a[4:7], v[110:113]\n\t"
    "s_waitcnt lgkmcnt(4)\n\t"
    "ds_read_b128 v[154:157], v4 offset:16384\n\t"
    "v_fma_f32 v54, -v56, v43, v54\n\t"
    "v_mfma_f32_16x16x32_bf16 v[114:117], v[158:161], a[0:3], v[114:117]\n\t"
    "s_add_u32 m0, s86, 0\n\t"
    "v_fma_f32 v55, -v57, v43, v55\n\t"
    "v_fma_f32 v50, v50, v49, v52\n\t"
    "v_mfma_f32_16x16x32_bf16 v[118:121], v[158:161], a[4:7], v[118:121]\n\t"
    "s_waitcnt lgkmcnt(4)\n\t"
    "ds_read_b128 v[158:161], v4 offset:9216\n\t"
    "v_fma_f32 v51, v51, v49, v53\n\t"
    "v_mfma_f32_16x16x32_bf16 v[122:125], v[162:165], a[0:3], v[122:125]\n\t"
    "s_add_u32 s60, s97, 81920\n\t"
    "buffer_load_dwordx4 v4, s[40:43], s60 offen offset:0 sc1 lds\n\t"
    "v_mfma_f32_16x16x32_bf16 v[126:129], v[162:165], a[4:7], v[126:129]\n\t"
    "s_waitcnt lgkmcnt(4)\n\t"
    "ds_read_b128 v[162:165], v4 offset:17408\n\t"
    "v_fma_f32 v54, v54, v49, v56\n\t"
    "v_mfma_f32_16x16x32_bf16 v[130:133], v[142:145], a[0:3], v[130:133]\n\t"
    "v_fma_f32 v55, v55, v49, v57\n\t"
    "v_accvgpr_read_b32 v24, a242\n\t"
    "v_mfma_f32_16x16x32_bf16 v[134:137], v[142:145], a[4:7], v[134:137]\n\t"
    "s_waitcnt lgkmcnt(4)\n\t"
    "ds_read_b128 v[142:145], v4 offset:10240\n\t"
    "v_accvgpr_read_b32 v25, a243\n\t"
    "v_mfma_f32_16x16x32_bf16 v[78:81], v[146:149], a[0:3], v[78:81]\n\t"
    "buffer_load_dwordx4 v4, s[40:43], s60 offen offset:1024 sc1 lds\n\t"
    "v_mfma_f32_16x16x32_bf16 v[138:141], v[146:149], a[4:7], v[138:141]\n\t"
    "s_waitcnt lgkmcnt(4)\n\t"
    "s_waitcnt vmcnt(12)\n\t"
    "s_barrier\n\t"
    "v_mfma_f32_16x16x32_bf16 v[82:85], v[150:153], a[8:11], v[82:85]\n\t"
    "s_waitcnt lgkmcnt(3)\n\t"
    "buffer_store_dword v60, v2, s[40:43], s84 offen sc1\n\t"
    "v_mfma_f32_16x16x32_bf16 v[82:85], v[154:157], a[16:19], v[82:85]\n\t"
    "ds_read_b128 v[146:149], v4 offset:18432\n\t"
    "v_cvt_pk_bf16_f32 v32, v50, v51\n\t"
    "v_mfma_f32_16x16x32_bf16 v[86:89], v[154:157], a[20:23], v[86:89]\n\t"
    "buffer_store_dwordx4 v[188:191], v4, s[40:43], s93 offen offset:1024 sc1\n\t"
    "v_mfma_f32_16x16x32_bf16 v[86:89], v[150:153], a[12:15], v[86:89]\n\t"
    "s_waitcnt lgkmcnt(3)\n\t"
    "ds_read_b128 v[150:153], v4 offset:11264\n\t"
    "v_cvt_pk_bf16_f32 v33, v54, v55\n\t"
    "v_mfma_f32_16x16x32_bf16 v[90:93], v[158:161], a[8:11], v[90:93]\n\t"
    "s_waitcnt lgkmcnt(3)\n\t"
    "ds_read_b128 v[154:157], v4 offset:19456\n\t"
    "v_lshlrev_b32 v50, 16, v24\n\t"
    "v_mfma_f32_16x16x32_bf16 v[90:93], v[162:165], a[16:19], v[90:93]\n\t"
    "s_add_u32 s84, s84, 32768\n\t"
    "buffer_load_dwordx4 v4, s[40:43], s60 offen offset:2048 sc1 lds\n\t"
    "v_mfma_f32_16x16x32_bf16 v[94:97], v[162:165], a[20:23], v[94:97]\n\t"
    "ds_read_b128 v[162:165], v4 offset:20480\n\t"
    "v_and_b32 v51, 0xffff0000, v24\n\t"
    "v_mfma_f32_16x16x32_bf16 v[94:97], v[158:161], a[12:15], v[94:97]\n\t"
    "s_waitcnt lgkmcnt(4)\n\t"
    "ds_read_b128 v[158:161], v4 offset:12288\n\t"
    "v_lshlrev_b32 v54, 16, v25\n\t"
    "v_mfma_f32_16x16x32_bf16 v[98:101], v[142:145], a[8:11], v[98:101]\n\t"
    "s_waitcnt lgkmcnt(4)\n\t"
    "v_and_b32 v55, 0xffff0000, v25\n\t"
    "v_sub_f32 v50, v50, v40\n\t"
    "v_mfma_f32_16x16x32_bf16 v[98:101], v[146:149], a[16:19], v[98:101]\n\t"
    "buffer_load_dwordx4 v4, s[40:43], s60 offen offset:3072 sc1 lds\n\t"
    "v_mfma_f32_16x16x32_bf16 v[102:105], v[146:149], a[20:23], v[102:105]\n\t"
    "s_lshl1_add_u32 m0, s70, 102400\n\t"
    "ds_read_b128 v[146:149], v4 offset:21504\n\t"
    "v_sub_f32 v51, v51, v40\n\t"
    "v_mfma_f32_16x16x32_bf16 v[102:105], v[142:145], a[12:15], v[102:105]\n\t"
    "s_waitcnt lgkmcnt(4)\n\t"
    "ds_read_b128 v[142:145], v4 offset:13312\n\t"
    "v_sub_f32 v54, v54, v40\n\t"
    "v_mfma_f32_16x16x32_bf16 v[106:109], v[150:153], a[8:11], v[106:109]\n\t"
    "s_waitcnt lgkmcnt(4)\n\t"
    "v_sub_f32 v55, v55, v40\n\t"
    "v_cvt_pk_bf16_f32 v51, v50, v51\n\t"
    "v_mfma_f32_16x16x32_bf16 v[106:109], v[154:157], a[16:19], v[106:109]\n\t"
    "s_add_u32 s60, s98, 81920\n\t"
    "buffer_load_dwordx4 v4, s[40:43], s60 offen offset:0 sc1 lds\n\t"
    "v_mfma_f32_16x16x32_bf16 v[110:113], v[154:157], a[20:23], v[110:113]\n\t"
    "ds_read_b128 v[154:157], v4 offset:22528\n\t"
    "v_cvt_pk_bf16_f32 v55, v54, v55\n\t"
    "v_mfma_f32_16x16x32_bf16 v[110:113], v[150:153], a[12:15], v[110:113]\n\t"
    "s_waitcnt lgkmcnt(3)\n\t"
    "ds_read_b128 v[150:153], v4 offset:14336\n\t"
    "v_lshlrev_b32 v50, 16, v51\n\t"
    "v_mfma_f32_16x16x32_bf16 v[114:117], v[158:161], a[8:11], v[114:117]\n\t"
    "v_lshlrev_b32 v54, 16, v55\n\t"
    "v_and_b32 v51, 0xffff0000, v51\n\t"
    "v_mfma_f32_16x16x32_bf16 v[114:117], v[162:165], a[16:19], v[114:117]\n\t"
    "buffer_load_dwordx4 v4, s[40:43], s60 offen offset:1024 sc1 lds\n\t"
    "v_mfma_f32_16x16x32_bf16 v[118:121], v[162:165], a[20:23], v[118:121]\n\t"
    "s_add_u32 m0, s86, 4294930432\n\t"
    "ds_read_b128 v[162:165], v4 offset:23552\n\t"
    "v_and_b32 v55, 0xffff0000, v55\n\t"
    "v_mfma_f32_16x16x32_bf16 v[118:121], v[158:161], a[12:15], v[118:121]\n\t"
    "s_waitcnt lgkmcnt(3)\n\t"
    "ds_read_b128 v[158:161], v4 offset:15360\n\t"
    "v_mul_f32 v52, v50, v48\n\t"
    "v_mfma_f32_16x16x32_bf16 v[122:125], v[142:145], a[8:11], v[122:125]\n\t"
    "v_mul_f32 v53, v51, v48\n\t"
    "v_mul_f32 v56, v54, v48\n\t"
    "v_mfma_f32_16x16x32_bf16 v[122:125], v[146:149], a[16:19], v[122:125]\n\t"
    "s_add_u32 s60, s68, 122880\n\t"
    "buffer_load_dwordx4 v4, s[40:43], s60 offen offset:0 sc1 lds\n\t"
    "v_mfma_f32_16x16x32_bf16 v[126:129], v[146:149], a[20:23], v[126:129]\n\t"
    "ds_read_b128 v[146:149], v4 offset:53248\n\t"
    "v_mul_f32 v57, v55, v48\n\t"
    "v_mfma_f32_16x16x32_bf16 v[126:129], v[142:145], a[12:15], v[126:129]\n\t"
    "s_waitcnt lgkmcnt(3)\n\t"
    "ds_read_b128 v[142:145], v4 offset:45056\n\t"
    "v_fma_f32 v50, -v52, v41, v50\n\t"
    "v_mfma_f32_16x16x32_bf16 v[130:133], v[150:153], a[8:11], v[130:133]\n\t"
    "v_fma_f32 v51, -v53, v41, v51\n\t"
    "v_fma_f32 v54, -v56, v41, v54\n\t"
    "v_mfma_f32_16x16x32_bf16 v[130:133], v[154:157], a[16:19], v[130:133]\n\t"
    "buffer_load_dwordx4 v4, s[40:43], s60 offen offset:1024 sc1 lds\n\t"
    "v_mfma_f32_16x16x32_bf16 v[134:137], v[154:157], a[20:23], v[134:137]\n\t"
    "buffer_store_dwordx4 v[244:247], v4, s[40:43], s94 offen offset:1024 sc1\n\t"
    "v_mfma_f32_16x16x32_bf16 v[134:137], v[150:153], a[12:15], v[134:137]\n\t"
    "s_waitcnt lgkmcnt(2)\n\t"
    "ds_read_b128 v[150:153], v4 offset:46080\n\t"
    "v_fma_f32 v55, -v57, v41, v55\n\t"
    "v_mfma_f32_16x16x32_bf16 v[78:81], v[158:161], a[8:11], v[78:81]\n\t"
    "ds_read_b128 v[154:157], v4 offset:54272\n\t"
    "v_fma_f32 v50, v50, v48, v52\n\t"
    "v_mfma_f32_16x16x32_bf16 v[78:81], v[162:165], a[16:19], v[78:81]\n\t"
    "buffer_load_dwordx4 v4, s[40:43], s60 offen offset:2048 sc1 lds\n\t"
    "v_mfma_f32_16x16x32_bf16 v[138:141], v[162:165], a[20:23], v[138:141]\n\t"
    "ds_read_b128 v[162:165], v4 offset:55296\n\t"
    "v_fma_f32 v51, v51, v48, v53\n\t"
    "v_mfma_f32_16x16x32_bf16 v[138:141], v[158:161], a[12:15], v[138:141]\n\t"
    "s_waitcnt lgkmcnt(3)\n\t"
    "ds_read_b128 v[158:161], v4 offset:47104\n\t"
    "v_fma_f32 v54, v54, v48, v56\n\t"
    "v_mfma_f32_16x16x32_bf16 v[82:85], v[142:145], a[24:27], v[82:85]\n\t"
    "v_fma_f32 v55, v55, v48, v57\n\t"
    "v_cndmask_b32_dpp v40, v40, v40, vcc row_ror:8 row_mask:0xf bank_mask:0xf\n\t"
    "v_mfma_f32_16x16x32_bf16 v[82:85], v[146:149], a[32:35], v[82:85]\n\t"
    "buffer_load_dwordx4 v4, s[40:43], s60 offen offset:3072 sc1 lds\n\t"
    "v_mfma_f32_16x16x32_bf16 v[86:89], v[146:149], a[36:39], v[86:89]\n\t"
    "ds_read_b128 v[146:149], v4 offset:56320\n\t"
    "v_cndmask_b32_dpp v41, v41, v41, vcc row_ror:8 row_mask:0xf bank_mask:0xf\n\t"
    "v_mfma_f32_16x16x32_bf16 v[86:89], v[142:145], a[28:31], v[86:89]\n\t"
    "s_waitcnt lgkmcnt(4)\n\t"
    "ds_read_b128 v[142:145], v4 offset:48128\n\t"
    "v_cndmask_b32_dpp v48, v48, v48, vcc row_ror:8 row_mask:0xf bank_mask:0xf\n\t"
    "v_mfma_f32_16x16x32_bf16 v[90:93], v[150:153], a[24:27], v[90:93]\n\t"
    "s_waitcnt lgkmcnt(4)\n\t"
    "v_cmp_ne_u32 vcc, s80, v59\n\t"
    "s_and_b32 s65, vcc_lo, 0xf07f\n\t"
    "s_cbranch_scc1 LS2_%=\n\t"
    "LR2_%=:\n\t"
    "v_mfma_f32_16x16x32_bf16 v[90:93], v[154:157], a[32:35], v[90:93]\n\t"
    "s_add_u32 m0, s86, 4294946816\n\t"
    "buffer_load_dwordx4 a[64:67], v5, s[40:43], s48 offen offset:2048 sc1\n\t"
    "v_mfma_f32_16x16x32_bf16 v[94:97], v[154:157], a[36:39], v[94:97]\n\t"
    "s_add_u32 s60, s69, 122880\n\t"
    "ds_read_b128 v[154:157], v4 offset:57344\n\t"
    "v_accvgpr_read_b32 v26, a246\n\t"
    "v_mfma_f32_16x16x32_bf16 v[94:97], v[150:153], a[28:31], v[94:97]\n\t"
    "s_waitcnt lgkmcnt(3)\n\t"
    "ds_read_b128 v[150:153], v4 offset:49152\n\t"
    "v_accvgpr_read_b32 v27, a247\n\t"
    "v_mfma_f32_16x16x32_bf16 v[98:101], v[158:161], a[24:27], v[98:101]\n\t"
    "v_accvgpr_read_b32 v28, a248\n\t"
    "v_accvgpr_read_b32 v29, a249\n\t"
    "v_mfma_f32_16x16x32_bf16 v[98:101], v[162:165], a[32:35], v[98:101]\n\t"
    "s_mov_b32 vcc_lo, s74\n\t"
    "buffer_load_dwordx4 a[68:71], v6, s[40:43], s48 offen offset:2048 sc1\n\t"
    "v_mfma_f32_16x16x32_bf16 v[102:105], v[162:165], a[36:39], v[102:105]\n\t"
    "s_mov_b32 vcc_hi, s74\n\t"
    "ds_read_b128 v[162:165], v4 offset:58368\n\t"
    "v_cvt_pk_bf16_f32 v34, v50, v51\n\t"
    "v_mfma_f32_16x16x32_bf16 v[102:105], v[158:161], a[28:31], v[102:105]\n\t"
    "s_waitcnt lgkmcnt(3)\n\t"
    "ds_read_b128 v[158:161], v4 offset:50176\n\t"
    "v_cvt_pk_bf16_f32 v35, v54, v55\n\t"
    "v_mfma_f32_16x16x32_bf16 v[106:109], v[142:145], a[24:27], v[106:109]\n\t"
    "v_lshlrev_b32 v50, 16, v26\n\t"
    "v_and_b32 v51, 0xffff0000, v26\n\t"
    "v_mfma_f32_16x16x32_bf16 v[106:109], v[146:149], a[32:35], v[106:109]\n\t"
    "buffer_load_dwordx4 a[72:75], v5, s[40:43], s49 offen offset:2048 sc1\n\t"
    "v_mfma_f32_16x16x32_bf16 v[110:113], v[146:149], a[36:39], v[110:113]\n\t"
    "ds_read_b128 v[146:149], v4 offset:59392\n\t"
    "v_lshlrev_b32 v54, 16, v27\n\t"
    "v_mfma_f32_16x16x32_bf16 v[110:113], v[142:145], a[28:31], v[110:113]\n\t"
    "s_waitcnt lgkmcnt(3)\n\t"
    "ds_read_b128 v[142:145], v4 offset:51200\n\t"
    "v_and_b32 v55, 0xffff0000, v27\n\t"
    "v_mfma_f32_16x16x32_bf16 v[114:117], v[150:153], a[24:27], v[114:117]\n\t"
    "v_sub_f32 v50, v50, v42\n\t"
    "v_sub_f32 v51, v51, v42\n\t"
    "v_mfma_f32_16x16x32_bf16 v[114:117], v[154:157], a[32:35], v[114:117]\n\t"
    "buffer_load_dwordx4 a[76:79], v6, s[40:43], s49 offen offset:2048 sc1\n\t"
    "v_mfma_f32_16x16x32_bf16 v[118:121], v[154:157], a[36:39], v[118:121]\n\t"
    "buffer_store_dwordx4 v[252:255], v4, s[40:43], s95 offen offset:1024 sc1\n\t"
    "v_mfma_f32_16x16x32_bf16 v[118:121], v[150:153], a[28:31], v[118:121]\n\t"
    "s_waitcnt lgkmcnt(2)\n\t"
    "ds_read_b128 v[150:153], v4 offset:52224\n\t"
    "v_sub_f32 v54, v54, v42\n\t"
    "v_mfma_f32_16x16x32_bf16 v[122:125], v[158:161], a[24:27], v[122:125]\n\t"
    "ds_read_b128 v[154:157], v4 offset:60416\n\t"
    "v_sub_f32 v55, v55, v42\n\t"
    "v_mfma_f32_16x16x32_bf16 v[122:125], v[162:165], a[32:35], v[122:125]\n\t"
    "buffer_load_dwordx4 a[80:83], v5, s[40:43], s50 offen offset:2048 sc1\n\t"
    "v_mfma_f32_16x16x32_bf16 v[126:129], v[162:165], a[36:39], v[126:129]\n\t"
    "ds_read_b128 v[162:165], v4 offset:32768\n\t"
    "v_cvt_pk_bf16_f32 v51, v50, v51\n\t"
    "v_mfma_f32_16x16x32_bf16 v[126:129], v[158:161], a[28:31], v[126:129]\n\t"
    "s_waitcnt lgkmcnt(3)\n\t"
    "ds_read_b128 v[158:161], v4 offset:24576\n\t"
    "v_cvt_pk_bf16_f32 v55, v54, v55\n\t"
    "v_mfma_f32_16x16x32_bf16 v[130:133], v[142:145], a[24:27], v[130:133]\n\t"
    "v_lshlrev_b32 v50, 16, v51\n\t"
    "v_lshlrev_b32 v54, 16, v55\n\t"
    "v_mfma_f32_16x16x32_bf16 v[130:133], v[146:149], a[32:35], v[130:133]\n\t"
    "buffer_load_dwordx4 a[84:87], v6, s[40:43], s50 offen offset:2048 sc1\n\t"
    "v_mfma_f32_16x16x32_bf16 v[134:137], v[146:149], a[36:39], v[134:137]\n\t"
    "ds_read_b128 v[146:149], v4 offset:33792\n\t"
    "v_and_b32 v51, 0xffff0000, v51\n\t"
    "v_mfma_f32_16x16x32_bf16 v[134:137], v[142:145], a[28:31], v[134:137]\n\t"
    "s_waitcnt lgkmcnt(4)\n\t"
    "ds_read_b128 v[142:145], v4 offset:25600\n\t"
    "v_and_b32 v55, 0xffff0000, v55\n\t"
    "v_mfma_f32_16x16x32_bf16 v[78:81], v[150:153], a[24:27], v[78:81]\n\t"
    "s_waitcnt lgkmcnt(4)\n\t"
    "v_mul_f32 v52, v50, v49\n\t"
    "v_mul_f32 v53, v51, v49\n\t"
    "v_mfma_f32_16x16x32_bf16 v[78:81], v[154:157], a[32:35], v[78:81]\n\t"
    "buffer_load_dwordx4 a[88:91], v5, s[40:43], s51 offen offset:2048 sc1\n\t"
    "v_mfma_f32_16x16x32_bf16 v[138:141], v[154:157], a[36:39], v[138:141]\n\t"
    "ds_read_b128 v[154:157], v4 offset:34816\n\t"
    "v_mul_f32 v56, v54, v49\n\t"
    "v_mfma_f32_16x16x32_bf16 v[138:141], v[150:153], a[28:31], v[138:141]\n\t"
    "s_waitcnt lgkmcnt(3)\n\t"
    "s_barrier\n\t"
    "v_mfma_f32_16x16x32_bf16 v[82:85], v[158:161], a[40:43], v[82:85]\n\t"
    "s_waitcnt vmcnt(29)\n\t"
    "ds_read_b128 v[150:153], v4 offset:26624\n\t"
    "v_mul_f32 v57, v55, v49\n\t"
    "v_mfma_f32_16x16x32_bf16 v[82:85], v[162:165], a[48:51], v[82:85]\n\t"
    "s_waitcnt vmcnt(28)\n\t"
    "buffer_load_dwordx4 a[92:95], v6, s[40:43], s51 offen offset:2048 sc1\n\t"
    "v_mfma_f32_16x16x32_bf16 v[86:89], v[162:165], a[52:55], v[86:89]\n\t"
    "ds_read_b128 v[162:165], v4 offset:35840\n\t"
    "v_fma_f32 v50, -v52, v43, v50\n\t"
    "v_mfma_f32_16x16x32_bf16 v[86:89], v[158:161], a[44:47], v[86:89]\n\t"
    "s_waitcnt lgkmcnt(3)\n\t"
    "ds_read_b128 v[158:161], v4 offset:27648\n\t"
    "v_fma_f32 v51, -v53, v43, v51\n\t"
    "v_mfma_f32_16x16x32_bf16 v[90:93], v[142:145], a[40:43], v[90:93]\n\t"
    "v_fma_f32 v54, -v56, v43, v54\n\t"
    "v_fma_f32 v55, -v57, v43, v55\n\t"
    "v_mfma_f32_16x16x32_bf16 v[90:93], v[146:149], a[48:51], v[90:93]\n\t"
    "buffer_load_dwordx4 a[96:99], v5, s[40:43], s52 offen offset:2048 sc1\n\t"
    "v_mfma_f32_16x16x32_bf16 v[94:97], v[146:149], a[52:55], v[94:97]\n\t"
    "ds_read_b128 v[146:149], v4 offset:36864\n\t"
    "v_fma_f32 v50, v50, v49, v52\n\t"
    "v_mfma_f32_16x16x32_bf16 v[94:97], v[142:145], a[44:47], v[94:97]\n\t"
    "s_waitcnt lgkmcnt(3)\n\t"
    "ds_read_b128 v[142:145], v4 offset:28672\n\t"
    "v_fma_f32 v51, v51, v49, v53\n\t"
    "v_mfma_f32_16x16x32_bf16 v[98:101], v[150:153], a[40:43], v[98:101]\n\t"
    "v_fma_f32 v54, v54, v49, v56\n\t"
    "v_fma_f32 v55, v55, v49, v57\n\t"
    "v_mfma_f32_16x16x32_bf16 v[98:101], v[154:157], a[48:51], v[98:101]\n\t"
    "buffer_load_dwordx4 a[100:103], v6, s[40:43], s52 offen offset:2048 sc1\n\t"
    "v_mfma_f32_16x16x32_bf16 v[102:105], v[154:157], a[52:55], v[102:105]\n\t"
    "ds_read_b128 v[154:157], v4 offset:37888\n\t"
    "v_cvt_pk_bf16_f32 v36, v50, v51\n\t"
    "v_mfma_f32_16x16x32_bf16 v[102:105], v[150:153], a[44:47], v[102:105]\n\t"
    "s_waitcnt lgkmcnt(3)\n\t"
    "ds_read_b128 v[150:153], v4 offset:29696\n\t"
    "v_cvt_pk_bf16_f32 v37, v54, v55\n\t"
    "v_mfma_f32_16x16x32_bf16 v[106:109], v[158:161], a[40:43], v[106:109]\n\t"
    "v_lshlrev_b32 v50, 16, v28\n\t"
    "v_and_b32 v51, 0xffff0000, v28\n\t"
    "v_mfma_f32_16x16x32_bf16 v[106:109], v[162:165], a[48:51], v[106:109]\n\t"
    "buffer_load_dwordx4 a[104:107], v5, s[40:43], s53 offen offset:2048 sc1\n\t"
    "v_mfma_f32_16x16x32_bf16 v[110:113], v[162:165], a[52:55], v[110:113]\n\t"
    "ds_read_b128 v[162:165], v4 offset:38912\n\t"
    "v_lshlrev_b32 v54, 16, v29\n\t"
    "v_mfma_f32_16x16x32_bf16 v[110:113], v[158:161], a[44:47], v[110:113]\n\t"
    "s_waitcnt lgkmcnt(3)\n\t"
    "ds_read_b128 v[158:161], v4 offset:30720\n\t"
    "v_and_b32 v55, 0xffff0000, v29\n\t"
    "v_mfma_f32_16x16x32_bf16 v[114:117], v[142:145], a[40:43], v[114:117]\n\t"
    "v_sub_f32 v50, v50, v40\n\t"
    "v_sub_f32 v51, v51, v40\n\t"
    "v_mfma_f32_16x16x32_bf16 v[114:117], v[146:149], a[48:51], v[114:117]\n\t"
    "buffer_load_dwordx4 a[108:111], v6, s[40:43], s53 offen offset:2048 sc1\n\t"
    "v_mfma_f32_16x16x32_bf16 v[118:121], v[146:149], a[52:55], v[118:121]\n\t"
    "ds_read_b128 v[146:149], v4 offset:39936\n\t"
    "v_sub_f32 v54, v54, v40\n\t"
    "v_mfma_f32_16x16x32_bf16 v[118:121], v[142:145], a[44:47], v[118:121]\n\t"
    "s_waitcnt lgkmcnt(3)\n\t"
    "ds_read_b128 v[142:145], v4 offset:31744\n\t"
    "v_sub_f32 v55, v55, v40\n\t"
    "v_mfma_f32_16x16x32_bf16 v[122:125], v[150:153], a[40:43], v[122:125]\n\t"
    "v_cvt_pk_bf16_f32 v51, v50, v51\n\t"
    "v_cvt_pk_bf16_f32 v55, v54, v55\n\t"
    "v_mfma_f32_16x16x32_bf16 v[122:125], v[154:157], a[48:51], v[122:125]\n\t"
    "buffer_load_dwordx4 a[112:115], v5, s[40:43], s54 offen offset:2048 sc1\n\t"
    "v_mfma_f32_16x16x32_bf16 v[126:129], v[154:157], a[52:55], v[126:129]\n\t"
    "ds_read_b128 v[154:157], v4 offset:62464\n\t"
    "v_lshlrev_b32 v50, 16, v51\n\t"
    "v_mfma_f32_16x16x32_bf16 v[126:129], v[150:153], a[44:47], v[126:129]\n\t"
    "s_waitcnt lgkmcnt(3)\n\t"
    "ds_read_b128 v[150:153], v4 offset:61440\n\t"
    "v_lshlrev_b32 v54, 16, v55\n\t"
    "v_mfma_f32_16x16x32_bf16 v[130:133], v[158:161], a[40:43], v[130:133]\n\t"
    "v_and_b32 v51, 0xffff0000, v51\n\t"
    "v_and_b32 v55, 0xffff0000, v55\n\t"
    "v_mfma_f32_16x16x32_bf16 v[130:133], v[162:165], a[48:51], v[130:133]\n\t"
    "buffer_load_dwordx4 a[116:119], v6, s[40:43], s54 offen offset:2048 sc1\n\t"
    "v_mfma_f32_16x16x32_bf16 v[134:137], v[162:165], a[52:55], v[134:137]\n\t"
    "ds_read_b128 v[162:165], v4 offset:64512\n\t"
    "v_mul_f32 v52, v50, v48\n\t"
    "v_mfma_f32_16x16x32_bf16 v[134:137], v[158:161], a[44:47], v[134:137]\n\t"
    "s_waitcnt lgkmcnt(3)\n\t"
    "ds_read_b128 v[158:161], v4 offset:63488\n\t"
    "v_mul_f32 v53, v51, v48\n\t"
    "v_mfma_f32_16x16x32_bf16 v[78:81], v[142:145], a[40:43], v[78:81]\n\t"
    "v_mul_f32 v56, v54, v48\n\t"
    "v_mul_f32 v57, v55, v48\n\t"
    "v_mfma_f32_16x16x32_bf16 v[78:81], v[146:149], a[48:51], v[78:81]\n\t"
    "buffer_load_dwordx4 v4, s[40:43], s60 offen offset:0 sc1 lds\n\t"
    "v_mfma_f32_16x16x32_bf16 v[138:141], v[146:149], a[52:55], v[138:141]\n\t"
    "ds_read_b128 v[146:149], v8 offset:1024\n\t"
    "v_fma_f32 v50, -v52, v41, v50\n\t"
    "v_mfma_f32_16x16x32_bf16 v[138:141], v[142:145], a[44:47], v[138:141]\n\t"
    "s_waitcnt vmcnt(35) lgkmcnt(3)\n\t"
    "ds_read_b128 v[142:145], v8 offset:0\n\t"
    "v_fma_f32 v51, -v53, v41, v51\n\t"
    "v_mfma_f32_16x16x32_bf16 v[82:85], v[150:153], a[56:59], v[82:85]\n\t"
    "s_waitcnt vmcnt(34)\n\t"
    "v_fma_f32 v54, -v56, v41, v54\n\t"
    "v_fma_f32 v55, -v57, v41, v55\n\t"
    "v_mfma_f32_16x16x32_bf16 v[86:89], v[150:153], a[60:63], v[86:89]\n\t"
    "buffer_load_dwordx4 v4, s[40:43], s60 offen offset:1024 sc1 lds\n\t"
    "v_mfma_f32_16x16x32_bf16 v[90:93], v[154:157], a[56:59], v[90:93]\n\t"
    "ds_read_b128 v[150:153], v8 offset:2048\n\t"
    "v_fma_f32 v50, v50, v48, v52\n\t"
    "v_mfma_f32_16x16x32_bf16 v[94:97], v[154:157], a[60:63], v[94:97]\n\t"
    "s_waitcnt lgkmcnt(3)\n\t"
    "ds_read_b128 v[154:157], v8 offset:3072\n\t"
    "v_fma_f32 v51, v51, v48, v53\n\t"
    "v_mfma_f32_16x16x32_bf16 v[98:101], v[158:161], a[56:59], v[98:101]\n\t"
    "v_fma_f32 v54, v54, v48, v56\n\t"
    "v_fma_f32 v55, v55, v48, v57\n\t"
    "v_mfma_f32_16x16x32_bf16 v[102:105], v[158:161], a[60:63], v[102:105]\n\t"
    "buffer_load_dwordx4 v4, s[40:43], s60 offen offset:2048 sc1 lds\n\t"
    "v_mfma_f32_16x16x32_bf16 v[106:109], v[162:165], a[56:59], v[106:109]\n\t"
    "ds_read_b128 v[158:161], v8 offset:4096\n\t"
    "v_and_b32 v48, 0x80, v4\n\t"
    "v_mfma_f32_16x16x32_bf16 v[110:113], v[162:165], a[60:63], v[110:113]\n\t"
    "s_waitcnt lgkmcnt(3)\n\t"
    "ds_read_b128 v[162:165], v8 offset:12288\n\t"
    "v_lshrrev_b32 v48, 2, v48\n\t"
    "v_mfma_f32_16x16x32_bf16 v[114:117], v[142:145], a[56:59], v[114:117]\n\t"
    "KFP15:\n\t"
    "s_cbranch_execz KFR2\n\t"
    "v_cvt_pk_bf16_f32 v38, v50, v51\n\t"
    "v_cvt_pk_bf16_f32 v39, v54, v55\n\t"
    "v_mfma_f32_16x16x32_bf16 v[118:121], v[142:145], a[60:63], v[118:121]\n\t"
    "buffer_load_dwordx4 v4, s[40:43], s60 offen offset:3072 sc1 lds\n\t"
    "v_mfma_f32_16x16x32_bf16 v[122:125], v[146:149], a[56:59], v[122:125]\n\t"
    "s_add_u32 m0, s86, 4294860800\n\t"
    "ds_read_b128 v[142:145], v8 offset:5120\n\t"
    "v_mfma_f32_16x16x32_bf16 v[126:129], v[146:149], a[60:63], v[126:129]\n\t"
    "s_waitcnt lgkmcnt(4)\n\t"
    "ds_read_b128 v[146:149], v8 offset:13312\n\t"
    "v_mfma_f32_16x16x32_bf16 v[130:133], v[150:153], a[56:59], v[130:133]\n\t"
    "s_add_u32 s60, s97, 122880\n\t"
    "v_mfma_f32_16x16x32_bf16 v[134:137], v[150:153], a[60:63], v[134:137]\n\t"
    "s_waitcnt lgkmcnt(4)\n\t"
    "buffer_load_dwordx4 v4, s[40:43], s60 offen offset:0 sc1 lds\n\t"
    "v_mfma_f32_16x16x32_bf16 v[78:81], v[154:157], a[56:59], v[78:81]\n\t"
    "ds_read_b128 v[150:153], v8 offset:6144\n\t"
    "v_mfma_f32_16x16x32_bf16 v[138:141], v[154:157], a[60:63], v[138:141]\n\t"
    "s_waitcnt lgkmcnt(4)\n\t"
    "s_waitcnt vmcnt(15)\n\t"
    "s_barrier\n\t"
    "v_mfma_f32_16x16x32_bf16 v[82:85], v[158:161], a[64:67], v[82:85]\n\t"
    "s_waitcnt lgkmcnt(3)\n\t"
    "buffer_store_dword v60, v2, s[40:43], s84 offen sc1\n\t"
    "v_mfma_f32_16x16x32_bf16 v[82:85], v[162:165], a[72:75], v[82:85]\n\t"
    "ds_read_b128 v[154:157], v8 offset:14336\n\t"
    "v_mfma_f32_16x16x32_bf16 v[86:89], v[162:165], a[76:79], v[86:89]\n\t"
    "buffer_load_dwordx4 a[120:123], v5, s[40:43], s48 offen offset:3072 sc1\n\t"
    "v_mfma_f32_16x16x32_bf16 v[86:89], v[158:161], a[68:71], v[86:89]\n\t"
    "s_waitcnt lgkmcnt(3)\n\t"
    "ds_read_b128 v[158:161], v8 offset:7168\n\t"
    "v_mfma_f32_16x16x32_bf16 v[90:93], v[142:145], a[64:67], v[90:93]\n\t"
    "s_waitcnt lgkmcnt(3)\n\t"
    "ds_read_b128 v[162:165], v8 offset:15360\n\t"
    "v_mfma_f32_16x16x32_bf16 v[90:93], v[146:149], a[72:75], v[90:93]\n\t"
    "s_add_u32 s84, s84, 32768\n\t"
    "v_mfma_f32_16x16x32_bf16 v[94:97], v[146:149], a[76:79], v[94:97]\n\t"
    "buffer_load_dwordx4 a[124:127], v6, s[40:43], s48 offen offset:3072 sc1\n\t"
    "v_mfma_f32_16x16x32_bf16 v[94:97], v[142:145], a[68:71], v[94:97]\n\t"
    "s_waitcnt lgkmcnt(3)\n\t"
    "ds_read_b128 v[142:145], v8 offset:8192\n\t"
    "v_mfma_f32_16x16x32_bf16 v[98:101], v[150:153], a[64:67], v[98:101]\n\t"
    "s_waitcnt lgkmcnt(3)\n\t"
    "ds_read_b128 v[146:149], v8 offset:16384\n\t"
    "v_mfma_f32_16x16x32_bf16 v[98:101], v[154:157], a[72:75], v[98:101]\n\t"
    "s_add_u32 s48, s48, 4096\n\t"
    "v_mfma_f32_16x16x32_bf16 v[102:105], v[154:157], a[76:79], v[102:105]\n\t"
    "buffer_load_dwordx4 a[128:131], v5, s[40:43], s49 offen offset:3072 sc1\n\t"
    "v_mfma_f32_16x16x32_bf16 v[102:105], v[150:153], a[68:71], v[102:105]\n\t"
    "s_waitcnt lgkmcnt(3)\n\t"
    "ds_read_b128 v[150:153], v8 offset:9216\n\t"
    "v_mfma_f32_16x16x32_bf16 v[106:109], v[158:161], a[64:67], v[106:109]\n\t"
    "s_waitcnt lgkmcnt(3)\n\t"
    "ds_read_b128 v[154:157], v8 offset:17408\n\t"
    "v_mfma_f32_16x16x32_bf16 v[106:109], v[162:165], a[72:75], v[106:109]\n\t"
    "v_mfma_f32_16x16x32_bf16 v[110:113], v[162:165], a[76:79], v[110:113]\n\t"
    "buffer_load_dwordx4 a[132:135], v6, s[40:43], s49 offen offset:3072 sc1\n\t"
    "v_mfma_f32_16x16x32_bf16 v[110:113], v[158:161], a[68:71], v[110:113]\n\t"
    "s_waitcnt lgkmcnt(3)\n\t"
    "ds_read_b128 v[158:161], v8 offset:10240\n\t"
    "v_mfma_f32_16x16x32_bf16 v[114:117], v[142:145], a[64:67], v[114:117]\n\t"
    "s_waitcnt lgkmcnt(3)\n\t"
    "ds_read_b128 v[162:165], v8 offset:18432\n\t"
    "v_mfma_f32_16x16x32_bf16 v[114:117], v[146:149], a[72:75], v[114:117]\n\t"
    "s_add_u32 s49, s49, 4096\n\t"
    "v_mfma_f32_16x16x32_bf16 v[118:121], v[146:149], a[76:79], v[118:121]\n\t"
    "s_mov_b32 s64, 49774592\n\t"
    "buffer_load_dwordx4 v4, s[40:43], s60 offen offset:1024 sc1 lds\n\t"
    "v_mfma_f32_16x16x32_bf16 v[118:121], v[142:145], a[68:71], v[118:121]\n\t"
    "s_waitcnt lgkmcnt(3)\n\t"
    "ds_read_b128 v[142:145], v8 offset:11264\n\t"
    "v_mfma_f32_16x16x32_bf16 v[122:125], v[150:153], a[64:67], v[122:125]\n\t"
    "s_waitcnt lgkmcnt(3)\n\t"
    "ds_read_b128 v[146:149], v8 offset:19456\n\t"
    "v_mfma_f32_16x16x32_bf16 v[122:125], v[154:157], a[72:75], v[122:125]\n\t"
    "v_mfma_f32_16x16x32_bf16 v[126:129], v[154:157], a[76:79], v[126:129]\n\t"
    "buffer_load_dwordx4 v4, s[40:43], s60 offen offset:2048 sc1 lds\n\t"
    "v_mfma_f32_16x16x32_bf16 v[126:129], v[150:153], a[68:71], v[126:129]\n\t"
    "s_waitcnt lgkmcnt(3)\n\t"
    "s_mov_b64 exec, 0xe7f\n\t"
    "buffer_load_dword v59, v7, s[40:43], s64 offen sc1\n\t"
    "s_mov_b64 exec, -1\n\t"
    "v_mfma_f32_16x16x32_bf16 v[130:133], v[158:161], a[64:67], v[130:133]\n\t"
    "s_waitcnt lgkmcnt(2)\n\t"
    "ds_read_b128 v[150:153], v8 offset:20480\n\t"
    "v_mfma_f32_16x16x32_bf16 v[130:133], v[162:165], a[72:75], v[130:133]\n\t"
    "ds_read_b128 v[154:157], v8 offset:28672\n\t"
    "v_mfma_f32_16x16x32_bf16 v[134:137], v[162:165], a[76:79], v[134:137]\n\t"
    "buffer_load_dwordx4 v4, s[40:43], s60 offen offset:3072 sc1 lds\n\t"
    "v_mfma_f32_16x16x32_bf16 v[134:137], v[158:161], a[68:71], v[134:137]\n\t"
    "s_waitcnt lgkmcnt(3)\n\t"
    "ds_read_b128 v[158:161], v8 offset:21504\n\t"
    "v_mfma_f32_16x16x32_bf16 v[78:81], v[142:145], a[64:67], v[78:81]\n\t"
    "s_waitcnt lgkmcnt(3)\n\t"
    "ds_read_b128 v[162:165], v8 offset:29696\n\t"
    "v_mfma_f32_16x16x32_bf16 v[78:81], v[146:149], a[72:75], v[78:81]\n\t"
    "s_lshl1_add_u32 m0, s70, 0\n\t"
    "v_mfma_f32_16x16x32_bf16 v[138:141], v[146:149], a[76:79], v[138:141]\n\t"
    "s_add_u32 s60, s98, 122880\n\t"
    "buffer_load_dwordx2 v[40:41], v13, s[40:43], s91 offen offset:0 sc1\n\t"
    "v_mfma_f32_16x16x32_bf16 v[138:141], v[142:145], a[68:71], v[138:141]\n\t"
    "s_waitcnt vmcnt(24) lgkmcnt(3)\n\t"
    "ds_read_b128 v[142:145], v8 offset:22528\n\t"
    "v_mfma_f32_16x16x32_bf16 v[82:85], v[150:153], a[80:83], v[82:85]\n\t"
    "s_waitcnt vmcnt(22) lgkmcnt(3)\n\t"
    "ds_read_b128 v[146:149], v8 offset:30720\n\t"
    "v_mfma_f32_16x16x32_bf16 v[82:85], v[154:157], a[88:91], v[82:85]\n\t"
    "s_waitcnt vmcnt(21)\n\t"
    "v_mfma_f32_16x16x32_bf16 v[86:89], v[154:157], a[92:95], v[86:89]\n\t"
    "buffer_load_dwordx4 a[136:139], v5, s[40:43], s50 offen offset:3072 sc1\n\t"
    "v_mfma_f32_16x16x32_bf16 v[86:89], v[150:153], a[84:87], v[86:89]\n\t"
    "s_waitcnt lgkmcnt(3)\n\t"
    "ds_read_b128 v[150:153], v8 offset:23552\n\t"
    "v_mfma_f32_16x16x32_bf16 v[90:93], v[158:161], a[80:83], v[90:93]\n\t"
    "s_waitcnt lgkmcnt(3)\n\t"
    "ds_read_b128 v[154:157], v8 offset:31744\n\t"
    "v_mfma_f32_16x16x32_bf16 v[90:93], v[162:165], a[88:91], v[90:93]\n\t"
    "v_mfma_f32_16x16x32_bf16 v[94:97], v[162:165], a[92:95], v[94:97]\n\t"
    "buffer_load_dwordx4 a[140:143], v6, s[40:43], s50 offen offset:3072 sc1\n\t"
    "v_mfma_f32_16x16x32_bf16 v[94:97], v[158:161], a[84:87], v[94:97]\n\t"
    "s_waitcnt lgkmcnt(3)\n\t"
    "ds_read_b128 v[158:161], v8 offset:24576\n\t"
    "v_mfma_f32_16x16x32_bf16 v[98:101], v[142:145], a[80:83], v[98:101]\n\t"
    "s_waitcnt lgkmcnt(3)\n\t"
    "ds_read_b128 v[162:165], v8 offset:32768\n\t"
    "v_mfma_f32_16x16x32_bf16 v[98:101], v[146:149], a[88:91], v[98:101]\n\t"
    "s_add_u32 s50, s50, 4096\n\t"
    "v_mfma_f32_16x16x32_bf16 v[102:105], v[146:149], a[92:95], v[102:105]\n\t"
    "buffer_load_dwordx4 a[144:147], v5, s[40:43], s51 offen offset:3072 sc1\n\t"
    "v_mfma_f32_16x16x32_bf16 v[102:105], v[142:145], a[84:87], v[102:105]\n\t"
    "s_waitcnt lgkmcnt(3)\n\t"
    "ds_read_b128 v[142:145], v8 offset:25600\n\t"
    "v_mfma_f32_16x16x32_bf16 v[106:109], v[150:153], a[80:83], v[106:109]\n\t"
    "s_waitcnt lgkmcnt(3)\n\t"
    "ds_read_b128 v[146:149], v8 offset:33792\n\t"
    "v_mfma_f32_16x16x32_bf16 v[106:109], v[154:157], a[88:91], v[106:109]\n\t"
    "v_mfma_f32_16x16x32_bf16 v[110:113], v[154:157], a[92:95], v[110:113]\n\t"
    "buffer_load_dwordx4 a[148:151], v6, s[40:43], s51 offen offset:3072 sc1\n\t"
    "v_mfma_f32_16x16x32_bf16 v[110:113], v[150:153], a[84:87], v[110:113]\n\t"
    "s_waitcnt lgkmcnt(3)\n\t"
    "ds_read_b128 v[150:153], v8 offset:26624\n\t"
    "v_mfma_f32_16x16x32_bf16 v[114:117], v[158:161], a[80:83], v[114:117]\n\t"
    "s_waitcnt lgkmcnt(3)\n\t"
    "ds_read_b128 v[154:157], v8 offset:34816\n\t"
    "v_mfma_f32_16x16x32_bf16 v[114:117], v[162:165], a[88:91], v[114:117]\n\t"
    "s_add_u32 s51, s51, 4096\n\t"
    "v_mfma_f32_16x16x32_bf16 v[118:121], v[162:165], a[92:95], v[118:121]\n\t"
    "buffer_load_dwordx2 v[42:43], v14, s[40:43], s91 offen offset:0 sc1\n\t"
    "v_mfma_f32_16x16x32_bf16 v[118:121], v[158:161], a[84:87], v[118:121]\n\t"
    "s_waitcnt lgkmcnt(3)\n\t"
    "ds_read_b128 v[158:161], v8 offset:27648\n\t"
    "v_mfma_f32_16x16x32_bf16 v[122:125], v[142:145], a[80:83], v[122:125]\n\t"
    "s_waitcnt lgkmcnt(3)\n\t"
    "ds_read_b128 v[162:165], v8 offset:35840\n\t"
    "v_mfma_f32_16x16x32_bf16 v[122:125], v[146:149], a[88:91], v[122:125]\n\t"
    "v_mfma_f32_16x16x32_bf16 v[126:129], v[146:149], a[92:95], v[126:129]\n\t"
    "buffer_load_dwordx2 v[44:45], v13, s[40:43], s91 offen offset:32 sc1\n\t"
    "v_mfma_f32_16x16x32_bf16 v[126:129], v[142:145], a[84:87], v[126:129]\n\t"
    "s_waitcnt lgkmcnt(3)\n\t"
    "ds_read_b128 v[142:145], v9 offset:16384\n\t"
    "v_cndmask_b32_dpp v13, v13, v13, vcc row_ror:8 row_mask:0xf bank_mask:0xf\n\t"
    "v_mfma_f32_16x16x32_bf16 v[130:133], v[150:153], a[80:83], v[130:133]\n\t"
    "s_waitcnt lgkmcnt(3)\n\t"
    "ds_read_b128 v[146:149], v9 offset:24576\n\t"
    "v_add_u32 v13, v13, v48\n\t"
    "v_mfma_f32_16x16x32_bf16 v[130:133], v[154:157], a[88:91], v[130:133]\n\t"
    "v_mfma_f32_16x16x32_bf16 v[134:137], v[154:157], a[92:95], v[134:137]\n\t"
    "buffer_load_dwordx2 v[46:47], v14, s[40:43], s91 offen offset:32 sc1\n\t"
    "v_mfma_f32_16x16x32_bf16 v[134:137], v[150:153], a[84:87], v[134:137]\n\t"
    "s_waitcnt lgkmcnt(3)\n\t"
    "ds_read_b128 v[150:153], v9 offset:17408\n\t"
    "v_mfma_f32_16x16x32_bf16 v[78:81], v[158:161], a[80:83], v[78:81]\n\t"
    "s_waitcnt lgkmcnt(3)\n\t"
    "ds_read_b128 v[154:157], v9 offset:25600\n\t"
    "v_mfma_f32_16x16x32_bf16 v[78:81], v[162:165], a[88:91], v[78:81]\n\t"
    "v_mfma_f32_16x16x32_bf16 v[138:141], v[162:165], a[92:95], v[138:141]\n\t"
    "buffer_load_dwordx2 v[48:49], v13, s[40:43], s62 offen offset:128 sc1\n\t"
    "v_mfma_f32_16x16x32_bf16 v[138:141], v[158:161], a[84:87], v[138:141]\n\t"
    "s_waitcnt vmcnt(28) lgkmcnt(3)\n\t"
    "s_barrier\n\t"
    "v_mfma_f32_16x16x32_bf16 v[82:85], v[142:145], a[96:99], v[82:85]\n\t"
    "s_waitcnt vmcnt(26) lgkmcnt(2)\n\t"
    "ds_read_b128 v[158:161], v9 offset:18432\n\t"
    "v_mfma_f32_16x16x32_bf16 v[82:85], v[146:149], a[104:107], v[82:85]\n\t"
    "s_waitcnt vmcnt(25)\n\t"
    "ds_read_b128 v[162:165], v9 offset:26624\n\t"
    "v_mfma_f32_16x16x32_bf16 v[86:89], v[146:149], a[108:111], v[86:89]\n\t"
    "buffer_load_dwordx4 a[152:155], v5, s[40:43], s52 offen offset:3072 sc1\n\t"
    "v_mfma_f32_16x16x32_bf16 v[86:89], v[142:145], a[100:103], v[86:89]\n\t"
    "s_waitcnt lgkmcnt(3)\n\t"
    "ds_read_b128 v[142:145], v9 offset:19456\n\t"
    "v_mfma_f32_16x16x32_bf16 v[90:93], v[150:153], a[96:99], v[90:93]\n\t"
    "s_waitcnt lgkmcnt(3)\n\t"
    "ds_read_b128 v[146:149], v9 offset:27648\n\t"
    "v_mfma_f32_16x16x32_bf16 v[90:93], v[154:157], a[104:107], v[90:93]\n\t"
    "s_add_u32 s62, s85, 10240\n\t"
    "v_mfma_f32_16x16x32_bf16 v[94:97], v[154:157], a[108:111], v[94:97]\n\t"
    "buffer_load_dwordx4 a[156:159], v6, s[40:43], s52 offen offset:3072 sc1\n\t"
    "v_mfma_f32_16x16x32_bf16 v[94:97], v[150:153], a[100:103], v[94:97]\n\t"
    "s_waitcnt lgkmcnt(3)\n\t"
    "ds_read_b128 v[150:153], v9 offset:20480\n\t"
    "v_mfma_f32_16x16x32_bf16 v[98:101], v[158:161], a[96:99], v[98:101]\n\t"
    "s_waitcnt lgkmcnt(3)\n\t"
    "ds_read_b128 v[154:157], v9 offset:28672\n\t"
    "v_mfma_f32_16x16x32_bf16 v[98:101], v[162:165], a[104:107], v[98:101]\n\t"
    "s_add_u32 s52, s52, 4096\n\t"
    "v_mfma_f32_16x16x32_bf16 v[102:105], v[162:165], a[108:111], v[102:105]\n\t"
    "buffer_load_dwordx4 a[160:163], v5, s[40:43], s53 offen offset:3072 sc1\n\t"
    "v_mfma_f32_16x16x32_bf16 v[102:105], v[158:161], a[100:103], v[102:105]\n\t"
    "s_waitcnt lgkmcnt(3)\n\t"
    "ds_read_b128 v[158:161], v9 offset:21504\n\t"
    "v_mfma_f32_16x16x32_bf16 v[106:109], v[142:145], a[96:99], v[106:109]\n\t"
    "s_waitcnt lgkmcnt(3)\n\t"
    "ds_read_b128 v[162:165], v9 offset:29696\n\t"
    "v_mfma_f32_16x16x32_bf16 v[106:109], v[146:149], a[104:107], v[106:109]\n\t"
    "v_mfma_f32_16x16x32_bf16 v[110:113], v[146:149], a[108:111], v[110:113]\n\t"
    "buffer_load_dwordx4 a[164:167], v6, s[40:43], s53 offen offset:3072 sc1\n\t"
    "v_mfma_f32_16x16x32_bf16 v[110:113], v[142:145], a[100:103], v[110:113]\n\t"
    "s_waitcnt lgkmcnt(3)\n\t"
    "ds_read_b128 v[142:145], v9 offset:22528\n\t"
    "v_mfma_f32_16x16x32_bf16 v[114:117], v[150:153], a[96:99], v[114:117]\n\t"
    "s_waitcnt lgkmcnt(3)\n\t"
    "ds_read_b128 v[146:149], v9 offset:30720\n\t"
    "v_mfma_f32_16x16x32_bf16 v[114:117], v[154:157], a[104:107], v[114:117]\n\t"
    "s_add_u32 s53, s53, 4096\n\t"
    "v_mfma_f32_16x16x32_bf16 v[118:121], v[154:157], a[108:111], v[118:121]\n\t"
    "buffer_load_dwordx4 v4, s[40:43], s60 offen offset:0 sc1 lds\n\t"
    "v_mfma_f32_16x16x32_bf16 v[118:121], v[150:153], a[100:103], v[118:121]\n\t"
    "s_waitcnt lgkmcnt(3)\n\t"
    "ds_read_b128 v[150:153], v9 offset:23552\n\t"
    "v_mfma_f32_16x16x32_bf16 v[122:125], v[158:161], a[96:99], v[122:125]\n\t"
    "s_waitcnt lgkmcnt(3)\n\t"
    "ds_read_b128 v[154:157], v9 offset:31744\n\t"
    "v_mfma_f32_16x16x32_bf16 v[122:125], v[162:165], a[104:107], v[122:125]\n\t"
    "v_mfma_f32_16x16x32_bf16 v[126:129], v[162:165], a[108:111], v[126:129]\n\t"
    "buffer_load_dwordx4 v4, s[40:43], s60 offen offset:1024 sc1 lds\n\t"
    "v_mfma_f32_16x16x32_bf16 v[126:129], v[158:161], a[100:103], v[126:129]\n\t"
    "s_waitcnt lgkmcnt(3)\n\t"
    "ds_read_b128 v[158:161], v8 offset:36864\n\t"
    "v_mfma_f32_16x16x32_bf16 v[130:133], v[142:145], a[96:99], v[130:133]\n\t"
    "s_waitcnt lgkmcnt(3)\n\t"
    "ds_read_b128 v[162:165], v8 offset:37888\n\t"
    "v_mfma_f32_16x16x32_bf16 v[130:133], v[146:149], a[104:107], v[130:133]\n\t"
    "s_add_u32 m0, s86, 4294828032\n\t"
    "v_mfma_f32_16x16x32_bf16 v[134:137], v[146:149], a[108:111], v[134:137]\n\t"
    "s_add_u32 s60, s68, 163840\n\t"
    "buffer_load_dwordx4 v4, s[40:43], s60 offen offset:0 sc1 lds\n\t"
    "v_mfma_f32_16x16x32_bf16 v[134:137], v[142:145], a[100:103], v[134:137]\n\t"
    "s_waitcnt lgkmcnt(3)\n\t"
    "ds_read_b128 v[142:145], v8 offset:38912\n\t"
    "v_mfma_f32_16x16x32_bf16 v[78:81], v[150:153], a[96:99], v[78:81]\n\t"
    "s_waitcnt lgkmcnt(3)\n\t"
    "ds_read_b128 v[146:149], v8 offset:39936\n\t"
    "v_mfma_f32_16x16x32_bf16 v[78:81], v[154:157], a[104:107], v[78:81]\n\t"
    "v_mfma_f32_16x16x32_bf16 v[138:141], v[154:157], a[108:111], v[138:141]\n\t"
    "buffer_load_dwordx4 v4, s[40:43], s60 offen offset:1024 sc1 lds\n\t"
    "v_mfma_f32_16x16x32_bf16 v[138:141], v[150:153], a[100:103], v[138:141]\n\t"
    "s_waitcnt vmcnt(32) lgkmcnt(3)\n\t"
    "ds_read_b128 v[150:153], v8 offset:40960\n\t"
    "v_mfma_f32_16x16x32_bf16 v[82:85], v[158:161], a[112:115], v[82:85]\n\t"
    "s_waitcnt vmcnt(31)\n\t"
    "ds_read_b128 v[154:157], v8 offset:41984\n\t"
    "v_mfma_f32_16x16x32_bf16 v[86:89], v[158:161], a[116:119], v[86:89]\n\t"
    "s_waitcnt lgkmcnt(4)\n\t"
    "ds_read_b128 v[158:161], v8 offset:43008\n\t"
    "v_mfma_f32_16x16x32_bf16 v[90:93], v[162:165], a[112:115], v[90:93]\n\t"
    "buffer_load_dwordx4 v4, s[40:43], s60 offen offset:2048 sc1 lds\n\t"
    "v_mfma_f32_16x16x32_bf16 v[94:97], v[162:165], a[116:119], v[94:97]\n\t"
    "s_waitcnt lgkmcnt(4)\n\t"
    "ds_read_b128 v[162:165], v8 offset:44032\n\t"
    "v_mfma_f32_16x16x32_bf16 v[98:101], v[142:145], a[112:115], v[98:101]\n\t"
    "v_mfma_f32_16x16x32_bf16 v[102:105], v[142:145], a[116:119], v[102:105]\n\t"
    "s_waitcnt lgkmcnt(4)\n\t"
    "ds_read_b128 v[142:145], v8 offset:45056\n\t"
    "v_mfma_f32_16x16x32_bf16 v[106:109], v[146:149], a[112:115], v[106:109]\n\t"
    "buffer_load_dwordx4 v4, s[40:43], s60 offen offset:3072 sc1 lds\n\t"
    "v_mfma_f32_16x16x32_bf16 v[110:113], v[146:149], a[116:119], v[110:113]\n\t"
    "s_waitcnt lgkmcnt(4)\n\t"
    "ds_read_b128 v[146:149], v8 offset:53248\n\t"
    "v_mfma_f32_16x16x32_bf16 v[114:117], v[150:153], a[112:115], v[114:117]\n\t"
    "s_add_u32 m0, s86, 4294844416\n\t"
    "v_mfma_f32_16x16x32_bf16 v[118:121], v[150:153], a[116:119], v[118:121]\n\t"
    "s_waitcnt lgkmcnt(4)\n\t"
    "ds_read_b128 v[150:153], v8 offset:46080\n\t"
    "v_mfma_f32_16x16x32_bf16 v[122:125], v[154:157], a[112:115], v[122:125]\n\t"
    "s_add_u32 s60, s69, 163840\n\t"
    "buffer_load_dwordx4 a[168:171], v5, s[40:43], s54 offen offset:3072 sc1\n\t"
    "v_mfma_f32_16x16x32_bf16 v[126:129], v[154:157], a[116:119], v[126:129]\n\t"
    "s_waitcnt lgkmcnt(4)\n\t"
    "ds_read_b128 v[154:157], v8 offset:54272\n\t"
    "v_mfma_f32_16x16x32_bf16 v[130:133], v[158:161], a[112:115], v[130:133]\n\t"
    "v_mfma_f32_16x16x32_bf16 v[134:137], v[158:161], a[116:119], v[134:137]\n\t"
    "s_waitcnt lgkmcnt(4)\n\t"
    "ds_read_b128 v[158:161], v8 offset:47104\n\t"
    "v_mfma_f32_16x16x32_bf16 v[78:81], v[162:165], a[112:115], v[78:81]\n\t"
    "buffer_load_dwordx4 a[172:175], v6, s[40:43], s54 offen offset:3072 sc1\n\t"
    "v_mfma_f32_16x16x32_bf16 v[138:141], v[162:165], a[116:119], v[138:141]\n\t"
    "s_waitcnt lgkmcnt(4)\n\t"
    "s_waitcnt vmcnt(21)\n\t"
    "s_barrier\n\t"
    "v_mfma_f32_16x16x32_bf16 v[82:85], v[142:145], a[120:123], v[82:85]\n\t"
    "s_waitcnt lgkmcnt(3)\n\t"
    "ds_read_b128 v[162:165], v8 offset:55296\n\t"
    "v_mfma_f32_16x16x32_bf16 v[82:85], v[146:149], a[128:131], v[82:85]\n\t"
    "v_cmp_ne_u32 vcc, s80, v59\n\t"
    "s_and_b32 s65, vcc_lo, 0xe7f\n\t"
    "s_cbranch_scc1 LS4_%=\n\t"
    "LR4_%=:\n\t"
    "v_mfma_f32_16x16x32_bf16 v[86:89], v[146:149], a[132:135], v[86:89]\n\t"
    "buffer_load_dwordx4 a[176:179], v5, s[40:43], s48 offen offset:0 sc1\n\t"
    "v_mfma_f32_16x16x32_bf16 v[86:89], v[142:145], a[124:127], v[86:89]\n\t"
    "s_waitcnt lgkmcnt(3)\n\t"
    "ds_read_b128 v[142:145], v8 offset:48128\n\t"
    "v_mfma_f32_16x16x32_bf16 v[90:93], v[150:153], a[120:123], v[90:93]\n\t"
    "s_waitcnt lgkmcnt(3)\n\t"
    "ds_read_b128 v[146:149], v8 offset:56320\n\t"
    "v_mfma_f32_16x16x32_bf16 v[90:93], v[154:157], a[128:131], v[90:93]\n\t"
    "s_add_u32 s54, s54, 4096\n\t"
    "v_mfma_f32_16x16x32_bf16 v[94:97], v[154:157], a[132:135], v[94:97]\n\t"
    "buffer_load_dwordx4 a[180:183], v6, s[40:43], s48 offen offset:0 sc1\n\t"
    "v_mfma_f32_16x16x32_bf16 v[94:97], v[150:153], a[124:127], v[94:97]\n\t"
    "s_waitcnt lgkmcnt(3)\n\t"
    "ds_read_b128 v[150:153], v8 offset:49152\n\t"
    "v_mfma_f32_16x16x32_bf16 v[98:101], v[158:161], a[120:123], v[98:101]\n\t"
    "s_waitcnt lgkmcnt(3)\n\t"
    "ds_read_b128 v[154:157], v8 offset:57344\n\t"
    "v_mfma_f32_16x16x32_bf16 v[98:101], v[162:165], a[128:131], v[98:101]\n\t"
    "v_mfma_f32_16x16x32_bf16 v[102:105], v[162:165], a[132:135], v[102:105]\n\t"
    "buffer_load_dwordx4 a[184:187], v5, s[40:43], s49 offen offset:0 sc1\n\t"
    "v_mfma_f32_16x16x32_bf16 v[102:105], v[158:161], a[124:127], v[102:105]\n\t"
    "s_waitcnt lgkmcnt(3)\n\t"
    "ds_read_b128 v[158:161], v8 offset:50176\n\t"
    "v_mfma_f32_16x16x32_bf16 v[106:109], v[142:145], a[120:123], v[106:109]\n\t"
    "s_waitcnt lgkmcnt(3)\n\t"
    "ds_read_b128 v[162:165], v8 offset:58368\n\t"
    "v_mfma_f32_16x16x32_bf16 v[106:109], v[146:149], a[128:131], v[106:109]\n\t"
    "v_mfma_f32_16x16x32_bf16 v[110:113], v[146:149], a[132:135], v[110:113]\n\t"
    "buffer_load_dwordx4 a[188:191], v6, s[40:43], s49 offen offset:0 sc1\n\t"
    "v_mfma_f32_16x16x32_bf16 v[110:113], v[142:145], a[124:127], v[110:113]\n\t"
    "s_waitcnt lgkmcnt(3)\n\t"
    "ds_read_b128 v[142:145], v8 offset:51200\n\t"
    "v_mfma_f32_16x16x32_bf16 v[114:117], v[150:153], a[120:123], v[114:117]\n\t"
    "s_waitcnt lgkmcnt(3)\n\t"
    "ds_read_b128 v[146:149], v8 offset:59392\n\t"
    "v_mfma_f32_16x16x32_bf16 v[114:117], v[154:157], a[128:131], v[114:117]\n\t"
    "v_mfma_f32_16x16x32_bf16 v[118:121], v[154:157], a[132:135], v[118:121]\n\t"
    "s_mov_b32 s64, 49807360\n\t"
    "buffer_load_dwordx4 v4, s[40:43], s60 offen offset:0 sc1 lds\n\t"
    "v_mfma_f32_16x16x32_bf16 v[118:121], v[150:153], a[124:127], v[118:121]\n\t"
    "s_waitcnt lgkmcnt(3)\n\t"
    "ds_read_b128 v[150:153], v8 offset:52224\n\t"
    "v_mfma_f32_16x16x32_bf16 v[122:125], v[158:161], a[120:123], v[122:125]\n\t"
    "s_waitcnt lgkmcnt(3)\n\t"
    "ds_read_b128 v[154:157], v8 offset:60416\n\t"
    "v_mfma_f32_16x16x32_bf16 v[122:125], v[162:165], a[128:131], v[122:125]\n\t"
    "v_mfma_f32_16x16x32_bf16 v[126:129], v[162:165], a[132:135], v[126:129]\n\t"
    "buffer_load_dwordx4 v4, s[40:43], s60 offen offset:1024 sc1 lds\n\t"
    "v_mfma_f32_16x16x32_bf16 v[126:129], v[158:161], a[124:127], v[126:129]\n\t"
    "s_waitcnt lgkmcnt(3)\n\t"
    "s_mov_b64 exec, 0x7f\n\t"
    "buffer_load_dword v59, v7, s[40:43], s64 offen sc1\n\t"
    "s_mov_b64 exec, -1\n\t"
    "v_mfma_f32_16x16x32_bf16 v[130:133], v[142:145], a[120:123], v[130:133]\n\t"
    "s_waitcnt lgkmcnt(2)\n\t"
    "ds_read_b128 v[158:161], v8 offset:61440\n\t"
    "v_mfma_f32_16x16x32_bf16 v[130:133], v[146:149], a[128:131], v[130:133]\n\t"
    "ds_read_b128 v[162:165], v9 offset:4096\n\t"
    "v_mfma_f32_16x16x32_bf16 v[134:137], v[146:149], a[132:135], v[134:137]\n\t"
    "buffer_load_dwordx4 v4, s[40:43], s60 offen offset:2048 sc1 lds\n\t"
    "v_mfma_f32_16x16x32_bf16 v[134:137], v[142:145], a[124:127], v[134:137]\n\t"
    "s_waitcnt lgkmcnt(3)\n\t"
    "ds_read_b128 v[142:145], v8 offset:62464\n\t"
    "v_mfma_f32_16x16x32_bf16 v[78:81], v[150:153], a[120:123], v[78:81]\n\t"
    "s_waitcnt lgkmcnt(3)\n\t"
    "ds_read_b128 v[146:149], v9 offset:5120\n\t"
    "v_lshlrev_b32 v50, 16, v30\n\t"
    "v_mfma_f32_16x16x32_bf16 v[78:81], v[154:157], a[128:131], v[78:81]\n\t"
    "v_and_b32 v51, 0xffff0000, v30\n\t"
    "s_waitcnt vmcnt(28)\n\t"
    "v_lshlrev_b32 v52, 16, v40\n\t"
    "v_mfma_f32_16x16x32_bf16 v[138:141], v[154:157], a[132:135], v[138:141]\n\t"
    "buffer_load_dwordx4 v4, s[40:43], s60 offen offset:3072 sc1 lds\n\t"
    "v_mfma_f32_16x16x32_bf16 v[138:141], v[150:153], a[124:127], v[138:141]\n\t"
    "s_waitcnt vmcnt(28) lgkmcnt(3)\n\t"
    "ds_read_b128 v[150:153], v8 offset:63488\n\t"
    "v_and_b32 v53, 0xffff0000, v40\n\t"
    "v_mfma_f32_16x16x32_bf16 v[82:85], v[158:161], a[136:139], v[82:85]\n\t"
    "s_waitcnt vmcnt(26) lgkmcnt(3)\n\t"
    "ds_read_b128 v[154:157], v9 offset:6144\n\t"
    "v_add_f32 v50, v50, v52\n\t"
    "v_mfma_f32_16x16x32_bf16 v[82:85], v[162:165], a[144:147], v[82:85]\n\t"
    "s_waitcnt vmcnt(25)\n\t"
    "s_add_u32 m0, s86, 4294877184\n\t"
    "v_add_f32 v51, v51, v53\n\t"
    "v_mfma_f32_16x16x32_bf16 v[86:89], v[162:165], a[148:151], v[86:89]\n\t"
    "s_add_u32 s60, s97, 163840\n\t"
    "ds_read_b128 v[162:165], v9 offset:7168\n\t"
    "v_mfma_f32_16x16x32_bf16 v[86:89], v[158:161], a[140:143], v[86:89]\n\t"
    "s_waitcnt lgkmcnt(4)\n\t"
    "buffer_load_dwordx4 v4, s[40:43], s60 offen offset:0 sc1 lds\n\t"
    "v_mfma_f32_16x16x32_bf16 v[90:93], v[142:145], a[136:139], v[90:93]\n\t"
    "s_waitcnt lgkmcnt(3)\n\t"
    "ds_read_b128 v[158:161], v8 offset:64512\n\t"
    "v_cvt_pk_bf16_f32 v51, v50, v51\n\t"
    "v_mfma_f32_16x16x32_bf16 v[90:93], v[146:149], a[144:147], v[90:93]\n\t"
    "v_lshlrev_b32 v50, 16, v51\n\t"
    "v_and_b32 v51, 0xffff0000, v51\n\t"
    "v_mfma_f32_16x16x32_bf16 v[94:97], v[146:149], a[148:151], v[94:97]\n\t"
    "ds_read_b128 v[146:149], v9 offset:8192\n\t"
    "v_mul_f32 v52, 0xbfb8aa3b, v50\n\t"
    "v_mfma_f32_16x16x32_bf16 v[94:97], v[142:145], a[140:143], v[94:97]\n\t"
    "s_waitcnt lgkmcnt(4)\n\t"
    "buffer_load_dwordx4 v4, s[40:43], s60 offen offset:1024 sc1 lds\n\t"
    "v_mfma_f32_16x16x32_bf16 v[98:101], v[150:153], a[136:139], v[98:101]\n\t"
    "s_waitcnt lgkmcnt(3)\n\t"
    "ds_read_b128 v[142:145], v9 offset:0\n\t"
    "v_mul_f32 v53, 0xbfb8aa3b, v51\n\t"
    "v_mfma_f32_16x16x32_bf16 v[98:101], v[154:157], a[144:147], v[98:101]\n\t"
    "v_exp_f32 v52, v52\n\t"
    "v_mfma_f32_16x16x32_bf16 v[102:105], v[154:157], a[148:151], v[102:105]\n\t"
    "ds_read_b128 v[154:157], v9 offset:9216\n\t"
    "v_add_f32 v52, 1.0, v52\n\t"
    "v_mfma_f32_16x16x32_bf16 v[102:105], v[150:153], a[140:143], v[102:105]\n\t"
    "s_waitcnt lgkmcnt(3)\n\t"
    "buffer_load_dwordx4 v4, s[40:43], s60 offen offset:2048 sc1 lds\n\t"
    "v_mfma_f32_16x16x32_bf16 v[106:109], v[158:161], a[136:139], v[106:109]\n\t"
    "ds_read_b128 v[150:153], v9 offset:1024\n\t"
    "v_lshlrev_b32 v54, 16, v31\n\t"
    "v_mfma_f32_16x16x32_bf16 v[106:109], v[162:165], a[144:147], v[106:109]\n\t"
    "v_exp_f32 v53, v53\n\t"
    "v_mfma_f32_16x16x32_bf16 v[110:113], v[162:165], a[148:151], v[110:113]\n\t"
    "ds_read_b128 v[162:165], v9 offset:10240\n\t"
    "v_add_f32 v53, 1.0, v53\n\t"
    "v_mfma_f32_16x16x32_bf16 v[110:113], v[158:161], a[140:143], v[110:113]\n\t"
    "s_waitcnt lgkmcnt(3)\n\t"
    "buffer_load_dwordx4 v4, s[40:43], s60 offen offset:3072 sc1 lds\n\t"
    "v_mfma_f32_16x16x32_bf16 v[114:117], v[142:145], a[136:139], v[114:117]\n\t"
    "ds_read_b128 v[158:161], v9 offset:2048\n\t"
    "v_and_b32 v55, 0xffff0000, v31\n\t"
    "v_mfma_f32_16x16x32_bf16 v[114:117], v[146:149], a[144:147], v[114:117]\n\t"
    "s_lshl1_add_u32 m0, s70, 73728\n\t"
    "v_rcp_f32 v52, v52\n\t"
    "v_mfma_f32_16x16x32_bf16 v[118:121], v[146:149], a[148:151], v[118:121]\n\t"
    "s_add_u32 s60, s98, 163840\n\t"
    "ds_read_b128 v[146:149], v9 offset:11264\n\t"
    "v_mul_f32 v50, v50, v52\n\t"
    "v_mfma_f32_16x16x32_bf16 v[118:121], v[142:145], a[140:143], v[118:121]\n\t"
    "s_waitcnt lgkmcnt(3)\n\t"
    "ds_read_b128 v[142:145], v9 offset:3072\n\t"
    "v_lshlrev_b32 v56, 16, v41\n\t"
    "v_mfma_f32_16x16x32_bf16 v[122:125], v[150:153], a[136:139], v[122:125]\n\t"
    "buffer_load_dwordx4 a[240:243], v5, s[40:43], s50 offen offset:0 sc1\n\t"
    "v_mfma_f32_16x16x32_bf16 v[122:125], v[154:157], a[144:147], v[122:125]\n\t"
    "v_rcp_f32 v53, v53\n\t"
    "v_mfma_f32_16x16x32_bf16 v[126:129], v[154:157], a[148:151], v[126:129]\n\t"
    "ds_read_b128 v[154:157], v4 offset:49152\n\t"
    "v_mul_f32 v51, v51, v53\n\t"
    "v_mfma_f32_16x16x32_bf16 v[126:129], v[150:153], a[140:143], v[126:129]\n\t"
    "s_waitcnt lgkmcnt(3)\n\t"
    "ds_read_b128 v[150:153], v4 offset:40960\n\t"
    "v_cvt_pk_bf16_f32 v30, v50, v51\n\t"
    "v_mfma_f32_16x16x32_bf16 v[130:133], v[158:161], a[136:139], v[130:133]\n\t"
    "buffer_load_dwordx4 a[244:247], v6, s[40:43], s50 offen offset:0 sc1\n\t"
    "v_mfma_f32_16x16x32_bf16 v[130:133], v[162:165], a[144:147], v[130:133]\n\t"
    "v_and_b32 v57, 0xffff0000, v41\n\t"
    "v_add_f32 v54, v54, v56\n\t"
    "v_mfma_f32_16x16x32_bf16 v[134:137], v[162:165], a[148:151], v[134:137]\n\t"
    "ds_read_b128 v[162:165], v4 offset:50176\n\t"
    "v_add_f32 v55, v55, v57\n\t"
    "v_mfma_f32_16x16x32_bf16 v[134:137], v[158:161], a[140:143], v[134:137]\n\t"
    "s_waitcnt lgkmcnt(3)\n\t"
    "ds_read_b128 v[158:161], v4 offset:41984\n\t"
    "v_cvt_pk_bf16_f32 v55, v54, v55\n\t"
    "v_mfma_f32_16x16x32_bf16 v[78:81], v[142:145], a[136:139], v[78:81]\n\t"
    "buffer_load_dwordx4 a[248:251], v5, s[40:43], s51 offen offset:0 sc1\n\t"
    "v_mfma_f32_16x16x32_bf16 v[78:81], v[146:149], a[144:147], v[78:81]\n\t"
    "v_lshlrev_b32 v54, 16, v55\n\t"
    "v_and_b32 v55, 0xffff0000, v55\n\t"
    "v_mfma_f32_16x16x32_bf16 v[138:141], v[146:149], a[148:151], v[138:141]\n\t"
    "ds_read_b128 v[146:149], v4 offset:51200\n\t"
    "v_mul_f32 v56, 0xbfb8aa3b, v54\n\t"
    "v_mfma_f32_16x16x32_bf16 v[138:141], v[142:145], a[140:143], v[138:141]\n\t"
    "s_waitcnt lgkmcnt(3)\n\t"
    "s_waitcnt vmcnt(18)\n\t"
    "s_barrier\n\t"
    "v_mfma_f32_16x16x32_bf16 v[82:85], v[150:153], a[152:155], v[82:85]\n\t"
    "buffer_load_dwordx4 a[252:255], v6, s[40:43], s51 offen offset:0 sc1\n\t"
    "v_mfma_f32_16x16x32_bf16 v[82:85], v[154:157], a[160:163], v[82:85]\n\t"
    "ds_read_b128 v[142:145], v4 offset:43008\n\t"
    "v_mul_f32 v57, 0xbfb8aa3b, v55\n\t"
    "v_mfma_f32_16x16x32_bf16 v[86:89], v[154:157], a[164:167], v[86:89]\n\t"
    "ds_read_b128 v[154:157], v4 offset:52224\n\t"
    "v_lshlrev_b32 v50, 16, v32\n\t"
    "v_mfma_f32_16x16x32_bf16 v[86:89], v[150:153], a[156:159], v[86:89]\n\t"
    "s_waitcnt lgkmcnt(3)\n\t"
    "ds_read_b128 v[150:153], v4 offset:44032\n\t"
    "v_and_b32 v51, 0xffff0000, v32\n\t"
    "v_mfma_f32_16x16x32_bf16 v[90:93], v[158:161], a[152:155], v[90:93]\n\t"
    "buffer_load_dwordx4 v[176:179], v5, s[40:43], s52 offen offset:0 sc1\n\t"
    "v_mfma_f32_16x16x32_bf16 v[90:93], v[162:165], a[160:163], v[90:93]\n\t"
    "v_exp_f32 v56, v56\n\t"
    "v_mfma_f32_16x16x32_bf16 v[94:97], v[162:165], a[164:167], v[94:97]\n\t"
    "ds_read_b128 v[162:165], v4 offset:53248\n\t"
    "v_add_f32 v56, 1.0, v56\n\t"
    "v_mfma_f32_16x16x32_bf16 v[94:97], v[158:161], a[156:159], v[94:97]\n\t"
    "s_waitcnt lgkmcnt(3)\n\t"
    "ds_read_b128 v[158:161], v4 offset:45056\n\t"
    "v_lshlrev_b32 v52, 16, v42\n\t"
    "v_mfma_f32_16x16x32_bf16 v[98:101], v[142:145], a[152:155], v[98:101]\n\t"
    "buffer_load_dwordx4 v[180:183], v6, s[40:43], s52 offen offset:0 sc1\n\t"
    "v_mfma_f32_16x16x32_bf16 v[98:101], v[146:149], a[160:163], v[98:101]\n\t"
    "v_exp_f32 v57, v57\n\t"
    "v_mfma_f32_16x16x32_bf16 v[102:105], v[146:149], a[164:167], v[102:105]\n\t"
    "ds_read_b128 v[146:149], v4 offset:54272\n\t"
    "v_add_f32 v57, 1.0, v57\n\t"
    "v_mfma_f32_16x16x32_bf16 v[102:105], v[142:145], a[156:159], v[102:105]\n\t"
    "s_waitcnt lgkmcnt(3)\n\t"
    "ds_read_b128 v[142:145], v4 offset:46080\n\t"
    "v_and_b32 v53, 0xffff0000, v42\n\t"
    "v_mfma_f32_16x16x32_bf16 v[106:109], v[150:153], a[152:155], v[106:109]\n\t"
    "buffer_load_dwordx4 v[184:187], v5, s[40:43], s53 offen offset:0 sc1\n\t"
    "v_mfma_f32_16x16x32_bf16 v[106:109], v[154:157], a[160:163], v[106:109]\n\t"
    "v_rcp_f32 v56, v56\n\t"
    "v_mfma_f32_16x16x32_bf16 v[110:113], v[154:157], a[164:167], v[110:113]\n\t"
    "ds_read_b128 v[154:157], v4 offset:55296\n\t"
    "v_mul_f32 v54, v54, v56\n\t"
    "v_mfma_f32_16x16x32_bf16 v[110:113], v[150:153], a[156:159], v[110:113]\n\t"
    "s_waitcnt lgkmcnt(3)\n\t"
    "ds_read_b128 v[150:153], v4 offset:47104\n\t"
    "v_add_f32 v50, v50, v52\n\t"
    "v_mfma_f32_16x16x32_bf16 v[114:117], v[158:161], a[152:155], v[114:117]\n\t"
    "buffer_load_dwordx4 v[188:191], v6, s[40:43], s53 offen offset:0 sc1\n\t"
    "v_mfma_f32_16x16x32_bf16 v[114:117], v[162:165], a[160:163], v[114:117]\n\t"
    "v_rcp_f32 v57, v57\n\t"
    "v_mfma_f32_16x16x32_bf16 v[118:121], v[162:165], a[164:167], v[118:121]\n\t"
    "ds_read_b128 v[162:165], v4 offset:56320\n\t"
    "v_mul_f32 v55, v55, v57\n\t"
    "v_mfma_f32_16x16x32_bf16 v[118:121], v[158:161], a[156:159], v[118:121]\n\t"
    "s_waitcnt lgkmcnt(3)\n\t"
    "ds_read_b128 v[158:161], v4 offset:48128\n\t"
    "v_cvt_pk_bf16_f32 v31, v54, v55\n\t"
    "v_mfma_f32_16x16x32_bf16 v[122:125], v[142:145], a[152:155], v[122:125]\n\t"
    "buffer_load_dwordx4 v4, s[40:43], s60 offen offset:0 sc1 lds\n\t"
    "v_mfma_f32_16x16x32_bf16 v[122:125], v[146:149], a[160:163], v[122:125]\n\t"
    "v_add_f32 v51, v51, v53\n\t"
    "v_cvt_pk_bf16_f32 v51, v50, v51\n\t"
    "v_mfma_f32_16x16x32_bf16 v[126:129], v[146:149], a[164:167], v[126:129]\n\t"
    "ds_read_b128 v[146:149], v4 offset:1024\n\t"
    "v_lshlrev_b32 v50, 16, v51\n\t"
    "v_mfma_f32_16x16x32_bf16 v[126:129], v[142:145], a[156:159], v[126:129]\n\t"
    "s_waitcnt lgkmcnt(3)\n\t"
    "ds_read_b128 v[142:145], v4 offset:0\n\t"
    "v_and_b32 v51, 0xffff0000, v51\n\t"
    "v_mfma_f32_16x16x32_bf16 v[130:133], v[150:153], a[152:155], v[130:133]\n\t"
    "buffer_load_dwordx4 v4, s[40:43], s60 offen offset:1024 sc1 lds\n\t"
    "v_mfma_f32_16x16x32_bf16 v[130:133], v[154:157], a[160:163], v[130:133]\n\t"
    "s_add_u32 m0, s86, 4294901760\n\t"
    "v_mul_f32 v52, 0xbfb8aa3b, v50\n\t"
    "v_mul_f32 v53, 0xbfb8aa3b, v51\n\t"
    "v_mfma_f32_16x16x32_bf16 v[134:137], v[154:157], a[164:167], v[134:137]\n\t"
    "s_add_u32 s60, s68, 204800\n\t"
    "ds_read_b128 v[154:157], v4 offset:3072\n\t"
    "v_lshlrev_b32 v54, 16, v33\n\t"
    "v_mfma_f32_16x16x32_bf16 v[134:137], v[150:153], a[156:159], v[134:137]\n\t"
    "s_waitcnt lgkmcnt(3)\n\t"
    "ds_read_b128 v[150:153], v4 offset:2048\n\t"
    "v_and_b32 v55, 0xffff0000, v33\n\t"
    "v_mfma_f32_16x16x32_bf16 v[78:81], v[158:161], a[152:155], v[78:81]\n\t"
    "buffer_load_dwordx4 v4, s[40:43], s60 offen offset:0 sc1 lds\n\t"
    "v_mfma_f32_16x16x32_bf16 v[78:81], v[162:165], a[160:163], v[78:81]\n\t"
    "v_exp_f32 v52, v52\n\t"
    "v_mfma_f32_16x16x32_bf16 v[138:141], v[162:165], a[164:167], v[138:141]\n\t"
    "ds_read_b128 v[162:165], v4 offset:5120\n\t"
    "v_add_f32 v52, 1.0, v52\n\t"
    "v_mfma_f32_16x16x32_bf16 v[138:141], v[158:161], a[156:159], v[138:141]\n\t"
    "s_waitcnt vmcnt(25) lgkmcnt(3)\n\t"
    "ds_read_b128 v[158:161], v4 offset:4096\n\t"
    "v_lshlrev_b32 v56, 16, v43\n\t"
    "v_mfma_f32_16x16x32_bf16 v[82:85], v[142:145], a[168:171], v[82:85]\n\t"
    "s_waitcnt vmcnt(24)\n\t"
    "buffer_load_dwordx4 v4, s[40:43], s60 offen offset:1024 sc1 lds\n\t"
    "v_mfma_f32_16x16x32_bf16 v[86:89], v[142:145], a[172:175], v[86:89]\n\t"
    "ds_read_b128 v[142:145], v4 offset:6144\n\t"
    "v_and_b32 v57, 0xffff0000, v43\n\t"
    "v_mfma_f32_16x16x32_bf16 v[90:93], v[146:149], a[168:171], v[90:93]\n\t"
    "v_exp_f32 v53, v53\n\t"
    "v_mfma_f32_16x16x32_bf16 v[94:97], v[146:149], a[172:175], v[94:97]\n\t"
    "KFP16:\n\t"
    "s_cbranch_execz KFR2\n\t"
    "s_waitcnt lgkmcnt(3)\n\t"
    "ds_read_b128 v[146:149], v4 offset:7168\n\t"
    "v_add_f32 v53, 1.0, v53\n\t"
    "v_mfma_f32_16x16x32_bf16 v[98:101], v[150:153], a[168:171], v[98:101]\n\t"
    "buffer_load_dwordx4 v4, s[40:43], s60 offen offset:2048 sc1 lds\n\t"
    "v_mfma_f32_16x16x32_bf16 v[102:105], v[150:153], a[172:175], v[102:105]\n\t"
    "ds_read_b128 v[150:153], v4 offset:8192\n\t"
    "v_add_f32 v54, v54, v56\n\t"
    "v_mfma_f32_16x16x32_bf16 v[106:109], v[154:157], a[168:171], v[106:109]\n\t"
    "v_rcp_f32 v52, v52\n\t"
    "v_mfma_f32_16x16x32_bf16 v[110:113], v[154:157], a[172:175], v[110:113]\n\t"
    "s_waitcnt lgkmcnt(3)\n\t"
    "ds_read_b128 v[154:157], v4 offset:16384\n\t"
    "v_mul_f32 v50, v50, v52\n\t"
    "v_mfma_f32_16x16x32_bf16 v[114:117], v[158:161], a[168:171], v[114:117]\n\t"
    "buffer_load_dwordx4 v4, s[40:43], s60 offen offset:3072 sc1 lds\n\t"
    "v_mfma_f32_16x16x32_bf16 v[118:121], v[158:161], a[172:175], v[118:121]\n\t"
    "s_add_u32 m0, s86, 4294963200\n\t"
    "ds_read_b128 v[158:161], v4 offset:9216\n\t"
    "v_mfma_f32_16x16x32_bf16 v[122:125], v[162:165], a[168:171], v[122:125]\n\t"
    "s_add_u32 s60, s69, 204800\n\t"
    "v_rcp_f32 v53, v53\n\t"
    "v_mfma_f32_16x16x32_bf16 v[126:129], v[162:165], a[172:175], v[126:129]\n\t"
    "s_waitcnt lgkmcnt(4)\n\t"
    "ds_read_b128 v[162:165], v4 offset:17408\n\t"
    "v_mul_f32 v51, v51, v53\n\t"
    "v_mfma_f32_16x16x32_bf16 v[130:133], v[142:145], a[168:171], v[130:133]\n\t"
    "buffer_load_dwordx4 v4, s[40:43], s60 offen offset:0 sc1 lds\n\t"
    "v_mfma_f32_16x16x32_bf16 v[134:137], v[142:145], a[172:175], v[134:137]\n\t"
    "s_waitcnt lgkmcnt(4)\n\t"
    "ds_read_b128 v[142:145], v4 offset:10240\n\t"
    "v_cvt_pk_bf16_f32 v32, v50, v51\n\t"
    "v_mfma_f32_16x16x32_bf16 v[78:81], v[146:149], a[168:171], v[78:81]\n\t"
    "v_add_f32 v55, v55, v57\n\t"
    "v_cvt_pk_bf16_f32 v55, v54, v55\n\t"
    "v_mfma_f32_16x16x32_bf16 v[138:141], v[146:149], a[172:175], v[138:141]\n\t"
    "s_waitcnt lgkmcnt(4)\n\t"
    "s_waitcnt vmcnt(15)\n\t"
    "s_barrier\n\t"
    "v_mfma_f32_16x16x32_bf16 v[82:85], v[150:153], a[176:179], v[82:85]\n\t"
    "s_waitcnt lgkmcnt(3)\n\t"
    "buffer_load_dwordx4 v4, s[40:43], s60 offen offset:1024 sc1 lds\n\t"
    "v_mfma_f32_16x16x32_bf16 v[82:85], v[154:157], a[184:187], v[82:85]\n\t"
    "v_cmp_ne_u32 vcc, s80, v59\n\t"
    "s_and_b32 s65, vcc_lo, 0x7f\n\t"
    "s_cbranch_scc1 LS5_%=\n\t"
    "LR5_%=:\n\t"
    "v_mfma_f32_16x16x32_bf16 v[86:89], v[154:157], a[188:191], v[86:89]\n\t"
    "ds_read_b128 v[146:149], v4 offset:18432\n\t"
    "v_lshlrev_b32 v54, 16, v55\n\t"
    "v_mfma_f32_16x16x32_bf16 v[86:89], v[150:153], a[180:183], v[86:89]\n\t"
    "s_waitcnt lgkmcnt(3)\n\t"
    "ds_read_b128 v[150:153], v4 offset:11264\n\t"
    "v_and_b32 v55, 0xffff0000, v55\n\t"
    "v_mfma_f32_16x16x32_bf16 v[90:93], v[158:161], a[176:179], v[90:93]\n\t"
    "s_waitcnt lgkmcnt(3)\n\t"
    "buffer_load_dwordx4 v4, s[40:43], s60 offen offset:2048 sc1 lds\n\t"
    "v_mfma_f32_16x16x32_bf16 v[90:93], v[162:165], a[184:187], v[90:93]\n\t"
    "s_not_b32 vcc_lo, s74\n\t"
    "ds_read_b128 v[154:157], v4 offset:19456\n\t"
    "v_mul_f32 v56, 0xbfb8aa3b, v54\n\t"
    "v_mfma_f32_16x16x32_bf16 v[94:97], v[162:165], a[188:191], v[94:97]\n\t"
    "s_not_b32 vcc_hi, s74\n\t"
    "ds_read_b128 v[162:165], v4 offset:20480\n\t"
    "v_mul_f32 v57, 0xbfb8aa3b, v55\n\t"
    "v_mfma_f32_16x16x32_bf16 v[94:97], v[158:161], a[180:183], v[94:97]\n\t"
    "s_waitcnt lgkmcnt(4)\n\t"
    "ds_read_b128 v[158:161], v4 offset:12288\n\t"
    "v_lshlrev_b32 v50, 16, v34\n\t"
    "v_mfma_f32_16x16x32_bf16 v[98:101], v[142:145], a[176:179], v[98:101]\n\t"
    "s_waitcnt lgkmcnt(4)\n\t"
    "buffer_load_dwordx4 v4, s[40:43], s60 offen offset:3072 sc1 lds\n\t"
    "v_mfma_f32_16x16x32_bf16 v[98:101], v[146:149], a[184:187], v[98:101]\n\t"
    "s_add_u32 m0, s86, 4294918144\n\t"
    "v_exp_f32 v56, v56\n\t"
    "v_mfma_f32_16x16x32_bf16 v[102:105], v[146:149], a[188:191], v[102:105]\n\t"
    "s_add_u32 s60, s97, 204800\n\t"
    "ds_read_b128 v[146:149], v4 offset:21504\n\t"
    "v_add_f32 v56, 1.0, v56\n\t"
    "v_mfma_f32_16x16x32_bf16 v[102:105], v[142:145], a[180:183], v[102:105]\n\t"
    "s_waitcnt lgkmcnt(4)\n\t"
    "ds_read_b128 v[142:145], v4 offset:13312\n\t"
    "v_and_b32 v51, 0xffff0000, v34\n\t"
    "v_mfma_f32_16x16x32_bf16 v[106:109], v[150:153], a[176:179], v[106:109]\n\t"
    "s_waitcnt lgkmcnt(4)\n\t"
    "buffer_load_dwordx4 v[192:195], v5, s[40:43], s54 offen offset:0 sc1\n\t"
    "v_mfma_f32_16x16x32_bf16 v[106:109], v[154:157], a[184:187], v[106:109]\n\t"
    "v_exp_f32 v57, v57\n\t"
    "v_mfma_f32_16x16x32_bf16 v[110:113], v[154:157], a[188:191], v[110:113]\n\t"
    "ds_read_b128 v[154:157], v4 offset:22528\n\t"
    "v_add_f32 v57, 1.0, v57\n\t"
    "v_mfma_f32_16x16x32_bf16 v[110:113], v[150:153], a[180:183], v[110:113]\n\t"
    "s_waitcnt lgkmcnt(3)\n\t"
    "ds_read_b128 v[150:153], v4 offset:14336\n\t"
    "v_lshlrev_b32 v52, 16, v44\n\t"
    "v_mfma_f32_16x16x32_bf16 v[114:117], v[158:161], a[176:179], v[114:117]\n\t"
    "buffer_load_dwordx4 v[196:199], v6, s[40:43], s54 offen offset:0 sc1\n\t"
    "v_mfma_f32_16x16x32_bf16 v[114:117], v[162:165], a[184:187], v[114:117]\n\t"
    "v_rcp_f32 v56, v56\n\t"
    "v_mfma_f32_16x16x32_bf16 v[118:121], v[162:165], a[188:191], v[118:121]\n\t"
    "ds_read_b128 v[162:165], v4 offset:23552\n\t"
    "v_mul_f32 v54, v54, v56\n\t"
    "v_mfma_f32_16x16x32_bf16 v[118:121], v[158:161], a[180:183], v[118:121]\n\t"
    "s_waitcnt lgkmcnt(3)\n\t"
    "ds_read_b128 v[158:161], v4 offset:15360\n\t"
    "v_and_b32 v53, 0xffff0000, v44\n\t"
    "v_mfma_f32_16x16x32_bf16 v[122:125], v[142:145], a[176:179], v[122:125]\n\t"
    "buffer_load_dwordx4 v[200:203], v5, s[40:43], s48 offen offset:1024 sc1\n\t"
    "v_mfma_f32_16x16x32_bf16 v[122:125], v[146:149], a[184:187], v[122:125]\n\t"
    "v_rcp_f32 v57, v57\n\t"
    "v_mfma_f32_16x16x32_bf16 v[126:129], v[146:149], a[188:191], v[126:129]\n\t"
    "ds_read_b128 v[146:149], v4 offset:32768\n\t"
    "v_mul_f32 v55, v55, v57\n\t"
    "v_mfma_f32_16x16x32_bf16 v[126:129], v[142:145], a[180:183], v[126:129]\n\t"
    "s_waitcnt lgkmcnt(3)\n\t"
    "ds_read_b128 v[142:145], v4 offset:24576\n\t"
    "v_cvt_pk_bf16_f32 v33, v54, v55\n\t"
    "v_mfma_f32_16x16x32_bf16 v[130:133], v[150:153], a[176:179], v[130:133]\n\t"
    "buffer_load_dwordx4 v[204:207], v6, s[40:43], s48 offen offset:1024 sc1\n\t"
    "v_mfma_f32_16x16x32_bf16 v[130:133], v[154:157], a[184:187], v[130:133]\n\t"
    "s_xor_b32 s48, s71, 0\n\t"
    "v_add_f32 v50, v50, v52\n\t"
    "v_add_f32 v51, v51, v53\n\t"
    "v_mfma_f32_16x16x32_bf16 v[134:137], v[154:157], a[188:191], v[134:137]\n\t"
    "s_mulk_i32 s48, 20480\n\t"
    "ds_read_b128 v[154:157], v4 offset:33792\n\t"
    "v_cvt_pk_bf16_f32 v51, v50, v51\n\t"
    "v_mfma_f32_16x16x32_bf16 v[134:137], v[150:153], a[180:183], v[134:137]\n\t"
    "s_waitcnt lgkmcnt(3)\n\t"
    "ds_read_b128 v[150:153], v4 offset:25600\n\t"
    "v_lshlrev_b32 v50, 16, v51\n\t"
    "v_mfma_f32_16x16x32_bf16 v[78:81], v[158:161], a[176:179], v[78:81]\n\t"
    "buffer_load_dwordx4 v[208:211], v5, s[40:43], s49 offen offset:1024 sc1\n\t"
    "v_mfma_f32_16x16x32_bf16 v[78:81], v[162:165], a[184:187], v[78:81]\n\t"
    "s_add_u32 s48, s48, s76\n\t"
    "v_and_b32 v51, 0xffff0000, v51\n\t"
    "v_mul_f32 v52, 0xbfb8aa3b, v50\n\t"
    "v_mfma_f32_16x16x32_bf16 v[138:141], v[162:165], a[188:191], v[138:141]\n\t"
    "ds_read_b128 v[162:165], v4 offset:34816\n\t"
    "v_mul_f32 v53, 0xbfb8aa3b, v51\n\t"
    "v_mfma_f32_16x16x32_bf16 v[138:141], v[158:161], a[180:183], v[138:141]\n\t"
    "s_waitcnt vmcnt(22) lgkmcnt(3)\n\t"
    "ds_read_b128 v[158:161], v4 offset:26624\n\t"
    "v_lshlrev_b32 v54, 16, v35\n\t"
    "v_mfma_f32_16x16x32_bf16 v[82:85], v[142:145], a[240:243], v[82:85]\n\t"
    "s_waitcnt vmcnt(20)\n\t"
    "buffer_load_dwordx4 v[212:215], v6, s[40:43], s49 offen offset:1024 sc1\n\t"
    "v_mfma_f32_16x16x32_bf16 v[82:85], v[146:149], a[248:251], v[82:85]\n\t"
    "s_waitcnt vmcnt(20)\n\t"
    "v_exp_f32 v52, v52\n\t"
    "v_mfma_f32_16x16x32_bf16 v[86:89], v[146:149], a[252:255], v[86:89]\n\t"
    "ds_read_b128 v[146:149], v4 offset:35840\n\t"
    "v_add_f32 v52, 1.0, v52\n\t"
    "v_mfma_f32_16x16x32_bf16 v[86:89], v[142:145], a[244:247], v[86:89]\n\t"
    "s_waitcnt lgkmcnt(3)\n\t"
    "ds_read_b128 v[142:145], v4 offset:27648\n\t"
    "v_and_b32 v55, 0xffff0000, v35\n\t"
    "v_mfma_f32_16x16x32_bf16 v[90:93], v[150:153], a[240:243], v[90:93]\n\t"
    "buffer_load_dwordx4 v4, s[40:43], s60 offen offset:0 sc1 lds\n\t"
    "v_mfma_f32_16x16x32_bf16 v[90:93], v[154:157], a[248:251], v[90:93]\n\t"
    "s_xor_b32 s49, s71, 1\n\t"
    "v_exp_f32 v53, v53\n\t"
    "v_mfma_f32_16x16x32_bf16 v[94:97], v[154:157], a[252:255], v[94:97]\n\t"
    "s_mulk_i32 s49, 20480\n\t"
    "ds_read_b128 v[154:157], v4 offset:36864\n\t"
    "v_add_f32 v53, 1.0, v53\n\t"
    "v_mfma_f32_16x16x32_bf16 v[94:97], v[150:153], a[244:247], v[94:97]\n\t"
    "s_waitcnt lgkmcnt(3)\n\t"
    "ds_read_b128 v[150:153], v4 offset:28672\n\t"
    "v_lshlrev_b32 v56, 16, v45\n\t"
    "v_mfma_f32_16x16x32_bf16 v[98:101], v[158:161], a[240:243], v[98:101]\n\t"
    "buffer_load_dwordx4 v4, s[40:43], s60 offen offset:1024 sc1 lds\n\t"
    "v_mfma_f32_16x16x32_bf16 v[98:101], v[162:165], a[248:251], v[98:101]\n\t"
    "s_add_u32 s49, s49, s76\n\t"
    "v_rcp_f32 v52, v52\n\t"
    "v_mfma_f32_16x16x32_bf16 v[102:105], v[162:165], a[252:255], v[102:105]\n\t"
    "ds_read_b128 v[162:165], v4 offset:37888\n\t"
    "v_mul_f32 v50, v50, v52\n\t"
    "v_mfma_f32_16x16x32_bf16 v[102:105], v[158:161], a[244:247], v[102:105]\n\t"
    "s_waitcnt lgkmcnt(3)\n\t"
    "ds_read_b128 v[158:161], v4 offset:29696\n\t"
    "v_and_b32 v57, 0xffff0000, v45\n\t"
    "v_mfma_f32_16x16x32_bf16 v[106:109], v[142:145], a[240:243], v[106:109]\n\t"
    "buffer_load_dwordx4 v4, s[40:43], s60 offen offset:2048 sc1 lds\n\t"
    "v_mfma_f32_16x16x32_bf16 v[106:109], v[146:149], a[248:251], v[106:109]\n\t"
    "v_rcp_f32 v53, v53\n\t"
    "v_mfma_f32_16x16x32_bf16 v[110:113], v[146:149], a[252:255], v[110:113]\n\t"
    "ds_read_b128 v[146:149], v4 offset:38912\n\t"
    "v_mul_f32 v51, v51, v53\n\t"
    "v_mfma_f32_16x16x32_bf16 v[110:113], v[142:145], a[244:247], v[110:113]\n\t"
    "s_waitcnt lgkmcnt(3)\n\t"
    "ds_read_b128 v[142:145], v4 offset:30720\n\t"
    "v_cvt_pk_bf16_f32 v34, v50, v51\n\t"
    "v_mfma_f32_16x16x32_bf16 v[114:117], v[150:153], a[240:243], v[114:117]\n\t"
    "buffer_load_dwordx4 v4, s[40:43], s60 offen offset:3072 sc1 lds\n\t"
    "v_mfma_f32_16x16x32_bf16 v[114:117], v[154:157], a[248:251], v[114:117]\n\t"
    "s_lshl1_add_u32 m0, s70, 114688\n\t"
    "v_add_f32 v54, v54, v56\n\t"
    "v_add_f32 v55, v55, v57\n\t"
    "v_mfma_f32_16x16x32_bf16 v[118:121], v[154:157], a[252:255], v[118:121]\n\t"
    "s_add_u32 s60, s98, 204800\n\t"
    "ds_read_b128 v[154:157], v4 offset:39936\n\t"
    "v_cvt_pk_bf16_f32 v55, v54, v55\n\t"
    "v_mfma_f32_16x16x32_bf16 v[118:121], v[150:153], a[244:247], v[118:121]\n\t"
    "s_waitcnt lgkmcnt(3)\n\t"
    "ds_read_b128 v[150:153], v4 offset:31744\n\t"
    "v_lshlrev_b32 v54, 16, v55\n\t"
    "v_mfma_f32_16x16x32_bf16 v[122:125], v[158:161], a[240:243], v[122:125]\n\t"
    "buffer_load_dwordx4 v4, s[40:43], s60 offen offset:0 sc1 lds\n\t"
    "v_mfma_f32_16x16x32_bf16 v[122:125], v[162:165], a[248:251], v[122:125]\n\t"
    "v_and_b32 v55, 0xffff0000, v55\n\t"
    "v_mul_f32 v56, 0xbfb8aa3b, v54\n\t"
    "v_mfma_f32_16x16x32_bf16 v[126:129], v[162:165], a[252:255], v[126:129]\n\t"
    "ds_read_b128 v[162:165], v8 offset:0\n\t"
    "v_mul_f32 v57, 0xbfb8aa3b, v55\n\t"
    "v_mfma_f32_16x16x32_bf16 v[126:129], v[158:161], a[244:247], v[126:129]\n\t"
    "s_waitcnt lgkmcnt(3)\n\t"
    "ds_read_b128 v[158:161], v4 offset:57344\n\t"
    "v_lshlrev_b32 v50, 16, v36\n\t"
    "v_mfma_f32_16x16x32_bf16 v[130:133], v[142:145], a[240:243], v[130:133]\n\t"
    "buffer_load_dwordx4 v4, s[40:43], s60 offen offset:1024 sc1 lds\n\t"
    "v_mfma_f32_16x16x32_bf16 v[130:133], v[146:149], a[248:251], v[130:133]\n\t"
    "s_add_u32 s60, s68, 0\n\t"
    "v_exp_f32 v56, v56\n\t"
    "v_mfma_f32_16x16x32_bf16 v[134:137], v[146:149], a[252:255], v[134:137]\n\t"
    "s_add_u32 m0, s70, 159744\n\t"
    "ds_read_b128 v[146:149], v8 offset:1024\n\t"
    "v_add_f32 v56, 1.0, v56\n\t"
    "v_mfma_f32_16x16x32_bf16 v[134:137], v[142:145], a[244:247], v[134:137]\n\t"
    "s_waitcnt lgkmcnt(3)\n\t"
    "ds_read_b128 v[142:145], v4 offset:58368\n\t"
    "v_and_b32 v51, 0xffff0000, v36\n\t"
    "v_mfma_f32_16x16x32_bf16 v[78:81], v[150:153], a[240:243], v[78:81]\n\t"
    "buffer_load_dwordx4 v[216:219], v5, s[40:43], s50 offen offset:1024 sc1\n\t"
    "v_mfma_f32_16x16x32_bf16 v[78:81], v[154:157], a[248:251], v[78:81]\n\t"
    "s_add_u32 s60, s60, s72\n\t"
    "v_exp_f32 v57, v57\n\t"
    "v_mfma_f32_16x16x32_bf16 v[138:141], v[154:157], a[252:255], v[138:141]\n\t"
    "ds_read_b128 v[154:157], v8 offset:2048\n\t"
    "v_add_f32 v57, 1.0, v57\n\t"
    "v_mfma_f32_16x16x32_bf16 v[138:141], v[150:153], a[244:247], v[138:141]\n\t"
    "s_waitcnt lgkmcnt(3)\n\t"
    "s_waitcnt vmcnt(13)\n\t"
    "s_barrier\n\t"
    "v_mfma_f32_16x16x32_bf16 v[82:85], v[158:161], v[176:179], v[82:85]\n\t"
    "buffer_load_dwordx4 v[220:223], v6, s[40:43], s50 offen offset:1024 sc1\n\t"
    "v_mfma_f32_16x16x32_bf16 v[82:85], v[162:165], v[184:187], v[82:85]\n\t"
    "ds_read_b128 v[150:153], v4 offset:59392\n\t"
    "v_lshlrev_b32 v52, 16, v46\n\t"
    "v_mfma_f32_16x16x32_bf16 v[86:89], v[162:165], v[188:191], v[86:89]\n\t"
    "ds_read_b128 v[162:165], v8 offset:3072\n\t"
    "v_and_b32 v53, 0xffff0000, v46\n\t"
    "v_mfma_f32_16x16x32_bf16 v[86:89], v[158:161], v[180:183], v[86:89]\n\t"
    "s_waitcnt lgkmcnt(3)\n\t"
    "ds_read_b128 v[158:161], v4 offset:60416\n\t"
    "v_add_f32 v50, v50, v52\n\t"
    "v_mfma_f32_16x16x32_bf16 v[90:93], v[142:145], v[176:179], v[90:93]\n\t"
    "buffer_load_dwordx4 v[224:227], v5, s[40:43], s51 offen offset:1024 sc1\n\t"
    "v_mfma_f32_16x16x32_bf16 v[90:93], v[146:149], v[184:187], v[90:93]\n\t"
    "v_rcp_f32 v56, v56\n\t"
    "v_mfma_f32_16x16x32_bf16 v[94:97], v[146:149], v[188:191], v[94:97]\n\t"
    "ds_read_b128 v[146:149], v8 offset:4096\n\t"
    "v_mul_f32 v54, v54, v56\n\t"
    "v_mfma_f32_16x16x32_bf16 v[94:97], v[142:145], v[180:183], v[94:97]\n\t"
    "s_waitcnt lgkmcnt(3)\n\t"
    "ds_read_b128 v[142:145], v4 offset:61440\n\t"
    "v_add_f32 v51, v51, v53\n\t"
    "v_mfma_f32_16x16x32_bf16 v[98:101], v[150:153], v[176:179], v[98:101]\n\t"
    "buffer_load_dwordx4 v[228:231], v6, s[40:43], s51 offen offset:1024 sc1\n\t"
    "v_mfma_f32_16x16x32_bf16 v[98:101], v[154:157], v[184:187], v[98:101]\n\t"
    "v_rcp_f32 v57, v57\n\t"
    "v_mfma_f32_16x16x32_bf16 v[102:105], v[154:157], v[188:191], v[102:105]\n\t"
    "ds_read_b128 v[154:157], v8 offset:5120\n\t"
    "v_mul_f32 v55, v55, v57\n\t"
    "v_mfma_f32_16x16x32_bf16 v[102:105], v[150:153], v[180:183], v[102:105]\n\t"
    "s_waitcnt lgkmcnt(3)\n\t"
    "ds_read_b128 v[150:153], v4 offset:62464\n\t"
    "v_cvt_pk_bf16_f32 v35, v54, v55\n\t"
    "v_mfma_f32_16x16x32_bf16 v[106:109], v[158:161], v[176:179], v[106:109]\n\t"
    "buffer_load_dwordx4 v[232:235], v5, s[40:43], s52 offen offset:1024 sc1\n\t"
    "v_mfma_f32_16x16x32_bf16 v[106:109], v[162:165], v[184:187], v[106:109]\n\t"
    "v_cvt_pk_bf16_f32 v51, v50, v51\n\t"
    "v_lshlrev_b32 v50, 16, v51\n\t"
    "v_mfma_f32_16x16x32_bf16 v[110:113], v[162:165], v[188:191], v[110:113]\n\t"
    "ds_read_b128 v[162:165], v8 offset:6144\n\t"
    "v_and_b32 v51, 0xffff0000, v51\n\t"
    "v_mfma_f32_16x16x32_bf16 v[110:113], v[158:161], v[180:183], v[110:113]\n\t"
    "s_waitcnt lgkmcnt(3)\n\t"
    "ds_read_b128 v[158:161], v4 offset:63488\n\t"
    "v_mul_f32 v52, 0xbfb8aa3b, v50\n\t"
    "v_mfma_f32_16x16x32_bf16 v[114:117], v[142:145], v[176:179], v[114:117]\n\t"
    "buffer_load_dwordx4 v[236:239], v6, s[40:43], s52 offen offset:1024 sc1\n\t"
    "v_mfma_f32_16x16x32_bf16 v[114:117], v[146:149], v[184:187], v[114:117]\n\t"
    "v_mul_f32 v53, 0xbfb8aa3b, v51\n\t"
    "v_lshlrev_b32 v54, 16, v37\n\t"
    "v_mfma_f32_16x16x32_bf16 v[118:121], v[146:149], v[188:191], v[118:121]\n\t"
    "ds_read_b128 v[146:149], v8 offset:7168\n\t"
    "v_and_b32 v55, 0xffff0000, v37\n\t"
    "v_mfma_f32_16x16x32_bf16 v[118:121], v[142:145], v[180:183], v[118:121]\n\t"
    "s_waitcnt lgkmcnt(3)\n\t"
    "ds_read_b128 v[142:145], v4 offset:64512\n\t"
    "v_lshlrev_b32 v56, 16, v47\n\t"
    "v_mfma_f32_16x16x32_bf16 v[122:125], v[150:153], v[176:179], v[122:125]\n\t"
    "buffer_load_dwordx4 v[240:243], v5, s[40:43], s53 offen offset:1024 sc1\n\t"
    "v_mfma_f32_16x16x32_bf16 v[122:125], v[154:157], v[184:187], v[122:125]\n\t"
    "v_exp_f32 v52, v52\n\t"
    "v_mfma_f32_16x16x32_bf16 v[126:129], v[154:157], v[188:191], v[126:129]\n\t"
    "ds_read_b128 v[154:157], v8 offset:9216\n\t"
    "v_add_f32 v52, 1.0, v52\n\t"
    "v_mfma_f32_16x16x32_bf16 v[126:129], v[150:153], v[180:183], v[126:129]\n\t"
    "s_waitcnt lgkmcnt(3)\n\t"
    "ds_read_b128 v[150:153], v8 offset:8192\n\t"
    "v_and_b32 v57, 0xffff0000, v47\n\t"
    "v_mfma_f32_16x16x32_bf16 v[130:133], v[158:161], v[176:179], v[130:133]\n\t"
    "buffer_load_dwordx4 v[244:247], v6, s[40:43], s53 offen offset:1024 sc1\n\t"
    "v_mfma_f32_16x16x32_bf16 v[130:133], v[162:165], v[184:187], v[130:133]\n\t"
    "v_exp_f32 v53, v53\n\t"
    "v_mfma_f32_16x16x32_bf16 v[134:137], v[162:165], v[188:191], v[134:137]\n\t"
    "ds_read_b128 v[162:165], v8 offset:11264\n\t"
    "v_add_f32 v53, 1.0, v53\n\t"
    "v_mfma_f32_16x16x32_bf16 v[134:137], v[158:161], v[180:183], v[134:137]\n\t"
    "s_waitcnt lgkmcnt(3)\n\t"
    "ds_read_b128 v[158:161], v8 offset:10240\n\t"
    "v_add_f32 v54, v54, v56\n\t"
    "v_mfma_f32_16x16x32_bf16 v[78:81], v[142:145], v[176:179], v[78:81]\n\t"
    "buffer_load_dwordx4 v4, s[40:43], s60 offen offset:0 sc1 lds\n\t"
    "v_mfma_f32_16x16x32_bf16 v[78:81], v[146:149], v[184:187], v[78:81]\n\t"
    "s_add_u32 s60, s69, 0\n\t"
    "v_rcp_f32 v52, v52\n\t"
    "v_mfma_f32_16x16x32_bf16 v[138:141], v[146:149], v[188:191], v[138:141]\n\t"
    "s_add_u32 m0, s70, 122880\n\t"
    "ds_read_b128 v[146:149], v8 offset:13312\n\t"
    "v_mul_f32 v50, v50, v52\n\t"
    "v_mfma_f32_16x16x32_bf16 v[138:141], v[142:145], v[180:183], v[138:141]\n\t"
    "s_waitcnt vmcnt(20) lgkmcnt(3)\n\t"
    "s_add_u32 s60, s60, s72\n\t"
    "ds_read_b128 v[142:145], v8 offset:12288\n\t"
    "v_mfma_f32_16x16x32_bf16 v[82:85], v[150:153], v[192:195], v[82:85]\n\t"
    "s_waitcnt vmcnt(19)\n\t"
    "buffer_load_dwordx4 v4, s[40:43], s60 offen offset:0 sc1 lds\n\t"
    "v_mfma_f32_16x16x32_bf16 v[86:89], v[150:153], v[196:199], v[86:89]\n\t"
    "s_add_u32 s60, s97, 0\n\t"
    "ds_read_b128 v[150:153], v8 offset:14336\n\t"
    "v_mfma_f32_16x16x32_bf16 v[90:93], v[154:157], v[192:195], v[90:93]\n\t"
    "s_add_u32 m0, s70, 126976\n\t"
    "v_rcp_f32 v53, v53\n\t"
    "v_mfma_f32_16x16x32_bf16 v[94:97], v[154:157], v[196:199], v[94:97]\n\t"
    "s_waitcnt lgkmcnt(3)\n\t"
    "s_add_u32 s60, s60, s72\n\t"
    "ds_read_b128 v[154:157], v8 offset:15360\n\t"
    "v_mfma_f32_16x16x32_bf16 v[98:101], v[158:161], v[192:195], v[98:101]\n\t"
    "buffer_load_dwordx4 v4, s[40:43], s60 offen offset:0 sc1 lds\n\t"
    "v_mfma_f32_16x16x32_bf16 v[102:105], v[158:161], v[196:199], v[102:105]\n\t"
    "s_add_u32 s60, s98, 0\n\t"
    "ds_read_b128 v[158:161], v8 offset:16384\n\t"
    "v_mfma_f32_16x16x32_bf16 v[106:109], v[162:165], v[192:195], v[106:109]\n\t"
    "s_add_u32 m0, s70, 131072\n\t"
    "v_mul_f32 v51, v51, v53\n\t"
    "v_cvt_pk_bf16_f32 v36, v50, v51\n\t"
    "v_mfma_f32_16x16x32_bf16 v[110:113], v[162:165], v[196:199], v[110:113]\n\t"
    "s_waitcnt lgkmcnt(3)\n\t"
    "s_add_u32 s60, s60, s73\n\t"
    "ds_read_b128 v[162:165], v8 offset:24576\n\t"
    "v_mfma_f32_16x16x32_bf16 v[114:117], v[142:145], v[192:195], v[114:117]\n\t"
    "buffer_load_dwordx4 v[248:251], v5, s[40:43], s54 offen offset:1024 sc1\n\t"
    "v_mfma_f32_16x16x32_bf16 v[118:121], v[142:145], v[196:199], v[118:121]\n\t"
    "ds_read_b128 v[142:145], v8 offset:17408\n\t"
    "v_add_f32 v55, v55, v57\n\t"
    "v_mfma_f32_16x16x32_bf16 v[122:125], v[146:149], v[192:195], v[122:125]\n\t"
    "v_cvt_pk_bf16_f32 v55, v54, v55\n\t"
    "v_lshlrev_b32 v54, 16, v55\n\t"
    "v_mfma_f32_16x16x32_bf16 v[126:129], v[146:149], v[196:199], v[126:129]\n\t"
    "s_waitcnt lgkmcnt(4)\n\t"
    "ds_read_b128 v[146:149], v8 offset:25600\n\t"
    "v_and_b32 v55, 0xffff0000, v55\n\t"
    "v_mfma_f32_16x16x32_bf16 v[130:133], v[150:153], v[192:195], v[130:133]\n\t"
    "buffer_load_dwordx4 v[252:255], v6, s[40:43], s54 offen offset:1024 sc1\n\t"
    "v_mfma_f32_16x16x32_bf16 v[134:137], v[150:153], v[196:199], v[134:137]\n\t"
    "s_waitcnt lgkmcnt(4)\n\t"
    "ds_read_b128 v[150:153], v8 offset:18432\n\t"
    "v_mul_f32 v56, 0xbfb8aa3b, v54\n\t"
    "v_mfma_f32_16x16x32_bf16 v[78:81], v[154:157], v[192:195], v[78:81]\n\t"
    "v_mul_f32 v57, 0xbfb8aa3b, v55\n\t"
    "v_lshlrev_b32 v50, 16, v38\n\t"
    "v_mfma_f32_16x16x32_bf16 v[138:141], v[154:157], v[196:199], v[138:141]\n\t"
    "s_waitcnt lgkmcnt(4)\n\t"
    "s_waitcnt vmcnt(13)\n\t"
    "s_barrier\n\t"
    "v_mfma_f32_16x16x32_bf16 v[82:85], v[158:161], v[200:203], v[82:85]\n\t"
    "s_waitcnt lgkmcnt(3)\n\t"
    "buffer_load_dwordx4 v4, s[40:43], s60 offen offset:0 sc1 lds\n\t"
    "v_mfma_f32_16x16x32_bf16 v[82:85], v[162:165], v[208:211], v[82:85]\n\t"
    "s_add_u32 s60, s68, 40960\n\t"
    "ds_read_b128 v[154:157], v8 offset:26624\n\t"
    "v_mfma_f32_16x16x32_bf16 v[86:89], v[162:165], v[212:215], v[86:89]\n\t"
    "s_add_u32 m0, s70, 135168\n\t"
    "ds_read_b128 v[162:165], v8 offset:31744\n\t"
    "v_mfma_f32_16x16x32_bf16 v[86:89], v[158:161], v[204:207], v[86:89]\n\t"
    "s_waitcnt lgkmcnt(4)\n\t"
    "s_add_u32 s60, s60, s72\n\t"
    "ds_read_b128 v[158:161], v8 offset:23552\n\t"
    "v_mfma_f32_16x16x32_bf16 v[90:93], v[142:145], v[200:203], v[90:93]\n\t"
    "s_waitcnt lgkmcnt(4)\n\t"
    "buffer_load_dwordx4 v4, s[40:43], s60 offen offset:0 sc1 lds\n\t"
    "v_mfma_f32_16x16x32_bf16 v[90:93], v[146:149], v[208:211], v[90:93]\n\t"
    "s_add_u32 s60, s69, 40960\n\t"
    "v_exp_f32 v56, v56\n\t"
    "v_mfma_f32_16x16x32_bf16 v[94:97], v[146:149], v[212:215], v[94:97]\n\t"
    "s_add_u32 m0, s70, 139264\n\t"
    "ds_read_b128 v[146:149], v9 offset:20480\n\t"
    "v_add_f32 v56, 1.0, v56\n\t"
    "v_mfma_f32_16x16x32_bf16 v[94:97], v[142:145], v[204:207], v[94:97]\n\t"
    "s_waitcnt lgkmcnt(4)\n\t"
    "s_add_u32 s60, s60, s72\n\t"
    "ds_read_b128 v[142:145], v9 offset:12288\n\t"
    "v_mfma_f32_16x16x32_bf16 v[98:101], v[150:153], v[200:203], v[98:101]\n\t"
    "s_waitcnt lgkmcnt(4)\n\t"
    "buffer_load_dwordx4 v4, s[40:43], s60 offen offset:0 sc1 lds\n\t"
    "v_mfma_f32_16x16x32_bf16 v[98:101], v[154:157], v[208:211], v[98:101]\n\t"
    "s_add_u32 s60, s97, 40960\n\t"
    "v_exp_f32 v57, v57\n\t"
    "v_mfma_f32_16x16x32_bf16 v[102:105], v[154:157], v[212:215], v[102:105]\n\t"
    "s_add_u32 m0, s70, 0\n\t"
    "ds_read_b128 v[154:157], v9 offset:21504\n\t"
    "v_add_f32 v57, 1.0, v57\n\t"
    "v_mfma_f32_16x16x32_bf16 v[102:105], v[150:153], v[204:207], v[102:105]\n\t"
    "s_waitcnt lgkmcnt(3)\n\t"
    "s_add_u32 s60, s60, s72\n\t"
    "ds_read_b128 v[150:153], v9 offset:13312\n\t"
    "v_mfma_f32_16x16x32_bf16 v[78:81], v[158:161], v[200:203], v[78:81]\n\t"
    "buffer_load_dwordx4 v4, s[40:43], s60 offen offset:0 sc1 lds\n\t"
    "v_mfma_f32_16x16x32_bf16 v[78:81], v[162:165], v[208:211], v[78:81]\n\t"
    "s_add_u32 s60, s68, 81920\n\t"
    "v_rcp_f32 v56, v56\n\t"
    "v_mfma_f32_16x16x32_bf16 v[138:141], v[162:165], v[212:215], v[138:141]\n\t"
    "s_add_u32 m0, s70, 4096\n\t"
    "ds_read_b128 v[162:165], v9 offset:22528\n\t"
    "v_mul_f32 v54, v54, v56\n\t"
    "v_mfma_f32_16x16x32_bf16 v[138:141], v[158:161], v[204:207], v[138:141]\n\t"
    "s_waitcnt vmcnt(16) lgkmcnt(3)\n\t"
    "s_add_u32 s60, s60, s72\n\t"
    "ds_read_b128 v[158:161], v9 offset:14336\n\t"
    "v_mfma_f32_16x16x32_bf16 v[82:85], v[142:145], v[216:219], v[82:85]\n\t"
    "s_waitcnt vmcnt(14)\n\t"
    "buffer_load_dwordx4 v4, s[40:43], s60 offen offset:0 sc1 lds\n\t"
    "v_mfma_f32_16x16x32_bf16 v[82:85], v[146:149], v[224:227], v[82:85]\n\t"
    "s_waitcnt vmcnt(14)\n\t"
    "s_add_u32 m0, s70, 40960\n\t"
    "v_and_b32 v51, 0xffff0000, v38\n\t"
    "v_mfma_f32_16x16x32_bf16 v[86:89], v[146:149], v[228:231], v[86:89]\n\t"
    "s_add_u32 s60, s69, 81920\n\t"
    "ds_read_b128 v[146:149], v9 offset:27648\n\t"
    "v_mfma_f32_16x16x32_bf16 v[86:89], v[142:145], v[220:223], v[86:89]\n\t"
    "s_waitcnt lgkmcnt(3)\n\t"
    "s_add_u32 s60, s60, s72\n\t"
    "ds_read_b128 v[142:145], v9 offset:19456\n\t"
    "v_mfma_f32_16x16x32_bf16 v[90:93], v[150:153], v[216:219], v[90:93]\n\t"
    "buffer_load_dwordx4 v4, s[40:43], s60 offen offset:0 sc1 lds\n\t"
    "v_mfma_f32_16x16x32_bf16 v[90:93], v[154:157], v[224:227], v[90:93]\n\t"
    "s_add_u32 s60, s97, 81920\n\t"
    "v_rcp_f32 v57, v57\n\t"
    "v_mfma_f32_16x16x32_bf16 v[94:97], v[154:157], v[228:231], v[94:97]\n\t"
    "s_add_u32 m0, s70, 45056\n\t"
    "ds_read_b128 v[154:157], v8 offset:40960\n\t"
    "v_mul_f32 v55, v55, v57\n\t"
    "v_mfma_f32_16x16x32_bf16 v[94:97], v[150:153], v[220:223], v[94:97]\n\t"
    "s_waitcnt lgkmcnt(3)\n\t"
    "s_add_u32 s60, s60, s72\n\t"
    "ds_read_b128 v[150:153], v8 offset:32768\n\t"
    "v_mfma_f32_16x16x32_bf16 v[98:101], v[158:161], v[216:219], v[98:101]\n\t"
    "buffer_load_dwordx4 v4, s[40:43], s60 offen offset:0 sc1 lds\n\t"
    "v_mfma_f32_16x16x32_bf16 v[98:101], v[162:165], v[224:227], v[98:101]\n\t"
    "s_add_u32 s60, s98, 81920\n\t"
    "v_cvt_pk_bf16_f32 v37, v54, v55\n\t"
    "v_lshlrev_b32 v52, 16, v48\n\t"
    "v_mfma_f32_16x16x32_bf16 v[102:105], v[162:165], v[228:231], v[102:105]\n\t"
    "s_add_u32 m0, s70, 49152\n\t"
    "ds_read_b128 v[162:165], v8 offset:41984\n\t"
    "v_and_b32 v53, 0xffff0000, v48\n\t"
    "v_mfma_f32_16x16x32_bf16 v[102:105], v[158:161], v[220:223], v[102:105]\n\t"
    "s_waitcnt lgkmcnt(3)\n\t"
    "s_add_u32 s60, s60, s73\n\t"
    "ds_read_b128 v[158:161], v8 offset:33792\n\t"
    "v_mfma_f32_16x16x32_bf16 v[78:81], v[142:145], v[216:219], v[78:81]\n\t"
    "buffer_load_dwordx4 v4, s[40:43], s60 offen offset:0 sc1 lds\n\t"
    "v_mfma_f32_16x16x32_bf16 v[78:81], v[146:149], v[224:227], v[78:81]\n\t"
    "s_add_u32 s60, s68, 122880\n\t"
    "v_add_f32 v50, v50, v52\n\t"
    "v_add_f32 v51, v51, v53\n\t"
    "v_mfma_f32_16x16x32_bf16 v[138:141], v[146:149], v[228:231], v[138:141]\n\t"
    "s_add_u32 m0, s70, 53248\n\t"
    "ds_read_b128 v[146:149], v8 offset:43008\n\t"
    "v_cvt_pk_bf16_f32 v51, v50, v51\n\t"
    "v_mfma_f32_16x16x32_bf16 v[138:141], v[142:145], v[220:223], v[138:141]\n\t"
    "s_waitcnt vmcnt(16) lgkmcnt(3)\n\t"
    "s_add_u32 s60, s60, s72\n\t"
    "ds_read_b128 v[142:145], v8 offset:34816\n\t"
    "v_mfma_f32_16x16x32_bf16 v[82:85], v[150:153], v[232:235], v[82:85]\n\t"
    "s_waitcnt vmcnt(14)\n\t"
    "buffer_load_dwordx4 v4, s[40:43], s60 offen offset:0 sc1 lds\n\t"
    "v_mfma_f32_16x16x32_bf16 v[82:85], v[154:157], v[240:243], v[82:85]\n\t"
    "s_waitcnt vmcnt(14)\n\t"
    "s_add_u32 s60, s69, 122880\n\t"
    "v_lshlrev_b32 v50, 16, v51\n\t"
    "v_mfma_f32_16x16x32_bf16 v[86:89], v[154:157], v[244:247], v[86:89]\n\t"
    "s_add_u32 m0, s70, 8192\n\t"
    "ds_read_b128 v[154:157], v8 offset:48128\n\t"
    "v_mfma_f32_16x16x32_bf16 v[86:89], v[150:153], v[236:239], v[86:89]\n\t"
    "s_waitcnt lgkmcnt(3)\n\t"
    "s_add_u32 s60, s60, s72\n\t"
    "ds_read_b128 v[150:153], v8 offset:39936\n\t"
    "v_mfma_f32_16x16x32_bf16 v[90:93], v[158:161], v[232:235], v[90:93]\n\t"
    "buffer_load_dwordx4 v4, s[40:43], s60 offen offset:0 sc1 lds\n\t"
    "v_mfma_f32_16x16x32_bf16 v[90:93], v[162:165], v[240:243], v[90:93]\n\t"
    "s_add_u32 s60, s97, 122880\n\t"
    "v_and_b32 v51, 0xffff0000, v51\n\t"
    "v_mul_f32 v52, 0xbfb8aa3b, v50\n\t"
    "v_mfma_f32_16x16x32_bf16 v[94:97], v[162:165], v[244:247], v[94:97]\n\t"
    "s_add_u32 m0, s70, 12288\n\t"
    "ds_read_b128 v[162:165], v8 offset:50176\n\t"
    "v_mul_f32 v53, 0xbfb8aa3b, v51\n\t"
    "v_mfma_f32_16x16x32_bf16 v[94:97], v[158:161], v[236:239], v[94:97]\n\t"
    "s_waitcnt lgkmcnt(3)\n\t"
    "s_add_u32 s60, s60, s72\n\t"
    "ds_read_b128 v[158:161], v8 offset:49152\n\t"
    "v_mfma_f32_16x16x32_bf16 v[98:101], v[142:145], v[232:235], v[98:101]\n\t"
    "buffer_load_dwordx4 v4, s[40:43], s60 offen offset:0 sc1 lds\n\t"
    "v_mfma_f32_16x16x32_bf16 v[98:101], v[146:149], v[240:243], v[98:101]\n\t"
    "s_add_u32 s60, s68, 163840\n\t"
    "v_exp_f32 v52, v52\n\t"
    "v_mfma_f32_16x16x32_bf16 v[102:105], v[146:149], v[244:247], v[102:105]\n\t"
    "s_add_u32 m0, s70, 16384\n\t"
    "ds_read_b128 v[146:149], v8 offset:56320\n\t"
    "v_add_f32 v52, 1.0, v52\n\t"
    "v_mfma_f32_16x16x32_bf16 v[102:105], v[142:145], v[236:239], v[102:105]\n\t"
    "s_waitcnt lgkmcnt(3)\n\t"
    "s_add_u32 s60, s60, s72\n\t"
    "ds_read_b128 v[142:145], v8 offset:51200\n\t"
    "v_mfma_f32_16x16x32_bf16 v[78:81], v[150:153], v[232:235], v[78:81]\n\t"
    "buffer_load_dwordx4 v4, s[40:43], s60 offen offset:0 sc1 lds\n\t"
    "v_mfma_f32_16x16x32_bf16 v[78:81], v[154:157], v[240:243], v[78:81]\n\t"
    "s_add_u32 s60, s69, 163840\n\t"
    "v_exp_f32 v53, v53\n\t"
    "v_mfma_f32_16x16x32_bf16 v[138:141], v[154:157], v[244:247], v[138:141]\n\t"
    "s_add_u32 m0, s70, 20480\n\t"
    "ds_read_b128 v[154:157], v8 offset:27648\n\t"
    "v_add_f32 v53, 1.0, v53\n\t"
    "v_mfma_f32_16x16x32_bf16 v[138:141], v[150:153], v[236:239], v[138:141]\n\t"
    "s_waitcnt vmcnt(13) lgkmcnt(3)\n\t"
    "s_add_u32 s60, s60, s72\n\t"
    "ds_read_b128 v[150:153], v8 offset:19456\n\t"
    "v_mfma_f32_16x16x32_bf16 v[82:85], v[158:161], v[248:251], v[82:85]\n\t"
    "s_waitcnt vmcnt(12)\n\t"
    "buffer_load_dwordx4 v4, s[40:43], s60 offen offset:0 sc1 lds\n\t"
    "v_mfma_f32_16x16x32_bf16 v[86:89], v[158:161], v[252:255], v[86:89]\n\t"
    "s_add_u32 s60, s97, 163840\n\t"
    "ds_read_b128 v[158:161], v8 offset:20480\n\t"
    "v_mfma_f32_16x16x32_bf16 v[90:93], v[162:165], v[248:251], v[90:93]\n\t"
    "s_add_u32 m0, s70, 24576\n\t"
    "v_rcp_f32 v52, v52\n\t"
    "v_mfma_f32_16x16x32_bf16 v[94:97], v[162:165], v[252:255], v[94:97]\n\t"
    "s_waitcnt lgkmcnt(3)\n\t"
    "s_add_u32 s60, s60, s72\n\t"
    "v_cvt_pk_bf16_f32 v82, v82, v83\n\t"
    "v_mfma_f32_16x16x32_bf16 v[98:101], v[142:145], v[248:251], v[98:101]\n\t"
    "ds_read_b128 v[162:165], v8 offset:28672\n\t"
    "v_cvt_pk_bf16_f32 v83, v84, v85\n\t"
    "v_mfma_f32_16x16x32_bf16 v[102:105], v[142:145], v[252:255], v[102:105]\n\t"
    "s_waitcnt vmcnt(7)\n\t"
    "s_barrier\n\t"
    "v_mfma_f32_16x16x32_bf16 v[78:81], v[146:149], v[248:251], v[78:81]\n\t"
    "ds_read_b128 v[142:145], v9 offset:15360\n\t"
    "v_cvt_pk_bf16_f32 v84, v86, v87\n\t"
    "v_mfma_f32_16x16x32_bf16 v[138:141], v[146:149], v[252:255], v[138:141]\n\t"
    "s_waitcnt lgkmcnt(3)\n\t"
    "ds_read_b128 v[146:149], v9 offset:23552\n\t"
    "v_cvt_pk_bf16_f32 v85, v88, v89\n\t"
    "v_mfma_f32_16x16x32_bf16 v[106:109], v[150:153], v[200:203], v[106:109]\n\t"
    "v_cvt_pk_bf16_f32 v86, v90, v91\n\t"
    "v_cvt_pk_bf16_f32 v87, v92, v93\n\t"
    "v_mfma_f32_16x16x32_bf16 v[106:109], v[154:157], v[208:211], v[106:109]\n\t"
    "v_cvt_pk_bf16_f32 v88, v94, v95\n\t"
    "v_cvt_pk_bf16_f32 v89, v96, v97\n\t"
    "v_mfma_f32_16x16x32_bf16 v[110:113], v[154:157], v[212:215], v[110:113]\n\t"
    "ds_read_b128 v[154:157], v9 offset:24576\n\t"
    "v_cvt_pk_bf16_f32 v170, v98, v99\n\t"
    "v_mfma_f32_16x16x32_bf16 v[110:113], v[150:153], v[204:207], v[110:113]\n\t"
    "s_waitcnt lgkmcnt(4)\n\t"
    "ds_read_b128 v[150:153], v9 offset:16384\n\t"
    "v_cvt_pk_bf16_f32 v171, v100, v101\n\t"
    "v_mfma_f32_16x16x32_bf16 v[114:117], v[158:161], v[200:203], v[114:117]\n\t"
    "s_waitcnt lgkmcnt(4)\n\t"
    "buffer_store_dwordx4 v[82:85], v4, s[40:43], s89 offen offset:0 sc1\n\t"
    "v_mfma_f32_16x16x32_bf16 v[114:117], v[162:165], v[208:211], v[114:117]\n\t"
    "v_cvt_pk_bf16_f32 v166, v78, v79\n\t"
    "v_cvt_pk_bf16_f32 v167, v80, v81\n\t"
    "v_mfma_f32_16x16x32_bf16 v[118:121], v[162:165], v[212:215], v[118:121]\n\t"
    "ds_read_b128 v[162:165], v8 offset:44032\n\t"
    "v_cndmask_b32_dpp v90, v166, v170, vcc row_ror:8 row_mask:0xf bank_mask:0xf\n\t"
    "v_mfma_f32_16x16x32_bf16 v[118:121], v[158:161], v[204:207], v[118:121]\n\t"
    "s_waitcnt lgkmcnt(4)\n\t"
    "ds_read_b128 v[158:161], v8 offset:35840\n\t"
    "v_cndmask_b32_dpp v91, v167, v171, vcc row_ror:8 row_mask:0xf bank_mask:0xf\n\t"
    "v_mfma_f32_16x16x32_bf16 v[106:109], v[142:145], v[216:219], v[106:109]\n\t"
    "s_waitcnt lgkmcnt(4)\n\t"
    "buffer_store_dwordx4 v[86:89], v4, s[40:43], s89 offen offset:1024 sc1\n\t"
    "v_mfma_f32_16x16x32_bf16 v[106:109], v[146:149], v[224:227], v[106:109]\n\t"
    "KFP17:\n\t"
    "s_cbranch_execz KFR2\n\t"
    "v_mov_b32 v92, v90\n\t"
    "v_mov_b32 v93, v91\n\t"
    "v_mfma_f32_16x16x32_bf16 v[110:113], v[146:149], v[228:231], v[110:113]\n\t"
    "ds_read_b128 v[146:149], v8 offset:45056\n\t"
    "v_permlane32_swap_b32 v90, v92\n\t"
    "v_mfma_f32_16x16x32_bf16 v[110:113], v[142:145], v[220:223], v[110:113]\n\t"
    "s_waitcnt lgkmcnt(3)\n\t"
    "ds_read_b128 v[142:145], v8 offset:36864\n\t"
    "v_permlane32_swap_b32 v91, v93\n\t"
    "v_mfma_f32_16x16x32_bf16 v[114:117], v[150:153], v[216:219], v[114:117]\n\t"
    "buffer_store_dwordx4 v[90:93], v12, s[40:43], s89 offen offset:2048 sc1\n\t"
    "v_mfma_f32_16x16x32_bf16 v[114:117], v[154:157], v[224:227], v[114:117]\n\t"
    "v_cvt_pk_bf16_f32 v172, v102, v103\n\t"
    "v_cvt_pk_bf16_f32 v173, v104, v105\n\t"
    "v_mfma_f32_16x16x32_bf16 v[118:121], v[154:157], v[228:231], v[118:121]\n\t"
    "ds_read_b128 v[154:157], v8 offset:53248\n\t"
    "v_cvt_pk_bf16_f32 v168, v138, v139\n\t"
    "v_mfma_f32_16x16x32_bf16 v[118:121], v[150:153], v[220:223], v[118:121]\n\t"
    "s_waitcnt lgkmcnt(3)\n\t"
    "ds_read_b128 v[150:153], v8 offset:52224\n\t"
    "v_cvt_pk_bf16_f32 v169, v140, v141\n\t"
    "v_mfma_f32_16x16x32_bf16 v[106:109], v[158:161], v[232:235], v[106:109]\n\t"
    "v_cndmask_b32_dpp v102, v168, v172, vcc row_ror:8 row_mask:0xf bank_mask:0xf\n\t"
    "v_cndmask_b32_dpp v103, v169, v173, vcc row_ror:8 row_mask:0xf bank_mask:0xf\n\t"
    "v_mfma_f32_16x16x32_bf16 v[106:109], v[162:165], v[240:243], v[106:109]\n\t"
    "s_mov_b32 vcc_lo, s74\n\t"
    "v_mov_b32 v104, v102\n\t"
    "v_mov_b32 v105, v103\n\t"
    "v_mfma_f32_16x16x32_bf16 v[110:113], v[162:165], v[244:247], v[110:113]\n\t"
    "s_mov_b32 vcc_hi, s74\n\t"
    "ds_read_b128 v[162:165], v8 offset:29696\n\t"
    "v_cndmask_b32_dpp v78, v98, v78, vcc row_ror:8 row_mask:0xf bank_mask:0xf\n\t"
    "v_mfma_f32_16x16x32_bf16 v[110:113], v[158:161], v[236:239], v[110:113]\n\t"
    "s_waitcnt lgkmcnt(3)\n\t"
    "ds_read_b128 v[158:161], v8 offset:21504\n\t"
    "v_cndmask_b32_dpp v79, v99, v79, vcc row_ror:8 row_mask:0xf bank_mask:0xf\n\t"
    "v_mfma_f32_16x16x32_bf16 v[114:117], v[142:145], v[232:235], v[114:117]\n\t"
    "v_cndmask_b32_dpp v80, v100, v80, vcc row_ror:8 row_mask:0xf bank_mask:0xf\n\t"
    "v_cndmask_b32_dpp v81, v101, v81, vcc row_ror:8 row_mask:0xf bank_mask:0xf\n\t"
    "v_mfma_f32_16x16x32_bf16 v[114:117], v[146:149], v[240:243], v[114:117]\n\t"
    "s_mov_b32 vcc_lo, s74\n\t"
    "v_permlane32_swap_b32 v102, v104\n\t"
    "v_permlane32_swap_b32 v103, v105\n\t"
    "v_mfma_f32_16x16x32_bf16 v[118:121], v[146:149], v[244:247], v[118:121]\n\t"
    "s_mov_b32 vcc_hi, s74\n\t"
    "buffer_load_dwordx4 v4, s[40:43], s60 offen offset:0 sc1 lds\n\t"
    "v_mfma_f32_16x16x32_bf16 v[118:121], v[142:145], v[236:239], v[118:121]\n\t"
    "s_waitcnt lgkmcnt(2)\n\t"
    "s_add_u32 s60, s98, 163840\n\t"
    "ds_read_b128 v[142:145], v8 offset:22528\n\t"
    "v_mfma_f32_16x16x32_bf16 v[106:109], v[150:153], v[248:251], v[106:109]\n\t"
    "s_add_u32 m0, s70, 28672\n\t"
    "ds_read_b128 v[146:149], v8 offset:30720\n\t"
    "v_mul_f32 v50, v50, v52\n\t"
    "v_mfma_f32_16x16x32_bf16 v[110:113], v[150:153], v[252:255], v[110:113]\n\t"
    "s_add_u32 s60, s60, s73\n\t"
    "ds_read_b128 v[150:153], v9 offset:17408\n\t"
    "v_mfma_f32_16x16x32_bf16 v[114:117], v[154:157], v[248:251], v[114:117]\n\t"
    "buffer_load_dwordx4 v4, s[40:43], s60 offen offset:0 sc1 lds\n\t"
    "v_mfma_f32_16x16x32_bf16 v[118:121], v[154:157], v[252:255], v[118:121]\n\t"
    "s_waitcnt lgkmcnt(3)\n\t"
    "s_add_u32 s60, s68, 204800\n\t"
    "v_cvt_pk_bf16_f32 v100, v106, v107\n\t"
    "v_mfma_f32_16x16x32_bf16 v[122:125], v[158:161], v[200:203], v[122:125]\n\t"
    "s_add_u32 m0, s70, 32768\n\t"
    "v_cvt_pk_bf16_f32 v98, v110, v111\n\t"
    "v_mfma_f32_16x16x32_bf16 v[122:125], v[162:165], v[208:211], v[122:125]\n\t"
    "s_add_u32 s60, s60, s72\n\t"
    "ds_read_b128 v[154:157], v9 offset:25600\n\t"
    "v_cvt_pk_bf16_f32 v96, v114, v115\n\t"
    "v_mfma_f32_16x16x32_bf16 v[126:129], v[162:165], v[212:215], v[126:129]\n\t"
    "ds_read_b128 v[162:165], v9 offset:26624\n\t"
    "v_cvt_pk_bf16_f32 v94, v118, v119\n\t"
    "v_mfma_f32_16x16x32_bf16 v[126:129], v[158:161], v[204:207], v[126:129]\n\t"
    "s_waitcnt lgkmcnt(4)\n\t"
    "ds_read_b128 v[158:161], v9 offset:18432\n\t"
    "v_cvt_pk_bf16_f32 v95, v120, v121\n\t"
    "v_mfma_f32_16x16x32_bf16 v[130:133], v[142:145], v[200:203], v[130:133]\n\t"
    "s_waitcnt lgkmcnt(4)\n\t"
    "v_cvt_pk_bf16_f32 v97, v116, v117\n\t"
    "v_cvt_pk_bf16_f32 v99, v112, v113\n\t"
    "v_mfma_f32_16x16x32_bf16 v[130:133], v[146:149], v[208:211], v[130:133]\n\t"
    "buffer_store_dwordx4 v[94:97], v4, s[40:43], s90 offen offset:0 sc1\n\t"
    "v_mfma_f32_16x16x32_bf16 v[134:137], v[146:149], v[212:215], v[134:137]\n\t"
    "ds_read_b128 v[146:149], v8 offset:46080\n\t"
    "v_cvt_pk_bf16_f32 v101, v108, v109\n\t"
    "v_mfma_f32_16x16x32_bf16 v[134:137], v[142:145], v[204:207], v[134:137]\n\t"
    "s_waitcnt lgkmcnt(4)\n\t"
    "ds_read_b128 v[142:145], v8 offset:37888\n\t"
    "v_cndmask_b32_dpp v114, v172, v168, vcc row_ror:8 row_mask:0xf bank_mask:0xf\n\t"
    "v_mfma_f32_16x16x32_bf16 v[122:125], v[150:153], v[216:219], v[122:125]\n\t"
    "s_waitcnt lgkmcnt(4)\n\t"
    "buffer_store_dwordx4 v[98:101], v4, s[40:43], s90 offen offset:1024 sc1\n\t"
    "v_mfma_f32_16x16x32_bf16 v[122:125], v[154:157], v[224:227], v[122:125]\n\t"
    "v_cndmask_b32_dpp v115, v173, v169, vcc row_ror:8 row_mask:0xf bank_mask:0xf\n\t"
    "v_mov_b32 v116, v114\n\t"
    "v_mfma_f32_16x16x32_bf16 v[126:129], v[154:157], v[228:231], v[126:129]\n\t"
    "ds_read_b128 v[154:157], v8 offset:47104\n\t"
    "v_mov_b32 v117, v115\n\t"
    "v_mfma_f32_16x16x32_bf16 v[126:129], v[150:153], v[220:223], v[126:129]\n\t"
    "s_waitcnt lgkmcnt(3)\n\t"
    "ds_read_b128 v[150:153], v8 offset:38912\n\t"
    "v_permlane32_swap_b32 v114, v116\n\t"
    "v_mfma_f32_16x16x32_bf16 v[130:133], v[158:161], v[216:219], v[130:133]\n\t"
    "buffer_store_dwordx4 v[102:105], v12, s[40:43], s90 offen offset:2048 sc1\n\t"
    "v_mfma_f32_16x16x32_bf16 v[130:133], v[162:165], v[224:227], v[130:133]\n\t"
    "v_permlane32_swap_b32 v115, v117\n\t"
    "v_lshlrev_b32 v54, 16, v39\n\t"
    "v_mfma_f32_16x16x32_bf16 v[134:137], v[162:165], v[228:231], v[134:137]\n\t"
    "buffer_load_dwordx4 v4, s[40:43], s60 offen offset:0 sc1 lds\n\t"
    "v_mfma_f32_16x16x32_bf16 v[134:137], v[158:161], v[220:223], v[134:137]\n\t"
    "s_waitcnt lgkmcnt(2)\n\t"
    "s_add_u32 s60, s69, 204800\n\t"
    "ds_read_b128 v[158:161], v8 offset:54272\n\t"
    "v_mfma_f32_16x16x32_bf16 v[122:125], v[142:145], v[232:235], v[122:125]\n\t"
    "s_add_u32 m0, s70, 36864\n\t"
    "ds_read_b128 v[162:165], v8 offset:55296\n\t"
    "v_mfma_f32_16x16x32_bf16 v[122:125], v[146:149], v[240:243], v[122:125]\n\t"
    "s_add_u32 s60, s60, s72\n\t"
    "v_rcp_f32 v53, v53\n\t"
    "v_mfma_f32_16x16x32_bf16 v[126:129], v[146:149], v[244:247], v[126:129]\n\t"
    "buffer_load_dwordx4 v4, s[40:43], s60 offen offset:0 sc1 lds\n\t"
    "v_mfma_f32_16x16x32_bf16 v[126:129], v[142:145], v[236:239], v[126:129]\n\t"
    "s_waitcnt lgkmcnt(2)\n\t"
    "s_add_u32 m0, s70, 57344\n\t"
    "ds_read_b128 v[142:145], v9 offset:29696\n\t"
    "v_mfma_f32_16x16x32_bf16 v[130:133], v[150:153], v[232:235], v[130:133]\n\t"
    "s_add_u32 s60, s97, 204800\n\t"
    "ds_read_b128 v[146:149], v9 offset:31744\n\t"
    "v_mfma_f32_16x16x32_bf16 v[130:133], v[154:157], v[240:243], v[130:133]\n\t"
    "s_add_u32 s60, s60, s72\n\t"
    "v_mul_f32 v51, v51, v53\n\t"
    "v_cvt_pk_bf16_f32 v38, v50, v51\n\t"
    "v_mfma_f32_16x16x32_bf16 v[134:137], v[154:157], v[244:247], v[134:137]\n\t"
    "buffer_load_dwordx4 v4, s[40:43], s60 offen offset:0 sc1 lds\n\t"
    "v_mfma_f32_16x16x32_bf16 v[134:137], v[150:153], v[236:239], v[134:137]\n\t"
    "s_waitcnt lgkmcnt(3)\n\t"
    "ds_read_b128 v[150:153], v9 offset:28672\n\t"
    "v_and_b32 v55, 0xffff0000, v39\n\t"
    "v_mfma_f32_16x16x32_bf16 v[122:125], v[158:161], v[248:251], v[122:125]\n\t"
    "ds_read_b128 v[154:157], v9 offset:30720\n\t"
    "v_lshlrev_b32 v56, 16, v49\n\t"
    "v_mfma_f32_16x16x32_bf16 v[126:129], v[158:161], v[252:255], v[126:129]\n\t"
    "s_waitcnt lgkmcnt(4)\n\t"
    "ds_read_b128 v[158:161], v8 offset:58368\n\t"
    "v_and_b32 v57, 0xffff0000, v49\n\t"
    "v_mfma_f32_16x16x32_bf16 v[130:133], v[162:165], v[248:251], v[130:133]\n\t"
    "v_add_f32 v54, v54, v56\n\t"
    "v_add_f32 v55, v55, v57\n\t"
    "v_mfma_f32_16x16x32_bf16 v[134:137], v[162:165], v[252:255], v[134:137]\n\t"
    "s_waitcnt lgkmcnt(4)\n\t"
    "ds_read_b128 v[162:165], v8 offset:60416\n\t"
    "v_cvt_pk_bf16_f32 v108, v122, v123\n\t"
    "v_mfma_f32_16x16x32_bf16 v[62:65], v[142:145], a[192:195], v[62:65]\n\t"
    "s_waitcnt lgkmcnt(4)\n\t"
    "v_cvt_pk_bf16_f32 v106, v126, v127\n\t"
    "v_cvt_pk_bf16_f32 v107, v128, v129\n\t"
    "v_mfma_f32_16x16x32_bf16 v[62:65], v[146:149], a[200:203], v[62:65]\n\t"
    "s_bfe_u32 s51, s87, 0x10009\n\t"
    "v_cvt_pk_bf16_f32 v109, v124, v125\n\t"
    "v_cvt_pk_bf16_f32 v112, v130, v131\n\t"
    "v_mfma_f32_16x16x32_bf16 v[66:69], v[146:149], a[204:207], v[66:69]\n\t"
    "s_mul_i32 s51, s51, 10048\n\t"
    "ds_read_b128 v[146:149], v8 offset:59392\n\t"
    "v_cvt_pk_bf16_f32 v110, v134, v135\n\t"
    "v_mfma_f32_16x16x32_bf16 v[66:69], v[142:145], a[196:199], v[66:69]\n\t"
    "s_waitcnt lgkmcnt(4)\n\t"
    "ds_read_b128 v[142:145], v8 offset:57344\n\t"
    "v_cvt_pk_bf16_f32 v111, v136, v137\n\t"
    "v_mfma_f32_16x16x32_bf16 v[70:73], v[150:153], a[192:195], v[70:73]\n\t"
    "s_waitcnt lgkmcnt(4)\n\t"
    "buffer_store_dwordx4 v[106:109], v4, s[40:43], s88 offen offset:0 sc1\n\t"
    "v_mfma_f32_16x16x32_bf16 v[70:73], v[154:157], a[200:203], v[70:73]\n\t"
    "s_mul_i32 s50, s71, 20416\n\t"
    "v_cvt_pk_bf16_f32 v113, v132, v133\n\t"
    "v_cvt_pk_bf16_f32 v55, v54, v55\n\t"
    "v_mfma_f32_16x16x32_bf16 v[74:77], v[154:157], a[204:207], v[74:77]\n\t"
    "s_add_u32 s50, s50, s51\n\t"
    "ds_read_b128 v[154:157], v8 offset:64512\n\t"
    "v_lshlrev_b32 v54, 16, v55\n\t"
    "v_mfma_f32_16x16x32_bf16 v[74:77], v[150:153], a[196:199], v[74:77]\n\t"
    "s_waitcnt lgkmcnt(3)\n\t"
    "ds_read_b128 v[150:153], v8 offset:62464\n\t"
    "v_and_b32 v55, 0xffff0000, v55\n\t"
    "v_mfma_f32_16x16x32_bf16 v[62:65], v[158:161], a[208:211], v[62:65]\n\t"
    "buffer_store_dwordx4 v[110:113], v4, s[40:43], s88 offen offset:1024 sc1\n\t"
    "v_mfma_f32_16x16x32_bf16 v[62:65], v[162:165], a[216:219], v[62:65]\n\t"
    "s_add_u32 s50, s50, s76\n\t"
    "v_mul_f32 v56, 0xbfb8aa3b, v54\n\t"
    "v_mul_f32 v57, 0xbfb8aa3b, v55\n\t"
    "v_mfma_f32_16x16x32_bf16 v[66:69], v[162:165], a[220:223], v[66:69]\n\t"
    "ds_read_b128 v[162:165], v8 offset:63488\n\t"
    "v_bfe_u32 v174, v4, 7, 1\n\t"
    "v_mfma_f32_16x16x32_bf16 v[66:69], v[158:161], a[212:215], v[66:69]\n\t"
    "s_waitcnt lgkmcnt(3)\n\t"
    "ds_read_b128 v[158:161], v8 offset:61440\n\t"
    "v_mul_i32_i24 v174, 0xffffd808, v174\n\t"
    "v_mfma_f32_16x16x32_bf16 v[70:73], v[142:145], a[208:211], v[70:73]\n\t"
    "buffer_store_dwordx4 v[114:117], v12, s[40:43], s88 offen offset:2048 sc1\n\t"
    "v_mfma_f32_16x16x32_bf16 v[70:73], v[146:149], a[216:219], v[70:73]\n\t"
    "v_exp_f32 v56, v56\n\t"
    "v_mfma_f32_16x16x32_bf16 v[74:77], v[146:149], a[220:223], v[74:77]\n\t"
    "ds_read_b128 v[146:149], v9 offset:5120\n\t"
    "v_add_f32 v56, 1.0, v56\n\t"
    "v_mfma_f32_16x16x32_bf16 v[74:77], v[142:145], a[212:215], v[74:77]\n\t"
    "s_waitcnt lgkmcnt(3)\n\t"
    "ds_read_b128 v[142:145], v9 offset:1024\n\t"
    "v_add_u32 v174, v174, v11\n\t"
    "v_mfma_f32_16x16x32_bf16 v[62:65], v[150:153], a[224:227], v[62:65]\n\t"
    "v_exp_f32 v57, v57\n\t"
    "v_mfma_f32_16x16x32_bf16 v[62:65], v[154:157], a[232:235], v[62:65]\n\t"
    "v_add_f32 v57, 1.0, v57\n\t"
    "v_lshlrev_b32 v122, 16, v20\n\t"
    "v_mfma_f32_16x16x32_bf16 v[66:69], v[154:157], a[236:239], v[66:69]\n\t"
    "ds_read_b128 v[154:157], v9 offset:4096\n\t"
    "v_and_b32 v123, 0xffff0000, v20\n\t"
    "v_mfma_f32_16x16x32_bf16 v[66:69], v[150:153], a[228:231], v[66:69]\n\t"
    "s_waitcnt lgkmcnt(3)\n\t"
    "ds_read_b128 v[150:153], v9 offset:0\n\t"
    "v_lshlrev_b32 v102, 16, v30\n\t"
    "v_mfma_f32_16x16x32_bf16 v[70:73], v[158:161], a[224:227], v[70:73]\n\t"
    "v_rcp_f32 v56, v56\n\t"
    "v_mfma_f32_16x16x32_bf16 v[70:73], v[162:165], a[232:235], v[70:73]\n\t"
    "v_rcp_f32 v57, v57\n\t"
    "v_mfma_f32_16x16x32_bf16 v[74:77], v[162:165], a[236:239], v[74:77]\n\t"
    "ds_read_b128 v[162:165], v9 offset:9216\n\t"
    "v_mul_f32 v54, v54, v56\n\t"
    "v_mfma_f32_16x16x32_bf16 v[74:77], v[158:161], a[228:231], v[74:77]\n\t"
    "s_waitcnt lgkmcnt(3)\n\t"
    "ds_read_b128 v[158:161], v9 offset:7168\n\t"
    "v_mul_f32 v55, v55, v57\n\t"
    "v_mfma_f32_16x16x32_bf16 v[62:65], v[142:145], a[0:3], v[62:65]\n\t"
    "v_cvt_pk_bf16_f32 v39, v54, v55\n\t"
    "v_and_b32 v103, 0xffff0000, v30\n\t"
    "v_mfma_f32_16x16x32_bf16 v[62:65], v[146:149], a[8:11], v[62:65]\n\t"
    "v_lshlrev_b32 v124, 16, v21\n\t"
    "v_and_b32 v125, 0xffff0000, v21\n\t"
    "v_mfma_f32_16x16x32_bf16 v[66:69], v[146:149], a[12:15], v[66:69]\n\t"
    "ds_read_b128 v[146:149], v9 offset:8192\n\t"
    "v_lshlrev_b32 v104, 16, v31\n\t"
    "v_mfma_f32_16x16x32_bf16 v[66:69], v[142:145], a[4:7], v[66:69]\n\t"
    "s_waitcnt lgkmcnt(3)\n\t"
    "ds_read_b128 v[142:145], v9 offset:6144\n\t"
    "v_and_b32 v105, 0xffff0000, v31\n\t"
    "v_mfma_f32_16x16x32_bf16 v[70:73], v[150:153], a[0:3], v[70:73]\n\t"
    "v_lshlrev_b32 v126, 16, v22\n\t"
    "v_and_b32 v127, 0xffff0000, v22\n\t"
    "v_mfma_f32_16x16x32_bf16 v[70:73], v[154:157], a[8:11], v[70:73]\n\t"
    "v_lshlrev_b32 v106, 16, v32\n\t"
    "v_and_b32 v107, 0xffff0000, v32\n\t"
    "v_mfma_f32_16x16x32_bf16 v[74:77], v[154:157], a[12:15], v[74:77]\n\t"
    "ds_read_b128 v[154:157], v4 offset:1024\n\t"
    "v_lshlrev_b32 v128, 16, v23\n\t"
    "v_mfma_f32_16x16x32_bf16 v[74:77], v[150:153], a[4:7], v[74:77]\n\t"
    "s_waitcnt lgkmcnt(3)\n\t"
    "ds_read_b128 v[150:153], v9 offset:11264\n\t"
    "v_and_b32 v129, 0xffff0000, v23\n\t"
    "v_mfma_f32_16x16x32_bf16 v[62:65], v[158:161], a[16:19], v[62:65]\n\t"
    "v_lshlrev_b32 v108, 16, v33\n\t"
    "v_and_b32 v109, 0xffff0000, v33\n\t"
    "v_mfma_f32_16x16x32_bf16 v[62:65], v[162:165], a[24:27], v[62:65]\n\t"
    "v_lshlrev_b32 v130, 16, v24\n\t"
    "v_and_b32 v131, 0xffff0000, v24\n\t"
    "v_mfma_f32_16x16x32_bf16 v[66:69], v[162:165], a[28:31], v[66:69]\n\t"
    "ds_read_b128 v[162:165], v4 offset:0\n\t"
    "v_lshlrev_b32 v110, 16, v34\n\t"
    "v_mfma_f32_16x16x32_bf16 v[66:69], v[158:161], a[20:23], v[66:69]\n\t"
    "s_waitcnt lgkmcnt(3)\n\t"
    "ds_read_b128 v[158:161], v9 offset:10240\n\t"
    "v_and_b32 v111, 0xffff0000, v34\n\t"
    "v_mfma_f32_16x16x32_bf16 v[70:73], v[142:145], a[16:19], v[70:73]\n\t"
    "v_lshlrev_b32 v132, 16, v25\n\t"
    "v_and_b32 v133, 0xffff0000, v25\n\t"
    "v_mfma_f32_16x16x32_bf16 v[70:73], v[146:149], a[24:27], v[70:73]\n\t"
    "v_lshlrev_b32 v112, 16, v35\n\t"
    "v_and_b32 v113, 0xffff0000, v35\n\t"
    "v_mfma_f32_16x16x32_bf16 v[74:77], v[146:149], a[28:31], v[74:77]\n\t"
    "ds_read_b128 v[146:149], v9 offset:3072\n\t"
    "v_lshlrev_b32 v134, 16, v26\n\t"
    "v_mfma_f32_16x16x32_bf16 v[74:77], v[142:145], a[20:23], v[74:77]\n\t"
    "s_waitcnt lgkmcnt(3)\n\t"
    "ds_read_b128 v[142:145], v4 offset:3072\n\t"
    "v_and_b32 v135, 0xffff0000, v26\n\t"
    "v_mfma_f32_16x16x32_bf16 v[62:65], v[150:153], a[32:35], v[62:65]\n\t"
    "v_lshlrev_b32 v114, 16, v36\n\t"
    "v_and_b32 v115, 0xffff0000, v36\n\t"
    "v_mfma_f32_16x16x32_bf16 v[62:65], v[154:157], a[40:43], v[62:65]\n\t"
    "v_lshlrev_b32 v136, 16, v27\n\t"
    "v_and_b32 v137, 0xffff0000, v27\n\t"
    "v_mfma_f32_16x16x32_bf16 v[66:69], v[154:157], a[44:47], v[66:69]\n\t"
    "ds_read_b128 v[154:157], v9 offset:2048\n\t"
    "v_lshlrev_b32 v116, 16, v37\n\t"
    "v_mfma_f32_16x16x32_bf16 v[66:69], v[150:153], a[36:39], v[66:69]\n\t"
    "s_waitcnt lgkmcnt(3)\n\t"
    "ds_read_b128 v[150:153], v4 offset:2048\n\t"
    "v_and_b32 v117, 0xffff0000, v37\n\t"
    "v_mfma_f32_16x16x32_bf16 v[70:73], v[158:161], a[32:35], v[70:73]\n\t"
    "v_lshlrev_b32 v138, 16, v28\n\t"
    "v_and_b32 v139, 0xffff0000, v28\n\t"
    "v_mfma_f32_16x16x32_bf16 v[70:73], v[162:165], a[40:43], v[70:73]\n\t"
    "v_lshlrev_b32 v118, 16, v38\n\t"
    "v_and_b32 v119, 0xffff0000, v38\n\t"
    "v_mfma_f32_16x16x32_bf16 v[74:77], v[162:165], a[44:47], v[74:77]\n\t"
    "ds_read_b128 v[162:165], v4 offset:7168\n\t"
    "v_lshlrev_b32 v140, 16, v29\n\t"
    "v_mfma_f32_16x16x32_bf16 v[74:77], v[158:161], a[36:39], v[74:77]\n\t"
    "s_waitcnt lgkmcnt(3)\n\t"
    "s_waitcnt vmcnt(3)\n\t"
    "s_barrier\n\t"
    "v_mfma_f32_16x16x32_bf16 v[62:65], v[142:145], a[48:51], v[62:65]\n\t"
    "ds_read_b128 v[158:161], v4 offset:5120\n\t"
    "v_and_b32 v141, 0xffff0000, v29\n\t"
    "v_mfma_f32_16x16x32_bf16 v[62:65], v[146:149], a[56:59], v[62:65]\n\t"
    "v_lshlrev_b32 v120, 16, v39\n\t"
    "v_and_b32 v121, 0xffff0000, v39\n\t"
    "v_mfma_f32_16x16x32_bf16 v[66:69], v[146:149], a[60:63], v[66:69]\n\t"
    "ds_read_b128 v[146:149], v4 offset:6144\n\t"
    "v_mfma_f32_16x16x32_bf16 v[66:69], v[142:145], a[52:55], v[66:69]\n\t"
    "s_waitcnt lgkmcnt(3)\n\t"
    "ds_read_b128 v[142:145], v4 offset:4096\n\t"
    "v_mfma_f32_16x16x32_bf16 v[70:73], v[150:153], a[48:51], v[70:73]\n\t"
    "v_mfma_f32_16x16x32_bf16 v[70:73], v[154:157], a[56:59], v[70:73]\n\t"
    "v_mfma_f32_16x16x32_bf16 v[74:77], v[154:157], a[60:63], v[74:77]\n\t"
    "ds_read_b128 v[154:157], v4 offset:44032\n\t"
    "v_mfma_f32_16x16x32_bf16 v[74:77], v[150:153], a[52:55], v[74:77]\n\t"
    "s_waitcnt lgkmcnt(3)\n\t"
    "ds_read_b128 v[150:153], v4 offset:41984\n\t"
    "v_mfma_f32_16x16x32_bf16 v[62:65], v[158:161], a[64:67], v[62:65]\n\t"
    "v_mfma_f32_16x16x32_bf16 v[62:65], v[162:165], a[72:75], v[62:65]\n\t"
    "v_mfma_f32_16x16x32_bf16 v[66:69], v[162:165], a[76:79], v[66:69]\n\t"
    "ds_read_b128 v[162:165], v4 offset:43008\n\t"
    "v_mfma_f32_16x16x32_bf16 v[66:69], v[158:161], a[68:71], v[66:69]\n\t"
    "s_waitcnt lgkmcnt(3)\n\t"
    "ds_read_b128 v[158:161], v4 offset:40960\n\t"
    "v_mfma_f32_16x16x32_bf16 v[70:73], v[142:145], a[64:67], v[70:73]\n\t"
    "v_mfma_f32_16x16x32_bf16 v[70:73], v[146:149], a[72:75], v[70:73]\n\t"
    "v_mfma_f32_16x16x32_bf16 v[74:77], v[146:149], a[76:79], v[74:77]\n\t"
    "ds_read_b128 v[146:149], v4 offset:48128\n\t"
    "v_mfma_f32_16x16x32_bf16 v[74:77], v[142:145], a[68:71], v[74:77]\n\t"
    "s_waitcnt lgkmcnt(3)\n\t"
    "ds_read_b128 v[142:145], v4 offset:46080\n\t"
    "v_mfma_f32_16x16x32_bf16 v[62:65], v[150:153], a[80:83], v[62:65]\n\t"
    "v_mfma_f32_16x16x32_bf16 v[62:65], v[154:157], a[88:91], v[62:65]\n\t"
    "v_mfma_f32_16x16x32_bf16 v[66:69], v[154:157], a[92:95], v[66:69]\n\t"
    "ds_read_b128 v[154:157], v4 offset:47104\n\t"
    "v_mfma_f32_16x16x32_bf16 v[66:69], v[150:153], a[84:87], v[66:69]\n\t"
    "s_waitcnt lgkmcnt(3)\n\t"
    "ds_read_b128 v[150:153], v4 offset:45056\n\t"
    "v_mfma_f32_16x16x32_bf16 v[70:73], v[158:161], a[80:83], v[70:73]\n\t"
    "v_mfma_f32_16x16x32_bf16 v[70:73], v[162:165], a[88:91], v[70:73]\n\t"
    "v_mfma_f32_16x16x32_bf16 v[74:77], v[162:165], a[92:95], v[74:77]\n\t"
    "ds_read_b128 v[162:165], v4 offset:54272\n\t"
    "v_mfma_f32_16x16x32_bf16 v[74:77], v[158:161], a[84:87], v[74:77]\n\t"
    "s_waitcnt lgkmcnt(3)\n\t"
    "ds_read_b128 v[158:161], v4 offset:50176\n\t"
    "v_mfma_f32_16x16x32_bf16 v[62:65], v[142:145], a[96:99], v[62:65]\n\t"
    "v_mfma_f32_16x16x32_bf16 v[62:65], v[146:149], a[104:107], v[62:65]\n\t"
    "v_mfma_f32_16x16x32_bf16 v[66:69], v[146:149], a[108:111], v[66:69]\n\t"
    "ds_read_b128 v[146:149], v4 offset:53248\n\t"
    "v_mfma_f32_16x16x32_bf16 v[66:69], v[142:145], a[100:103], v[66:69]\n\t"
    "s_waitcnt lgkmcnt(3)\n\t"
    "ds_read_b128 v[142:145], v4 offset:49152\n\t"
    "v_mfma_f32_16x16x32_bf16 v[70:73], v[150:153], a[96:99], v[70:73]\n\t"
    "v_mfma_f32_16x16x32_bf16 v[70:73], v[154:157], a[104:107], v[70:73]\n\t"
    "v_mfma_f32_16x16x32_bf16 v[74:77], v[154:157], a[108:111], v[74:77]\n\t"
    "ds_read_b128 v[154:157], v4 offset:9216\n\t"
    "v_mfma_f32_16x16x32_bf16 v[74:77], v[150:153], a[100:103], v[74:77]\n\t"
    "s_waitcnt lgkmcnt(3)\n\t"
    "ds_read_b128 v[150:153], v4 offset:56320\n\t"
    "v_mfma_f32_16x16x32_bf16 v[62:65], v[158:161], a[112:115], v[62:65]\n\t"
    "v_mfma_f32_16x16x32_bf16 v[62:65], v[162:165], a[120:123], v[62:65]\n\t"
    "v_mfma_f32_16x16x32_bf16 v[66:69], v[162:165], a[124:127], v[66:69]\n\t"
    "ds_read_b128 v[162:165], v4 offset:8192\n\t"
    "v_mfma_f32_16x16x32_bf16 v[66:69], v[158:161], a[116:119], v[66:69]\n\t"
    "s_waitcnt lgkmcnt(3)\n\t"
    "ds_read_b128 v[158:161], v4 offset:55296\n\t"
    "v_mfma_f32_16x16x32_bf16 v[70:73], v[142:145], a[112:115], v[70:73]\n\t"
    "v_mfma_f32_16x16x32_bf16 v[70:73], v[146:149], a[120:123], v[70:73]\n\t"
    "buffer_load_dwordx4 v[82:85], v4, s[40:43], s62 offen offset:0 sc1\n\t"
    "v_mfma_f32_16x16x32_bf16 v[74:77], v[146:149], a[124:127], v[74:77]\n\t"
    "ds_read_b128 v[146:149], v4 offset:13312\n\t"
    "v_mfma_f32_16x16x32_bf16 v[74:77], v[142:145], a[116:119], v[74:77]\n\t"
    "s_waitcnt lgkmcnt(3)\n\t"
    "ds_read_b128 v[142:145], v4 offset:11264\n\t"
    "v_mfma_f32_16x16x32_bf16 v[62:65], v[150:153], a[128:131], v[62:65]\n\t"
    "buffer_load_dwordx4 v[86:89], v4, s[40:43], s62 offen offset:1024 sc1\n\t"
    "v_mfma_f32_16x16x32_bf16 v[62:65], v[154:157], a[136:139], v[62:65]\n\t"
    "v_mfma_f32_16x16x32_bf16 v[66:69], v[154:157], a[140:143], v[66:69]\n\t"
    "ds_read_b128 v[154:157], v4 offset:12288\n\t"
    "v_mfma_f32_16x16x32_bf16 v[66:69], v[150:153], a[132:135], v[66:69]\n\t"
    "s_waitcnt lgkmcnt(3)\n\t"
    "buffer_load_dwordx2 v[90:91], v10, s[40:43], s62 offen offset:2048 sc1\n\t"
    "v_mfma_f32_16x16x32_bf16 v[70:73], v[158:161], a[128:131], v[70:73]\n\t"
    "ds_read_b128 v[150:153], v4 offset:10240\n\t"
    "v_mfma_f32_16x16x32_bf16 v[70:73], v[162:165], a[136:139], v[70:73]\n\t"
    "s_add_u32 s62, s85, 20480\n\t"
    "v_mfma_f32_16x16x32_bf16 v[74:77], v[162:165], a[140:143], v[74:77]\n\t"
    "buffer_load_dwordx4 v[92:95], v4, s[40:43], s62 offen offset:0 sc1\n\t"
    "v_mfma_f32_16x16x32_bf16 v[74:77], v[158:161], a[132:135], v[74:77]\n\t"
    "s_waitcnt lgkmcnt(2)\n\t"
    "ds_read_b128 v[158:161], v4 offset:15360\n\t"
    "v_mfma_f32_16x16x32_bf16 v[62:65], v[142:145], a[144:147], v[62:65]\n\t"
    "ds_read_b128 v[162:165], v4 offset:52224\n\t"
    "v_mfma_f32_16x16x32_bf16 v[62:65], v[146:149], a[152:155], v[62:65]\n\t"
    "buffer_load_dwordx4 v[96:99], v4, s[40:43], s62 offen offset:1024 sc1\n\t"
    "v_mfma_f32_16x16x32_bf16 v[66:69], v[146:149], a[156:159], v[66:69]\n\t"
    "ds_read_b128 v[146:149], v4 offset:51200\n\t"
    "v_mfma_f32_16x16x32_bf16 v[66:69], v[142:145], a[148:151], v[66:69]\n\t"
    "s_waitcnt lgkmcnt(3)\n\t"
    "ds_read_b128 v[142:145], v4 offset:14336\n\t"
    "v_mfma_f32_16x16x32_bf16 v[70:73], v[150:153], a[144:147], v[70:73]\n\t"
    "buffer_load_dwordx2 v[100:101], v10, s[40:43], s62 offen offset:2048 sc1\n\t"
    "v_mfma_f32_16x16x32_bf16 v[70:73], v[154:157], a[152:155], v[70:73]\n\t"
    "s_add_u32 s62, s85, 0\n\t"
    "v_mfma_f32_16x16x32_bf16 v[74:77], v[154:157], a[156:159], v[74:77]\n\t"
    "ds_read_b128 v[154:157], v4 offset:19456\n\t"
    "v_mfma_f32_16x16x32_bf16 v[74:77], v[150:153], a[148:151], v[74:77]\n\t"
    "s_waitcnt lgkmcnt(4)\n\t"
    "buffer_load_dwordx4 v[40:43], v4, s[40:43], s62 offen offset:0 sc1\n\t"
    "v_mfma_f32_16x16x32_bf16 v[62:65], v[158:161], a[160:163], v[62:65]\n\t"
    "s_waitcnt lgkmcnt(3)\n\t"
    "ds_read_b128 v[150:153], v4 offset:17408\n\t"
    "v_mfma_f32_16x16x32_bf16 v[62:65], v[162:165], a[168:171], v[62:65]\n\t"
    "v_mfma_f32_16x16x32_bf16 v[66:69], v[162:165], a[172:175], v[66:69]\n\t"
    "buffer_load_dwordx4 v[44:47], v4, s[40:43], s62 offen offset:1024 sc1\n\t"
    "v_mfma_f32_16x16x32_bf16 v[66:69], v[158:161], a[164:167], v[66:69]\n\t"
    "s_waitcnt lgkmcnt(2)\n\t"
    "ds_read_b128 v[158:161], v4 offset:16384\n\t"
    "v_mfma_f32_16x16x32_bf16 v[70:73], v[142:145], a[160:163], v[70:73]\n\t"
    "ds_read_b128 v[162:165], v4 offset:18432\n\t"
    "v_mfma_f32_16x16x32_bf16 v[70:73], v[146:149], a[168:171], v[70:73]\n\t"
    "buffer_load_dwordx2 v[48:49], v10, s[40:43], s62 offen offset:2048 sc1\n\t"
    "v_mfma_f32_16x16x32_bf16 v[74:77], v[146:149], a[172:175], v[74:77]\n\t"
    "ds_read_b128 v[146:149], v4 offset:23552\n\t"
    "v_mfma_f32_16x16x32_bf16 v[74:77], v[142:145], a[164:167], v[74:77]\n\t"
    "s_waitcnt lgkmcnt(3)\n\t"
    "ds_read_b128 v[142:145], v4 offset:21504\n\t"
    "v_mfma_f32_16x16x32_bf16 v[62:65], v[150:153], a[176:179], v[62:65]\n\t"
    "v_mfma_f32_16x16x32_bf16 v[62:65], v[154:157], a[184:187], v[62:65]\n\t"
    "v_mfma_f32_16x16x32_bf16 v[66:69], v[154:157], a[188:191], v[66:69]\n\t"
    "ds_read_b128 v[154:157], v4 offset:22528\n\t"
    "v_mfma_f32_16x16x32_bf16 v[66:69], v[150:153], a[180:183], v[66:69]\n\t"
    "s_waitcnt lgkmcnt(4)\n\t"
    "ds_read_b128 v[150:153], v4 offset:20480\n\t"
    "v_mfma_f32_16x16x32_bf16 v[70:73], v[158:161], a[176:179], v[70:73]\n\t"
    "s_waitcnt lgkmcnt(4)\n\t"
    "v_mfma_f32_16x16x32_bf16 v[70:73], v[162:165], a[184:187], v[70:73]\n\t"
    "v_mfma_f32_16x16x32_bf16 v[74:77], v[162:165], a[188:191], v[74:77]\n\t"
    "ds_read_b128 v[162:165], v4 offset:27648\n\t"
    "v_mfma_f32_16x16x32_bf16 v[74:77], v[158:161], a[180:183], v[74:77]\n\t"
    "s_waitcnt lgkmcnt(3)\n\t"
    "ds_read_b128 v[158:161], v4 offset:25600\n\t"
    "v_mfma_f32_16x16x32_bf16 v[62:65], v[142:145], a[240:243], v[62:65]\n\t"
    "v_mfma_f32_16x16x32_bf16 v[62:65], v[146:149], a[248:251], v[62:65]\n\t"
    "v_mfma_f32_16x16x32_bf16 v[66:69], v[146:149], a[252:255], v[66:69]\n\t"
    "ds_read_b128 v[146:149], v4 offset:26624\n\t"
    "v_mfma_f32_16x16x32_bf16 v[66:69], v[142:145], a[244:247], v[66:69]\n\t"
    "s_waitcnt lgkmcnt(3)\n\t"
    "ds_read_b128 v[142:145], v4 offset:24576\n\t"
    "v_mfma_f32_16x16x32_bf16 v[70:73], v[150:153], a[240:243], v[70:73]\n\t"
    "v_mfma_f32_16x16x32_bf16 v[70:73], v[154:157], a[248:251], v[70:73]\n\t"
    "v_mfma_f32_16x16x32_bf16 v[74:77], v[154:157], a[252:255], v[74:77]\n\t"
    "ds_read_b128 v[154:157], v4 offset:33792\n\t"
    "v_mfma_f32_16x16x32_bf16 v[74:77], v[150:153], a[244:247], v[74:77]\n\t"
    "s_waitcnt lgkmcnt(3)\n\t"
    "ds_read_b128 v[150:153], v4 offset:29696\n\t"
    "v_mfma_f32_16x16x32_bf16 v[62:65], v[158:161], v[176:179], v[62:65]\n\t"
    "v_mfma_f32_16x16x32_bf16 v[62:65], v[162:165], v[184:187], v[62:65]\n\t"
    "v_mfma_f32_16x16x32_bf16 v[66:69], v[162:165], v[188:191], v[66:69]\n\t"
    "ds_read_b128 v[162:165], v4 offset:32768\n\t"
    "v_mfma_f32_16x16x32_bf16 v[66:69], v[158:161], v[180:183], v[66:69]\n\t"
    "s_waitcnt lgkmcnt(3)\n\t"
    "ds_read_b128 v[158:161], v4 offset:28672\n\t"
    "v_mfma_f32_16x16x32_bf16 v[70:73], v[142:145], v[176:179], v[70:73]\n\t"
    "v_mfma_f32_16x16x32_bf16 v[70:73], v[146:149], v[184:187], v[70:73]\n\t"
    "v_mfma_f32_16x16x32_bf16 v[74:77], v[146:149], v[188:191], v[74:77]\n\t"
    "ds_read_b128 v[146:149], v4 offset:37888\n\t"
    "v_mfma_f32_16x16x32_bf16 v[74:77], v[142:145], v[180:183], v[74:77]\n\t"
    "s_waitcnt lgkmcnt(3)\n\t"
    "ds_read_b128 v[142:145], v4 offset:35840\n\t"
    "v_mfma_f32_16x16x32_bf16 v[62:65], v[150:153], v[192:195], v[62:65]\n\t"
    "v_mfma_f32_16x16x32_bf16 v[62:65], v[154:157], v[200:203], v[62:65]\n\t"
    "v_mfma_f32_16x16x32_bf16 v[66:69], v[154:157], v[204:207], v[66:69]\n\t"
    "ds_read_b128 v[154:157], v4 offset:36864\n\t"
    "v_mfma_f32_16x16x32_bf16 v[66:69], v[150:153], v[196:199], v[66:69]\n\t"
    "s_waitcnt lgkmcnt(3)\n\t"
    "ds_read_b128 v[150:153], v4 offset:34816\n\t"
    "v_mfma_f32_16x16x32_bf16 v[70:73], v[158:161], v[192:195], v[70:73]\n\t"
    "v_mfma_f32_16x16x32_bf16 v[70:73], v[162:165], v[200:203], v[70:73]\n\t"
    "v_mfma_f32_16x16x32_bf16 v[74:77], v[162:165], v[204:207], v[74:77]\n\t"
    "ds_read_b128 v[162:165], v4 offset:58368\n\t"
    "v_mfma_f32_16x16x32_bf16 v[74:77], v[158:161], v[196:199], v[74:77]\n\t"
    "s_waitcnt lgkmcnt(3)\n\t"
    "ds_read_b128 v[158:161], v4 offset:39936\n\t"
    "v_mfma_f32_16x16x32_bf16 v[62:65], v[142:145], v[208:211], v[62:65]\n\t"
    "v_mfma_f32_16x16x32_bf16 v[62:65], v[146:149], v[216:219], v[62:65]\n\t"
    "v_mfma_f32_16x16x32_bf16 v[66:69], v[146:149], v[220:223], v[66:69]\n\t"
    "ds_read_b128 v[146:149], v4 offset:57344\n\t"
    "v_mfma_f32_16x16x32_bf16 v[66:69], v[142:145], v[212:215], v[66:69]\n\t"
    "s_waitcnt lgkmcnt(3)\n\t"
    "ds_read_b128 v[142:145], v4 offset:38912\n\t"
    "v_mfma_f32_16x16x32_bf16 v[70:73], v[150:153], v[208:211], v[70:73]\n\t"
    "v_mfma_f32_16x16x32_bf16 v[70:73], v[154:157], v[216:219], v[70:73]\n\t"
    "s_waitcnt vmcnt(8)\n\t"
    "v_max3_u32 v50, v82, v83, 0\n\t"
    "v_max3_u32 v51, v84, v85, 0\n\t"
    "v_mfma_f32_16x16x32_bf16 v[74:77], v[154:157], v[220:223], v[74:77]\n\t"
    "ds_read_b128 v[154:157], v4 offset:31744\n\t"
    "s_waitcnt vmcnt(7)\n\t"
    "v_max3_u32 v50, v86, v87, v50\n\t"
    "v_mfma_f32_16x16x32_bf16 v[74:77], v[150:153], v[212:215], v[74:77]\n\t"
    "s_waitcnt lgkmcnt(3)\n\t"
    "ds_read_b128 v[150:153], v4 offset:60416\n\t"
    "v_max3_u32 v51, v88, v89, v51\n\t"
    "v_mfma_f32_16x16x32_bf16 v[62:65], v[158:161], v[224:227], v[62:65]\n\t"
    "s_waitcnt vmcnt(6)\n\t"
    "v_max3_u32 v50, v90, v91, v50\n\t"
    "s_waitcnt vmcnt(5)\n\t"
    "v_max3_u32 v51, v92, v93, v51\n\t"
    "v_mfma_f32_16x16x32_bf16 v[62:65], v[162:165], v[232:235], v[62:65]\n\t"
    "v_max3_u32 v50, v94, v95, v50\n\t"
    "s_waitcnt vmcnt(4)\n\t"
    "v_max3_u32 v51, v96, v97, v51\n\t"
    "v_mfma_f32_16x16x32_bf16 v[66:69], v[162:165], v[236:239], v[66:69]\n\t"
    "ds_read_b128 v[162:165], v4 offset:30720\n\t"
    "v_max3_u32 v50, v98, v99, v50\n\t"
    "v_mfma_f32_16x16x32_bf16 v[66:69], v[158:161], v[228:231], v[66:69]\n\t"
    "s_waitcnt lgkmcnt(3)\n\t"
    "ds_read_b128 v[158:161], v4 offset:59392\n\t"
    "s_waitcnt vmcnt(3)\n\t"
    "v_max3_u32 v51, v100, v101, v51\n\t"
    "v_mfma_f32_16x16x32_bf16 v[70:73], v[142:145], v[224:227], v[70:73]\n\t"
    "s_waitcnt vmcnt(2)\n\t"
    "v_max3_u32 v50, v40, v41, v50\n\t"
    "v_max3_u32 v51, v42, v43, v51\n\t"
    "v_mfma_f32_16x16x32_bf16 v[70:73], v[146:149], v[232:235], v[70:73]\n\t"
    "s_waitcnt vmcnt(1)\n\t"
    "v_max3_u32 v50, v44, v45, v50\n\t"
    "v_max3_u32 v51, v46, v47, v51\n\t"
    "v_mfma_f32_16x16x32_bf16 v[74:77], v[146:149], v[236:239], v[74:77]\n\t"
    "s_waitcnt vmcnt(0)\n\t"
    "v_max3_u32 v50, v48, v49, v50\n\t"
    "v_max_u32 v50, v50, v51\n\t"
    "v_mfma_f32_16x16x32_bf16 v[74:77], v[142:145], v[228:231], v[74:77]\n\t"
    "s_waitcnt lgkmcnt(2)\n\t"
    "v_cmp_eq_u32 vcc, -1, v50\n\t"
    "s_cbranch_vccnz LSX_%=\n\t"
    "LRX_%=:\n\t"
    "v_mfma_f32_16x16x32_bf16 v[62:65], v[150:153], v[240:243], v[62:65]\n\t"
    "v_mfma_f32_16x16x32_bf16 v[62:65], v[154:157], v[248:251], v[62:65]\n\t"
    "v_mfma_f32_16x16x32_bf16 v[66:69], v[154:157], v[252:255], v[66:69]\n\t"
    "v_mfma_f32_16x16x32_bf16 v[66:69], v[150:153], v[244:247], v[66:69]\n\t"
    "s_waitcnt lgkmcnt(0)\n\t"
    "v_mfma_f32_16x16x32_bf16 v[70:73], v[158:161], v[240:243], v[70:73]\n\t"
    "v_mfma_f32_16x16x32_bf16 v[70:73], v[162:165], v[248:251], v[70:73]\n\t"
    "v_mfma_f32_16x16x32_bf16 v[74:77], v[162:165], v[252:255], v[74:77]\n\t"
    "v_mfma_f32_16x16x32_bf16 v[74:77], v[158:161], v[244:247], v[74:77]\n\t"
    "s_nop 0\n\t"
    "v_dot2_f32_bf16 v62, v82, s56, v62\n\t"
    "v_dot2_f32_bf16 v63, v82, s57, v63\n\t"
    "v_dot2_f32_bf16 v64, v83, s56, v64\n\t"
    "v_dot2_f32_bf16 v65, v83, s57, v65\n\t"
    "v_dot2_f32_bf16 v66, v84, s56, v66\n\t"
    "v_dot2_f32_bf16 v67, v84, s57, v67\n\t"
    "v_dot2_f32_bf16 v68, v85, s56, v68\n\t"
    "v_dot2_f32_bf16 v69, v85, s57, v69\n\t"
    "v_dot2_f32_bf16 v70, v86, s56, v70\n\t"
    "v_dot2_f32_bf16 v71, v86, s57, v71\n\t"
    "v_dot2_f32_bf16 v72, v87, s56, v72\n\t"
    "v_dot2_f32_bf16 v73, v87, s57, v73\n\t"
    "v_dot2_f32_bf16 v74, v88, s56, v74\n\t"
    "v_dot2_f32_bf16 v75, v88, s57, v75\n\t"
    "v_dot2_f32_bf16 v76, v89, s56, v76\n\t"
    "v_dot2_f32_bf16 v77, v89, s57, v77\n\t"
    "v_dot2_f32_bf16 v78, v90, s56, v78\n\t"
    "v_dot2_f32_bf16 v79, v90, s57, v79\n\t"
    "v_dot2_f32_bf16 v80, v91, s56, v80\n\t"
    "v_dot2_f32_bf16 v81, v91, s57, v81\n\t"
    "v_dot2_f32_bf16 v62, v92, s56, v62\n\t"
    "v_dot2_f32_bf16 v63, v92, s57, v63\n\t"
    "v_dot2_f32_bf16 v64, v93, s56, v64\n\t"
    "v_dot2_f32_bf16 v65, v93, s57, v65\n\t"
    "v_dot2_f32_bf16 v66, v94, s56, v66\n\t"
    "v_dot2_f32_bf16 v67, v94, s57, v67\n\t"
    "v_dot2_f32_bf16 v68, v95, s56, v68\n\t"
    "v_dot2_f32_bf16 v69, v95, s57, v69\n\t"
    "v_dot2_f32_bf16 v70, v96, s56, v70\n\t"
    "v_dot2_f32_bf16 v71, v96, s57, v71\n\t"
    "v_dot2_f32_bf16 v72, v97, s56, v72\n\t"
    "v_dot2_f32_bf16 v73, v97, s57, v73\n\t"
    "v_dot2_f32_bf16 v74, v98, s56, v74\n\t"
    "v_dot2_f32_bf16 v75, v98, s57, v75\n\t"
    "v_dot2_f32_bf16 v76, v99, s56, v76\n\t"
    "v_dot2_f32_bf16 v77, v99, s57, v77\n\t"
    "v_dot2_f32_bf16 v78, v100, s56, v78\n\t"
    "v_dot2_f32_bf16 v79, v100, s57, v79\n\t"
    "v_dot2_f32_bf16 v80, v101, s56, v80\n\t"
    "v_dot2_f32_bf16 v81, v101, s57, v81\n\t"
    "v_dot2_f32_bf16 v62, v40, s56, v62\n\t"
    "v_dot2_f32_bf16 v63, v40, s57, v63\n\t"
    "v_dot2_f32_bf16 v64, v41, s56, v64\n\t"
    "v_dot2_f32_bf16 v65, v41, s57, v65\n\t"
    "v_dot2_f32_bf16 v66, v42, s56, v66\n\t"
    "v_dot2_f32_bf16 v67, v42, s57, v67\n\t"
    "v_dot2_f32_bf16 v68, v43, s56, v68\n\t"
    "v_dot2_f32_bf16 v69, v43, s57, v69\n\t"
    "v_dot2_f32_bf16 v70, v44, s56, v70\n\t"
    "v_dot2_f32_bf16 v71, v44, s57, v71\n\t"
    "v_dot2_f32_bf16 v72, v45, s56, v72\n\t"
    "v_dot2_f32_bf16 v73, v45, s57, v73\n\t"
    "v_dot2_f32_bf16 v74, v46, s56, v74\n\t"
    "v_dot2_f32_bf16 v75, v46, s57, v75\n\t"
    "v_dot2_f32_bf16 v76, v47, s56, v76\n\t"
    "v_dot2_f32_bf16 v77, v47, s57, v77\n\t"
    "v_dot2_f32_bf16 v78, v48, s56, v78\n\t"
    "v_dot2_f32_bf16 v79, v48, s57, v79\n\t"
    "v_dot2_f32_bf16 v80, v49, s56, v80\n\t"
    "v_dot2_f32_bf16 v81, v49, s57, v81\n\t"
    "v_pk_fma_f32 v[122:123], v[62:63], v[102:103], v[122:123]\n\t"
    "v_pk_fma_f32 v[124:125], v[64:65], v[104:105], v[124:125]\n\t"
    "v_pk_fma_f32 v[130:131], v[70:71], v[110:111], v[130:131]\n\t"
    "v_pk_fma_f32 v[132:133], v[72:73], v[112:113], v[132:133]\n\t"
    "v_cvt_pk_bf16_f32 v82, v122, v123\n\t"
    "v_cvt_pk_bf16_f32 v83, v124, v125\n\t"
    "v_cvt_pk_bf16_f32 v84, v130, v131\n\t"
    "v_cvt_pk_bf16_f32 v85, v132, v133\n\t"
    "buffer_store_dwordx4 v[82:85], v11, s[44:47], s48 offen offset:0 sc1\n\t"
    "v_pk_fma_f32 v[126:127], v[66:67], v[106:107], v[126:127]\n\t"
    "v_pk_fma_f32 v[128:129], v[68:69], v[108:109], v[128:129]\n\t"
    "v_pk_fma_f32 v[134:135], v[74:75], v[114:115], v[134:135]\n\t"
    "v_pk_fma_f32 v[136:137], v[76:77], v[116:117], v[136:137]\n\t"
    "v_cvt_pk_bf16_f32 v86, v126, v127\n\t"
    "v_cvt_pk_bf16_f32 v87, v128, v129\n\t"
    "v_cvt_pk_bf16_f32 v88, v134, v135\n\t"
    "v_cvt_pk_bf16_f32 v89, v136, v137\n\t"
    "buffer_store_dwordx4 v[86:89], v11, s[44:47], s49 offen offset:0 sc1\n\t"
    "v_pk_fma_f32 v[138:139], v[78:79], v[118:119], v[138:139]\n\t"
    "v_pk_fma_f32 v[140:141], v[80:81], v[120:121], v[140:141]\n\t"
    "v_cvt_pk_bf16_f32 v90, v138, v139\n\t"
    "v_cvt_pk_bf16_f32 v91, v140, v141\n\t"
    "buffer_store_dwordx2 v[90:91], v174, s[44:47], s50 offen offset:128 sc1\n\t"
    "s_endpgm\n\t"
    "KFASL:\n\t"
    "s_mov_b32 s65, 0x100000\n\t"
    "s_bfm_b64 exec, 8, 0\n\t"
    "KFALL:\n\t"
    "buffer_load_dword v45, v61, s[40:43], s95 offen sc1\n\t"
    "s_sub_u32 s65, s65, 1\n\t"
    "s_waitcnt vmcnt(0)\n\t"
    "s_cbranch_scc1 KFADL\n\t"
    "v_cmp_ne_u32 vcc, s80, v45\n\t"
    "s_cbranch_vccnz KFALL\n\t"
    "KFADL:\n\t"
    "s_mov_b64 exec, -1\n\t"
    "s_branch KFARL\n\t"
    "KFASM:\n\t"
    "s_mov_b32 s65, 0x100000\n\t"
    "s_bfm_b64 exec, 10, 0\n\t"
    "KFALM:\n\t"
    "buffer_load_dword v27, v61, s[40:43], s94 offen sc1\n\t"
    "s_sub_u32 s65, s65, 1\n\t"
    "s_waitcnt vmcnt(0)\n\t"
    "s_cbranch_scc1 KFADM\n\t"
    "v_cmp_ne_u32 vcc, s80, v27\n\t"
    "s_cbranch_vccnz KFALM\n\t"
    "KFADM:\n\t"
    "s_mov_b64 exec, -1\n\t"
    "s_branch KFARM\n\t"
    "KFASW:\n\t"
    "s_mov_b32 s65, 0x100000\n\t"
    "s_bfm_b64 exec, 60, 0\n\t"
    "KFALW:\n\t"
    "buffer_load_dword v27, v61, s[40:43], s93 offen sc1\n\t"
    "s_sub_u32 s65, s65, 1\n\t"
    "s_waitcnt vmcnt(0)\n\t"
    "s_cbranch_scc1 KFADW\n\t"
    "v_cmp_ne_u32 vcc, s80, v27\n\t"
    "s_cbranch_vccnz KFALW\n\t"
    "KFADW:\n\t"
    "s_mov_b64 exec, -1\n\t"
    "s_branch KFARW\n\t"
    "KFASY:\n\t"
    "s_mov_b32 s65, 0x100000\n\t"
    "s_bfm_b64 exec, 12, 0\n\t"
    "KFALY:\n\t"
    "buffer_load_dword v27, v61, s[40:43], s75 offen sc1\n\t"
    "s_sub_u32 s65, s65, 1\n\t"
    "s_waitcnt vmcnt(0)\n\t"
    "s_cbranch_scc1 KFADY\n\t"
    "v_cmp_ne_u32 vcc, s80, v27\n\t"
    "s_cbranch_vccnz KFALY\n\t"
    "KFADY:\n\t"
    "s_mov_b64 exec, -1\n\t"
    "s_branch KFARY\n\t"
    "KFASK:\n\t"
    "s_mov_b32 s65, 0x100000\n\t"
    "s_mov_b32 s63, 49315840\n\t"
    "KFALK:\n\t"
    "buffer_load_dwordx4 v[44:47], v4, s[40:43], s63 offen offset:0 sc1\n\t"
    "buffer_load_dwordx4 v[48:51], v4, s[40:43], s63 offen offset:1024 sc1\n\t"
    "buffer_load_dwordx4 v[52:55], v4, s[40:43], s63 offen offset:2048 sc1\n\t"
    "buffer_load_dwordx4 v[56:59], v4, s[40:43], s63 offen offset:3072 sc1\n\t"
    "s_sub_u32 s65, s65, 1\n\t"
    "s_waitcnt vmcnt(0)\n\t"
    "s_cbranch_scc1 KFARK\n\t"
    "v_xor_b32 v45, s80, v45\n\t"
    "v_xor_b32 v47, s80, v47\n\t"
    "v_xor_b32 v49, s80, v49\n\t"
    "v_xor_b32 v51, s80, v51\n\t"
    "v_xor_b32 v53, s80, v53\n\t"
    "v_xor_b32 v55, s80, v55\n\t"
    "v_xor_b32 v57, s80, v57\n\t"
    "v_xor_b32 v59, s80, v59\n\t"
    "v_or3_b32 v45, v45, v47, v49\n\t"
    "v_or3_b32 v51, v51, v53, v55\n\t"
    "v_or3_b32 v45, v45, v57, v59\n\t"
    "v_or_b32 v45, v45, v51\n\t"
    "v_cmp_ne_u32 vcc, 0, v45\n\t"
    "s_cbranch_vccnz KFALK\n\t"
    "s_branch KFARK\n\t"
    "LS0_%=:\n\t"
    "s_mov_b32 s63, 0x100000\n\t"
    "s_mov_b32 s64, 49643520\n\t"
    "s_mov_b64 exec, 0x7f\n\t"
    "LL0_%=:\n\t"
    "buffer_load_dword v59, v7, s[40:43], s64 offen sc1\n\t"
    "s_sub_u32 s63, s63, 1\n\t"
    "s_waitcnt vmcnt(0)\n\t"
    "s_cbranch_scc1 LD0_%=\n\t"
    "v_cmp_ne_u32 vcc, s80, v59\n\t"
    "s_cbranch_vccnz LL0_%=\n\t"
    "LD0_%=:\n\t"
    "s_mov_b64 exec, -1\n\t"
    "s_branch LR0_%=\n\t"
    "LS2_%=:\n\t"
    "s_mov_b32 s63, 0x100000\n\t"
    "s_mov_b32 s64, 49709056\n\t"
    "s_mov_b64 exec, 0xf07f\n\t"
    "LL2_%=:\n\t"
    "buffer_load_dword v59, v7, s[40:43], s64 offen sc1\n\t"
    "s_sub_u32 s63, s63, 1\n\t"
    "s_waitcnt vmcnt(0)\n\t"
    "s_cbranch_scc1 LD2_%=\n\t"
    "v_cmp_ne_u32 vcc, s80, v59\n\t"
    "s_cbranch_vccnz LL2_%=\n\t"
    "LD2_%=:\n\t"
    "s_mov_b64 exec, -1\n\t"
    "s_branch LR2_%=\n\t"
    "LS4_%=:\n\t"
    "s_mov_b32 s63, 0x100000\n\t"
    "s_mov_b32 s64, 49774592\n\t"
    "s_mov_b64 exec, 0xe7f\n\t"
    "LL4_%=:\n\t"
    "buffer_load_dword v59, v7, s[40:43], s64 offen sc1\n\t"
    "s_sub_u32 s63, s63, 1\n\t"
    "s_waitcnt vmcnt(0)\n\t"
    "s_cbranch_scc1 LD4_%=\n\t"
    "v_cmp_ne_u32 vcc, s80, v59\n\t"
    "s_cbranch_vccnz LL4_%=\n\t"
    "LD4_%=:\n\t"
    "s_mov_b64 exec, -1\n\t"
    "s_branch LR4_%=\n\t"
    "LS5_%=:\n\t"
    "s_mov_b32 s63, 0x100000\n\t"
    "s_mov_b32 s64, 49807360\n\t"
    "s_mov_b64 exec, 0x7f\n\t"
    "LL5_%=:\n\t"
    "buffer_load_dword v59, v7, s[40:43], s64 offen sc1\n\t"
    "s_sub_u32 s63, s63, 1\n\t"
    "s_waitcnt vmcnt(0)\n\t"
    "s_cbranch_scc1 LD5_%=\n\t"
    "v_cmp_ne_u32 vcc, s80, v59\n\t"
    "s_cbranch_vccnz LL5_%=\n\t"
    "LD5_%=:\n\t"
    "s_mov_b64 exec, -1\n\t"
    "s_branch LR5_%=\n\t"
    "LSX_%=:\n\t"
    "s_mov_b32 s63, 0x100000\n\t"
    "LLX_%=:\n\t"
    "s_add_u32 s62, s85, 10240\n\t"
    "buffer_load_dwordx4 v[82:85], v4, s[40:43], s62 offen offset:0 sc1\n\t"
    "buffer_load_dwordx4 v[86:89], v4, s[40:43], s62 offen offset:1024 sc1\n\t"
    "buffer_load_dwordx2 v[90:91], v10, s[40:43], s62 offen offset:2048 sc1\n\t"
    "s_add_u32 s62, s85, 20480\n\t"
    "buffer_load_dwordx4 v[92:95], v4, s[40:43], s62 offen offset:0 sc1\n\t"
    "buffer_load_dwordx4 v[96:99], v4, s[40:43], s62 offen offset:1024 sc1\n\t"
    "buffer_load_dwordx2 v[100:101], v10, s[40:43], s62 offen offset:2048 sc1\n\t"
    "s_add_u32 s62, s85, 0\n\t"
    "buffer_load_dwordx4 v[40:43], v4, s[40:43], s62 offen offset:0 sc1\n\t"
    "buffer_load_dwordx4 v[44:47], v4, s[40:43], s62 offen offset:1024 sc1\n\t"
    "buffer_load_dwordx2 v[48:49], v10, s[40:43], s62 offen offset:2048 sc1\n\t"
    "s_sub_u32 s63, s63, 1\n\t"
    "s_waitcnt vmcnt(0)\n\t"
    "s_cbranch_scc1 LRX_%=\n\t"
    "v_max3_u32 v50, v82, v83, 0\n\t"
    "v_max3_u32 v51, v84, v85, 0\n\t"
    "v_max3_u32 v50, v86, v87, v50\n\t"
    "v_max3_u32 v51, v88, v89, v51\n\t"
    "v_max3_u32 v50, v90, v91, v50\n\t"
    "v_max3_u32 v51, v92, v93, v51\n\t"
    "v_max3_u32 v50, v94, v95, v50\n\t"
    "v_max3_u32 v51, v96, v97, v51\n\t"
    "v_max3_u32 v50, v98, v99, v50\n\t"
    "v_max3_u32 v51, v100, v101, v51\n\t"
    "v_max3_u32 v50, v40, v41, v50\n\t"
    "v_max3_u32 v51, v42, v43, v51\n\t"
    "v_max3_u32 v50, v44, v45, v50\n\t"
    "v_max3_u32 v51, v46, v47, v51\n\t"
    "v_max3_u32 v50, v48, v49, v50\n\t"
    "v_max_u32 v50, v50, v51\n\t"
    "v_cmp_eq_u32 vcc, -1, v50\n\t"
    "s_cbranch_vccnz LLX_%=\n\t"
    "s_branch LRX_%=\n\t"
    "KFP18:\n\t"
    "s_branch KFR2\n\t"
    "LE_%=:\n\t"
      : CARRY, KEPT("+"), "+{v[24:55]}"(k0), REDO("+") :: CLOB_V, "vcc", "scc", "memory");
  }

  asm volatile("KF_END:");
}
#undef x

extern "C" void kernel_launch(void* const* d_in, const int* in_sizes, int n_in, void* d_out, int out_size, void* d_ws, size_t ws_size,
                              hipStream_t stream) {
  (void)hipFuncSetAttribute((const void*)kf_fused, hipFuncAttributeMaxDynamicSharedMemorySize, 163840);
  kf_fused<<<256, 256, 163840, stream>>>((const u16*)d_in[0], (char*)d_ws + ((size_t)WSOFF << 20), (const u16*)d_in[3], (const u16*)d_in[6], (const u16*)d_in[1], (const u16*)d_in[2],
                                         (const u16*)d_in[4], (const u16*)d_in[7], (const u16*)d_in[8], (const u16*)d_in[9], (u16*)d_out);
}
```
